```python
import torch

def _softmax(x, axis=-1):
    return torch.softmax(x, dim=axis)

def _gelu(x):
    return torch.nn.functional.gelu(x, approximate='tanh')

def _silu(x):
    return torch.nn.functional.silu(x)

def _sigmoid(x):
    return torch.sigmoid(x)

def _layernorm(x, axis=-1, eps=1e-05):
    m = x.mean(dim=axis, keepdim=True)
    v = x.var(dim=axis, keepdim=True, unbiased=False)
    return (x - m) / torch.sqrt(v + eps)

def _scatter_add(tgt, idx, src, axis=0):
    return tgt.clone().index_add_(axis, idx, src)

def _segment_sum(src, seg, n):
    out = torch.zeros((n,) + src.shape[1:], dtype=src.dtype)
    return out.index_add_(0, seg, src)

def _pad(x, pw):
    flat = tuple((v for pair in reversed(pw) for v in pair))
    return torch.nn.functional.pad(x, flat)
kernel_name = 'sequential_preLN__grouped_kv_mqa__mixer_gmlp__prompt'

def setup_inputs():

    _w = lambda a: (lambda t: t.to(torch.bfloat16) if t.is_floating_point() else t)(torch.as_tensor(a))

    def _A_setup():
        B, S, D, H = (1, 2048, 1024, 32)
        Dh = D // H
        return {'x': _w((lambda *s: torch.randn(*s, dtype=torch.bfloat16) * 0.05)(B, S, D)), 'wq': _w((lambda *s: torch.randn(*s, dtype=torch.bfloat16) * 0.05)(D, H * Dh)), 'wk': _w((lambda *s: torch.randn(*s, dtype=torch.bfloat16) * 0.05)(D, Dh)), 'wv': _w((lambda *s: torch.randn(*s, dtype=torch.bfloat16) * 0.05)(D, Dh)), 'wo': _w((lambda *s: torch.randn(*s, dtype=torch.bfloat16) * 0.05)(H * Dh, D))}
    _A_in = _A_setup()

    def _B_setup():
        B, S, D, F = (1, 2048, 1024, 3584)
        Fh = F // 2
        return {'x': _w((lambda *s: torch.randn(*s, dtype=torch.bfloat16) * 0.05)(B, S, D)), 'w_in': _w((lambda *s: torch.randn(*s, dtype=torch.bfloat16) * 1.6)(D, 2 * Fh)), 'w_sp': _w(0.01 * (lambda *s: torch.randn(*s, dtype=torch.bfloat16) * 1.6)(S, S)), 'b_sp': _w(1.0 + 0.01 * (lambda *s: torch.randn(*s, dtype=torch.bfloat16) * 0.05)(S)), 'w_out': _w((lambda *s: torch.randn(*s, dtype=torch.bfloat16) * 0.05)(Fh, D))}
    _B_in = _B_setup()
    return {'A_x': _A_in['x'], 'A_wq': _A_in['wq'], 'A_wk': _A_in['wk'], 'A_wv': _A_in['wv'], 'A_wo': _A_in['wo'], 'B_x': _B_in['x'], 'B_w_in': _B_in['w_in'], 'B_w_sp': _B_in['w_sp'], 'B_b_sp': _B_in['b_sp'], 'B_w_out': _B_in['w_out']}

def reference(A_x, A_wq, A_wk, A_wv, A_wo, B_x, B_w_in, B_w_sp, B_b_sp, B_w_out):

    def _A_ref(x, wq, wk, wv, wo):
        B, S, D = x.shape
        H = 32
        Dh = D // H
        q = (lambda x, *d: x.permute(*d))((x @ wq).reshape(B, S, H, Dh), 0, 2, 1, 3)
        k = x @ wk
        v = x @ wv
        scores = torch.einsum('bhqd,bkd->bhqk', q, k) * Dh ** (-0.5)
        p = _softmax(scores, axis=-1)
        o = torch.einsum('bhqk,bkd->bhqd', p, v)
        return (lambda x, *d: x.permute(*d))(o, 0, 2, 1, 3).reshape(B, S, H * Dh) @ wo

    def _B_ref(x, w_in, w_sp, b_sp, w_out):
        B, S, D = x.shape
        F = w_in.shape[-1]
        Fh = F // 2
        z = _gelu(torch.einsum('bsd,df->bsf', x, w_in))
        z1 = z[..., :Fh].clone()
        z2 = _layernorm(z[..., Fh:], axis=-1)
        g = torch.einsum('ts,bsf->btf', w_sp, z2) + b_sp[None, :, None]
        return torch.einsum('bsf,fd->bsd', z1 * g, w_out)
    x = A_x
    h = x + _A_ref(_layernorm(x, axis=-1), A_wq, A_wk, A_wv, A_wo)
    h2 = h + _B_ref(_layernorm(h, axis=-1), B_w_in, B_w_sp, B_b_sp, B_w_out)
    return h2
```

```cpp
#include <hip/hip_runtime.h>
#include <hip/hip_bf16.h>
typedef unsigned short u16;
static __device__ __forceinline__ float bf2f(u16 b) { return __uint_as_float(((unsigned)b) << 16); }
static __device__ __forceinline__ u16 f2bf(float f) { __hip_bfloat16 h = __float2bfloat16(f); return *(u16*)&h; }

__global__ void k_ln(const u16* in, int ld, int n, u16* out, int ldo) {
    __shared__ float red[2][4];
    const u16* p = in + (size_t)blockIdx.x * ld;
    float s = 0.f;
    for (int i = threadIdx.x; i < n; i += 256) s += bf2f(p[i]);
    for (int o = 32; o; o >>= 1) s += __shfl_xor(s, o);
    if ((threadIdx.x & 63) == 0) red[0][threadIdx.x >> 6] = s;
    __syncthreads();
    float m = (red[0][0] + red[0][1] + red[0][2] + red[0][3]) / n;
    float q = 0.f;
    for (int i = threadIdx.x; i < n; i += 256) { float d = bf2f(p[i]) - m; q += d * d; }
    for (int o = 32; o; o >>= 1) q += __shfl_xor(q, o);
    if ((threadIdx.x & 63) == 0) red[1][threadIdx.x >> 6] = q;
    __syncthreads();
    float r = rsqrtf((red[1][0] + red[1][1] + red[1][2] + red[1][3]) / n + 1e-5f);
    for (int i = threadIdx.x; i < n; i += 256) out[(size_t)blockIdx.x * ldo + i] = f2bf((bf2f(p[i]) - m) * r);
}
template <int MODE>
__global__ void k_gemm(const u16* A, int lda, const u16* B, int ldb, u16* C, int ldc, int K, const u16* aux, int ldaux, const u16* bias) {
    __shared__ float As[32][33], Bs[32][33];
    int tx = threadIdx.x & 31, ty = threadIdx.x >> 5;
    int r0 = blockIdx.y * 32, c0 = blockIdx.x * 32;
    float acc[4] = {0, 0, 0, 0};
    for (int k0 = 0; k0 < K; k0 += 32) {
        for (int i = 0; i < 4; ++i) {
            As[ty + 8 * i][tx] = bf2f(A[(size_t)(r0 + ty + 8 * i) * lda + k0 + tx]);
            Bs[ty + 8 * i][tx] = bf2f(B[(size_t)(k0 + ty + 8 * i) * ldb + c0 + tx]);
        }
        __syncthreads();
        for (int k = 0; k < 32; ++k) { float b = Bs[k][tx];
            for (int i = 0; i < 4; ++i) acc[i] += As[ty + 8 * i][k] * b; }
        __syncthreads();
    }
    for (int i = 0; i < 4; ++i) {
        int r = r0 + ty + 8 * i, c = c0 + tx; float v = acc[i];
        if (MODE == 1) v += bf2f(aux[(size_t)r * ldaux + c]);
        if (MODE == 2) v = 0.5f * v * (1.f + tanhf(0.7978845608028654f * (v + 0.044715f * v * v * v)));
        if (MODE == 3) v = (v + bf2f(bias[r])) * bf2f(aux[(size_t)r * ldaux + c]);
        C[(size_t)r * ldc + c] = f2bf(v);
    }
}
__global__ void k_attn(const u16* q, const u16* k, const u16* v, u16* o) {
    int row = blockIdx.x * 4 + (threadIdx.x >> 6), lane = threadIdx.x & 63;
    int t = row >> 5, h = row & 31;
    float qq[32];
    for (int d = 0; d < 32; ++d) qq[d] = bf2f(q[t * 1024 + h * 32 + d]) * 0.17677669529663687f;
    float m = -1e30f, l = 0.f, acc[32];
    for (int d = 0; d < 32; ++d) acc[d] = 0.f;
    for (int j = lane; j < 2048; j += 64) {
        float s = 0.f;
        for (int d = 0; d < 32; ++d) s += qq[d] * bf2f(k[j * 32 + d]);
        float mn = fmaxf(m, s), a = __expf(m - mn), p = __expf(s - mn);
        l = l * a + p;
        for (int d = 0; d < 32; ++d) acc[d] = acc[d] * a + p * bf2f(v[j * 32 + d]);
        m = mn;
    }
    float mg = m;
    for (int o2 = 32; o2; o2 >>= 1) mg = fmaxf(mg, __shfl_xor(mg, o2));
    float a = __expf(m - mg); l *= a;
    for (int o2 = 32; o2; o2 >>= 1) l += __shfl_xor(l, o2);
    for (int d = 0; d < 32; ++d) { float x = acc[d] * a; for (int o2 = 32; o2; o2 >>= 1) x += __shfl_xor(x, o2); if (lane == d) o[t * 1024 + h * 32 + d] = f2bf(x / l); }
}
extern "C" void kernel_launch(void* const* d_in, const int* in_sizes, int n_in, void* d_out, int out_size, void* d_ws, size_t ws_size, hipStream_t stream) {
    const u16 *x = (const u16*)d_in[0], *wq = (const u16*)d_in[1], *wk = (const u16*)d_in[2], *wv = (const u16*)d_in[3], *wo = (const u16*)d_in[4];
    const u16 *win = (const u16*)d_in[6], *wsp = (const u16*)d_in[7], *bsp = (const u16*)d_in[8], *wout = (const u16*)d_in[9];
    u16* ws = (u16*)d_ws; const size_t M1 = 2048 * 1024;
    u16 *xn = ws, *q = ws + M1, *k = ws + 2 * M1, *v = k + 2048 * 32, *o = ws + 3 * M1, *h = ws + 4 * M1, *hn = ws + 5 * M1, *z = ws + 6 * M1, *z2n = ws + 10 * M1, *u = ws + 12 * M1;
    k_ln<<<2048, 256, 0, stream>>>(x, 1024, 1024, xn, 1024);
    k_gemm<0><<<dim3(32, 64), 256, 0, stream>>>(xn, 1024, wq, 1024, q, 1024, 1024, nullptr, 0, nullptr);
    k_gemm<0><<<dim3(1, 64), 256, 0, stream>>>(xn, 1024, wk, 32, k, 32, 1024, nullptr, 0, nullptr);
    k_gemm<0><<<dim3(1, 64), 256, 0, stream>>>(xn, 1024, wv, 32, v, 32, 1024, nullptr, 0, nullptr);
    k_attn<<<2048 * 32 / 4, 256, 0, stream>>>(q, k, v, o);
    k_gemm<1><<<dim3(32, 64), 256, 0, stream>>>(o, 1024, wo, 1024, h, 1024, 1024, x, 1024, nullptr);
    k_ln<<<2048, 256, 0, stream>>>(h, 1024, 1024, hn, 1024);
    k_gemm<2><<<dim3(112, 64), 256, 0, stream>>>(hn, 1024, win, 3584, z, 3584, 1024, nullptr, 0, nullptr);
    k_ln<<<2048, 256, 0, stream>>>(z + 1792, 3584, 1792, z2n, 1792);
    k_gemm<3><<<dim3(56, 64), 256, 0, stream>>>(wsp, 2048, z2n, 1792, u, 1792, 2048, z, 3584, bsp);
    k_gemm<1><<<dim3(32, 64), 256, 0, stream>>>(u, 1792, wout, 1024, (u16*)d_out, 1024, 1792, h, 1024, nullptr);
}
```

```cpp
#include <hip/hip_runtime.h>

__global__ __attribute__((aligned(4096))) __launch_bounds__(256, 1) void sequential_preLN__grouped_kv_mqa__mixer_gmlp__prompt_kernel(
    const void* x, const void* wq, const void* wk, const void* wv, const void* wo, const void* w_in, const void* w_sp,
    const void* b_sp, const void* w_out, void* out, void* ws, void* ws_pad) {
  __shared__ __attribute__((aligned(1024))) char lds[163840];
  asm volatile("s_mov_b64 s[4:5], %0\n s_mov_b32 s6, %1\n v_mov_b32 v0, %2\n .p2align 3\n"
"s_getpc_b64 s[70:71]\n"
"LPC_%=:\n"
"s_load_dwordx16 s[72:87], s[4:5], 0x0\n"
"s_load_dwordx8 s[88:95], s[4:5], 0x40\n"
"v_lshrrev_b32 v5, 6, v0\n"
"v_and_b32 v1, 63, v0\n"
"v_readfirstlane_b32 s7, v5\n"
"s_lshr_b32 s58, s6, 3\n"
"s_and_b32 s59, s58, 3\n"
"s_lshl_b32 s59, s59, 5\n"
"v_and_b32 v6, 7, v0\n"
"v_lshl_add_u32 v6, v5, 3, v6\n"
"v_add_u32 v7, s59, v6\n"
"v_lshlrev_b32 v7, 7, v7\n"
"global_load_dword a255, v7, s[70:71]\n"
"s_lshl_b32 s59, s58, 5\n"
"v_add_u32 v0, s59, v6\n"
"v_lshlrev_b32 v0, 7, v0\n"
"v_lshlrev_b32 v2, 4, v1\n"
"v_and_b32 v3, 15, v1\n"
"v_lshrrev_b32 v4, 4, v1\n"
"s_and_b32 s57, s6, 7\n"
"s_lshr_b32 s8, s57, 1\n"
"s_lshl_b32 s8, s8, 2\n"
"s_lshr_b32 s59, s58, 3\n"
"s_add_u32 s8, s8, s59\n"
"s_and_b32 s9, s57, 1\n"
"s_lshl_b32 s9, s9, 3\n"
"s_and_b32 s59, s58, 7\n"
"s_add_u32 s9, s9, s59\n"
"s_lshl_b32 s57, s8, 3\n"
"s_lshr_b32 s58, s9, 1\n"
"s_add_u32 s61, s57, s58\n"
"s_lshl_b32 s57, s61, 15\n"
"s_lshl_b32 s58, s7, 9\n"
"s_add_u32 s57, s57, s58\n"
"s_lshl_b32 s58, s7, 13\n"
"v_lshrrev_b32 v32, 5, v1\n"
"v_and_b32 v33, 31, v1\n"
"v_xor_b32 v33, v33, v32\n"
"v_lshlrev_b32 v33, 4, v33\n"
"v_lshl_add_u32 v36, v32, 11, v33\n"
"s_mov_b32 s14, 4194304\n"
"s_mov_b32 s15, 0x20000\n"
"v_xor_b32 v37, 4128, v36\n"
"v_xor_b32 v38, 8256, v36\n"
"v_xor_b32 v39, 12384, v36\n"
"v_xor_b32 v40, 16512, v36\n"
"v_xor_b32 v41, 20640, v36\n"
"v_xor_b32 v42, 24768, v36\n"
"v_xor_b32 v43, 28896, v36\n"
"s_waitcnt lgkmcnt(0)\n"
"s_mov_b32 s12, s72\n"
"s_and_b32 s13, s73, 0xffff\n"
"s_add_u32 m0, s58, 0\n"
"s_nop 0\n"
"buffer_load_dwordx4 v36, s[12:15], s57 offen lds\n"
"s_add_u32 m0, s58, 1024\n"
"s_nop 0\n"
"buffer_load_dwordx4 v37, s[12:15], s57 offen lds\n"
"s_add_u32 m0, s58, 2048\n"
"s_nop 0\n"
"buffer_load_dwordx4 v38, s[12:15], s57 offen lds\n"
"s_add_u32 m0, s58, 3072\n"
"s_nop 0\n"
"buffer_load_dwordx4 v39, s[12:15], s57 offen lds\n"
"s_add_u32 m0, s58, 4096\n"
"s_nop 0\n"
"buffer_load_dwordx4 v40, s[12:15], s57 offen lds\n"
"s_add_u32 m0, s58, 5120\n"
"s_nop 0\n"
"buffer_load_dwordx4 v41, s[12:15], s57 offen lds\n"
"s_add_u32 m0, s58, 6144\n"
"s_nop 0\n"
"buffer_load_dwordx4 v42, s[12:15], s57 offen lds\n"
"s_add_u32 m0, s58, 7168\n"
"s_nop 0\n"
"buffer_load_dwordx4 v43, s[12:15], s57 offen lds\n"
"s_mov_b32 s48, s92\n"
"s_and_b32 s49, s93, 0xffff\n"
"s_mov_b32 s50, 57671680\n"
"s_mov_b32 s51, 0x20000\n"
"s_mov_b32 s16, s74\n"
"s_and_b32 s17, s75, 0xffff\n"
"s_mov_b32 s18, 2097152\n"
"s_mov_b32 s19, 0x20000\n"
"s_bitcmp1_b32 s9, 0\n"
"s_cselect_b32 s76, s78, s76\n"
"s_cselect_b32 s77, s79, s77\n"
"s_mov_b32 s20, s76\n"
"s_and_b32 s21, s77, 0xffff\n"
"s_mov_b32 s22, 65536\n"
"s_mov_b32 s23, 0x20000\n"
"s_lshl_b32 s60, s7, 14\n"
"s_lshr_b32 s4, s6, 3\n"
"s_lshl_b32 s4, s4, 2\n"
"s_add_u32 s4, s4, s7\n"
"s_lshl_b32 s4, s4, 11\n"
"s_and_b32 s5, s6, 1\n"
"s_lshl_b32 s5, s5, 10\n"
"s_add_u32 s4, s4, s5\n"
"s_sub_u32 s5, 0x180, s16\n"
"s_and_b32 s5, s5, 0x380\n"
"s_add_u32 s4, s4, s5\n"
"s_add_u32 s4, s4, s16\n"
"s_addc_u32 s5, s17, 0\n"
"s_load_dword s3, s[4:5], 0\n"
"s_load_dword s3, s[4:5], 64\n"
"s_load_dword s3, s[4:5], 262144\n"
"s_load_dword s3, s[4:5], 262208\n"
"s_load_dword s3, s[4:5], 524288\n"
"s_load_dword s3, s[4:5], 524352\n"
"s_load_dword s3, s[4:5], 786432\n"
"s_load_dword s3, s[4:5], 786496\n"
"s_add_u32 s4, s4, 0x100000\n"
"s_addc_u32 s5, s5, 0\n"
"s_load_dword s3, s[4:5], 0\n"
"s_load_dword s3, s[4:5], 64\n"
"s_load_dword s3, s[4:5], 262144\n"
"s_load_dword s3, s[4:5], 262208\n"
"s_load_dword s3, s[4:5], 524288\n"
"s_load_dword s3, s[4:5], 524352\n"
"s_lshr_b32 s4, s6, 1\n"
"s_lshl2_add_u32 s4, s4, s7\n"
"s_lshl_b32 s4, s4, 12\n"
"s_and_b32 s5, s6, 1\n"
"s_lshl_b32 s5, s5, 10\n"
"s_add_u32 s4, s4, s5\n"
"v_and_b32 v63, 32, v1\n"
"v_add_u32 v63, v63, v1\n"
"v_lshlrev_b32 v63, 5, v63\n"
"buffer_load_dword a254, v63, s[16:19], s4 offen\n"
"buffer_load_dwordx2 v[12:13], off, s[48:51], 0 sc1\n"
"s_lshl2_add_u32 s62, s6, s7\n"
"s_lshl_b32 s62, s62, 6\n"
"s_add_u32 s62, s62, 57081856\n"
"v_and_b32 v63, 0, v1\n"
"v_lshlrev_b32 v63, 6, v63\n"
"v_mov_b32 v62, 0\n"
"s_mov_b64 exec, 1\n"
"buffer_atomic_or v62, v63, s[48:51], s62 offen sc1\n"
"s_mov_b64 exec, -1\n"
"v_add_u32 v63, LPC_%=-LPC_%=, v0\n"
"v_min_u32 v63, LEND_%=-LPC_%=-4, v63\n"
"global_load_dword a255, v63, s[70:71]\n"
"s_lshl_b32 s57, s8, 2\n"
"s_add_u32 s57, s57, s7\n"
"s_lshl_b32 s52, s57, 11\n"
"s_lshl_b32 s54, s57, 7\n"
"s_lshl_b32 s59, s9, 3\n"
"s_add_u32 s53, s54, s59\n"
"s_lshl_b32 s55, s7, 10\n"
"s_lshl_b32 s56, s9, 7\n"
"s_mul_i32 s69, s7, 7168\n"
"v_lshlrev_b32 v10, 3, v3\n"
"v_lshrrev_b32 v32, 3, v1\n"
"v_bfe_u32 v33, v1, 4, 1\n"
"s_and_b32 s57, s7, 1\n"
"s_lshl_b32 s57, s57, 1\n"
"v_or_b32 v33, s57, v33\n"
"v_lshlrev_b32 v33, 1, v33\n"
"v_and_b32 v34, 7, v1\n"
"v_xor_b32 v33, v33, v34\n"
"v_lshlrev_b32 v11, 4, v33\n"
"s_lshl_b32 s57, s7, 3\n"
"v_add_u32 v32, s57, v32\n"
"v_lshl_add_u32 v9, v32, 11, v11\n"
"v_bfe_u32 v32, v1, 2, 2\n"
"v_lshl_add_u32 v35, v4, 3, v32\n"
"v_lshlrev_b32 v33, 7, v35\n"
"v_and_b32 v34, 3, v1\n"
"v_lshl_add_u32 v33, v34, 3, v33\n"
"v_lshlrev_b32 v35, 6, v35\n"
"v_lshl_add_u32 v35, v34, 3, v35\n"
"s_and_b32 s57, s9, 1\n"
"s_lshl_b32 s59, s57, 13\n"
"s_add_u32 s59, s59, s60\n"
"s_add_u32 s59, s59, 0x18000\n"
"v_add_u32 v46, s59, v35\n"
"v_xor_b32 v47, 8192, v46\n"
"v_lshrrev_b32 v32, 1, v32\n"
"v_and_b32 v34, 1, v4\n"
"v_lshl_or_b32 v32, v34, 1, v32\n"
"v_xor_b32 v34, 0, v32\n"
"v_lshl_add_u32 v5, v34, 5, v33\n"
"v_xor_b32 v34, 1, v32\n"
"v_lshl_add_u32 v6, v34, 5, v33\n"
"v_xor_b32 v34, 2, v32\n"
"v_lshl_add_u32 v7, v34, 5, v33\n"
"v_xor_b32 v34, 3, v32\n"
"v_lshl_add_u32 v8, v34, 5, v33\n"
"v_xor_b32 v32, v4, v3\n"
"v_lshlrev_b32 v32, 4, v32\n"
"v_lshl_add_u32 v32, v3, 9, v32\n"
"s_lshl_b32 s59, s57, 8\n"
"s_add_u32 s59, s59, s58\n"
"v_add_u32 v36, s59, v32\n"
"v_mov_b32 v48, 0x3f803f80\n"
"v_mov_b32 v49, 0x3f803f80\n"
"v_mov_b32 v50, 0x3f803f80\n"
"v_mov_b32 v51, 0x3f803f80\n"
"v_add_u32 v45, s58, v10\n"
"v_xor_b32 v37, 64, v36\n"
"v_xor_b32 v38, 128, v36\n"
"v_xor_b32 v39, 192, v36\n"
"v_xor_b32 v40, 256, v36\n"
"v_xor_b32 v41, 320, v36\n"
"v_xor_b32 v42, 384, v36\n"
"v_xor_b32 v43, 448, v36\n"
"v_and_b32_e64 v33, 3, v3\n"
"v_cmp_eq_u32 s[72:73], 1, v33\n"
"v_cmp_eq_u32 s[74:75], 2, v33\n"
"v_cmp_eq_u32 s[76:77], 3, v33\n"
"v_lshrrev_b32 v33, 2, v3\n"
"v_cmp_eq_u32 s[78:79], v33, v4\n"
"s_lshl_b32 s58, s7, 3\n"
"s_lshl_b32 s57, s57, 2\n"
"s_add_u32 s58, s58, s57\n"
"s_mul_i32 s58, s58, 131072\n"
"s_lshl_b32 s59, s61, 10\n"
"s_add_u32 s58, s58, s59\n"
"s_add_u32 s58, s58, 2097152\n"
"s_mov_b32 s24, s80\n"
"s_and_b32 s25, s81, 0xffff\n"
"s_mov_b32 s26, 2097152\n"
"s_mov_b32 s27, 0x20000\n"
"s_mov_b32 s28, s82\n"
"s_and_b32 s29, s83, 0xffff\n"
"s_mov_b32 s30, 7340032\n"
"s_mov_b32 s31, 0x20000\n"
"s_mov_b32 s32, s84\n"
"s_and_b32 s33, s85, 0xffff\n"
"s_mov_b32 s34, 8388608\n"
"s_mov_b32 s35, 0x20000\n"
"s_mov_b32 s36, s86\n"
"s_and_b32 s37, s87, 0xffff\n"
"s_mov_b32 s38, 4096\n"
"s_mov_b32 s39, 0x20000\n"
"s_mov_b32 s40, s88\n"
"s_and_b32 s41, s89, 0xffff\n"
"s_mov_b32 s42, 3670016\n"
"s_mov_b32 s43, 0x20000\n"
"s_mov_b32 s44, s90\n"
"s_and_b32 s45, s91, 0xffff\n"
"s_mov_b32 s46, 4194304\n"
"s_mov_b32 s47, 0x20000\n"
"s_cmp_lt_u32 s6, 224\n"
"s_cselect_b32 s63, 0, 32\n"
"s_sub_u32 s63, s6, s63\n"
"s_lshr_b32 s65, s63, 2\n"
"s_and_b32 s68, s63, 3\n"
"s_mul_i32 s88, s68, 1835008\n"
"s_lshl_b32 s81, s65, 7\n"
"s_add_u32 s88, s88, s81\n"
"s_lshl_b32 s65, s65, 1\n"
"s_lshr_b32 s62, s7, 1\n"
"s_add_u32 s65, s65, s62\n"
"s_cmp_lt_u32 s65, 56\n"
"s_cselect_b32 s81, 0, 56\n"
"s_cselect_b32 s82, 33, 0\n"
"s_sub_u32 s65, s65, s81\n"
"v_bfe_u32 v61, v1, 3, 1\n"
"s_mul_i32 s81, s65, 37\n"
"s_lshr_b32 s81, s81, 8\n"
"s_mul_i32 s83, s81, 7\n"
"s_sub_u32 s83, s65, s83\n"
"s_and_b32 s84, s7, 1\n"
"s_lshl_b32 s85, s83, 1\n"
"s_add_u32 s85, s85, s84\n"
"s_sub_u32 s86, s85, 1\n"
"s_cmp_gt_u32 s83, 3\n"
"s_cselect_b32 s85, s86, s85\n"
"v_and_b32 v254, 1, v4\n"
"s_mul_i32 s86, s84, 7\n"
"s_add_u32 s86, s86, 6\n"
"s_cmp_eq_u32 s83, 3\n"
"s_cselect_b32 s85, s86, s85\n"
"s_cselect_b32 s86, 3, 4\n"
"s_cselect_b32 s87, 5, 3\n"
"s_mul_i32 s81, s81, 14\n"
"s_add_u32 s85, s85, s81\n"
"s_lshl_b32 s85, s85, 10\n"
"s_mul_i32 s81, s82, 114688\n"
"v_lshl_or_b32 v61, v254, 1, v61\n"
"s_add_u32 s85, s85, s81\n"
"s_add_u32 s85, s85, 30408704\n"
"s_mul_i32 s81, s68, 917504\n"
"s_add_u32 s66, s85, s81\n"
"s_lshl_b32 s81, s68, 8\n"
"s_add_u32 s67, s85, s81\n"
"s_add_u32 s67, s67, 3670016\n"
"s_cmp_lt_u32 s6, 224\n"
"s_cselect_b32 s66, s66, 57671680\n"
"s_cselect_b32 s67, s67, 57671680\n"
"s_cselect_b32 s88, s88, 57671680\n"
"s_lshl_b32 s84, s84, s87\n"
"s_lshl_b32 s62, s62, 6\n"
"s_add_u32 s84, s84, s62\n"
"v_and_b32 v60, 3, v1\n"
"v_lshlrev_b32 v60, s86, v60\n"
"v_add_u32 v60, s84, v60\n"
"v_lshlrev_b32 v61, 5, v61\n"
"v_xor_b32_e64 v60, v60, v61\n"
"v_bfe_u32 v61, v1, 2, 2\n"
"v_lshl_add_u32 v61, v4, 3, v61\n"
"v_lshl_add_u32 v244, v61, 7, v60\n"
"v_bfe_u32 v60, v1, 3, 2\n"
"v_lshrrev_b32 v61, 5, v1\n"
"v_lshl_add_u32 v254, v61, 4, v60\n"
"s_lshl_b32 s81, s7, 2\n"
"v_add_u32 v254, s81, v254\n"
"v_mul_u32_u24 v254, 7168, v254\n"
"v_add_u32 v245, v254, v11\n"
"v_lshl_add_u32 v254, v61, 3, v60\n"
"s_lshr_b32 s81, s7, 1\n"
"s_lshl_b32 s81, s81, 4\n"
"s_and_b32 s82, s7, 1\n"
"s_lshl_b32 s82, s82, 2\n"
"s_add_u32 s81, s81, s82\n"
"v_add_u32 v254, s81, v254\n"
"v_lshl_add_u32 v11, v254, 11, v11\n"
"s_lshl_b32 s89, s52, 6\n"
"s_lshl_b32 s82, s9, 8\n"
"s_add_u32 s89, s89, s82\n"
"s_lshl_b32 s90, s7, 13\n"
"s_add_u32 s90, s90, 65536\n"
"v_lshrrev_b32 v60, 1, v4\n"
"v_xor_b32 v60, v60, v3\n"
"v_lshlrev_b32 v60, 4, v60\n"
"v_lshl_add_u32 v60, v3, 8, v60\n"
"v_and_b32 v61, 1, v4\n"
"v_lshl_add_u32 v60, v61, 3, v60\n"
"v_add_u32 v246, s90, v60\n"
"v_xor_b32 v61, v4, v3\n"
"v_lshlrev_b32_e64 v61, 4, v61\n"
"v_lshl_add_u32 v248, v4, 12, v61\n"
"v_xor_b32 v249, 16448, v248\n"
"v_xor_b32 v250, 32896, v248\n"
"v_xor_b32 v251, 49344, v248\n"
"s_lshl_b32 s91, s52, 5\n"
"s_add_u32 s91, s91, s56\n"
"s_lshl_b32 s92, s7, 12\n"
"s_add_u32 s92, s92, 98304\n"
"v_lshrrev_b32 v60, 3, v1\n"
"v_and_b32 v61, 7, v1\n"
"v_xor_b32 v61, v61, v60\n"
"v_lshlrev_b32 v61, 4, v61\n"
"v_lshl_add_u32 v252, v60, 11, v61\n"
"v_lshrrev_b32 v60, 1, v4\n"
"v_and_b32 v61, 7, v3\n"
"v_xor_b32 v60, v60, v61\n"
"v_lshlrev_b32 v60, 4, v60\n"
"v_lshl_add_u32 v60, v3, 7, v60\n"
"v_and_b32 v61, 1, v4\n"
"v_lshl_add_u32 v60, v61, 3, v60\n"
"v_add_u32 v247, s92, v60\n"
"v_lshlrev_b32 v253, 1, v3\n"
"s_lshl_b32 s94, s6, 2\n"
"s_add_u32 s94, s94, s7\n"
"s_mul_i32 s80, s94, 3584\n"
"s_lshr_b32 s94, s6, 1\n"
"s_lshl2_add_u32 s94, s94, s7\n"
"s_lshl_b32 s94, s94, 12\n"
"s_and_b32 s82, s6, 1\n"
"s_lshl_b32 s82, s82, 10\n"
"s_add_u32 s94, s94, s82\n"
"v_mov_b32 v192, 0\n"
"v_mov_b32 v193, 0\n"
"v_mov_b32 v194, 0\n"
"v_mov_b32 v195, 0\n"
"v_mov_b32 v196, 0\n"
"v_mov_b32 v197, 0\n"
"v_mov_b32 v198, 0\n"
"v_mov_b32 v199, 0\n"
"v_mov_b32 v200, 0\n"
"v_mov_b32 v201, 0\n"
"v_mov_b32 v202, 0\n"
"v_mov_b32 v203, 0\n"
"v_mov_b32 v204, 0\n"
"v_mov_b32 v205, 0\n"
"v_mov_b32 v206, 0\n"
"v_mov_b32 v207, 0\n"
"v_mov_b32 v208, 0\n"
"v_mov_b32 v209, 0\n"
"v_mov_b32 v210, 0\n"
"v_mov_b32 v211, 0\n"
"v_mov_b32 v212, 0\n"
"v_mov_b32 v213, 0\n"
"v_mov_b32 v214, 0\n"
"v_mov_b32 v215, 0\n"
"v_mov_b32 v220, 0xdddd\n"
"v_add_u32 v255, s55, v2\n"
"v_lshrrev_b32 v255, 2, v255\n"
"v_and_b32_e64 v60, 7, v1\n"
"v_mov_b32 v61, 0x3f803f80\n"
"v_cmp_eq_u32 s[82:83], 0, v60\n"
"v_cmp_eq_u32 s[84:85], 4, v60\n"
"v_add_u32 v221, 0x10000, v2\n"
"v_add_u32 v222, 0x20000, v2\n"
"v_cndmask_b32 v216, 0, v61, s[82:83]\n"
"v_cndmask_b32 v217, 0, v61, s[82:83]\n"
"v_cndmask_b32 v218, 0, v61, s[84:85]\n"
"v_cndmask_b32 v219, 0, v61, s[84:85]\n"
"s_mov_b64 exec, 0\n"
"s_cmp_eq_u32 s7, 0\n"
"s_cbranch_scc1 LPG1_%=\n"
"s_cmp_eq_u32 s7, 1\n"
"s_cbranch_scc1 LPG2_%=\n"
"s_cmp_eq_u32 s7, 2\n"
"s_cbranch_scc1 LPG3_%=\n"
"s_branch LPG4_%=\n"
"LWRa_%=:\n"
"s_mov_b64 exec, -1\n"
"s_add_u32 m0, s60, 98304\n"
"s_add_u32 s64, s60, 0\n"
"buffer_load_dwordx4 v2, s[20:23], s64 offen lds\n"
"buffer_load_dwordx4 v2, s[20:23], s64 offen offset:1024 lds\n"
"buffer_load_dwordx4 v2, s[20:23], s64 offen offset:2048 lds\n"
"buffer_load_dwordx4 v2, s[20:23], s64 offen offset:3072 lds\n"
"s_add_u32 m0, s60, 102400\n"
"s_add_u32 s64, s60, 4096\n"
"buffer_load_dwordx4 v2, s[20:23], s64 offen lds\n"
"buffer_load_dwordx4 v2, s[20:23], s64 offen offset:1024 lds\n"
"buffer_load_dwordx4 v2, s[20:23], s64 offen offset:2048 lds\n"
"buffer_load_dwordx4 v2, s[20:23], s64 offen offset:3072 lds\n"
"s_add_u32 m0, s60, 106496\n"
"s_add_u32 s64, s60, 8192\n"
"buffer_load_dwordx4 v2, s[20:23], s64 offen lds\n"
"buffer_load_dwordx4 v2, s[20:23], s64 offen offset:1024 lds\n"
"buffer_load_dwordx4 v2, s[20:23], s64 offen offset:2048 lds\n"
"buffer_load_dwordx4 v2, s[20:23], s64 offen offset:3072 lds\n"
"s_add_u32 m0, s60, 110592\n"
"s_add_u32 s64, s60, 12288\n"
"buffer_load_dwordx4 v2, s[20:23], s64 offen lds\n"
"buffer_load_dwordx4 v2, s[20:23], s64 offen offset:1024 lds\n"
"buffer_load_dwordx4 v2, s[20:23], s64 offen offset:2048 lds\n"
"buffer_load_dwordx4 v2, s[20:23], s64 offen offset:3072 lds\n"
"s_waitcnt vmcnt(20)\n"
"ds_read_b128 v[64:67], v36\n"
"ds_read_b128 v[68:71], v37\n"
"ds_read_b128 v[72:75], v38\n"
"ds_read_b128 v[76:79], v39\n"
"ds_read_b128 v[80:83], v40\n"
"ds_read_b128 v[84:87], v41\n"
"ds_read_b128 v[88:91], v42\n"
"ds_read_b128 v[92:95], v43\n"
"s_waitcnt lgkmcnt(0)\n"
"v_mfma_f32_16x16x32_bf16 v[52:55], v[48:51], v[64:67], 0\n"
"s_add_u32 m0, s55, 32768\n"
"s_add_u32 s64, s56, 0\n"
"v_mfma_f32_16x16x32_bf16 v[56:59], v[64:67], v[64:67], 0\n"
"buffer_load_dwordx4 v9, s[16:19], s64 offen lds\n"
"v_mfma_f32_16x16x32_bf16 v[52:55], v[48:51], v[68:71], v[52:55]\n"
"s_add_u32 m0, s55, 36864\n"
"s_add_u32 s64, s56, 65536\n"
"v_mfma_f32_16x16x32_bf16 v[56:59], v[68:71], v[68:71], v[56:59]\n"
"buffer_load_dwordx4 v9, s[16:19], s64 offen lds\n"
"v_mfma_f32_16x16x32_bf16 v[52:55], v[48:51], v[72:75], v[52:55]\n"
"s_add_u32 m0, s55, 40960\n"
"s_add_u32 s64, s56, 131072\n"
"v_mfma_f32_16x16x32_bf16 v[56:59], v[72:75], v[72:75], v[56:59]\n"
"v_mfma_f32_16x16x32_bf16 v[52:55], v[48:51], v[76:79], v[52:55]\n"
"buffer_load_dwordx4 v9, s[16:19], s64 offen lds\n"
"v_mfma_f32_16x16x32_bf16 v[56:59], v[76:79], v[76:79], v[56:59]\n"
"s_add_u32 m0, s55, 45056\n"
"s_add_u32 s64, s56, 196608\n"
"v_mfma_f32_16x16x32_bf16 v[52:55], v[48:51], v[80:83], v[52:55]\n"
"v_mfma_f32_16x16x32_bf16 v[56:59], v[80:83], v[80:83], v[56:59]\n"
"buffer_load_dwordx4 v9, s[16:19], s64 offen lds\n"
"v_mfma_f32_16x16x32_bf16 v[52:55], v[48:51], v[84:87], v[52:55]\n"
"s_add_u32 m0, s55, 49152\n"
"s_add_u32 s64, s56, 262144\n"
"v_mfma_f32_16x16x32_bf16 v[56:59], v[84:87], v[84:87], v[56:59]\n"
"v_mfma_f32_16x16x32_bf16 v[52:55], v[48:51], v[88:91], v[52:55]\n"
"buffer_load_dwordx4 v9, s[16:19], s64 offen lds\n"
"v_mfma_f32_16x16x32_bf16 v[56:59], v[88:91], v[88:91], v[56:59]\n"
"s_add_u32 m0, s55, 53248\n"
"s_add_u32 s64, s56, 327680\n"
"v_mfma_f32_16x16x32_bf16 v[52:55], v[48:51], v[92:95], v[52:55]\n"
"v_mfma_f32_16x16x32_bf16 v[56:59], v[92:95], v[92:95], v[56:59]\n"
"buffer_load_dwordx4 v9, s[16:19], s64 offen lds\n"
"v_mov_b32 v62, 0\n"
"s_lshl2_add_u32 s62, s6, s7\n"
"s_lshl_b32 s62, s62, 8\n"
"s_add_u32 s62, s62, 1572864\n"
"v_lshlrev_b32 v63, 2, v1\n"
"buffer_store_dword v62, v63, s[48:51], s62 offen sc1\n"
"s_waitcnt vmcnt(25)\n"
"v_readfirstlane_b32 s10, v12\n"
"v_readfirstlane_b32 s11, v13\n"
"v_mov_b32 v60, -1\n"
"s_add_u32 s10, s10, 1\n"
"s_addc_u32 s11, s11, 0\n"
"v_mov_b32 v12, s10\n"
"v_mov_b32_e64 v13, s11\n"
"v_cndmask_b32 v53, v56, v57, s[72:73]\n"
"v_cndmask_b32 v53, v53, v58, s[74:75]\n"
"v_cndmask_b32 v53, v53, v59, s[76:77]\n"
"s_mov_b64 exec, s[78:79]\n"
"ds_write_b64 v45, v[52:53]\n"
"s_mov_b64 exec, -1\n"
"v_lshlrev_b32 v128, 16, v64\n"
"v_and_b32 v129, 0xffff0000, v64\n"
"v_lshlrev_b32 v130, 16, v65\n"
"v_and_b32 v131, 0xffff0000, v65\n"
"v_lshlrev_b32 v132, 16, v66\n"
"v_and_b32 v133, 0xffff0000, v66\n"
"v_lshlrev_b32 v134, 16, v67\n"
"v_and_b32 v135, 0xffff0000, v67\n"
"s_waitcnt lgkmcnt(0)\n"
"s_barrier\n"
"ds_read_b64 v[52:53], v10\n"
"ds_read_b64 v[54:55], v10 offset:8192\n"
"ds_read_b64 v[56:57], v10 offset:16384\n"
"ds_read_b64 v[58:59], v10 offset:24576\n"
"v_lshlrev_b32 v136, 16, v68\n"
"s_add_u32 m0, s55, 57344\n"
"s_add_u32 s64, s56, 393216\n"
"v_and_b32 v137, 0xffff0000, v68\n"
"v_lshlrev_b32 v138, 16, v69\n"
"v_and_b32 v139, 0xffff0000, v69\n"
"v_lshlrev_b32 v140, 16, v70\n"
"v_and_b32 v141, 0xffff0000, v70\n"
"v_lshlrev_b32 v142, 16, v71\n"
"v_and_b32 v143, 0xffff0000, v71\n"
"v_lshlrev_b32 v144, 16, v72\n"
"v_and_b32 v145, 0xffff0000, v72\n"
"v_lshlrev_b32 v146, 16, v73\n"
"v_and_b32 v147, 0xffff0000, v73\n"
"v_lshlrev_b32 v148, 16, v74\n"
"buffer_load_dwordx4 v9, s[16:19], s64 offen lds\n"
"v_and_b32 v149, 0xffff0000, v74\n"
"s_add_u32 m0, s55, 61440\n"
"s_add_u32 s64, s56, 458752\n"
"v_lshlrev_b32 v150, 16, v75\n"
"v_and_b32 v151, 0xffff0000, v75\n"
"v_lshlrev_b32 v152, 16, v76\n"
"v_and_b32 v153, 0xffff0000, v76\n"
"v_lshlrev_b32 v154, 16, v77\n"
"v_and_b32 v155, 0xffff0000, v77\n"
"v_lshlrev_b32 v156, 16, v78\n"
"v_and_b32 v157, 0xffff0000, v78\n"
"v_lshlrev_b32 v158, 16, v79\n"
"v_and_b32 v159, 0xffff0000, v79\n"
"s_waitcnt lgkmcnt(2)\n"
"v_pk_add_f32 v[52:53], v[52:53], v[54:55]\n"
"s_waitcnt lgkmcnt(0)\n"
"v_pk_add_f32 v[56:57], v[56:57], v[58:59]\n"
"buffer_load_dwordx4 v9, s[16:19], s64 offen lds\n"
"v_pk_add_f32 v[52:53], v[52:53], v[56:57]\n"
"s_add_u32 m0, s55, 65536\n"
"s_add_u32 s64, s56, 524288\n"
"v_mul_f32 v52, 0x3a800000, v52\n"
"v_mul_f32 v53, 0x3a800000, v53\n"
"v_fma_f32 v53, -v52, v52, v53\n"
"v_max_f32 v53, 0, v53\n"
"v_add_f32 v53, 0x3727c5ac, v53\n"
"v_rsq_f32 v54, v53\n"
"v_and_b32 v62, 3, v1\n"
"v_lshlrev_b32 v62, 2, v62\n"
"v_and_b32 v61, 0x3c0, v2\n"
"v_or_b32_e64 v62, v61, v62\n"
"v_mul_f32 v56, -v52, v54\n"
"buffer_load_dwordx4 v9, s[16:19], s64 offen lds\n"
"v_pk_fma_f32 v[128:129], v[128:129], v[54:55], v[56:57] op_sel_hi:[1,0,0]\n"
"v_pk_fma_f32 v[130:131], v[130:131], v[54:55], v[56:57] op_sel_hi:[1,0,0]\n"
"v_pk_fma_f32 v[132:133], v[132:133], v[54:55], v[56:57] op_sel_hi:[1,0,0]\n"
"v_pk_fma_f32 v[134:135], v[134:135], v[54:55], v[56:57] op_sel_hi:[1,0,0]\n"
"v_cvt_pk_bf16_f32 v96, v128, v129\n"
"v_cvt_pk_bf16_f32 v97, v130, v131\n"
"v_cvt_pk_bf16_f32 v98, v132, v133\n"
"v_cvt_pk_bf16_f32 v99, v134, v135\n"
"buffer_store_dwordx4 v[96:99], v2, s[48:51], s58 offen sc1\n"
"s_add_u32 s58, s58, 131072\n"
"v_pk_fma_f32 v[136:137], v[136:137], v[54:55], v[56:57] op_sel_hi:[1,0,0]\n"
"v_pk_fma_f32 v[138:139], v[138:139], v[54:55], v[56:57] op_sel_hi:[1,0,0]\n"
"v_pk_fma_f32 v[140:141], v[140:141], v[54:55], v[56:57] op_sel_hi:[1,0,0]\n"
"v_pk_fma_f32 v[142:143], v[142:143], v[54:55], v[56:57] op_sel_hi:[1,0,0]\n"
"v_cvt_pk_bf16_f32 v100, v136, v137\n"
"v_cvt_pk_bf16_f32 v101, v138, v139\n"
"v_cvt_pk_bf16_f32 v102, v140, v141\n"
"v_cvt_pk_bf16_f32 v103, v142, v143\n"
"buffer_store_dwordx4 v[100:103], v2, s[48:51], s58 offen sc1\n"
"s_add_u32 s58, s58, 131072\n"
"v_pk_fma_f32 v[144:145], v[144:145], v[54:55], v[56:57] op_sel_hi:[1,0,0]\n"
"v_pk_fma_f32 v[146:147], v[146:147], v[54:55], v[56:57] op_sel_hi:[1,0,0]\n"
"v_pk_fma_f32 v[148:149], v[148:149], v[54:55], v[56:57] op_sel_hi:[1,0,0]\n"
"v_pk_fma_f32 v[150:151], v[150:151], v[54:55], v[56:57] op_sel_hi:[1,0,0]\n"
"v_cvt_pk_bf16_f32 v104, v144, v145\n"
"v_cvt_pk_bf16_f32 v105, v146, v147\n"
"v_cvt_pk_bf16_f32 v106, v148, v149\n"
"v_cvt_pk_bf16_f32 v107, v150, v151\n"
"buffer_store_dwordx4 v[104:107], v2, s[48:51], s58 offen sc1\n"
"s_add_u32 s58, s58, 131072\n"
"v_pk_fma_f32 v[152:153], v[152:153], v[54:55], v[56:57] op_sel_hi:[1,0,0]\n"
"v_pk_fma_f32 v[154:155], v[154:155], v[54:55], v[56:57] op_sel_hi:[1,0,0]\n"
"v_pk_fma_f32 v[156:157], v[156:157], v[54:55], v[56:57] op_sel_hi:[1,0,0]\n"
"v_pk_fma_f32 v[158:159], v[158:159], v[54:55], v[56:57] op_sel_hi:[1,0,0]\n"
"v_cvt_pk_bf16_f32 v108, v152, v153\n"
"v_cvt_pk_bf16_f32 v109, v154, v155\n"
"v_cvt_pk_bf16_f32 v110, v156, v157\n"
"v_cvt_pk_bf16_f32 v111, v158, v159\n"
"buffer_store_dwordx4 v[108:111], v2, s[48:51], s58 offen sc1\n"
"s_add_u32 s58, s58, 131072\n"
"s_lshl_b32 s57, s53, 5\n"
"s_add_u32 s57, s57, 524288\n"
"v_lshlrev_b32 v62, 2, v1\n"
"buffer_store_dword v60, v62, s[48:51], s57 offen sc1\n"
"v_lshlrev_b32 v58, 16, v80\n"
"s_add_u32 m0, s55, 69632\n"
"s_add_u32 s64, s56, 589824\n"
"v_and_b32 v59, 0xffff0000, v80\n"
"buffer_load_dwordx4 v9, s[16:19], s64 offen lds\n"
"v_pk_fma_f32 v[58:59], v[58:59], v[54:55], v[56:57] op_sel_hi:[1,0,0]\n"
"s_add_u32 m0, s55, 73728\n"
"s_add_u32 s64, s56, 655360\n"
"v_cvt_pk_bf16_f32 v112, v58, v59\n"
"v_lshlrev_b32 v58, 16, v81\n"
"v_and_b32 v59, 0xffff0000, v81\n"
"v_pk_fma_f32 v[58:59], v[58:59], v[54:55], v[56:57] op_sel_hi:[1,0,0]\n"
"v_cvt_pk_bf16_f32 v113, v58, v59\n"
"v_lshlrev_b32_e64 v58, 16, v82\n"
"v_and_b32 v59, 0xffff0000, v82\n"
"v_pk_fma_f32 v[58:59], v[58:59], v[54:55], v[56:57] op_sel_hi:[1,0,0]\n"
"v_cvt_pk_bf16_f32 v114, v58, v59\n"
"v_lshlrev_b32_e64 v58, 16, v83\n"
"v_and_b32 v59, 0xffff0000, v83\n"
"v_pk_fma_f32 v[58:59], v[58:59], v[54:55], v[56:57] op_sel_hi:[1,0,0]\n"
"v_cvt_pk_bf16_f32 v115, v58, v59\n"
"v_lshlrev_b32_e64 v58, 16, v84\n"
"v_and_b32 v59, 0xffff0000, v84\n"
"v_pk_fma_f32 v[58:59], v[58:59], v[54:55], v[56:57] op_sel_hi:[1,0,0]\n"
"v_cvt_pk_bf16_f32 v116, v58, v59\n"
"buffer_load_dwordx4 v9, s[16:19], s64 offen lds\n"
"v_lshlrev_b32 v58, 16, v85\n"
"s_add_u32 m0, s55, 77824\n"
"s_add_u32 s64, s56, 720896\n"
"v_and_b32 v59, 0xffff0000, v85\n"
"v_pk_fma_f32 v[58:59], v[58:59], v[54:55], v[56:57] op_sel_hi:[1,0,0]\n"
"v_cvt_pk_bf16_f32 v117, v58, v59\n"
"v_lshlrev_b32 v58, 16, v86\n"
"v_and_b32 v59, 0xffff0000, v86\n"
"v_pk_fma_f32 v[58:59], v[58:59], v[54:55], v[56:57] op_sel_hi:[1,0,0]\n"
"v_cvt_pk_bf16_f32 v118, v58, v59\n"
"v_lshlrev_b32_e64 v58, 16, v87\n"
"v_and_b32 v59, 0xffff0000, v87\n"
"v_pk_fma_f32 v[58:59], v[58:59], v[54:55], v[56:57] op_sel_hi:[1,0,0]\n"
"v_cvt_pk_bf16_f32 v119, v58, v59\n"
"v_lshlrev_b32_e64 v58, 16, v88\n"
"v_and_b32 v59, 0xffff0000, v88\n"
"v_pk_fma_f32 v[58:59], v[58:59], v[54:55], v[56:57] op_sel_hi:[1,0,0]\n"
"v_cvt_pk_bf16_f32 v120, v58, v59\n"
"v_lshlrev_b32_e64 v58, 16, v89\n"
"v_and_b32 v59, 0xffff0000, v89\n"
"v_pk_fma_f32 v[58:59], v[58:59], v[54:55], v[56:57] op_sel_hi:[1,0,0]\n"
"v_cvt_pk_bf16_f32 v121, v58, v59\n"
"buffer_load_dwordx4 v9, s[16:19], s64 offen lds\n"
"v_lshlrev_b32_e64 v58, 16, v90\n"
"v_and_b32 v59, 0xffff0000, v90\n"
"v_pk_fma_f32 v[58:59], v[58:59], v[54:55], v[56:57] op_sel_hi:[1,0,0]\n"
"v_cvt_pk_bf16_f32 v122, v58, v59\n"
"v_lshlrev_b32_e64 v58, 16, v91\n"
"v_and_b32 v59, 0xffff0000, v91\n"
"LPG1_%=:\n"
"s_cbranch_execz LPG5_%=\n"
"v_pk_fma_f32 v[58:59], v[58:59], v[54:55], v[56:57] op_sel_hi:[1,0,0]\n"
"v_cvt_pk_bf16_f32 v123, v58, v59\n"
"v_lshlrev_b32 v58, 16, v92\n"
"v_and_b32 v59, 0xffff0000, v92\n"
"v_pk_fma_f32 v[58:59], v[58:59], v[54:55], v[56:57] op_sel_hi:[1,0,0]\n"
"v_cvt_pk_bf16_f32 v124, v58, v59\n"
"v_lshlrev_b32_e64 v58, 16, v93\n"
"v_and_b32 v59, 0xffff0000, v93\n"
"v_pk_fma_f32 v[58:59], v[58:59], v[54:55], v[56:57] op_sel_hi:[1,0,0]\n"
"v_cvt_pk_bf16_f32 v125, v58, v59\n"
"v_lshlrev_b32_e64 v58, 16, v94\n"
"v_and_b32 v59, 0xffff0000, v94\n"
"v_pk_fma_f32 v[58:59], v[58:59], v[54:55], v[56:57] op_sel_hi:[1,0,0]\n"
"v_cvt_pk_bf16_f32 v126, v58, v59\n"
"v_lshlrev_b32_e64 v58, 16, v95\n"
"v_and_b32 v59, 0xffff0000, v95\n"
"v_pk_fma_f32 v[58:59], v[58:59], v[54:55], v[56:57] op_sel_hi:[1,0,0]\n"
"v_cvt_pk_bf16_f32 v127, v58, v59\n"
"v_lshlrev_b32_e64 v32, 7, v3\n"
"v_lshl_add_u32 v33, v4, 5, v32\n"
"v_lshl_add_u32 v32, v4, 4, v32\n"
"v_lshlrev_b32 v34, 9, v4\n"
"v_lshl_add_u32 v34, v3, 4, v34\n"
"v_lshlrev_b32 v36, 6, v3\n"
"v_lshl_add_u32 v36, v4, 4, v36\n"
"v_and_b32 v37, 31, v1\n"
"v_lshlrev_b32 v37, 6, v37\n"
"v_lshrrev_b32 v38, 5, v1\n"
"v_lshl_add_u32 v37, v38, 5, v37\n"
"s_waitcnt vmcnt(18)\n"
"ds_read_b64_tr_b16 v[128:129], v46\n"
"ds_read_b64_tr_b16 v[130:131], v46 offset:256\n"
"ds_read_b64_tr_b16 v[132:133], v46 offset:32\n"
"ds_read_b64_tr_b16 v[134:135], v46 offset:288\n"
"ds_read_b64_tr_b16 v[136:137], v46 offset:2048\n"
"ds_read_b64_tr_b16 v[138:139], v46 offset:2304\n"
"ds_read_b64_tr_b16 v[140:141], v46 offset:2080\n"
"ds_read_b64_tr_b16 v[142:143], v46 offset:2336\n"
"ds_read_b64_tr_b16 v[144:145], v46 offset:4096\n"
"ds_read_b64_tr_b16 v[146:147], v46 offset:4352\n"
"ds_read_b64_tr_b16 v[148:149], v46 offset:4128\n"
"ds_read_b64_tr_b16 v[150:151], v46 offset:4384\n"
"ds_read_b64_tr_b16 v[152:153], v46 offset:6144\n"
"ds_read_b64_tr_b16 v[154:155], v46 offset:6400\n"
"ds_read_b64_tr_b16 v[156:157], v46 offset:6176\n"
"ds_read_b64_tr_b16 v[158:159], v46 offset:6432\n"
"ds_read_b64_tr_b16 v[160:161], v47\n"
"ds_read_b64_tr_b16 v[162:163], v47 offset:256\n"
"ds_read_b64_tr_b16 v[164:165], v47 offset:32\n"
"ds_read_b64_tr_b16 v[166:167], v47 offset:288\n"
"ds_read_b64_tr_b16 v[168:169], v47 offset:2048\n"
"ds_read_b64_tr_b16 v[170:171], v47 offset:2304\n"
"ds_read_b64_tr_b16 v[172:173], v47 offset:2080\n"
"ds_read_b64_tr_b16 v[174:175], v47 offset:2336\n"
"ds_read_b64_tr_b16 v[176:177], v47 offset:4096\n"
"ds_read_b64_tr_b16 v[178:179], v47 offset:4352\n"
"ds_read_b64_tr_b16 v[180:181], v47 offset:4128\n"
"ds_read_b64_tr_b16 v[182:183], v47 offset:4384\n"
"ds_read_b64_tr_b16 v[184:185], v47 offset:6144\n"
"ds_read_b64_tr_b16 v[186:187], v47 offset:6400\n"
"ds_read_b64_tr_b16 v[188:189], v47 offset:6176\n"
"ds_read_b64_tr_b16 v[190:191], v47 offset:6432\n"
"s_lshl_b32 s57, s8, 4\n"
"s_add_u32 s57, s57, s9\n"
"s_lshl_b32 s95, s57, 4\n"
"s_add_u32 s95, s95, 57114624\n"
"s_lshl_b32 s57, s57, 2\n"
"s_add_u32 s57, s57, s7\n"
"s_lshl_b32 s57, s57, 3\n"
"s_waitcnt vmcnt(4)\n"
"s_lshl2_add_u32 s62, s57, 57081856\n"
"s_mov_b64 exec, 1\n"
"buffer_store_dwordx2 v[12:13], off, s[48:51], s62 sc1\n"
"s_mov_b64 exec, -1\n"
"s_lshl_b32 s57, s7, 13\n"
"s_add_u32 s57, s57, 1024\n"
"s_lshr_b32 s59, s61, 1\n"
"s_lshl_b32 s59, s59, 12\n"
"s_add_u32 s59, s59, 1048576\n"
"s_and_b32 s60, s61, 1\n"
"s_bitcmp1_b32 s9, 0\n"
"s_cbranch_scc1 Lo1_%=\n"
"s_waitcnt lgkmcnt(15)\n"
"v_mfma_f32_16x16x32_bf16 v[52:55], v[128:131], v[96:99], 0\n"
"v_mfma_f32_16x16x32_bf16 v[56:59], v[132:135], v[96:99], 0\n"
"v_mfma_f32_16x16x32_bf16 v[52:55], v[136:139], v[100:103], v[52:55]\n"
"v_mfma_f32_16x16x32_bf16 v[56:59], v[140:143], v[100:103], v[56:59]\n"
"v_mfma_f32_16x16x32_bf16 v[52:55], v[144:147], v[104:107], v[52:55]\n"
"v_mfma_f32_16x16x32_bf16 v[56:59], v[148:151], v[104:107], v[56:59]\n"
"v_mfma_f32_16x16x32_bf16 v[52:55], v[152:155], v[108:111], v[52:55]\n"
"v_mfma_f32_16x16x32_bf16 v[56:59], v[156:159], v[108:111], v[56:59]\n"
"s_waitcnt lgkmcnt(14)\n"
"v_mfma_f32_16x16x32_bf16 v[52:55], v[160:163], v[112:115], v[52:55]\n"
"s_waitcnt lgkmcnt(12)\n"
"v_mfma_f32_16x16x32_bf16 v[56:59], v[164:167], v[112:115], v[56:59]\n"
"s_waitcnt lgkmcnt(10)\n"
"v_mfma_f32_16x16x32_bf16 v[52:55], v[168:171], v[116:119], v[52:55]\n"
"s_waitcnt lgkmcnt(8)\n"
"v_mfma_f32_16x16x32_bf16 v[56:59], v[172:175], v[116:119], v[56:59]\n"
"s_waitcnt lgkmcnt(6)\n"
"v_mfma_f32_16x16x32_bf16 v[52:55], v[176:179], v[120:123], v[52:55]\n"
"s_waitcnt lgkmcnt(4)\n"
"v_mfma_f32_16x16x32_bf16 v[56:59], v[180:183], v[120:123], v[56:59]\n"
"s_waitcnt lgkmcnt(2)\n"
"v_mfma_f32_16x16x32_bf16 v[52:55], v[184:187], v[124:127], v[52:55]\n"
"s_waitcnt lgkmcnt(0)\n"
"v_mfma_f32_16x16x32_bf16 v[56:59], v[188:191], v[124:127], v[56:59]\n"
"s_lshl_b32 s60, s60, 8\n"
"v_mov_b32 v35, -1\n"
"v_add_u32 v32, s57, v32\n"
"v_add_u32 v39, 64, v32\n"
"s_branch Lj2_%=\n"
"Lo1_%=:\n"
"s_waitcnt lgkmcnt(15)\n"
"v_mfma_f32_16x16x32_bf16 v[52:55], v[96:99], v[128:131], 0\n"
"v_mfma_f32_16x16x32_bf16 v[56:59], v[96:99], v[132:135], 0\n"
"v_mfma_f32_16x16x32_bf16 v[52:55], v[100:103], v[136:139], v[52:55]\n"
"v_mfma_f32_16x16x32_bf16 v[56:59], v[100:103], v[140:143], v[56:59]\n"
"v_mfma_f32_16x16x32_bf16 v[52:55], v[104:107], v[144:147], v[52:55]\n"
"v_mfma_f32_16x16x32_bf16 v[56:59], v[104:107], v[148:151], v[56:59]\n"
"v_mfma_f32_16x16x32_bf16 v[52:55], v[108:111], v[152:155], v[52:55]\n"
"v_mfma_f32_16x16x32_bf16 v[56:59], v[108:111], v[156:159], v[56:59]\n"
"s_waitcnt lgkmcnt(14)\n"
"v_mfma_f32_16x16x32_bf16 v[52:55], v[112:115], v[160:163], v[52:55]\n"
"s_waitcnt lgkmcnt(12)\n"
"v_mfma_f32_16x16x32_bf16 v[56:59], v[112:115], v[164:167], v[56:59]\n"
"s_waitcnt lgkmcnt(10)\n"
"v_mfma_f32_16x16x32_bf16 v[52:55], v[116:119], v[168:171], v[52:55]\n"
"s_waitcnt lgkmcnt(8)\n"
"v_mfma_f32_16x16x32_bf16 v[56:59], v[116:119], v[172:175], v[56:59]\n"
"s_waitcnt lgkmcnt(6)\n"
"v_mfma_f32_16x16x32_bf16 v[52:55], v[120:123], v[176:179], v[52:55]\n"
"s_waitcnt lgkmcnt(4)\n"
"v_mfma_f32_16x16x32_bf16 v[56:59], v[120:123], v[180:183], v[56:59]\n"
"s_waitcnt lgkmcnt(2)\n"
"v_mfma_f32_16x16x32_bf16 v[52:55], v[124:127], v[184:187], v[52:55]\n"
"s_waitcnt lgkmcnt(0)\n"
"v_mfma_f32_16x16x32_bf16 v[56:59], v[124:127], v[188:191], v[56:59]\n"
"s_lshl_b32 s60, s60, 10\n"
"s_add_u32 s60, s60, 2048\n"
"v_mov_b32 v35, 0\n"
"v_mov_b32 v33, v37\n"
"v_mov_b32 v34, v2\n"
"v_add_u32 v32, s57, v36\n"
"v_add_u32 v39, 1024, v32\n"
"Lj2_%=:\n"
"s_add_u32 s59, s59, s60\n"
"ds_write_b128 v32, v[52:55]\n"
"s_nop 0\n"
"ds_write_b128 v39, v[56:59]\n"
"s_cmp_eq_u32 s7, 3\n"
"s_cselect_b32 s59, s59, 57671680\n"
"s_cselect_b32 s95, s95, 57671680\n"
"s_waitcnt lgkmcnt(0)\n"
"s_barrier\n"
"ds_read_b128 v[64:67], v33 offset:1024\n"
"ds_read_b128 v[68:71], v33 offset:1040\n"
"ds_read_b128 v[72:75], v33 offset:9216\n"
"ds_read_b128 v[76:79], v33 offset:9232\n"
"ds_read_b128 v[80:83], v33 offset:17408\n"
"ds_read_b128 v[84:87], v33 offset:17424\n"
"ds_read_b128 v[88:91], v33 offset:25600\n"
"ds_read_b128 v[92:95], v33 offset:25616\n"
"s_waitcnt lgkmcnt(5)\n"
"v_pk_add_f32 v[64:65], v[64:65], v[72:73]\n"
"s_waitcnt lgkmcnt(1)\n"
"v_pk_add_f32 v[80:81], v[80:81], v[88:89]\n"
"v_pk_add_f32 v[64:65], v[64:65], v[80:81]\n"
"v_cvt_pk_bf16_f32 v96, v64, v65\n"
"v_pk_add_f32 v[66:67], v[66:67], v[74:75]\n"
"v_pk_add_f32 v[82:83], v[82:83], v[90:91]\n"
"v_pk_add_f32 v[66:67], v[66:67], v[82:83]\n"
"v_cvt_pk_bf16_f32 v97, v66, v67\n"
"v_pk_add_f32 v[68:69], v[68:69], v[76:77]\n"
"s_waitcnt lgkmcnt(0)\n"
"s_nop 0\n"
"v_pk_add_f32 v[84:85], v[84:85], v[92:93]\n"
"v_pk_add_f32 v[68:69], v[68:69], v[84:85]\n"
"v_cvt_pk_bf16_f32 v98, v68, v69\n"
"v_pk_add_f32 v[70:71], v[70:71], v[78:79]\n"
"v_pk_add_f32 v[86:87], v[86:87], v[94:95]\n"
"v_pk_add_f32 v[70:71], v[70:71], v[86:87]\n"
"v_cvt_pk_bf16_f32 v99, v70, v71\n"
"buffer_store_dwordx4 v[96:99], v34, s[48:51], s59 offen sc1\n"
"v_lshlrev_b32 v10, 5, v3\n"
"v_mov_b32 v36, 0\n"
"s_add_u32 m0, s55, 81920\n"
"s_add_u32 s64, s56, 786432\n"
"v_lshlrev_b32 v37, 16, v96\n"
"buffer_load_dwordx4 v9, s[16:19], s64 offen lds\n"
"v_and_b32 v38, 0xffff0000, v96\n"
"s_add_u32 m0, s55, 86016\n"
"s_add_u32 s64, s56, 851968\n"
"v_fmac_f32 v36, v37, v37\n"
"buffer_load_dwordx4 v9, s[16:19], s64 offen lds\n"
"v_fmac_f32 v36, v38, v38\n"
"s_add_u32 m0, s55, 90112\n"
"s_add_u32 s64, s56, 917504\n"
"v_lshlrev_b32 v37, 16, v97\n"
"v_and_b32 v38, 0xffff0000, v97\n"
"v_fmac_f32 v36, v37, v37\n"
"v_fmac_f32 v36, v38, v38\n"
"v_lshlrev_b32_e64 v37, 16, v98\n"
"v_and_b32 v38, 0xffff0000, v98\n"
"buffer_load_dwordx4 v9, s[16:19], s64 offen lds\n"
"v_fmac_f32 v36, v37, v37\n"
"s_add_u32 m0, s55, 94208\n"
"s_add_u32 s64, s56, 983040\n"
"v_fmac_f32 v36, v38, v38\n"
"v_lshlrev_b32 v37, 16, v99\n"
"v_and_b32 v38, 0xffff0000, v99\n"
"v_fmac_f32 v36, v37, v37\n"
"v_fmac_f32 v36, v38, v38\n"
"v_mov_b32 v37, v36\n"
"s_nop 1\n"
"v_permlane16_swap_b32 v36, v37\n"
"v_add_f32 v36, v36, v37\n"
"v_mov_b32 v37, v36\n"
"s_nop 1\n"
"v_permlane32_swap_b32 v36, v37\n"
"v_add_f32 v36, v36, v37\n"
"buffer_load_dwordx4 v9, s[16:19], s64 offen lds\n"
"s_nop 0\n"
"v_max_f32_dpp v36, v36, v36 row_ror:8 row_mask:0xf bank_mask:0xf\n"
"s_add_u32 m0, s55, 98304\n"
"s_add_u32 s64, s56, 1048576\n"
"v_max_f32_dpp v36, v36, v36 row_ror:4 row_mask:0xf bank_mask:0xf\n"
"s_nop 1\n"
"v_max_f32_dpp v36, v36, v36 row_ror:2 row_mask:0xf bank_mask:0xf\n"
"s_nop 1\n"
"v_max_f32_dpp v36, v36, v36 row_ror:1 row_mask:0xf bank_mask:0xf\n"
"v_and_b32 v101, v35, v36\n"
"v_and_b32 v103, v35, v36\n"
"v_mov_b32 v100, v12\n"
"v_mov_b32 v102, v13\n"
"buffer_load_dwordx4 v9, s[16:19], s64 offen lds\n"
"s_add_u32 m0, s55, 102400\n"
"s_add_u32 s64, s56, 1114112\n"
"buffer_load_dwordx4 v9, s[16:19], s64 offen lds\n"
"s_add_u32 m0, s55, 106496\n"
"s_add_u32 s64, s56, 1179648\n"
"buffer_load_dwordx4 v9, s[16:19], s64 offen lds\n"
"s_add_u32 m0, s55, 110592\n"
"s_add_u32 s64, s56, 1245184\n"
"buffer_load_dwordx4 v9, s[16:19], s64 offen lds\n"
"s_add_u32 m0, s55, 114688\n"
"s_add_u32 s64, s56, 1310720\n"
"buffer_load_dwordx4 v9, s[16:19], s64 offen lds\n"
"s_add_u32 m0, s55, 118784\n"
"s_add_u32 s64, s56, 1376256\n"
"buffer_load_dwordx4 v9, s[16:19], s64 offen lds\n"
"s_add_u32 m0, s55, 122880\n"
"s_add_u32 s64, s56, 1441792\n"
"s_lshl2_add_u32 s62, s54, 57081856\n"
"buffer_load_dwordx2 v[60:61], v10, s[48:51], s62 offen sc1\n"
"buffer_load_dwordx4 v9, s[16:19], s64 offen lds\n"
"s_add_u32 m0, s55, 126976\n"
"s_add_u32 s64, s56, 1507328\n"
"buffer_load_dwordx4 v9, s[16:19], s64 offen lds\n"
"s_add_u32 m0, s55, 131072\n"
"s_add_u32 s64, s56, 1572864\n"
"buffer_load_dwordx4 v9, s[16:19], s64 offen lds\n"
"s_add_u32 m0, s55, 135168\n"
"s_add_u32 s64, s56, 1638400\n"
"buffer_load_dwordx4 v9, s[16:19], s64 offen lds\n"
"s_add_u32 m0, s55, 139264\n"
"s_add_u32 s64, s56, 1703936\n"
"buffer_load_dwordx4 v9, s[16:19], s64 offen lds\n"
"s_add_u32 m0, s55, 143360\n"
"s_add_u32 s64, s56, 1769472\n"
"buffer_load_dwordx4 v9, s[16:19], s64 offen lds\n"
"s_add_u32 m0, s55, 147456\n"
"s_add_u32 s64, s56, 1835008\n"
"buffer_load_dwordx4 v9, s[16:19], s64 offen lds\n"
"s_add_u32 m0, s55, 151552\n"
"s_add_u32 s64, s56, 1900544\n"
"buffer_load_dwordx4 v9, s[16:19], s64 offen lds\n"
"s_waitcnt vmcnt(8)\n"
"v_cmp_ne_u64 vcc, s[10:11], v[60:61]\n"
"s_nop 4\n"
"s_cbranch_vccz Ld4_%=\n"
"s_mov_b32 s65, 0\n"
"Lp3_%=:\n"
"s_lshl2_add_u32 s62, s54, 57081856\n"
"buffer_load_dwordx2 v[60:61], v10, s[48:51], s62 offen sc1\n"
"s_add_u32 s65, s65, 1\n"
"s_cmp_gt_u32 s65, 131072\n"
"s_waitcnt vmcnt(0)\n"
"s_cbranch_scc1 Ld4_%=\n"
"v_cmp_ne_u64 vcc, s[10:11], v[60:61]\n"
"s_nop 4\n"
"s_cbranch_vccnz Lp3_%=\n"
"Ld4_%=:\n"
"s_add_u32 s63, s52, 2097152\n"
"buffer_load_dwordx4 a[0:3], v2, s[48:51], s63 offen sc1\n"
"buffer_load_dwordx4 a[4:7], v2, s[48:51], s63 offen offset:1024 sc1\n"
"s_add_u32 s63, s52, 2228224\n"
"buffer_load_dwordx4 a[8:11], v2, s[48:51], s63 offen sc1\n"
"buffer_load_dwordx4 a[12:15], v2, s[48:51], s63 offen offset:1024 sc1\n"
"s_add_u32 s63, s52, 2359296\n"
"buffer_load_dwordx4 a[16:19], v2, s[48:51], s63 offen sc1\n"
"buffer_load_dwordx4 a[20:23], v2, s[48:51], s63 offen offset:1024 sc1\n"
"s_add_u32 s63, s52, 2490368\n"
"buffer_load_dwordx4 a[24:27], v2, s[48:51], s63 offen sc1\n"
"buffer_load_dwordx4 a[28:31], v2, s[48:51], s63 offen offset:1024 sc1\n"
"s_add_u32 s63, s52, 2621440\n"
"buffer_load_dwordx4 a[32:35], v2, s[48:51], s63 offen sc1\n"
"buffer_load_dwordx4 a[36:39], v2, s[48:51], s63 offen offset:1024 sc1\n"
"s_add_u32 s63, s52, 2752512\n"
"buffer_load_dwordx4 a[40:43], v2, s[48:51], s63 offen sc1\n"
"buffer_load_dwordx4 a[44:47], v2, s[48:51], s63 offen offset:1024 sc1\n"
"v_mov_b32 v32, v244\n"
"v_mov_b32 v33, v245\n"
"v_mov_b32 v34, v246\n"
"v_mov_b32 v35, v247\n"
"v_mov_b32 v40, v248\n"
"v_mov_b32 v41, v249\n"
"v_mov_b32 v42, v250\n"
"v_mov_b32 v43, v251\n"
"v_mov_b32 v44, v252\n"
"v_mov_b32 v45, v253\n"
"s_lshl_b32 s61, s7, 12\n"
"s_barrier\n"
"ds_read_b64_tr_b16 v[224:225], v5 offset:32768\n"
"ds_read_b64_tr_b16 v[226:227], v5 offset:33280\n"
"ds_read_b64_tr_b16 v[228:229], v6 offset:32768\n"
"ds_read_b64_tr_b16 v[230:231], v6 offset:33280\n"
"ds_read_b64_tr_b16 v[232:233], v7 offset:32768\n"
"ds_read_b64_tr_b16 v[234:235], v7 offset:33280\n"
"ds_read_b64_tr_b16 v[236:237], v8 offset:32768\n"
"ds_read_b64_tr_b16 v[238:239], v8 offset:33280\n"
"ds_read_b64_tr_b16 v[240:241], v5 offset:36864\n"
"ds_read_b64_tr_b16 v[242:243], v5 offset:37376\n"
"s_mov_b64 exec, 1\n"
"buffer_store_dwordx4 v[100:103], off, s[48:51], s95 sc1\n"
"s_mov_b64 exec, -1\n"
"s_waitcnt vmcnt(12) lgkmcnt(8)\n"
"v_mfma_f32_16x16x32_bf16 v[64:67], v[224:227], a[0:3], 0\n"
"ds_read_b64_tr_b16 v[244:245], v6 offset:36864\n"
"s_add_u32 m0, s55, 155648\n"
"s_add_u32 s64, s56, 1966080\n"
"s_waitcnt vmcnt(11)\n"
"v_mfma_f32_16x16x32_bf16 v[68:71], v[224:227], a[4:7], 0\n"
"ds_read_b64_tr_b16 v[246:247], v6 offset:37376\n"
"s_add_u32 s63, s52, 2883584\n"
"s_waitcnt lgkmcnt(8)\n"
"v_mfma_f32_16x16x32_bf16 v[72:75], v[228:231], a[0:3], 0\n"
"ds_read_b64_tr_b16 v[224:225], v7 offset:36864\n"
"buffer_load_dwordx4 v9, s[16:19], s64 offen lds\n"
"v_mfma_f32_16x16x32_bf16 v[76:79], v[228:231], a[4:7], 0\n"
"ds_read_b64_tr_b16 v[226:227], v7 offset:37376\n"
"s_waitcnt lgkmcnt(6)\n"
"v_mfma_f32_16x16x32_bf16 v[80:83], v[232:235], a[0:3], 0\n"
"ds_read_b64_tr_b16 v[228:229], v8 offset:36864\n"
"buffer_load_dwordx4 a[48:51], v2, s[48:51], s63 offen sc1\n"
"v_mfma_f32_16x16x32_bf16 v[84:87], v[232:235], a[4:7], 0\n"
"ds_read_b64_tr_b16 v[230:231], v8 offset:37376\n"
"v_mfma_f32_16x16x32_bf16 v[88:91], v[236:239], a[0:3], 0\n"
"ds_read_b64_tr_b16 v[232:233], v5 offset:40960\n"
"buffer_load_dwordx4 a[52:55], v2, s[48:51], s63 offen offset:1024 sc1\n"
"v_mfma_f32_16x16x32_bf16 v[92:95], v[236:239], a[4:7], 0\n"
"ds_read_b64_tr_b16 v[234:235], v5 offset:41472\n"
"s_waitcnt vmcnt(12) lgkmcnt(6)\n"
"v_mfma_f32_16x16x32_bf16 v[64:67], v[240:243], a[8:11], v[64:67]\n"
"ds_read_b64_tr_b16 v[236:237], v6 offset:40960\n"
"s_add_u32 m0, s55, 159744\n"
"s_add_u32 s64, s56, 2031616\n"
"v_mfma_f32_16x16x32_bf16 v[68:71], v[240:243], a[12:15], v[68:71]\n"
"ds_read_b64_tr_b16 v[238:239], v6 offset:41472\n"
"s_add_u32 s63, s52, 3014656\n"
"v_mfma_f32_16x16x32_bf16 v[72:75], v[244:247], a[8:11], v[72:75]\n"
"ds_read_b64_tr_b16 v[240:241], v7 offset:40960\n"
"buffer_load_dwordx4 v9, s[16:19], s64 offen lds\n"
"v_mfma_f32_16x16x32_bf16 v[76:79], v[244:247], a[12:15], v[76:79]\n"
"ds_read_b64_tr_b16 v[242:243], v7 offset:41472\n"
"s_waitcnt lgkmcnt(6)\n"
"v_mfma_f32_16x16x32_bf16 v[80:83], v[224:227], a[8:11], v[80:83]\n"
"ds_read_b64_tr_b16 v[244:245], v8 offset:40960\n"
"buffer_load_dwordx4 a[56:59], v2, s[48:51], s63 offen sc1\n"
"v_mfma_f32_16x16x32_bf16 v[84:87], v[224:227], a[12:15], v[84:87]\n"
"ds_read_b64_tr_b16 v[246:247], v8 offset:41472\n"
"v_mov_b32 v176, 0\n"
"v_mfma_f32_16x16x32_bf16 v[88:91], v[228:231], a[8:11], v[88:91]\n"
"ds_read_b64_tr_b16 v[224:225], v5 offset:45056\n"
"buffer_load_dwordx4 a[60:63], v2, s[48:51], s63 offen offset:1024 sc1\n"
"v_mfma_f32_16x16x32_bf16 v[92:95], v[228:231], a[12:15], v[92:95]\n"
"ds_read_b64_tr_b16 v[226:227], v5 offset:45568\n"
"s_waitcnt vmcnt(13) lgkmcnt(6)\n"
"v_mfma_f32_16x16x32_bf16 v[64:67], v[232:235], a[16:19], v[64:67]\n"
"ds_read_b64_tr_b16 v[228:229], v6 offset:45056\n"
"s_add_u32 s63, s52, 3145728\n"
"v_mfma_f32_16x16x32_bf16 v[68:71], v[232:235], a[20:23], v[68:71]\n"
"ds_read_b64_tr_b16 v[230:231], v6 offset:45568\n"
"v_mov_b32 v177, 0\n"
"v_mfma_f32_16x16x32_bf16 v[72:75], v[236:239], a[16:19], v[72:75]\n"
"ds_read_b64_tr_b16 v[232:233], v7 offset:45056\n"
"buffer_load_dwordx4 a[64:67], v2, s[48:51], s63 offen sc1\n"
"v_mfma_f32_16x16x32_bf16 v[76:79], v[236:239], a[20:23], v[76:79]\n"
"ds_read_b64_tr_b16 v[234:235], v7 offset:45568\n"
"s_waitcnt lgkmcnt(6)\n"
"v_mfma_f32_16x16x32_bf16 v[80:83], v[240:243], a[16:19], v[80:83]\n"
"ds_read_b64_tr_b16 v[236:237], v8 offset:45056\n"
"buffer_load_dwordx4 a[68:71], v2, s[48:51], s63 offen offset:1024 sc1\n"
"v_mfma_f32_16x16x32_bf16 v[84:87], v[240:243], a[20:23], v[84:87]\n"
"ds_read_b64_tr_b16 v[238:239], v8 offset:45568\n"
"v_mov_b32 v178, 0\n"
"v_mfma_f32_16x16x32_bf16 v[88:91], v[244:247], a[16:19], v[88:91]\n"
"ds_read_b64_tr_b16 v[240:241], v5 offset:49152\n"
"v_mov_b32 v179, 0\n"
"v_mfma_f32_16x16x32_bf16 v[92:95], v[244:247], a[20:23], v[92:95]\n"
"ds_read_b64_tr_b16 v[242:243], v5 offset:49664\n"
"s_waitcnt vmcnt(13) lgkmcnt(6)\n"
"v_mfma_f32_16x16x32_bf16 v[64:67], v[224:227], a[24:27], v[64:67]\n"
"ds_read_b64_tr_b16 v[244:245], v6 offset:49152\n"
"s_add_u32 s63, s52, 3276800\n"
"v_mfma_f32_16x16x32_bf16 v[68:71], v[224:227], a[28:31], v[68:71]\n"
"ds_read_b64_tr_b16 v[246:247], v6 offset:49664\n"
"v_mov_b32 v180, 0\n"
"v_mfma_f32_16x16x32_bf16 v[72:75], v[228:231], a[24:27], v[72:75]\n"
"ds_read_b64_tr_b16 v[224:225], v7 offset:49152\n"
"buffer_load_dwordx4 a[72:75], v2, s[48:51], s63 offen sc1\n"
"v_mfma_f32_16x16x32_bf16 v[76:79], v[228:231], a[28:31], v[76:79]\n"
"ds_read_b64_tr_b16 v[226:227], v7 offset:49664\n"
"s_waitcnt lgkmcnt(6)\n"
"v_mfma_f32_16x16x32_bf16 v[80:83], v[232:235], a[24:27], v[80:83]\n"
"ds_read_b64_tr_b16 v[228:229], v8 offset:49152\n"
"buffer_load_dwordx4 a[76:79], v2, s[48:51], s63 offen offset:1024 sc1\n"
"v_mfma_f32_16x16x32_bf16 v[84:87], v[232:235], a[28:31], v[84:87]\n"
"ds_read_b64_tr_b16 v[230:231], v8 offset:49664\n"
"v_mov_b32 v181, 0\n"
"v_mfma_f32_16x16x32_bf16 v[88:91], v[236:239], a[24:27], v[88:91]\n"
"ds_read_b64_tr_b16 v[232:233], v5 offset:53248\n"
"v_mov_b32 v182, 0\n"
"v_mfma_f32_16x16x32_bf16 v[92:95], v[236:239], a[28:31], v[92:95]\n"
"ds_read_b64_tr_b16 v[234:235], v5 offset:53760\n"
"s_waitcnt vmcnt(13) lgkmcnt(6)\n"
"v_mfma_f32_16x16x32_bf16 v[64:67], v[240:243], a[32:35], v[64:67]\n"
"ds_read_b64_tr_b16 v[236:237], v6 offset:53248\n"
"s_add_u32 s63, s52, 3407872\n"
"v_mfma_f32_16x16x32_bf16 v[68:71], v[240:243], a[36:39], v[68:71]\n"
"ds_read_b64_tr_b16 v[238:239], v6 offset:53760\n"
"v_mov_b32 v183, 0\n"
"v_mfma_f32_16x16x32_bf16 v[72:75], v[244:247], a[32:35], v[72:75]\n"
"ds_read_b64_tr_b16 v[240:241], v7 offset:53248\n"
"buffer_load_dwordx4 a[80:83], v2, s[48:51], s63 offen sc1\n"
"v_mfma_f32_16x16x32_bf16 v[76:79], v[244:247], a[36:39], v[76:79]\n"
"ds_read_b64_tr_b16 v[242:243], v7 offset:53760\n"
"s_waitcnt lgkmcnt(6)\n"
"v_mfma_f32_16x16x32_bf16 v[80:83], v[224:227], a[32:35], v[80:83]\n"
"ds_read_b64_tr_b16 v[244:245], v8 offset:53248\n"
"buffer_load_dwordx4 a[84:87], v2, s[48:51], s63 offen offset:1024 sc1\n"
"v_mfma_f32_16x16x32_bf16 v[84:87], v[224:227], a[36:39], v[84:87]\n"
"ds_read_b64_tr_b16 v[246:247], v8 offset:53760\n"
"v_mov_b32 v184, 0\n"
"v_mfma_f32_16x16x32_bf16 v[88:91], v[228:231], a[32:35], v[88:91]\n"
"ds_read_b64_tr_b16 v[224:225], v5 offset:57344\n"
"v_mov_b32 v185, 0\n"
"v_mfma_f32_16x16x32_bf16 v[92:95], v[228:231], a[36:39], v[92:95]\n"
"ds_read_b64_tr_b16 v[226:227], v5 offset:57856\n"
"s_waitcnt vmcnt(13) lgkmcnt(6)\n"
"v_mfma_f32_16x16x32_bf16 v[64:67], v[232:235], a[40:43], v[64:67]\n"
"ds_read_b64_tr_b16 v[228:229], v6 offset:57344\n"
"s_add_u32 s63, s52, 3538944\n"
"v_mfma_f32_16x16x32_bf16 v[68:71], v[232:235], a[44:47], v[68:71]\n"
"ds_read_b64_tr_b16 v[230:231], v6 offset:57856\n"
"v_mov_b32 v186, 0\n"
"v_mfma_f32_16x16x32_bf16 v[72:75], v[236:239], a[40:43], v[72:75]\n"
"ds_read_b64_tr_b16 v[232:233], v7 offset:57344\n"
"buffer_load_dwordx4 a[88:91], v2, s[48:51], s63 offen sc1\n"
"v_mfma_f32_16x16x32_bf16 v[76:79], v[236:239], a[44:47], v[76:79]\n"
"ds_read_b64_tr_b16 v[234:235], v7 offset:57856\n"
"s_waitcnt lgkmcnt(6)\n"
"v_mfma_f32_16x16x32_bf16 v[80:83], v[240:243], a[40:43], v[80:83]\n"
"ds_read_b64_tr_b16 v[236:237], v8 offset:57344\n"
"buffer_load_dwordx4 a[92:95], v2, s[48:51], s63 offen offset:1024 sc1\n"
"v_mfma_f32_16x16x32_bf16 v[84:87], v[240:243], a[44:47], v[84:87]\n"
"ds_read_b64_tr_b16 v[238:239], v8 offset:57856\n"
"v_mov_b32 v187, 0\n"
"v_mfma_f32_16x16x32_bf16 v[88:91], v[244:247], a[40:43], v[88:91]\n"
"ds_read_b64_tr_b16 v[240:241], v5 offset:61440\n"
"v_mov_b32 v188, 0\n"
"v_mfma_f32_16x16x32_bf16 v[92:95], v[244:247], a[44:47], v[92:95]\n"
"ds_read_b64_tr_b16 v[242:243], v5 offset:61952\n"
"s_waitcnt vmcnt(11) lgkmcnt(6)\n"
"v_mfma_f32_16x16x32_bf16 v[64:67], v[224:227], a[48:51], v[64:67]\n"
"ds_read_b64_tr_b16 v[244:245], v6 offset:61440\n"
"s_add_u32 s63, s52, 3670016\n"
"v_mfma_f32_16x16x32_bf16 v[68:71], v[224:227], a[52:55], v[68:71]\n"
"ds_read_b64_tr_b16 v[246:247], v6 offset:61952\n"
"v_mov_b32 v189, 0\n"
"v_mfma_f32_16x16x32_bf16 v[72:75], v[228:231], a[48:51], v[72:75]\n"
"ds_read_b64_tr_b16 v[224:225], v7 offset:61440\n"
"buffer_load_dwordx4 a[96:99], v2, s[48:51], s63 offen sc1\n"
"v_mfma_f32_16x16x32_bf16 v[76:79], v[228:231], a[52:55], v[76:79]\n"
"ds_read_b64_tr_b16 v[226:227], v7 offset:61952\n"
"s_waitcnt lgkmcnt(6)\n"
"v_mfma_f32_16x16x32_bf16 v[80:83], v[232:235], a[48:51], v[80:83]\n"
"ds_read_b64_tr_b16 v[228:229], v8 offset:61440\n"
"buffer_load_dwordx4 a[100:103], v2, s[48:51], s63 offen offset:1024 sc1\n"
"v_mfma_f32_16x16x32_bf16 v[84:87], v[232:235], a[52:55], v[84:87]\n"
"ds_read_b64_tr_b16 v[230:231], v8 offset:61952\n"
"v_mov_b32 v190, 0\n"
"v_mfma_f32_16x16x32_bf16 v[88:91], v[236:239], a[48:51], v[88:91]\n"
"v_add_u32 v5, 65536, v5\n"
"v_add_u32 v6, 65536, v6\n"
"v_add_u32 v7, 65536, v7\n"
"v_add_u32 v8, 65536, v8\n"
"ds_read_b64_tr_b16 v[232:233], v5\n"
"v_mfma_f32_16x16x32_bf16 v[92:95], v[236:239], a[52:55], v[92:95]\n"
"ds_read_b64_tr_b16 v[234:235], v5 offset:512\n"
"s_waitcnt vmcnt(10) lgkmcnt(6)\n"
"v_mfma_f32_16x16x32_bf16 v[64:67], v[240:243], a[56:59], v[64:67]\n"
"ds_read_b64_tr_b16 v[236:237], v6\n"
"s_add_u32 s63, s52, 3801088\n"
"v_mfma_f32_16x16x32_bf16 v[68:71], v[240:243], a[60:63], v[68:71]\n"
"ds_read_b64_tr_b16 v[238:239], v6 offset:512\n"
"v_mov_b32 v191, 0\n"
"v_mfma_f32_16x16x32_bf16 v[72:75], v[244:247], a[56:59], v[72:75]\n"
"ds_read_b64_tr_b16 v[240:241], v7\n"
"buffer_load_dwordx4 a[104:107], v2, s[48:51], s63 offen sc1\n"
"v_mfma_f32_16x16x32_bf16 v[76:79], v[244:247], a[60:63], v[76:79]\n"
"ds_read_b64_tr_b16 v[242:243], v7 offset:512\n"
"s_waitcnt lgkmcnt(6)\n"
"v_mfma_f32_16x16x32_bf16 v[80:83], v[224:227], a[56:59], v[80:83]\n"
"ds_read_b64_tr_b16 v[244:245], v8\n"
"buffer_load_dwordx4 a[108:111], v2, s[48:51], s63 offen offset:1024 sc1\n"
"v_mfma_f32_16x16x32_bf16 v[84:87], v[224:227], a[60:63], v[84:87]\n"
"ds_read_b64_tr_b16 v[246:247], v8 offset:512\n"
"v_mfma_f32_16x16x32_bf16 v[88:91], v[228:231], a[56:59], v[88:91]\n"
"ds_read_b64_tr_b16 v[224:225], v5 offset:4096\n"
"v_mfma_f32_16x16x32_bf16 v[92:95], v[228:231], a[60:63], v[92:95]\n"
"ds_read_b64_tr_b16 v[226:227], v5 offset:4608\n"
"s_waitcnt vmcnt(10) lgkmcnt(6)\n"
"v_mfma_f32_16x16x32_bf16 v[64:67], v[232:235], a[64:67], v[64:67]\n"
"ds_read_b64_tr_b16 v[228:229], v6 offset:4096\n"
"s_add_u32 s63, s52, 3932160\n"
"v_mfma_f32_16x16x32_bf16 v[68:71], v[232:235], a[68:71], v[68:71]\n"
"ds_read_b64_tr_b16 v[230:231], v6 offset:4608\n"
"v_mfma_f32_16x16x32_bf16 v[72:75], v[236:239], a[64:67], v[72:75]\n"
"ds_read_b64_tr_b16 v[232:233], v7 offset:4096\n"
"buffer_load_dwordx4 a[112:115], v2, s[48:51], s63 offen sc1\n"
"v_mfma_f32_16x16x32_bf16 v[76:79], v[236:239], a[68:71], v[76:79]\n"
"ds_read_b64_tr_b16 v[234:235], v7 offset:4608\n"
"s_waitcnt lgkmcnt(6)\n"
"v_mfma_f32_16x16x32_bf16 v[80:83], v[240:243], a[64:67], v[80:83]\n"
"ds_read_b64_tr_b16 v[236:237], v8 offset:4096\n"
"buffer_load_dwordx4 a[116:119], v2, s[48:51], s63 offen offset:1024 sc1\n"
"v_mfma_f32_16x16x32_bf16 v[84:87], v[240:243], a[68:71], v[84:87]\n"
"ds_read_b64_tr_b16 v[238:239], v8 offset:4608\n"
"v_mfma_f32_16x16x32_bf16 v[88:91], v[244:247], a[64:67], v[88:91]\n"
"ds_read_b64_tr_b16 v[240:241], v5 offset:8192\n"
"v_mfma_f32_16x16x32_bf16 v[92:95], v[244:247], a[68:71], v[92:95]\n"
"ds_read_b64_tr_b16 v[242:243], v5 offset:8704\n"
"s_waitcnt vmcnt(10) lgkmcnt(6)\n"
"v_mfma_f32_16x16x32_bf16 v[64:67], v[224:227], a[72:75], v[64:67]\n"
"ds_read_b64_tr_b16 v[244:245], v6 offset:8192\n"
"s_add_u32 s63, s52, 4063232\n"
"v_mfma_f32_16x16x32_bf16 v[68:71], v[224:227], a[76:79], v[68:71]\n"
"ds_read_b64_tr_b16 v[246:247], v6 offset:8704\n"
"v_mfma_f32_16x16x32_bf16 v[72:75], v[228:231], a[72:75], v[72:75]\n"
"ds_read_b64_tr_b16 v[224:225], v7 offset:8192\n"
"buffer_load_dwordx4 a[120:123], v2, s[48:51], s63 offen sc1\n"
"v_mfma_f32_16x16x32_bf16 v[76:79], v[228:231], a[76:79], v[76:79]\n"
"ds_read_b64_tr_b16 v[226:227], v7 offset:8704\n"
"s_waitcnt lgkmcnt(6)\n"
"v_mfma_f32_16x16x32_bf16 v[80:83], v[232:235], a[72:75], v[80:83]\n"
"ds_read_b64_tr_b16 v[228:229], v8 offset:8192\n"
"buffer_load_dwordx4 a[124:127], v2, s[48:51], s63 offen offset:1024 sc1\n"
"v_mfma_f32_16x16x32_bf16 v[84:87], v[232:235], a[76:79], v[84:87]\n"
"ds_read_b64_tr_b16 v[230:231], v8 offset:8704\n"
"v_mfma_f32_16x16x32_bf16 v[88:91], v[236:239], a[72:75], v[88:91]\n"
"ds_read_b64_tr_b16 v[232:233], v5 offset:12288\n"
"v_mfma_f32_16x16x32_bf16 v[92:95], v[236:239], a[76:79], v[92:95]\n"
"ds_read_b64_tr_b16 v[234:235], v5 offset:12800\n"
"s_waitcnt vmcnt(10) lgkmcnt(6)\n"
"v_mfma_f32_16x16x32_bf16 v[64:67], v[240:243], a[80:83], v[64:67]\n"
"ds_read_b64_tr_b16 v[236:237], v6 offset:12288\n"
"s_add_u32 s63, s52, 4194304\n"
"v_mfma_f32_16x16x32_bf16 v[68:71], v[240:243], a[84:87], v[68:71]\n"
"ds_read_b64_tr_b16 v[238:239], v6 offset:12800\n"
"v_mfma_f32_16x16x32_bf16 v[72:75], v[244:247], a[80:83], v[72:75]\n"
"ds_read_b64_tr_b16 v[240:241], v7 offset:12288\n"
"buffer_load_dwordx4 a[128:131], v2, s[48:51], s63 offen sc1\n"
"v_mfma_f32_16x16x32_bf16 v[76:79], v[244:247], a[84:87], v[76:79]\n"
"ds_read_b64_tr_b16 v[242:243], v7 offset:12800\n"
"s_waitcnt lgkmcnt(6)\n"
"v_mfma_f32_16x16x32_bf16 v[80:83], v[224:227], a[80:83], v[80:83]\n"
"ds_read_b64_tr_b16 v[244:245], v8 offset:12288\n"
"buffer_load_dwordx4 a[132:135], v2, s[48:51], s63 offen offset:1024 sc1\n"
"v_mfma_f32_16x16x32_bf16 v[84:87], v[224:227], a[84:87], v[84:87]\n"
"ds_read_b64_tr_b16 v[246:247], v8 offset:12800\n"
"LPG2_%=:\n"
"s_cbranch_execz LPG6_%=\n"
"v_mfma_f32_16x16x32_bf16 v[88:91], v[228:231], a[80:83], v[88:91]\n"
"ds_read_b64_tr_b16 v[224:225], v5 offset:16384\n"
"v_mfma_f32_16x16x32_bf16 v[92:95], v[228:231], a[84:87], v[92:95]\n"
"ds_read_b64_tr_b16 v[226:227], v5 offset:16896\n"
"s_waitcnt vmcnt(10) lgkmcnt(6)\n"
"v_mfma_f32_16x16x32_bf16 v[64:67], v[232:235], a[88:91], v[64:67]\n"
"ds_read_b64_tr_b16 v[228:229], v6 offset:16384\n"
"s_add_u32 s63, s52, 4325376\n"
"v_mfma_f32_16x16x32_bf16 v[68:71], v[232:235], a[92:95], v[68:71]\n"
"ds_read_b64_tr_b16 v[230:231], v6 offset:16896\n"
"v_mfma_f32_16x16x32_bf16 v[72:75], v[236:239], a[88:91], v[72:75]\n"
"ds_read_b64_tr_b16 v[232:233], v7 offset:16384\n"
"buffer_load_dwordx4 a[136:139], v2, s[48:51], s63 offen sc1\n"
"v_mfma_f32_16x16x32_bf16 v[76:79], v[236:239], a[92:95], v[76:79]\n"
"ds_read_b64_tr_b16 v[234:235], v7 offset:16896\n"
"s_waitcnt lgkmcnt(6)\n"
"v_mfma_f32_16x16x32_bf16 v[80:83], v[240:243], a[88:91], v[80:83]\n"
"ds_read_b64_tr_b16 v[236:237], v8 offset:16384\n"
"buffer_load_dwordx4 a[140:143], v2, s[48:51], s63 offen offset:1024 sc1\n"
"v_mfma_f32_16x16x32_bf16 v[84:87], v[240:243], a[92:95], v[84:87]\n"
"ds_read_b64_tr_b16 v[238:239], v8 offset:16896\n"
"v_mfma_f32_16x16x32_bf16 v[88:91], v[244:247], a[88:91], v[88:91]\n"
"ds_read_b64_tr_b16 v[240:241], v5 offset:20480\n"
"v_mfma_f32_16x16x32_bf16 v[92:95], v[244:247], a[92:95], v[92:95]\n"
"ds_read_b64_tr_b16 v[242:243], v5 offset:20992\n"
"s_waitcnt vmcnt(10) lgkmcnt(6)\n"
"v_mfma_f32_16x16x32_bf16 v[64:67], v[224:227], a[96:99], v[64:67]\n"
"ds_read_b64_tr_b16 v[244:245], v6 offset:20480\n"
"s_add_u32 s63, s52, 4456448\n"
"v_mfma_f32_16x16x32_bf16 v[68:71], v[224:227], a[100:103], v[68:71]\n"
"ds_read_b64_tr_b16 v[246:247], v6 offset:20992\n"
"v_mfma_f32_16x16x32_bf16 v[72:75], v[228:231], a[96:99], v[72:75]\n"
"ds_read_b64_tr_b16 v[224:225], v7 offset:20480\n"
"buffer_load_dwordx4 a[144:147], v2, s[48:51], s63 offen sc1\n"
"v_mfma_f32_16x16x32_bf16 v[76:79], v[228:231], a[100:103], v[76:79]\n"
"ds_read_b64_tr_b16 v[226:227], v7 offset:20992\n"
"s_waitcnt lgkmcnt(6)\n"
"v_mfma_f32_16x16x32_bf16 v[80:83], v[232:235], a[96:99], v[80:83]\n"
"ds_read_b64_tr_b16 v[228:229], v8 offset:20480\n"
"buffer_load_dwordx4 a[148:151], v2, s[48:51], s63 offen offset:1024 sc1\n"
"v_mfma_f32_16x16x32_bf16 v[84:87], v[232:235], a[100:103], v[84:87]\n"
"ds_read_b64_tr_b16 v[230:231], v8 offset:20992\n"
"v_mfma_f32_16x16x32_bf16 v[88:91], v[236:239], a[96:99], v[88:91]\n"
"ds_read_b64_tr_b16 v[232:233], v5 offset:24576\n"
"v_mfma_f32_16x16x32_bf16 v[92:95], v[236:239], a[100:103], v[92:95]\n"
"ds_read_b64_tr_b16 v[234:235], v5 offset:25088\n"
"s_waitcnt vmcnt(10) lgkmcnt(6)\n"
"v_mfma_f32_16x16x32_bf16 v[64:67], v[240:243], a[104:107], v[64:67]\n"
"ds_read_b64_tr_b16 v[236:237], v6 offset:24576\n"
"s_add_u32 s63, s52, 4587520\n"
"v_mfma_f32_16x16x32_bf16 v[68:71], v[240:243], a[108:111], v[68:71]\n"
"ds_read_b64_tr_b16 v[238:239], v6 offset:25088\n"
"v_mfma_f32_16x16x32_bf16 v[72:75], v[244:247], a[104:107], v[72:75]\n"
"ds_read_b64_tr_b16 v[240:241], v7 offset:24576\n"
"buffer_load_dwordx4 a[152:155], v2, s[48:51], s63 offen sc1\n"
"v_mfma_f32_16x16x32_bf16 v[76:79], v[244:247], a[108:111], v[76:79]\n"
"ds_read_b64_tr_b16 v[242:243], v7 offset:25088\n"
"s_waitcnt lgkmcnt(6)\n"
"v_mfma_f32_16x16x32_bf16 v[80:83], v[224:227], a[104:107], v[80:83]\n"
"ds_read_b64_tr_b16 v[244:245], v8 offset:24576\n"
"buffer_load_dwordx4 a[156:159], v2, s[48:51], s63 offen offset:1024 sc1\n"
"v_mfma_f32_16x16x32_bf16 v[84:87], v[224:227], a[108:111], v[84:87]\n"
"ds_read_b64_tr_b16 v[246:247], v8 offset:25088\n"
"v_mfma_f32_16x16x32_bf16 v[88:91], v[228:231], a[104:107], v[88:91]\n"
"ds_read_b64_tr_b16 v[224:225], v5 offset:28672\n"
"v_mfma_f32_16x16x32_bf16 v[92:95], v[228:231], a[108:111], v[92:95]\n"
"ds_read_b64_tr_b16 v[226:227], v5 offset:29184\n"
"s_waitcnt vmcnt(10) lgkmcnt(6)\n"
"v_mfma_f32_16x16x32_bf16 v[64:67], v[232:235], a[112:115], v[64:67]\n"
"ds_read_b64_tr_b16 v[228:229], v6 offset:28672\n"
"s_add_u32 s63, s52, 4718592\n"
"v_mfma_f32_16x16x32_bf16 v[68:71], v[232:235], a[116:119], v[68:71]\n"
"ds_read_b64_tr_b16 v[230:231], v6 offset:29184\n"
"v_mfma_f32_16x16x32_bf16 v[72:75], v[236:239], a[112:115], v[72:75]\n"
"ds_read_b64_tr_b16 v[232:233], v7 offset:28672\n"
"buffer_load_dwordx4 a[160:163], v2, s[48:51], s63 offen sc1\n"
"v_mfma_f32_16x16x32_bf16 v[76:79], v[236:239], a[116:119], v[76:79]\n"
"ds_read_b64_tr_b16 v[234:235], v7 offset:29184\n"
"s_waitcnt lgkmcnt(6)\n"
"v_mfma_f32_16x16x32_bf16 v[80:83], v[240:243], a[112:115], v[80:83]\n"
"ds_read_b64_tr_b16 v[236:237], v8 offset:28672\n"
"buffer_load_dwordx4 a[164:167], v2, s[48:51], s63 offen offset:1024 sc1\n"
"v_mfma_f32_16x16x32_bf16 v[84:87], v[240:243], a[116:119], v[84:87]\n"
"ds_read_b64_tr_b16 v[238:239], v8 offset:29184\n"
"v_mfma_f32_16x16x32_bf16 v[88:91], v[244:247], a[112:115], v[88:91]\n"
"s_barrier\n"
"ds_read_b64_tr_b16 v[240:241], v5 offset:32768\n"
"v_mfma_f32_16x16x32_bf16 v[92:95], v[244:247], a[116:119], v[92:95]\n"
"ds_read_b64_tr_b16 v[242:243], v5 offset:33280\n"
"s_waitcnt vmcnt(10) lgkmcnt(6)\n"
"v_mfma_f32_16x16x32_bf16 v[64:67], v[224:227], a[120:123], v[64:67]\n"
"ds_read_b64_tr_b16 v[244:245], v6 offset:32768\n"
"s_add_u32 s63, s52, 4849664\n"
"v_mfma_f32_16x16x32_bf16 v[68:71], v[224:227], a[124:127], v[68:71]\n"
"ds_read_b64_tr_b16 v[246:247], v6 offset:33280\n"
"v_mfma_f32_16x16x32_bf16 v[72:75], v[228:231], a[120:123], v[72:75]\n"
"ds_read_b64_tr_b16 v[224:225], v7 offset:32768\n"
"buffer_load_dwordx4 a[168:171], v2, s[48:51], s63 offen sc1\n"
"v_mfma_f32_16x16x32_bf16 v[76:79], v[228:231], a[124:127], v[76:79]\n"
"ds_read_b64_tr_b16 v[226:227], v7 offset:33280\n"
"s_waitcnt lgkmcnt(6)\n"
"v_mfma_f32_16x16x32_bf16 v[80:83], v[232:235], a[120:123], v[80:83]\n"
"ds_read_b64_tr_b16 v[228:229], v8 offset:32768\n"
"buffer_load_dwordx4 a[172:175], v2, s[48:51], s63 offen offset:1024 sc1\n"
"v_mfma_f32_16x16x32_bf16 v[84:87], v[232:235], a[124:127], v[84:87]\n"
"ds_read_b64_tr_b16 v[230:231], v8 offset:33280\n"
"v_mfma_f32_16x16x32_bf16 v[88:91], v[236:239], a[120:123], v[88:91]\n"
"ds_read_b64_tr_b16 v[232:233], v5 offset:36864\n"
"v_mfma_f32_16x16x32_bf16 v[92:95], v[236:239], a[124:127], v[92:95]\n"
"ds_read_b64_tr_b16 v[234:235], v5 offset:37376\n"
"s_waitcnt vmcnt(10) lgkmcnt(6)\n"
"s_add_u32 s62, s55, 57114624\n"
"buffer_load_dwordx4 v[128:131], v2, s[48:51], s62 offen sc1\n"
"v_mfma_f32_16x16x32_bf16 v[64:67], v[240:243], a[128:131], v[64:67]\n"
"ds_read_b64_tr_b16 v[236:237], v6 offset:36864\n"
"s_add_u32 s63, s52, 4980736\n"
"v_mfma_f32_16x16x32_bf16 v[68:71], v[240:243], a[132:135], v[68:71]\n"
"ds_read_b64_tr_b16 v[238:239], v6 offset:37376\n"
"v_mfma_f32_16x16x32_bf16 v[72:75], v[244:247], a[128:131], v[72:75]\n"
"ds_read_b64_tr_b16 v[240:241], v7 offset:36864\n"
"buffer_load_dwordx4 a[176:179], v2, s[48:51], s63 offen sc1\n"
"v_mfma_f32_16x16x32_bf16 v[76:79], v[244:247], a[132:135], v[76:79]\n"
"ds_read_b64_tr_b16 v[242:243], v7 offset:37376\n"
"s_waitcnt lgkmcnt(6)\n"
"v_mfma_f32_16x16x32_bf16 v[80:83], v[224:227], a[128:131], v[80:83]\n"
"ds_read_b64_tr_b16 v[244:245], v8 offset:36864\n"
"buffer_load_dwordx4 a[180:183], v2, s[48:51], s63 offen offset:1024 sc1\n"
"v_mfma_f32_16x16x32_bf16 v[84:87], v[224:227], a[132:135], v[84:87]\n"
"ds_read_b64_tr_b16 v[246:247], v8 offset:37376\n"
"v_mfma_f32_16x16x32_bf16 v[88:91], v[228:231], a[128:131], v[88:91]\n"
"ds_read_b64_tr_b16 v[224:225], v5 offset:40960\n"
"v_mfma_f32_16x16x32_bf16 v[92:95], v[228:231], a[132:135], v[92:95]\n"
"ds_read_b64_tr_b16 v[226:227], v5 offset:41472\n"
"s_waitcnt vmcnt(11) lgkmcnt(6)\n"
"v_mfma_f32_16x16x32_bf16 v[64:67], v[232:235], a[136:139], v[64:67]\n"
"ds_read_b64_tr_b16 v[228:229], v6 offset:40960\n"
"s_add_u32 s63, s52, 5111808\n"
"v_mfma_f32_16x16x32_bf16 v[68:71], v[232:235], a[140:143], v[68:71]\n"
"ds_read_b64_tr_b16 v[230:231], v6 offset:41472\n"
"v_mfma_f32_16x16x32_bf16 v[72:75], v[236:239], a[136:139], v[72:75]\n"
"ds_read_b64_tr_b16 v[232:233], v7 offset:40960\n"
"buffer_load_dwordx4 a[184:187], v2, s[48:51], s63 offen sc1\n"
"v_mfma_f32_16x16x32_bf16 v[76:79], v[236:239], a[140:143], v[76:79]\n"
"ds_read_b64_tr_b16 v[234:235], v7 offset:41472\n"
"s_waitcnt lgkmcnt(6)\n"
"v_mfma_f32_16x16x32_bf16 v[80:83], v[240:243], a[136:139], v[80:83]\n"
"ds_read_b64_tr_b16 v[236:237], v8 offset:40960\n"
"buffer_load_dwordx4 a[188:191], v2, s[48:51], s63 offen offset:1024 sc1\n"
"v_mfma_f32_16x16x32_bf16 v[84:87], v[240:243], a[140:143], v[84:87]\n"
"ds_read_b64_tr_b16 v[238:239], v8 offset:41472\n"
"v_mfma_f32_16x16x32_bf16 v[88:91], v[244:247], a[136:139], v[88:91]\n"
"ds_read_b64_tr_b16 v[240:241], v5 offset:45056\n"
"v_mfma_f32_16x16x32_bf16 v[92:95], v[244:247], a[140:143], v[92:95]\n"
"ds_read_b64_tr_b16 v[242:243], v5 offset:45568\n"
"s_waitcnt vmcnt(11) lgkmcnt(6)\n"
"v_mfma_f32_16x16x32_bf16 v[64:67], v[224:227], a[144:147], v[64:67]\n"
"ds_read_b64_tr_b16 v[244:245], v6 offset:45056\n"
"s_add_u32 s63, s52, 5242880\n"
"v_mfma_f32_16x16x32_bf16 v[68:71], v[224:227], a[148:151], v[68:71]\n"
"ds_read_b64_tr_b16 v[246:247], v6 offset:45568\n"
"v_mfma_f32_16x16x32_bf16 v[72:75], v[228:231], a[144:147], v[72:75]\n"
"ds_read_b64_tr_b16 v[224:225], v7 offset:45056\n"
"buffer_load_dwordx4 a[192:195], v2, s[48:51], s63 offen sc1\n"
"v_mfma_f32_16x16x32_bf16 v[76:79], v[228:231], a[148:151], v[76:79]\n"
"ds_read_b64_tr_b16 v[226:227], v7 offset:45568\n"
"s_waitcnt lgkmcnt(6)\n"
"v_mfma_f32_16x16x32_bf16 v[80:83], v[232:235], a[144:147], v[80:83]\n"
"ds_read_b64_tr_b16 v[228:229], v8 offset:45056\n"
"buffer_load_dwordx4 a[196:199], v2, s[48:51], s63 offen offset:1024 sc1\n"
"v_mfma_f32_16x16x32_bf16 v[84:87], v[232:235], a[148:151], v[84:87]\n"
"ds_read_b64_tr_b16 v[230:231], v8 offset:45568\n"
"v_mfma_f32_16x16x32_bf16 v[88:91], v[236:239], a[144:147], v[88:91]\n"
"ds_read_b64_tr_b16 v[232:233], v5 offset:49152\n"
"v_mfma_f32_16x16x32_bf16 v[92:95], v[236:239], a[148:151], v[92:95]\n"
"ds_read_b64_tr_b16 v[234:235], v5 offset:49664\n"
"s_waitcnt vmcnt(11) lgkmcnt(6)\n"
"v_mfma_f32_16x16x32_bf16 v[64:67], v[240:243], a[152:155], v[64:67]\n"
"ds_read_b64_tr_b16 v[236:237], v6 offset:49152\n"
"s_add_u32 s63, s52, 5373952\n"
"v_mfma_f32_16x16x32_bf16 v[68:71], v[240:243], a[156:159], v[68:71]\n"
"ds_read_b64_tr_b16 v[238:239], v6 offset:49664\n"
"v_mfma_f32_16x16x32_bf16 v[72:75], v[244:247], a[152:155], v[72:75]\n"
"ds_read_b64_tr_b16 v[240:241], v7 offset:49152\n"
"buffer_load_dwordx4 a[200:203], v2, s[48:51], s63 offen sc1\n"
"v_mfma_f32_16x16x32_bf16 v[76:79], v[244:247], a[156:159], v[76:79]\n"
"ds_read_b64_tr_b16 v[242:243], v7 offset:49664\n"
"s_waitcnt lgkmcnt(6)\n"
"v_mfma_f32_16x16x32_bf16 v[80:83], v[224:227], a[152:155], v[80:83]\n"
"ds_read_b64_tr_b16 v[244:245], v8 offset:49152\n"
"buffer_load_dwordx4 a[204:207], v2, s[48:51], s63 offen offset:1024 sc1\n"
"v_mfma_f32_16x16x32_bf16 v[84:87], v[224:227], a[156:159], v[84:87]\n"
"ds_read_b64_tr_b16 v[246:247], v8 offset:49664\n"
"v_mfma_f32_16x16x32_bf16 v[88:91], v[228:231], a[152:155], v[88:91]\n"
"ds_read_b64_tr_b16 v[224:225], v5 offset:53248\n"
"v_mfma_f32_16x16x32_bf16 v[92:95], v[228:231], a[156:159], v[92:95]\n"
"ds_read_b64_tr_b16 v[226:227], v5 offset:53760\n"
"s_waitcnt vmcnt(11) lgkmcnt(6)\n"
"v_mfma_f32_16x16x32_bf16 v[64:67], v[232:235], a[160:163], v[64:67]\n"
"ds_read_b64_tr_b16 v[228:229], v6 offset:53248\n"
"s_add_u32 s63, s52, 5505024\n"
"v_mfma_f32_16x16x32_bf16 v[68:71], v[232:235], a[164:167], v[68:71]\n"
"ds_read_b64_tr_b16 v[230:231], v6 offset:53760\n"
"v_mfma_f32_16x16x32_bf16 v[72:75], v[236:239], a[160:163], v[72:75]\n"
"ds_read_b64_tr_b16 v[232:233], v7 offset:53248\n"
"buffer_load_dwordx4 a[208:211], v2, s[48:51], s63 offen sc1\n"
"v_mfma_f32_16x16x32_bf16 v[76:79], v[236:239], a[164:167], v[76:79]\n"
"ds_read_b64_tr_b16 v[234:235], v7 offset:53760\n"
"s_waitcnt lgkmcnt(6)\n"
"v_mfma_f32_16x16x32_bf16 v[80:83], v[240:243], a[160:163], v[80:83]\n"
"ds_read_b64_tr_b16 v[236:237], v8 offset:53248\n"
"buffer_load_dwordx4 a[212:215], v2, s[48:51], s63 offen offset:1024 sc1\n"
"v_mfma_f32_16x16x32_bf16 v[84:87], v[240:243], a[164:167], v[84:87]\n"
"ds_read_b64_tr_b16 v[238:239], v8 offset:53760\n"
"v_mfma_f32_16x16x32_bf16 v[88:91], v[244:247], a[160:163], v[88:91]\n"
"ds_read_b64_tr_b16 v[240:241], v5 offset:57344\n"
"v_mfma_f32_16x16x32_bf16 v[92:95], v[244:247], a[164:167], v[92:95]\n"
"ds_read_b64_tr_b16 v[242:243], v5 offset:57856\n"
"s_waitcnt vmcnt(11) lgkmcnt(6)\n"
"v_mfma_f32_16x16x32_bf16 v[64:67], v[224:227], a[168:171], v[64:67]\n"
"ds_read_b64_tr_b16 v[244:245], v6 offset:57344\n"
"s_add_u32 s63, s52, 5636096\n"
"v_mfma_f32_16x16x32_bf16 v[68:71], v[224:227], a[172:175], v[68:71]\n"
"ds_read_b64_tr_b16 v[246:247], v6 offset:57856\n"
"v_mfma_f32_16x16x32_bf16 v[72:75], v[228:231], a[168:171], v[72:75]\n"
"ds_read_b64_tr_b16 v[224:225], v7 offset:57344\n"
"buffer_load_dwordx4 a[216:219], v2, s[48:51], s63 offen sc1\n"
"v_mfma_f32_16x16x32_bf16 v[76:79], v[228:231], a[172:175], v[76:79]\n"
"ds_read_b64_tr_b16 v[226:227], v7 offset:57856\n"
"s_waitcnt lgkmcnt(6)\n"
"v_mfma_f32_16x16x32_bf16 v[80:83], v[232:235], a[168:171], v[80:83]\n"
"ds_read_b64_tr_b16 v[228:229], v8 offset:57344\n"
"buffer_load_dwordx4 a[220:223], v2, s[48:51], s63 offen offset:1024 sc1\n"
"v_mfma_f32_16x16x32_bf16 v[84:87], v[232:235], a[172:175], v[84:87]\n"
"ds_read_b64_tr_b16 v[230:231], v8 offset:57856\n"
"v_mfma_f32_16x16x32_bf16 v[88:91], v[236:239], a[168:171], v[88:91]\n"
"ds_read_b64_tr_b16 v[232:233], v5 offset:61440\n"
"v_mfma_f32_16x16x32_bf16 v[92:95], v[236:239], a[172:175], v[92:95]\n"
"ds_read_b64_tr_b16 v[234:235], v5 offset:61952\n"
"s_waitcnt vmcnt(10) lgkmcnt(6)\n"
"v_xor_b32 v144, s10, v128\n"
"s_add_u32 s63, s52, 5767168\n"
"v_xor_b32 v145, s11, v130\n"
"buffer_load_dwordx4 a[224:227], v2, s[48:51], s63 offen sc1\n"
"v_or_b32 v146, v144, v145\n"
"buffer_load_dwordx4 a[228:231], v2, s[48:51], s63 offen offset:1024 sc1\n"
"v_cmp_ne_u32 vcc, 0, v146\n"
"s_nop 4\n"
"s_cbranch_vccz Ld6_%=\n"
"s_mov_b32 s65, 0\n"
"Lp5_%=:\n"
"s_add_u32 s62, s55, 57114624\n"
"buffer_load_dwordx4 v[128:131], v2, s[48:51], s62 offen sc1\n"
"s_add_u32 s65, s65, 1\n"
"s_cmp_gt_u32 s65, 131072\n"
"s_waitcnt vmcnt(0)\n"
"s_cbranch_scc1 Ld6_%=\n"
"v_xor_b32 v144, s10, v128\n"
"v_xor_b32 v145, s11, v130\n"
"v_or_b32 v146, v144, v145\n"
"v_cmp_ne_u32 vcc, 0, v146\n"
"s_nop 4\n"
"s_cbranch_vccnz Lp5_%=\n"
"Ld6_%=:\n"
"ds_write_b32 v255, v129 offset:32768\n"
"v_mfma_f32_16x16x32_bf16 v[64:67], v[240:243], a[176:179], v[64:67]\n"
"ds_read_b64_tr_b16 v[236:237], v6 offset:61440\n"
"v_mfma_f32_16x16x32_bf16 v[68:71], v[240:243], a[180:183], v[68:71]\n"
"ds_read_b64_tr_b16 v[238:239], v6 offset:61952\n"
"v_mfma_f32_16x16x32_bf16 v[72:75], v[244:247], a[176:179], v[72:75]\n"
"ds_read_b64_tr_b16 v[240:241], v7 offset:61440\n"
"v_mfma_f32_16x16x32_bf16 v[76:79], v[244:247], a[180:183], v[76:79]\n"
"ds_read_b64_tr_b16 v[242:243], v7 offset:61952\n"
"s_waitcnt lgkmcnt(7)\n"
"v_mfma_f32_16x16x32_bf16 v[80:83], v[224:227], a[176:179], v[80:83]\n"
"ds_read_b64_tr_b16 v[244:245], v8 offset:61440\n"
"v_mfma_f32_16x16x32_bf16 v[84:87], v[224:227], a[180:183], v[84:87]\n"
"ds_read_b64_tr_b16 v[246:247], v8 offset:61952\n"
"v_mfma_f32_16x16x32_bf16 v[88:91], v[228:231], a[176:179], v[88:91]\n"
"s_waitcnt lgkmcnt(6)\n"
"s_barrier\n"
"s_nop 0\n"
"v_add_u32 v5, 65536, v5\n"
"v_add_u32 v6, 65536, v6\n"
"v_add_u32 v7, 65536, v7\n"
"v_add_u32 v8, 65536, v8\n"
"ds_read_b64_tr_b16 v[224:225], v5\n"
"v_mfma_f32_16x16x32_bf16 v[92:95], v[228:231], a[180:183], v[92:95]\n"
"ds_read_b64_tr_b16 v[226:227], v5 offset:512\n"
"s_waitcnt vmcnt(10) lgkmcnt(6)\n"
"ds_read_b128 v[128:131], v2 offset:32768\n"
"v_mfma_f32_16x16x32_bf16 v[64:67], v[232:235], a[184:187], v[64:67]\n"
"ds_read_b64_tr_b16 v[228:229], v6\n"
"s_add_u32 s63, s52, 5898240\n"
"v_mfma_f32_16x16x32_bf16 v[68:71], v[232:235], a[188:191], v[68:71]\n"
"ds_read_b64_tr_b16 v[230:231], v6 offset:512\n"
"v_mfma_f32_16x16x32_bf16 v[72:75], v[236:239], a[184:187], v[72:75]\n"
"ds_read_b64_tr_b16 v[232:233], v7\n"
"buffer_load_dwordx4 a[232:235], v2, s[48:51], s63 offen sc1\n"
"v_mfma_f32_16x16x32_bf16 v[76:79], v[236:239], a[188:191], v[76:79]\n"
"ds_read_b64_tr_b16 v[234:235], v7 offset:512\n"
"s_waitcnt lgkmcnt(7)\n"
"v_mfma_f32_16x16x32_bf16 v[80:83], v[240:243], a[184:187], v[80:83]\n"
"ds_read_b64_tr_b16 v[236:237], v8\n"
"buffer_load_dwordx4 a[236:239], v2, s[48:51], s63 offen offset:1024 sc1\n"
"v_mfma_f32_16x16x32_bf16 v[84:87], v[240:243], a[188:191], v[84:87]\n"
"ds_read_b64_tr_b16 v[238:239], v8 offset:512\n"
"s_add_u32 m0, s61, 0\n"
"v_mfma_f32_16x16x32_bf16 v[88:91], v[244:247], a[184:187], v[88:91]\n"
"ds_read_b64_tr_b16 v[240:241], v5 offset:4096\n"
"s_add_u32 s64, s61, 1048576\n"
"v_mfma_f32_16x16x32_bf16 v[92:95], v[244:247], a[188:191], v[92:95]\n"
"ds_read_b64_tr_b16 v[242:243], v5 offset:4608\n"
"s_waitcnt vmcnt(10) lgkmcnt(6)\n"
"v_mfma_f32_16x16x32_bf16 v[64:67], v[224:227], a[192:195], v[64:67]\n"
"ds_read_b64_tr_b16 v[244:245], v6 offset:4096\n"
"s_add_u32 s63, s52, 6029312\n"
"v_mfma_f32_16x16x32_bf16 v[68:71], v[224:227], a[196:199], v[68:71]\n"
"ds_read_b64_tr_b16 v[246:247], v6 offset:4608\n"
"v_mfma_f32_16x16x32_bf16 v[72:75], v[228:231], a[192:195], v[72:75]\n"
"ds_read_b64_tr_b16 v[224:225], v7 offset:4096\n"
"buffer_load_dwordx4 a[240:243], v2, s[48:51], s63 offen sc1\n"
"v_mfma_f32_16x16x32_bf16 v[76:79], v[228:231], a[196:199], v[76:79]\n"
"ds_read_b64_tr_b16 v[226:227], v7 offset:4608\n"
"s_waitcnt lgkmcnt(6)\n"
"v_mfma_f32_16x16x32_bf16 v[80:83], v[232:235], a[192:195], v[80:83]\n"
"ds_read_b64_tr_b16 v[228:229], v8 offset:4096\n"
"buffer_load_dwordx4 a[244:247], v2, s[48:51], s63 offen offset:1024 sc1\n"
"v_mfma_f32_16x16x32_bf16 v[84:87], v[232:235], a[196:199], v[84:87]\n"
"ds_read_b64_tr_b16 v[230:231], v8 offset:4608\n"
"v_max3_f32 v144, v128, v129, v130\n"
"v_mfma_f32_16x16x32_bf16 v[88:91], v[236:239], a[192:195], v[88:91]\n"
"ds_read_b64_tr_b16 v[232:233], v5 offset:8192\n"
"v_max_f32 v144, v144, v131\n"
"v_mfma_f32_16x16x32_bf16 v[92:95], v[236:239], a[196:199], v[92:95]\n"
"ds_read_b64_tr_b16 v[234:235], v5 offset:8704\n"
"s_waitcnt vmcnt(10) lgkmcnt(6)\n"
"v_mfma_f32_16x16x32_bf16 v[64:67], v[240:243], a[200:203], v[64:67]\n"
"ds_read_b64_tr_b16 v[236:237], v6 offset:8192\n"
"s_add_u32 s63, s52, 6160384\n"
"v_mfma_f32_16x16x32_bf16 v[68:71], v[240:243], a[204:207], v[68:71]\n"
"ds_read_b64_tr_b16 v[238:239], v6 offset:8704\n"
"v_max_f32_dpp v144, v144, v144 row_ror:8 row_mask:0xf bank_mask:0xf\n"
"v_mfma_f32_16x16x32_bf16 v[72:75], v[244:247], a[200:203], v[72:75]\n"
"ds_read_b64_tr_b16 v[240:241], v7 offset:8192\n"
"buffer_load_dwordx4 a[248:251], v2, s[48:51], s63 offen sc1\n"
"v_mfma_f32_16x16x32_bf16 v[76:79], v[244:247], a[204:207], v[76:79]\n"
"ds_read_b64_tr_b16 v[242:243], v7 offset:8704\n"
"s_waitcnt lgkmcnt(6)\n"
"v_mfma_f32_16x16x32_bf16 v[80:83], v[224:227], a[200:203], v[80:83]\n"
"ds_read_b64_tr_b16 v[244:245], v8 offset:8192\n"
"buffer_load_dwordx4 a[252:255], v2, s[48:51], s63 offen offset:1024 sc1\n"
"v_mfma_f32_16x16x32_bf16 v[84:87], v[224:227], a[204:207], v[84:87]\n"
"ds_read_b64_tr_b16 v[246:247], v8 offset:8704\n"
"v_max_f32_dpp v144, v144, v144 row_ror:4 row_mask:0xf bank_mask:0xf\n"
"v_mfma_f32_16x16x32_bf16 v[88:91], v[228:231], a[200:203], v[88:91]\n"
"ds_read_b64_tr_b16 v[224:225], v5 offset:12288\n"
"v_max_f32_dpp v144, v144, v144 row_ror:2 row_mask:0xf bank_mask:0xf\n"
"v_mfma_f32_16x16x32_bf16 v[92:95], v[228:231], a[204:207], v[92:95]\n"
"ds_read_b64_tr_b16 v[226:227], v5 offset:12800\n"
"s_waitcnt vmcnt(10) lgkmcnt(6)\n"
"v_mfma_f32_16x16x32_bf16 v[64:67], v[232:235], a[208:211], v[64:67]\n"
"ds_read_b64_tr_b16 v[228:229], v6 offset:12288\n"
"v_max_f32_dpp v144, v144, v144 row_ror:1 row_mask:0xf bank_mask:0xf\n"
"v_mfma_f32_16x16x32_bf16 v[68:71], v[232:235], a[212:215], v[68:71]\n"
"ds_read_b64_tr_b16 v[230:231], v6 offset:12800\n"
"v_mov_b32 v145, v144\n"
"v_mfma_f32_16x16x32_bf16 v[72:75], v[236:239], a[208:211], v[72:75]\n"
"ds_read_b64_tr_b16 v[232:233], v7 offset:12288\n"
"v_permlane16_swap_b32 v144, v145\n"
"v_mfma_f32_16x16x32_bf16 v[76:79], v[236:239], a[212:215], v[76:79]\n"
"ds_read_b64_tr_b16 v[234:235], v7 offset:12800\n"
"s_waitcnt lgkmcnt(6)\n"
"v_mfma_f32_16x16x32_bf16 v[80:83], v[240:243], a[208:211], v[80:83]\n"
"ds_read_b64_tr_b16 v[236:237], v8 offset:12288\n"
"v_max_f32 v144, v144, v145\n"
"v_mfma_f32_16x16x32_bf16 v[84:87], v[240:243], a[212:215], v[84:87]\n"
"ds_read_b64_tr_b16 v[238:239], v8 offset:12800\n"
"v_mfma_f32_16x16x32_bf16 v[88:91], v[244:247], a[208:211], v[88:91]\n"
"ds_read_b64_tr_b16 v[240:241], v5 offset:16384\n"
"v_mov_b32 v145, v144\n"
"v_mfma_f32_16x16x32_bf16 v[92:95], v[244:247], a[212:215], v[92:95]\n"
"ds_read_b64_tr_b16 v[242:243], v5 offset:16896\n"
"s_waitcnt vmcnt(8) lgkmcnt(6)\n"
"v_mfma_f32_16x16x32_bf16 v[64:67], v[224:227], a[216:219], v[64:67]\n"
"ds_read_b64_tr_b16 v[244:245], v6 offset:16384\n"
"v_permlane32_swap_b32 v144, v145\n"
"v_mfma_f32_16x16x32_bf16 v[68:71], v[224:227], a[220:223], v[68:71]\n"
"ds_read_b64_tr_b16 v[246:247], v6 offset:16896\n"
"v_mfma_f32_16x16x32_bf16 v[72:75], v[228:231], a[216:219], v[72:75]\n"
"ds_read_b64_tr_b16 v[224:225], v7 offset:16384\n"
"buffer_load_dwordx4 v2, s[48:51], s64 offen sc1 lds\n"
"v_mfma_f32_16x16x32_bf16 v[76:79], v[228:231], a[220:223], v[76:79]\n"
"ds_read_b64_tr_b16 v[226:227], v7 offset:16896\n"
"s_waitcnt lgkmcnt(6)\n"
"v_mfma_f32_16x16x32_bf16 v[80:83], v[232:235], a[216:219], v[80:83]\n"
"ds_read_b64_tr_b16 v[228:229], v8 offset:16384\n"
"v_max_f32 v46, v144, v145\n"
"v_mfma_f32_16x16x32_bf16 v[84:87], v[232:235], a[220:223], v[84:87]\n"
"ds_read_b64_tr_b16 v[230:231], v8 offset:16896\n"
"v_mfma_f32_16x16x32_bf16 v[88:91], v[236:239], a[216:219], v[88:91]\n"
"ds_read_b64_tr_b16 v[232:233], v5 offset:20480\n"
"buffer_load_dwordx4 v2, s[48:51], s64 offen offset:1024 sc1 lds\n"
"v_mfma_f32_16x16x32_bf16 v[92:95], v[236:239], a[220:223], v[92:95]\n"
"ds_read_b64_tr_b16 v[234:235], v5 offset:20992\n"
"s_waitcnt vmcnt(8) lgkmcnt(6)\n"
"v_mfma_f32_16x16x32_bf16 v[64:67], v[240:243], a[224:227], v[64:67]\n"
"ds_read_b64_tr_b16 v[236:237], v6 offset:20480\n"
"v_mfma_f32_16x16x32_bf16 v[68:71], v[240:243], a[228:231], v[68:71]\n"
"ds_read_b64_tr_b16 v[238:239], v6 offset:20992\n"
"v_mfma_f32_16x16x32_bf16 v[72:75], v[244:247], a[224:227], v[72:75]\n"
"ds_read_b64_tr_b16 v[240:241], v7 offset:20480\n"
"buffer_load_dwordx4 v2, s[48:51], s64 offen offset:2048 sc1 lds\n"
"v_mfma_f32_16x16x32_bf16 v[76:79], v[244:247], a[228:231], v[76:79]\n"
"ds_read_b64_tr_b16 v[242:243], v7 offset:20992\n"
"s_waitcnt lgkmcnt(6)\n"
"v_mfma_f32_16x16x32_bf16 v[80:83], v[224:227], a[224:227], v[80:83]\n"
"ds_read_b64_tr_b16 v[244:245], v8 offset:20480\n"
"v_mfma_f32_16x16x32_bf16 v[84:87], v[224:227], a[228:231], v[84:87]\n"
"ds_read_b64_tr_b16 v[246:247], v8 offset:20992\n"
"v_mfma_f32_16x16x32_bf16 v[88:91], v[228:231], a[224:227], v[88:91]\n"
"ds_read_b64_tr_b16 v[224:225], v5 offset:24576\n"
"buffer_load_dwordx4 v2, s[48:51], s64 offen offset:3072 sc1 lds\n"
"v_mfma_f32_16x16x32_bf16 v[92:95], v[228:231], a[228:231], v[92:95]\n"
"ds_read_b64_tr_b16 v[226:227], v5 offset:25088\n"
"s_waitcnt vmcnt(8) lgkmcnt(6)\n"
"v_mfma_f32_16x16x32_bf16 v[64:67], v[232:235], a[232:235], v[64:67]\n"
"ds_read_b64_tr_b16 v[228:229], v6 offset:24576\n"
"s_add_u32 m0, s61, 16384\n"
"s_add_u32 s64, s61, 1064960\n"
"v_mfma_f32_16x16x32_bf16 v[68:71], v[232:235], a[236:239], v[68:71]\n"
"ds_read_b64_tr_b16 v[230:231], v6 offset:25088\n"
"v_mfma_f32_16x16x32_bf16 v[72:75], v[236:239], a[232:235], v[72:75]\n"
"ds_read_b64_tr_b16 v[232:233], v7 offset:24576\n"
"buffer_load_dwordx4 v2, s[48:51], s64 offen sc1 lds\n"
"v_mfma_f32_16x16x32_bf16 v[76:79], v[236:239], a[236:239], v[76:79]\n"
"ds_read_b64_tr_b16 v[234:235], v7 offset:25088\n"
"s_waitcnt lgkmcnt(6)\n"
"v_mfma_f32_16x16x32_bf16 v[80:83], v[240:243], a[232:235], v[80:83]\n"
"ds_read_b64_tr_b16 v[236:237], v8 offset:24576\n"
"v_mfma_f32_16x16x32_bf16 v[84:87], v[240:243], a[236:239], v[84:87]\n"
"ds_read_b64_tr_b16 v[238:239], v8 offset:25088\n"
"v_mfma_f32_16x16x32_bf16 v[88:91], v[244:247], a[232:235], v[88:91]\n"
"ds_read_b64_tr_b16 v[240:241], v5 offset:28672\n"
"buffer_load_dwordx4 v2, s[48:51], s64 offen offset:1024 sc1 lds\n"
"v_mfma_f32_16x16x32_bf16 v[92:95], v[244:247], a[236:239], v[92:95]\n"
"ds_read_b64_tr_b16 v[242:243], v5 offset:29184\n"
"s_waitcnt vmcnt(8) lgkmcnt(6)\n"
"v_mfma_f32_16x16x32_bf16 v[64:67], v[224:227], a[240:243], v[64:67]\n"
"ds_read_b64_tr_b16 v[244:245], v6 offset:28672\n"
"v_mfma_f32_16x16x32_bf16 v[68:71], v[224:227], a[244:247], v[68:71]\n"
"ds_read_b64_tr_b16 v[246:247], v6 offset:29184\n"
"v_mfma_f32_16x16x32_bf16 v[72:75], v[228:231], a[240:243], v[72:75]\n"
"ds_read_b64_tr_b16 v[224:225], v7 offset:28672\n"
"buffer_load_dwordx4 v2, s[48:51], s64 offen offset:2048 sc1 lds\n"
"v_mfma_f32_16x16x32_bf16 v[76:79], v[228:231], a[244:247], v[76:79]\n"
"ds_read_b64_tr_b16 v[226:227], v7 offset:29184\n"
"s_waitcnt lgkmcnt(6)\n"
"v_mfma_f32_16x16x32_bf16 v[80:83], v[232:235], a[240:243], v[80:83]\n"
"ds_read_b64_tr_b16 v[228:229], v8 offset:28672\n"
"v_mfma_f32_16x16x32_bf16 v[84:87], v[232:235], a[244:247], v[84:87]\n"
"ds_read_b64_tr_b16 v[230:231], v8 offset:29184\n"
"v_mfma_f32_16x16x32_bf16 v[88:91], v[236:239], a[240:243], v[88:91]\n"
"v_mfma_f32_16x16x32_bf16 v[92:95], v[236:239], a[244:247], v[92:95]\n"
"s_waitcnt vmcnt(7) lgkmcnt(6)\n"
"buffer_load_dwordx4 v2, s[48:51], s64 offen offset:3072 sc1 lds\n"
"v_mfma_f32_16x16x32_bf16 v[64:67], v[240:243], a[248:251], v[64:67]\n"
"v_mfma_f32_16x16x32_bf16 v[68:71], v[240:243], a[252:255], v[68:71]\n"
"s_waitcnt lgkmcnt(4)\n"
"v_mfma_f32_16x16x32_bf16 v[72:75], v[244:247], a[248:251], v[72:75]\n"
"v_mfma_f32_16x16x32_bf16 v[76:79], v[244:247], a[252:255], v[76:79]\n"
"s_waitcnt lgkmcnt(2)\n"
"v_mfma_f32_16x16x32_bf16 v[80:83], v[224:227], a[248:251], v[80:83]\n"
"v_mfma_f32_16x16x32_bf16 v[84:87], v[224:227], a[252:255], v[84:87]\n"
"s_waitcnt lgkmcnt(0)\n"
"v_mfma_f32_16x16x32_bf16 v[88:91], v[228:231], a[248:251], v[88:91]\n"
"v_mfma_f32_16x16x32_bf16 v[92:95], v[228:231], a[252:255], v[92:95]\n"
"v_mov_b32 v36, 0x3e8293ee\n"
"v_pk_mul_f32 v[64:65], v[64:65], v[36:37] op_sel_hi:[1,0]\n"
"v_pk_mul_f32 v[66:67], v[66:67], v[36:37] op_sel_hi:[1,0]\n"
"v_cvt_pk_bf16_f32 v96, v64, v65\n"
"v_cvt_pk_bf16_f32 v97, v66, v67\n"
"v_pk_mul_f32 v[68:69], v[68:69], v[36:37] op_sel_hi:[1,0]\n"
"v_pk_mul_f32 v[70:71], v[70:71], v[36:37] op_sel_hi:[1,0]\n"
"v_cvt_pk_bf16_f32 v98, v68, v69\n"
"v_cvt_pk_bf16_f32 v99, v70, v71\n"
"v_pk_mul_f32 v[72:73], v[72:73], v[36:37] op_sel_hi:[1,0]\n"
"v_pk_mul_f32 v[74:75], v[74:75], v[36:37] op_sel_hi:[1,0]\n"
"v_cvt_pk_bf16_f32 v100, v72, v73\n"
"v_cvt_pk_bf16_f32 v101, v74, v75\n"
"v_pk_mul_f32 v[76:77], v[76:77], v[36:37] op_sel_hi:[1,0]\n"
"v_pk_mul_f32 v[78:79], v[78:79], v[36:37] op_sel_hi:[1,0]\n"
"v_cvt_pk_bf16_f32 v102, v76, v77\n"
"v_cvt_pk_bf16_f32 v103, v78, v79\n"
"v_pk_mul_f32 v[80:81], v[80:81], v[36:37] op_sel_hi:[1,0]\n"
"v_pk_mul_f32 v[82:83], v[82:83], v[36:37] op_sel_hi:[1,0]\n"
"v_cvt_pk_bf16_f32 v104, v80, v81\n"
"v_cvt_pk_bf16_f32 v105, v82, v83\n"
"v_pk_mul_f32 v[84:85], v[84:85], v[36:37] op_sel_hi:[1,0]\n"
"v_pk_mul_f32 v[86:87], v[86:87], v[36:37] op_sel_hi:[1,0]\n"
"v_cvt_pk_bf16_f32 v106, v84, v85\n"
"v_cvt_pk_bf16_f32 v107, v86, v87\n"
"v_pk_mul_f32 v[88:89], v[88:89], v[36:37] op_sel_hi:[1,0]\n"
"v_pk_mul_f32 v[90:91], v[90:91], v[36:37] op_sel_hi:[1,0]\n"
"v_cvt_pk_bf16_f32 v108, v88, v89\n"
"v_cvt_pk_bf16_f32 v109, v90, v91\n"
"v_pk_mul_f32 v[92:93], v[92:93], v[36:37] op_sel_hi:[1,0]\n"
"v_pk_mul_f32 v[94:95], v[94:95], v[36:37] op_sel_hi:[1,0]\n"
"v_cvt_pk_bf16_f32 v110, v92, v93\n"
"v_cvt_pk_bf16_f32 v111, v94, v95\n"
"v_permlane16_swap_b32 v96, v98\n"
"v_permlane16_swap_b32 v97, v99\n"
"v_permlane16_swap_b32 v100, v102\n"
"v_permlane16_swap_b32 v101, v103\n"
"v_permlane16_swap_b32 v104, v106\n"
"v_permlane16_swap_b32 v105, v107\n"
"v_permlane16_swap_b32 v108, v110\n"
"v_permlane16_swap_b32 v109, v111\n"
"v_dot2_f32_bf16 v47, v96, v96, 0\n"
"v_dot2_f32_bf16 v48, v104, v104, 0\n"
"v_dot2_f32_bf16 v47, v97, v97, v47\n"
"v_dot2_f32_bf16 v48, v105, v105, v48\n"
"v_dot2_f32_bf16 v47, v98, v98, v47\n"
"v_dot2_f32_bf16 v48, v106, v106, v48\n"
"v_dot2_f32_bf16 v47, v99, v99, v47\n"
"v_dot2_f32_bf16 v48, v107, v107, v48\n"
"v_dot2_f32_bf16 v47, v100, v100, v47\n"
"v_dot2_f32_bf16 v48, v108, v108, v48\n"
"v_dot2_f32_bf16 v47, v101, v101, v47\n"
"v_dot2_f32_bf16 v48, v109, v109, v48\n"
"v_dot2_f32_bf16 v47, v102, v102, v47\n"
"v_dot2_f32_bf16 v48, v110, v110, v48\n"
"v_dot2_f32_bf16 v47, v103, v103, v47\n"
"v_dot2_f32_bf16 v48, v111, v111, v48\n"
"s_waitcnt vmcnt(4)\n"
"s_barrier\n"
"ds_read_b128 a[0:3], v2\n"
"ds_read_b128 a[4:7], v2 offset:1024\n"
"ds_read_b128 a[16:19], v2 offset:4096\n"
"ds_read_b128 a[20:23], v2 offset:5120\n"
"ds_read_b128 a[8:11], v2 offset:2048\n"
"ds_read_b128 a[12:15], v2 offset:3072\n"
"v_mov_b32 v52, v47\n"
"v_mov_b32 v53, v48\n"
"v_mul_f32 v46, 0x3f808000, v46\n"
"v_permlane32_swap_b32 v47, v52\n"
"v_permlane32_swap_b32 v48, v53\n"
"v_add_f32 v47, v47, v52\n"
"v_add_f32 v48, v48, v53\n"
"v_mul_f32 v47, v47, v46\n"
"v_mul_f32 v48, v48, v46\n"
"v_sqrt_f32 v47, v47\n"
"v_sqrt_f32 v48, v48\n"
"v_sub_f32 v112, 0, v47\n"
"v_sub_f32 v113, 0, v47\n"
"v_sub_f32 v114, 0, v47\n"
"v_sub_f32 v115, 0, v47\n"
"v_sub_f32 v116, 0, v47\n"
"v_sub_f32 v117, 0, v47\n"
"v_sub_f32 v118, 0, v47\n"
"v_sub_f32 v119, 0, v47\n"
"v_sub_f32 v120, 0, v47\n"
"v_sub_f32 v121, 0, v47\n"
"v_sub_f32 v122, 0, v47\n"
"v_sub_f32 v123, 0, v47\n"
"v_sub_f32 v124, 0, v47\n"
"v_sub_f32 v125, 0, v47\n"
"LPG3_%=:\n"
"s_cbranch_execz LPG7_%=\n"
"v_sub_f32 v126, 0, v47\n"
"v_sub_f32 v127, 0, v47\n"
"s_waitcnt lgkmcnt(5)\n"
"s_nop 0\n"
"v_mfma_f32_32x32x16_bf16 v[144:159], a[0:3], v[96:99], v[112:127]\n"
"s_waitcnt lgkmcnt(4)\n"
"v_mfma_f32_32x32x16_bf16 v[144:159], a[4:7], v[100:103], v[144:159]\n"
"v_sub_f32 v128, 0, v48\n"
"v_sub_f32 v129, 0, v48\n"
"v_sub_f32 v130, 0, v48\n"
"v_sub_f32 v131, 0, v48\n"
"v_sub_f32 v132, 0, v48\n"
"v_sub_f32 v133, 0, v48\n"
"v_sub_f32 v134, 0, v48\n"
"v_sub_f32 v135, 0, v48\n"
"v_sub_f32 v136, 0, v48\n"
"v_sub_f32 v137, 0, v48\n"
"v_sub_f32 v138, 0, v48\n"
"v_sub_f32 v139, 0, v48\n"
"v_sub_f32 v140, 0, v48\n"
"v_sub_f32 v141, 0, v48\n"
"v_sub_f32 v142, 0, v48\n"
"v_sub_f32 v143, 0, v48\n"
"s_nop 1\n"
"v_mfma_f32_32x32x16_bf16 v[160:175], a[0:3], v[104:107], v[128:143]\n"
"v_mfma_f32_32x32x16_bf16 v[160:175], a[4:7], v[108:111], v[160:175]\n"
"v_max3_f32 v49, v144, v145, v146\n"
"v_max3_f32 v49, v49, v147, v148\n"
"v_max3_f32 v49, v49, v149, v150\n"
"v_max3_f32 v49, v49, v151, v152\n"
"v_max3_f32 v49, v49, v153, v154\n"
"v_max3_f32 v49, v49, v155, v156\n"
"v_max3_f32 v49, v49, v157, v158\n"
"v_max3_f32 v49, v49, v159, v159\n"
"s_add_u32 m0, s61, 114688\n"
"s_add_u32 s64, s61, 1081344\n"
"buffer_load_dwordx4 v2, s[48:51], s64 offen sc1 lds\n"
"buffer_load_dwordx4 v2, s[48:51], s64 offen offset:1024 sc1 lds\n"
"buffer_load_dwordx4 v2, s[48:51], s64 offen offset:2048 sc1 lds\n"
"buffer_load_dwordx4 v2, s[48:51], s64 offen offset:3072 sc1 lds\n"
"v_max3_f32 v50, v160, v161, v162\n"
"v_max3_f32 v50, v50, v163, v164\n"
"v_max3_f32 v50, v50, v165, v166\n"
"v_max3_f32 v50, v50, v167, v168\n"
"v_max3_f32 v50, v50, v169, v170\n"
"v_max3_f32 v50, v50, v171, v172\n"
"v_max3_f32 v50, v50, v173, v174\n"
"v_max3_f32 v50, v50, v175, v175\n"
"v_mov_b32 v52, v49\n"
"v_mov_b32 v53, v50\n"
"s_nop 0\n"
"v_permlane32_swap_b32 v49, v52\n"
"v_permlane32_swap_b32 v50, v53\n"
"v_max_f32 v36, v49, v52\n"
"v_max_f32 v37, v50, v53\n"
"v_min_f32 v36, v36, v37\n"
"v_cmp_nle_f32 vcc, 3266576384, v36\n"
"s_nop 4\n"
"s_cbranch_vccz Lf7_%=\n"
"s_mov_b32 s57, 1048576\n"
"v_mov_b32 v49, 0xff800000\n"
"v_mov_b32 v50, 0xff800000\n"
"Lm8_%=:\n"
"buffer_load_dwordx4 v[56:59], v2, s[48:51], s57 offen sc1\n"
"buffer_load_dwordx4 v[60:63], v2, s[48:51], s57 offen offset:1024 sc1\n"
"s_waitcnt vmcnt(0)\n"
"v_mfma_f32_32x32x16_bf16 v[144:159], v[56:59], v[96:99], 0\n"
"v_mfma_f32_32x32x16_bf16 v[144:159], v[60:63], v[100:103], v[144:159]\n"
"v_mfma_f32_32x32x16_bf16 v[160:175], v[56:59], v[104:107], 0\n"
"v_mfma_f32_32x32x16_bf16 v[160:175], v[60:63], v[108:111], v[160:175]\n"
"s_nop 15\n"
"s_nop 0\n"
"v_max3_f32 v49, v144, v145, v49\n"
"v_max3_f32 v49, v49, v146, v147\n"
"v_max3_f32 v49, v49, v148, v149\n"
"v_max3_f32 v49, v49, v150, v151\n"
"v_max3_f32 v49, v49, v152, v153\n"
"v_max3_f32 v49, v49, v154, v155\n"
"v_max3_f32 v49, v49, v156, v157\n"
"v_max3_f32 v49, v49, v158, v159\n"
"v_max3_f32 v50, v160, v161, v50\n"
"v_max3_f32 v50, v50, v162, v163\n"
"v_max3_f32 v50, v50, v164, v165\n"
"v_max3_f32 v50, v50, v166, v167\n"
"v_max3_f32 v50, v50, v168, v169\n"
"v_max3_f32 v50, v50, v170, v171\n"
"v_max3_f32 v50, v50, v172, v173\n"
"v_max3_f32 v50, v50, v174, v175\n"
"s_add_u32 s57, s57, 4096\n"
"s_cmp_lt_u32 s57, 1310720\n"
"s_cbranch_scc1 Lm8_%=\n"
"v_mov_b32 v36, v49\n"
"s_nop 1\n"
"v_permlane32_swap_b32 v49, v36\n"
"v_max_f32 v47, v49, v36\n"
"v_mov_b32 v36, v50\n"
"s_nop 1\n"
"v_permlane32_swap_b32 v50, v36\n"
"v_max_f32 v48, v50, v36\n"
"v_sub_f32 v112, 0, v47\n"
"v_sub_f32 v113, 0, v47\n"
"v_sub_f32 v114, 0, v47\n"
"v_sub_f32 v115, 0, v47\n"
"v_sub_f32 v116, 0, v47\n"
"v_sub_f32 v117, 0, v47\n"
"v_sub_f32 v118, 0, v47\n"
"v_sub_f32 v119, 0, v47\n"
"v_sub_f32 v120, 0, v47\n"
"v_sub_f32 v121, 0, v47\n"
"v_sub_f32 v122, 0, v47\n"
"v_sub_f32 v123, 0, v47\n"
"v_sub_f32 v124, 0, v47\n"
"v_sub_f32 v125, 0, v47\n"
"v_sub_f32 v126, 0, v47\n"
"v_sub_f32 v127, 0, v47\n"
"s_nop 1\n"
"v_mfma_f32_32x32x16_bf16 v[144:159], a[0:3], v[96:99], v[112:127]\n"
"v_mfma_f32_32x32x16_bf16 v[144:159], a[4:7], v[100:103], v[144:159]\n"
"v_sub_f32 v128, 0, v48\n"
"v_sub_f32 v129, 0, v48\n"
"v_sub_f32 v130, 0, v48\n"
"v_sub_f32 v131, 0, v48\n"
"v_sub_f32 v132, 0, v48\n"
"v_sub_f32 v133, 0, v48\n"
"v_sub_f32 v134, 0, v48\n"
"v_sub_f32 v135, 0, v48\n"
"v_sub_f32 v136, 0, v48\n"
"v_sub_f32 v137, 0, v48\n"
"v_sub_f32 v138, 0, v48\n"
"v_sub_f32 v139, 0, v48\n"
"v_sub_f32 v140, 0, v48\n"
"v_sub_f32 v141, 0, v48\n"
"v_sub_f32 v142, 0, v48\n"
"v_sub_f32 v143, 0, v48\n"
"s_nop 1\n"
"v_mfma_f32_32x32x16_bf16 v[160:175], a[0:3], v[104:107], v[128:143]\n"
"v_mfma_f32_32x32x16_bf16 v[160:175], a[4:7], v[108:111], v[160:175]\n"
"s_nop 15\n"
"Lf7_%=:\n"
"v_exp_f32 v144, v144\n"
"v_exp_f32 v145, v145\n"
"v_exp_f32 v146, v146\n"
"v_exp_f32 v147, v147\n"
"v_exp_f32 v148, v148\n"
"v_exp_f32 v149, v149\n"
"s_waitcnt lgkmcnt(3)\n"
"v_mfma_f32_32x32x16_bf16 v[160:175], a[16:19], v[96:99], v[112:127]\n"
"s_add_u32 m0, s61, 131072\n"
"v_exp_f32 v150, v150\n"
"v_exp_f32 v151, v151\n"
"v_exp_f32 v152, v152\n"
"s_waitcnt lgkmcnt(2)\n"
"v_mfma_f32_32x32x16_bf16 v[160:175], a[20:23], v[100:103], v[160:175]\n"
"ds_read_b128 a[32:35], v2 offset:8192\n"
"ds_read_b128 a[36:39], v2 offset:9216\n"
"v_exp_f32 v153, v153\n"
"v_exp_f32 v154, v154\n"
"v_exp_f32 v155, v155\n"
"s_waitcnt lgkmcnt(3)\n"
"v_smfmac_f32_32x32x32_bf16 v[176:191], a[8:11], v[144:151], v220\n"
"s_add_u32 s64, s61, 1097728\n"
"buffer_load_dwordx4 v2, s[48:51], s64 offen sc1 lds\n"
"v_exp_f32 v156, v156\n"
"v_exp_f32 v157, v157\n"
"v_exp_f32 v158, v158\n"
"v_smfmac_f32_16x16x64_bf16 v[208:211], v[216:219], v[144:151], v220\n"
"ds_read_b128 a[24:27], v2 offset:6144\n"
"ds_read_b128 a[28:31], v2 offset:7168\n"
"v_exp_f32 v159, v159\n"
"v_exp_f32 v160, v160\n"
"v_exp_f32 v161, v161\n"
"s_waitcnt lgkmcnt(4)\n"
"v_smfmac_f32_32x32x32_bf16 v[176:191], a[12:15], v[152:159], v220\n"
"buffer_load_dwordx4 v2, s[48:51], s64 offen offset:1024 sc1 lds\n"
"s_bfe_u32 s20, s6, 0x10002\n"
"v_exp_f32 v162, v162\n"
"v_exp_f32 v163, v163\n"
"v_exp_f32 v164, v164\n"
"v_smfmac_f32_16x16x64_bf16 v[208:211], v[216:219], v[152:159], v220\n"
"s_waitcnt lgkmcnt(2)\n"
"ds_read_b128 a[48:51], v2 offset:12288\n"
"ds_read_b128 a[52:55], v2 offset:13312\n"
"v_exp_f32 v165, v165\n"
"v_mfma_f32_32x32x16_bf16 v[144:159], a[32:35], v[96:99], v[112:127]\n"
"s_lshl_b32 s20, s20, 3\n"
"v_exp_f32 v166, v166\n"
"v_exp_f32 v167, v167\n"
"v_exp_f32 v168, v168\n"
"v_mfma_f32_32x32x16_bf16 v[144:159], a[36:39], v[100:103], v[144:159]\n"
"s_waitcnt lgkmcnt(2)\n"
"v_exp_f32 v169, v169\n"
"v_exp_f32 v170, v170\n"
"v_exp_f32 v171, v171\n"
"v_smfmac_f32_32x32x32_bf16 v[176:191], a[24:27], v[160:167], v220\n"
"buffer_load_dwordx4 v2, s[48:51], s64 offen offset:2048 sc1 lds\n"
"s_lshr_b32 s59, s6, 5\n"
"v_exp_f32 v172, v172\n"
"v_exp_f32 v173, v173\n"
"v_exp_f32 v174, v174\n"
"v_smfmac_f32_16x16x64_bf16 v[208:211], v[216:219], v[160:167], v220\n"
"ds_read_b128 a[40:43], v2 offset:10240\n"
"ds_read_b128 a[44:47], v2 offset:11264\n"
"v_exp_f32 v175, v175\n"
"v_exp_f32 v144, v144\n"
"v_exp_f32 v145, v145\n"
"v_smfmac_f32_32x32x32_bf16 v[176:191], a[28:31], v[168:175], v220\n"
"s_waitcnt vmcnt(7)\n"
"s_barrier\n"
"v_exp_f32 v146, v146\n"
"v_exp_f32 v147, v147\n"
"v_exp_f32 v148, v148\n"
"v_smfmac_f32_16x16x64_bf16 v[208:211], v[216:219], v[168:175], v220\n"
"s_waitcnt lgkmcnt(2)\n"
"ds_read_b128 a[64:67], v2 offset:16384\n"
"ds_read_b128 a[68:71], v2 offset:17408\n"
"v_exp_f32 v149, v149\n"
"v_mfma_f32_32x32x16_bf16 v[160:175], a[48:51], v[96:99], v[112:127]\n"
"s_add_u32 s20, s20, s59\n"
"v_exp_f32 v150, v150\n"
"v_exp_f32 v151, v151\n"
"v_exp_f32 v152, v152\n"
"v_mfma_f32_32x32x16_bf16 v[160:175], a[52:55], v[100:103], v[160:175]\n"
"s_waitcnt lgkmcnt(2)\n"
"v_exp_f32 v153, v153\n"
"v_exp_f32 v154, v154\n"
"v_exp_f32 v155, v155\n"
"v_smfmac_f32_32x32x32_bf16 v[176:191], a[40:43], v[144:151], v220\n"
"buffer_load_dwordx4 v2, s[48:51], s64 offen offset:3072 sc1 lds\n"
"s_add_u32 m0, s61, 147456\n"
"v_exp_f32 v156, v156\n"
"v_exp_f32 v157, v157\n"
"v_exp_f32 v158, v158\n"
"v_smfmac_f32_16x16x64_bf16 v[208:211], v[216:219], v[144:151], v220\n"
"ds_read_b128 a[56:59], v2 offset:14336\n"
"ds_read_b128 a[60:63], v2 offset:15360\n"
"v_exp_f32 v159, v159\n"
"v_exp_f32 v160, v160\n"
"v_exp_f32 v161, v161\n"
"v_smfmac_f32_32x32x32_bf16 v[176:191], a[44:47], v[152:159], v220\n"
"s_add_u32 s64, s61, 1114112\n"
"buffer_load_dwordx4 v2, s[48:51], s64 offen sc1 lds\n"
"v_exp_f32 v162, v162\n"
"v_exp_f32 v163, v163\n"
"v_exp_f32 v164, v164\n"
"v_smfmac_f32_16x16x64_bf16 v[208:211], v[216:219], v[152:159], v220\n"
"s_waitcnt lgkmcnt(2)\n"
"ds_read_b128 a[80:83], v2 offset:20480\n"
"ds_read_b128 a[84:87], v2 offset:21504\n"
"v_exp_f32 v165, v165\n"
"v_mfma_f32_32x32x16_bf16 v[144:159], a[64:67], v[96:99], v[112:127]\n"
"s_bfe_u32 s21, s6, 0x20003\n"
"v_exp_f32 v166, v166\n"
"v_exp_f32 v167, v167\n"
"v_exp_f32 v168, v168\n"
"v_mfma_f32_32x32x16_bf16 v[144:159], a[68:71], v[100:103], v[144:159]\n"
"s_waitcnt lgkmcnt(2)\n"
"v_exp_f32 v169, v169\n"
"v_exp_f32 v170, v170\n"
"v_exp_f32 v171, v171\n"
"v_smfmac_f32_32x32x32_bf16 v[176:191], a[56:59], v[160:167], v220\n"
"buffer_load_dwordx4 v2, s[48:51], s64 offen offset:1024 sc1 lds\n"
"s_and_b32 s59, s6, 2\n"
"v_exp_f32 v172, v172\n"
"v_exp_f32 v173, v173\n"
"v_exp_f32 v174, v174\n"
"v_smfmac_f32_16x16x64_bf16 v[208:211], v[216:219], v[160:167], v220\n"
"ds_read_b128 a[72:75], v2 offset:18432\n"
"ds_read_b128 a[76:79], v2 offset:19456\n"
"v_exp_f32 v175, v175\n"
"v_exp_f32 v144, v144\n"
"v_exp_f32 v145, v145\n"
"v_smfmac_f32_32x32x32_bf16 v[176:191], a[60:63], v[168:175], v220\n"
"buffer_load_dwordx4 v2, s[48:51], s64 offen offset:2048 sc1 lds\n"
"s_lshl1_add_u32 s21, s59, s21\n"
"v_exp_f32 v146, v146\n"
"v_exp_f32 v147, v147\n"
"v_exp_f32 v148, v148\n"
"v_smfmac_f32_16x16x64_bf16 v[208:211], v[216:219], v[168:175], v220\n"
"s_waitcnt lgkmcnt(2)\n"
"ds_read_b128 a[96:99], v2 offset:24576\n"
"ds_read_b128 a[100:103], v2 offset:25600\n"
"v_exp_f32 v149, v149\n"
"v_mfma_f32_32x32x16_bf16 v[160:175], a[80:83], v[96:99], v[112:127]\n"
"s_and_b32 s59, s6, 1\n"
"v_exp_f32 v150, v150\n"
"v_exp_f32 v151, v151\n"
"v_exp_f32 v152, v152\n"
"v_mfma_f32_32x32x16_bf16 v[160:175], a[84:87], v[100:103], v[160:175]\n"
"s_waitcnt lgkmcnt(2)\n"
"v_exp_f32 v153, v153\n"
"v_exp_f32 v154, v154\n"
"v_exp_f32 v155, v155\n"
"v_smfmac_f32_32x32x32_bf16 v[176:191], a[72:75], v[144:151], v220\n"
"buffer_load_dwordx4 v2, s[48:51], s64 offen offset:3072 sc1 lds\n"
"s_lshl3_add_u32 s21, s59, s21\n"
"v_exp_f32 v156, v156\n"
"v_exp_f32 v157, v157\n"
"v_exp_f32 v158, v158\n"
"v_smfmac_f32_16x16x64_bf16 v[208:211], v[216:219], v[144:151], v220\n"
"ds_read_b128 a[88:91], v2 offset:22528\n"
"ds_read_b128 a[92:95], v2 offset:23552\n"
"v_exp_f32 v159, v159\n"
"v_exp_f32 v160, v160\n"
"v_exp_f32 v161, v161\n"
"v_smfmac_f32_32x32x32_bf16 v[176:191], a[76:79], v[152:159], v220\n"
"s_lshl2_add_u32 s59, s20, s7\n"
"s_lshl_b32 s22, s59, 11\n"
"v_exp_f32 v162, v162\n"
"v_exp_f32 v163, v163\n"
"v_exp_f32 v164, v164\n"
"v_smfmac_f32_16x16x64_bf16 v[208:211], v[216:219], v[152:159], v220\n"
"s_waitcnt lgkmcnt(2)\n"
"ds_read_b128 a[112:115], v2 offset:28672\n"
"ds_read_b128 a[116:119], v2 offset:29696\n"
"v_exp_f32 v165, v165\n"
"v_mfma_f32_32x32x16_bf16 v[144:159], a[96:99], v[96:99], v[112:127]\n"
"s_lshl_b32 s23, s59, 7\n"
"v_exp_f32 v166, v166\n"
"v_exp_f32 v167, v167\n"
"v_exp_f32 v168, v168\n"
"v_mfma_f32_32x32x16_bf16 v[144:159], a[100:103], v[100:103], v[144:159]\n"
"s_waitcnt lgkmcnt(2)\n"
"v_exp_f32 v169, v169\n"
"v_exp_f32 v170, v170\n"
"v_exp_f32 v171, v171\n"
"v_smfmac_f32_32x32x32_bf16 v[176:191], a[88:91], v[160:167], v220\n"
"s_lshl3_add_u32 s4, s21, s23\n"
"s_xor_b32 s5, s4, 64\n"
"v_exp_f32 v172, v172\n"
"v_exp_f32 v173, v173\n"
"v_exp_f32 v174, v174\n"
"v_smfmac_f32_16x16x64_bf16 v[208:211], v[216:219], v[160:167], v220\n"
"ds_read_b128 a[104:107], v2 offset:26624\n"
"ds_read_b128 a[108:111], v2 offset:27648\n"
"v_exp_f32 v175, v175\n"
"v_exp_f32 v144, v144\n"
"v_exp_f32 v145, v145\n"
"v_smfmac_f32_32x32x32_bf16 v[176:191], a[92:95], v[168:175], v220\n"
"s_waitcnt vmcnt(8)\n"
"s_barrier\n"
"v_exp_f32 v146, v146\n"
"v_exp_f32 v147, v147\n"
"v_exp_f32 v148, v148\n"
"v_smfmac_f32_16x16x64_bf16 v[208:211], v[216:219], v[168:175], v220\n"
"s_waitcnt lgkmcnt(2)\n"
"ds_read_b128 a[128:131], v221 offset:49152\n"
"ds_read_b128 a[132:135], v221 offset:50176\n"
"v_exp_f32 v149, v149\n"
"v_mfma_f32_32x32x16_bf16 v[160:175], a[112:115], v[96:99], v[112:127]\n"
"s_add_u32 m0, s55, 32768\n"
"v_exp_f32 v150, v150\n"
"v_exp_f32 v151, v151\n"
"v_exp_f32 v152, v152\n"
"v_mfma_f32_32x32x16_bf16 v[160:175], a[116:119], v[100:103], v[160:175]\n"
"s_waitcnt lgkmcnt(2)\n"
"v_exp_f32 v153, v153\n"
"v_exp_f32 v154, v154\n"
"v_exp_f32 v155, v155\n"
"v_smfmac_f32_32x32x32_bf16 v[176:191], a[104:107], v[144:151], v220\n"
"s_add_u32 s57, s88, 0\n"
"buffer_load_dwordx4 v33, s[28:31], s57 offen lds\n"
"v_exp_f32 v156, v156\n"
"v_exp_f32 v157, v157\n"
"v_exp_f32 v158, v158\n"
"v_smfmac_f32_16x16x64_bf16 v[208:211], v[216:219], v[144:151], v220\n"
"ds_read_b128 a[120:123], v2 offset:30720\n"
"ds_read_b128 a[124:127], v2 offset:31744\n"
"v_exp_f32 v159, v159\n"
"v_exp_f32 v160, v160\n"
"v_exp_f32 v161, v161\n"
"v_smfmac_f32_32x32x32_bf16 v[176:191], a[108:111], v[152:159], v220\n"
"s_add_u32 m0, s61, 0\n"
"s_add_u32 s64, s61, 1130496\n"
"v_exp_f32 v162, v162\n"
"v_exp_f32 v163, v163\n"
"v_exp_f32 v164, v164\n"
"v_smfmac_f32_16x16x64_bf16 v[208:211], v[216:219], v[152:159], v220\n"
"s_waitcnt lgkmcnt(2)\n"
"ds_read_b128 a[144:147], v221 offset:53248\n"
"ds_read_b128 a[148:151], v221 offset:54272\n"
"v_exp_f32 v165, v165\n"
"v_mfma_f32_32x32x16_bf16 v[144:159], a[128:131], v[96:99], v[112:127]\n"
"s_lshr_b32 s93, s22, 5\n"
"v_exp_f32 v166, v166\n"
"v_exp_f32 v167, v167\n"
"v_exp_f32 v168, v168\n"
"v_mfma_f32_32x32x16_bf16 v[144:159], a[132:135], v[100:103], v[144:159]\n"
"s_waitcnt lgkmcnt(2)\n"
"v_exp_f32 v169, v169\n"
"v_exp_f32 v170, v170\n"
"v_exp_f32 v171, v171\n"
"v_smfmac_f32_32x32x32_bf16 v[176:191], a[120:123], v[160:167], v220\n"
"buffer_load_dwordx4 v2, s[48:51], s64 offen sc1 lds\n"
"s_lshl_b32 s0, s54, 1\n"
"v_exp_f32 v172, v172\n"
"v_exp_f32 v173, v173\n"
"v_exp_f32 v174, v174\n"
"v_smfmac_f32_16x16x64_bf16 v[208:211], v[216:219], v[160:167], v220\n"
"ds_read_b128 a[136:139], v221 offset:51200\n"
"ds_read_b128 a[140:143], v221 offset:52224\n"
"v_exp_f32 v175, v175\n"
"v_exp_f32 v144, v144\n"
"v_exp_f32 v145, v145\n"
"v_smfmac_f32_32x32x32_bf16 v[176:191], a[124:127], v[168:175], v220\n"
"buffer_load_dwordx4 v2, s[48:51], s64 offen offset:1024 sc1 lds\n"
"s_lshl_b32 s1, s23, 1\n"
"v_exp_f32 v146, v146\n"
"v_exp_f32 v147, v147\n"
"v_exp_f32 v148, v148\n"
"v_smfmac_f32_16x16x64_bf16 v[208:211], v[216:219], v[168:175], v220\n"
"s_waitcnt lgkmcnt(2)\n"
"ds_read_b128 a[160:163], v221 offset:57344\n"
"ds_read_b128 a[164:167], v221 offset:58368\n"
"v_exp_f32 v149, v149\n"
"v_mfma_f32_32x32x16_bf16 v[160:175], a[144:147], v[96:99], v[112:127]\n"
"v_mov_b32 v84, 0x3f803f80\n"
"v_exp_f32 v150, v150\n"
"v_exp_f32 v151, v151\n"
"v_exp_f32 v152, v152\n"
"v_mfma_f32_32x32x16_bf16 v[160:175], a[148:151], v[100:103], v[160:175]\n"
"s_waitcnt lgkmcnt(2)\n"
"v_exp_f32 v153, v153\n"
"v_exp_f32 v154, v154\n"
"v_exp_f32 v155, v155\n"
"v_smfmac_f32_32x32x32_bf16 v[176:191], a[136:139], v[144:151], v220\n"
"buffer_load_dwordx4 v2, s[48:51], s64 offen offset:2048 sc1 lds\n"
"v_exp_f32 v156, v156\n"
"v_exp_f32 v157, v157\n"
"v_exp_f32 v158, v158\n"
"v_smfmac_f32_16x16x64_bf16 v[208:211], v[216:219], v[144:151], v220\n"
"ds_read_b128 a[152:155], v221 offset:55296\n"
"ds_read_b128 a[156:159], v221 offset:56320\n"
"v_exp_f32 v159, v159\n"
"v_exp_f32 v160, v160\n"
"v_exp_f32 v161, v161\n"
"v_smfmac_f32_32x32x32_bf16 v[176:191], a[140:143], v[152:159], v220\n"
"buffer_load_dwordx4 v2, s[48:51], s64 offen offset:3072 sc1 lds\n"
"s_add_u32 m0, s55, 36864\n"
"v_exp_f32 v162, v162\n"
"v_exp_f32 v163, v163\n"
"v_exp_f32 v164, v164\n"
"v_smfmac_f32_16x16x64_bf16 v[208:211], v[216:219], v[152:159], v220\n"
"s_waitcnt lgkmcnt(2)\n"
"ds_read_b128 a[176:179], v221 offset:61440\n"
"ds_read_b128 a[180:183], v221 offset:62464\n"
"v_exp_f32 v165, v165\n"
"v_mfma_f32_32x32x16_bf16 v[144:159], a[160:163], v[96:99], v[112:127]\n"
"s_add_u32 s57, s88, 229376\n"
"v_exp_f32 v166, v166\n"
"v_exp_f32 v167, v167\n"
"v_exp_f32 v168, v168\n"
"v_mfma_f32_32x32x16_bf16 v[144:159], a[164:167], v[100:103], v[144:159]\n"
"s_waitcnt lgkmcnt(2)\n"
"v_exp_f32 v169, v169\n"
"v_exp_f32 v170, v170\n"
"v_exp_f32 v171, v171\n"
"v_smfmac_f32_32x32x32_bf16 v[176:191], a[152:155], v[160:167], v220\n"
"buffer_load_dwordx4 v33, s[28:31], s57 offen lds\n"
"s_add_u32 m0, s92, 0\n"
"v_exp_f32 v172, v172\n"
"v_exp_f32 v173, v173\n"
"v_exp_f32 v174, v174\n"
"v_smfmac_f32_16x16x64_bf16 v[208:211], v[216:219], v[160:167], v220\n"
"ds_read_b128 a[168:171], v221 offset:59392\n"
"ds_read_b128 a[172:175], v221 offset:60416\n"
"v_exp_f32 v175, v175\n"
"v_exp_f32 v144, v144\n"
"v_exp_f32 v145, v145\n"
"v_smfmac_f32_32x32x32_bf16 v[176:191], a[156:159], v[168:175], v220\n"
"s_waitcnt vmcnt(10)\n"
"s_barrier\n"
"v_exp_f32 v146, v146\n"
"v_exp_f32 v147, v147\n"
"v_exp_f32 v148, v148\n"
"v_smfmac_f32_16x16x64_bf16 v[208:211], v[216:219], v[168:175], v220\n"
"s_waitcnt lgkmcnt(2)\n"
"ds_read_b128 a[192:195], v222\n"
"ds_read_b128 a[196:199], v222 offset:1024\n"
"v_exp_f32 v149, v149\n"
"v_mfma_f32_32x32x16_bf16 v[160:175], a[176:179], v[96:99], v[112:127]\n"
"s_add_u32 s57, s91, 0\n"
"v_exp_f32 v150, v150\n"
"v_exp_f32 v151, v151\n"
"v_exp_f32 v152, v152\n"
"v_mfma_f32_32x32x16_bf16 v[160:175], a[180:183], v[100:103], v[160:175]\n"
"s_waitcnt lgkmcnt(2)\n"
"v_exp_f32 v153, v153\n"
"v_exp_f32 v154, v154\n"
"v_exp_f32 v155, v155\n"
"v_smfmac_f32_32x32x32_bf16 v[176:191], a[168:171], v[144:151], v220\n"
"buffer_load_dwordx4 v44, s[12:15], s57 offen lds\n"
"s_add_u32 m0, s55, 40960\n"
"v_exp_f32 v156, v156\n"
"v_exp_f32 v157, v157\n"
"v_exp_f32 v158, v158\n"
"v_smfmac_f32_16x16x64_bf16 v[208:211], v[216:219], v[144:151], v220\n"
"ds_read_b128 a[184:187], v221 offset:63488\n"
"ds_read_b128 a[188:191], v221 offset:64512\n"
"v_exp_f32 v159, v159\n"
"v_exp_f32 v160, v160\n"
"v_exp_f32 v161, v161\n"
"v_smfmac_f32_32x32x32_bf16 v[176:191], a[172:175], v[152:159], v220\n"
"s_add_u32 s57, s88, 458752\n"
"buffer_load_dwordx4 v33, s[28:31], s57 offen lds\n"
"v_exp_f32 v162, v162\n"
"v_exp_f32 v163, v163\n"
"v_exp_f32 v164, v164\n"
"v_smfmac_f32_16x16x64_bf16 v[208:211], v[216:219], v[152:159], v220\n"
"s_waitcnt lgkmcnt(2)\n"
"ds_read_b128 a[208:211], v222 offset:4096\n"
"ds_read_b128 a[212:215], v222 offset:5120\n"
"v_exp_f32 v165, v165\n"
"v_mfma_f32_32x32x16_bf16 v[144:159], a[192:195], v[96:99], v[112:127]\n"
"s_add_u32 m0, s61, 16384\n"
"v_exp_f32 v166, v166\n"
"v_exp_f32 v167, v167\n"
"v_exp_f32 v168, v168\n"
"v_mfma_f32_32x32x16_bf16 v[144:159], a[196:199], v[100:103], v[144:159]\n"
"s_waitcnt lgkmcnt(2)\n"
"v_exp_f32 v169, v169\n"
"v_exp_f32 v170, v170\n"
"v_exp_f32 v171, v171\n"
"v_smfmac_f32_32x32x32_bf16 v[176:191], a[184:187], v[160:167], v220\n"
"s_add_u32 s64, s61, 1146880\n"
"buffer_load_dwordx4 v2, s[48:51], s64 offen sc1 lds\n"
"v_exp_f32 v172, v172\n"
"v_exp_f32 v173, v173\n"
"v_exp_f32 v174, v174\n"
"v_smfmac_f32_16x16x64_bf16 v[208:211], v[216:219], v[160:167], v220\n"
"ds_read_b128 a[200:203], v222 offset:2048\n"
"ds_read_b128 a[204:207], v222 offset:3072\n"
"v_exp_f32 v175, v175\n"
"v_exp_f32 v144, v144\n"
"v_exp_f32 v145, v145\n"
"v_smfmac_f32_32x32x32_bf16 v[176:191], a[188:191], v[168:175], v220\n"
"buffer_load_dwordx4 v2, s[48:51], s64 offen offset:1024 sc1 lds\n"
"v_exp_f32 v146, v146\n"
"v_exp_f32 v147, v147\n"
"v_exp_f32 v148, v148\n"
"v_smfmac_f32_16x16x64_bf16 v[208:211], v[216:219], v[168:175], v220\n"
"s_waitcnt lgkmcnt(2)\n"
"ds_read_b128 a[224:227], v222 offset:8192\n"
"ds_read_b128 a[228:231], v222 offset:9216\n"
"v_exp_f32 v149, v149\n"
"v_mfma_f32_32x32x16_bf16 v[160:175], a[208:211], v[96:99], v[112:127]\n"
"v_mov_b32 v85, 0x3f803f80\n"
"v_exp_f32 v150, v150\n"
"v_exp_f32 v151, v151\n"
"v_exp_f32 v152, v152\n"
"v_mfma_f32_32x32x16_bf16 v[160:175], a[212:215], v[100:103], v[160:175]\n"
"s_waitcnt lgkmcnt(2)\n"
"v_exp_f32 v153, v153\n"
"v_exp_f32 v154, v154\n"
"v_exp_f32 v155, v155\n"
"v_smfmac_f32_32x32x32_bf16 v[176:191], a[200:203], v[144:151], v220\n"
"buffer_load_dwordx4 v2, s[48:51], s64 offen offset:2048 sc1 lds\n"
"v_exp_f32 v156, v156\n"
"v_exp_f32 v157, v157\n"
"v_exp_f32 v158, v158\n"
"v_smfmac_f32_16x16x64_bf16 v[208:211], v[216:219], v[144:151], v220\n"
"ds_read_b128 a[216:219], v222 offset:6144\n"
"ds_read_b128 a[220:223], v222 offset:7168\n"
"v_exp_f32 v159, v159\n"
"v_exp_f32 v160, v160\n"
"v_exp_f32 v161, v161\n"
"v_smfmac_f32_32x32x32_bf16 v[176:191], a[204:207], v[152:159], v220\n"
"buffer_load_dwordx4 v2, s[48:51], s64 offen offset:3072 sc1 lds\n"
"s_add_u32 m0, s92, 1024\n"
"v_exp_f32 v162, v162\n"
"v_exp_f32 v163, v163\n"
"v_exp_f32 v164, v164\n"
"v_smfmac_f32_16x16x64_bf16 v[208:211], v[216:219], v[152:159], v220\n"
"s_waitcnt lgkmcnt(2)\n"
"ds_read_b128 a[240:243], v222 offset:12288\n"
"ds_read_b128 a[244:247], v222 offset:13312\n"
"v_exp_f32 v165, v165\n"
"v_mfma_f32_32x32x16_bf16 v[144:159], a[224:227], v[96:99], v[112:127]\n"
"s_add_u32 s57, s91, 16384\n"
"v_exp_f32 v166, v166\n"
"v_exp_f32 v167, v167\n"
"v_exp_f32 v168, v168\n"
"v_mfma_f32_32x32x16_bf16 v[144:159], a[228:231], v[100:103], v[144:159]\n"
"s_waitcnt lgkmcnt(2)\n"
"v_exp_f32 v169, v169\n"
"v_exp_f32 v170, v170\n"
"v_exp_f32 v171, v171\n"
"v_smfmac_f32_32x32x32_bf16 v[176:191], a[216:219], v[160:167], v220\n"
"buffer_load_dwordx4 v44, s[12:15], s57 offen lds\n"
"v_exp_f32 v172, v172\n"
"v_exp_f32 v173, v173\n"
"v_exp_f32 v174, v174\n"
"v_smfmac_f32_16x16x64_bf16 v[208:211], v[216:219], v[160:167], v220\n"
"ds_read_b128 a[232:235], v222 offset:10240\n"
"ds_read_b128 a[236:239], v222 offset:11264\n"
"v_exp_f32 v175, v175\n"
"v_exp_f32 v144, v144\n"
"v_exp_f32 v145, v145\n"
"v_smfmac_f32_32x32x32_bf16 v[176:191], a[220:223], v[168:175], v220\n"
"s_add_u32 m0, s55, 45056\n"
"s_add_u32 s57, s88, 688128\n"
"v_exp_f32 v146, v146\n"
"v_exp_f32 v147, v147\n"
"v_exp_f32 v148, v148\n"
"v_smfmac_f32_16x16x64_bf16 v[208:211], v[216:219], v[168:175], v220\n"
"ds_read_b128 a[248:251], v222 offset:14336\n"
"ds_read_b128 a[252:255], v222 offset:15360\n"
"v_exp_f32 v149, v149\n"
"v_mfma_f32_32x32x16_bf16 v[160:175], a[0:3], v[104:107], v[128:143]\n"
"v_mov_b32 v86, 0x3f803f80\n"
"v_exp_f32 v150, v150\n"
"v_exp_f32 v151, v151\n"
"v_exp_f32 v152, v152\n"
"v_mfma_f32_32x32x16_bf16 v[160:175], a[4:7], v[108:111], v[160:175]\n"
"s_waitcnt lgkmcnt(2)\n"
"v_exp_f32 v153, v153\n"
"v_exp_f32 v154, v154\n"
"v_exp_f32 v155, v155\n"
"v_smfmac_f32_32x32x32_bf16 v[176:191], a[232:235], v[144:151], v220\n"
"buffer_load_dwordx4 v33, s[28:31], s57 offen lds\n"
"v_exp_f32 v156, v156\n"
"v_exp_f32 v157, v157\n"
"v_exp_f32 v158, v158\n"
"v_smfmac_f32_16x16x64_bf16 v[208:211], v[216:219], v[144:151], v220\n"
"v_mov_b32 v87, 0x3f803f80\n"
"v_mov_b32 v90, 0\n"
"v_exp_f32 v159, v159\n"
"v_exp_f32 v160, v160\n"
"v_exp_f32 v161, v161\n"
"v_smfmac_f32_32x32x32_bf16 v[176:191], a[236:239], v[152:159], v220\n"
"s_waitcnt vmcnt(14) lgkmcnt(0)\n"
"s_barrier\n"
"v_exp_f32 v162, v162\n"
"v_exp_f32 v163, v163\n"
"v_exp_f32 v164, v164\n"
"v_smfmac_f32_16x16x64_bf16 v[208:211], v[216:219], v[152:159], v220\n"
"ds_read_b128 a[0:3], v222 offset:16384\n"
"v_exp_f32 v165, v165\n"
"v_mfma_f32_32x32x16_bf16 v[144:159], a[240:243], v[96:99], v[112:127]\n"
"v_mov_b32 v91, 0\n"
"v_exp_f32 v166, v166\n"
"v_exp_f32 v167, v167\n"
"v_exp_f32 v168, v168\n"
"v_mfma_f32_32x32x16_bf16 v[144:159], a[244:247], v[100:103], v[144:159]\n"
"s_mul_i32 s81, s9, 524288\n"
"v_exp_f32 v169, v169\n"
"v_exp_f32 v170, v170\n"
"v_exp_f32 v171, v171\n"
"v_smfmac_f32_32x32x32_bf16 v[192:207], a[8:11], v[160:167], v220\n"
"s_add_u32 s81, s81, s52\n"
"v_exp_f32 v172, v172\n"
"v_exp_f32 v173, v173\n"
"v_exp_f32 v174, v174\n"
"v_smfmac_f32_16x16x64_bf16 v[212:215], v[216:219], v[160:167], v220\n"
"ds_read_b128 a[4:7], v222 offset:17408\n"
"v_exp_f32 v175, v175\n"
"v_exp_f32 v144, v144\n"
"v_exp_f32 v145, v145\n"
"v_smfmac_f32_32x32x32_bf16 v[192:207], a[12:15], v[168:175], v220\n"
"s_add_u32 m0, s61, 114688\n"
"s_add_u32 s64, s61, 1163264\n"
"v_exp_f32 v146, v146\n"
"v_exp_f32 v147, v147\n"
"v_exp_f32 v148, v148\n"
"v_smfmac_f32_16x16x64_bf16 v[212:215], v[216:219], v[168:175], v220\n"
"ds_read_b128 a[8:11], v222 offset:18432\n"
"v_exp_f32 v149, v149\n"
"v_mfma_f32_32x32x16_bf16 v[160:175], a[16:19], v[104:107], v[128:143]\n"
"v_exp_f32 v150, v150\n"
"v_exp_f32 v151, v151\n"
"v_exp_f32 v152, v152\n"
"v_mfma_f32_32x32x16_bf16 v[160:175], a[20:23], v[108:111], v[160:175]\n"
"s_waitcnt lgkmcnt(1)\n"
"v_exp_f32 v153, v153\n"
"v_exp_f32 v154, v154\n"
"v_exp_f32 v155, v155\n"
"v_smfmac_f32_32x32x32_bf16 v[176:191], a[248:251], v[144:151], v220\n"
"buffer_load_dwordx4 v2, s[48:51], s64 offen sc1 lds\n"
"v_exp_f32 v156, v156\n"
"v_exp_f32 v157, v157\n"
"v_exp_f32 v158, v158\n"
"v_smfmac_f32_16x16x64_bf16 v[208:211], v[216:219], v[144:151], v220\n"
"ds_read_b128 a[12:15], v222 offset:19456\n"
"v_exp_f32 v159, v159\n"
"v_exp_f32 v160, v160\n"
"v_exp_f32 v161, v161\n"
"v_smfmac_f32_32x32x32_bf16 v[176:191], a[252:255], v[152:159], v220\n"
"buffer_load_dwordx4 v2, s[48:51], s64 offen offset:1024 sc1 lds\n"
"v_exp_f32 v162, v162\n"
"v_exp_f32 v163, v163\n"
"v_exp_f32 v164, v164\n"
"v_smfmac_f32_16x16x64_bf16 v[208:211], v[216:219], v[152:159], v220\n"
"ds_read_b128 a[16:19], v222 offset:20480\n"
"v_exp_f32 v165, v165\n"
"v_mfma_f32_32x32x16_bf16 v[144:159], a[0:3], v[96:99], v[112:127]\n"
"v_exp_f32 v166, v166\n"
"v_exp_f32 v167, v167\n"
"v_exp_f32 v168, v168\n"
"v_mfma_f32_32x32x16_bf16 v[144:159], a[4:7], v[100:103], v[144:159]\n"
"v_exp_f32 v169, v169\n"
"v_exp_f32 v170, v170\n"
"v_exp_f32 v171, v171\n"
"v_smfmac_f32_32x32x32_bf16 v[192:207], a[24:27], v[160:167], v220\n"
"buffer_load_dwordx4 v2, s[48:51], s64 offen offset:2048 sc1 lds\n"
"v_exp_f32 v172, v172\n"
"v_exp_f32 v173, v173\n"
"v_exp_f32 v174, v174\n"
"v_smfmac_f32_16x16x64_bf16 v[212:215], v[216:219], v[160:167], v220\n"
"ds_read_b128 a[20:23], v222 offset:21504\n"
"v_exp_f32 v175, v175\n"
"v_exp_f32 v144, v144\n"
"v_exp_f32 v145, v145\n"
"v_smfmac_f32_32x32x32_bf16 v[192:207], a[28:31], v[168:175], v220\n"
"buffer_load_dwordx4 v2, s[48:51], s64 offen offset:3072 sc1 lds\n"
"s_add_u32 m0, s61, 131072\n"
"v_exp_f32 v146, v146\n"
"v_exp_f32 v147, v147\n"
"v_exp_f32 v148, v148\n"
"v_smfmac_f32_16x16x64_bf16 v[212:215], v[216:219], v[168:175], v220\n"
"ds_read_b128 a[24:27], v222 offset:22528\n"
"s_add_u32 s64, s61, 1179648\n"
"v_exp_f32 v149, v149\n"
"v_mfma_f32_32x32x16_bf16 v[160:175], a[32:35], v[104:107], v[128:143]\n"
"v_exp_f32 v150, v150\n"
"v_exp_f32 v151, v151\n"
"v_exp_f32 v152, v152\n"
"v_mfma_f32_32x32x16_bf16 v[160:175], a[36:39], v[108:111], v[160:175]\n"
"s_waitcnt lgkmcnt(1)\n"
"v_exp_f32 v153, v153\n"
"v_exp_f32 v154, v154\n"
"v_exp_f32 v155, v155\n"
"v_smfmac_f32_32x32x32_bf16 v[176:191], a[8:11], v[144:151], v220\n"
"buffer_load_dwordx4 v2, s[48:51], s64 offen sc1 lds\n"
"v_exp_f32 v156, v156\n"
"v_exp_f32 v157, v157\n"
"v_exp_f32 v158, v158\n"
"v_smfmac_f32_16x16x64_bf16 v[208:211], v[216:219], v[144:151], v220\n"
"LPG4_%=:\n"
"s_cbranch_execz LPG8_%=\n"
"ds_read_b128 a[28:31], v222 offset:23552\n"
"v_exp_f32 v159, v159\n"
"v_exp_f32 v160, v160\n"
"v_exp_f32 v161, v161\n"
"v_smfmac_f32_32x32x32_bf16 v[176:191], a[12:15], v[152:159], v220\n"
"buffer_load_dwordx4 v2, s[48:51], s64 offen offset:1024 sc1 lds\n"
"v_exp_f32 v162, v162\n"
"v_exp_f32 v163, v163\n"
"v_exp_f32 v164, v164\n"
"v_smfmac_f32_16x16x64_bf16 v[208:211], v[216:219], v[152:159], v220\n"
"ds_read_b128 a[32:35], v222 offset:24576\n"
"v_exp_f32 v165, v165\n"
"v_mfma_f32_32x32x16_bf16 v[144:159], a[16:19], v[96:99], v[112:127]\n"
"v_exp_f32 v166, v166\n"
"v_exp_f32 v167, v167\n"
"v_exp_f32 v168, v168\n"
"v_mfma_f32_32x32x16_bf16 v[144:159], a[20:23], v[100:103], v[144:159]\n"
"v_exp_f32 v169, v169\n"
"v_exp_f32 v170, v170\n"
"v_exp_f32 v171, v171\n"
"v_smfmac_f32_32x32x32_bf16 v[192:207], a[40:43], v[160:167], v220\n"
"buffer_load_dwordx4 v2, s[48:51], s64 offen offset:2048 sc1 lds\n"
"v_exp_f32 v172, v172\n"
"v_exp_f32 v173, v173\n"
"v_exp_f32 v174, v174\n"
"v_smfmac_f32_16x16x64_bf16 v[212:215], v[216:219], v[160:167], v220\n"
"ds_read_b128 a[36:39], v222 offset:25600\n"
"v_exp_f32 v175, v175\n"
"v_exp_f32 v144, v144\n"
"v_exp_f32 v145, v145\n"
"v_smfmac_f32_32x32x32_bf16 v[192:207], a[44:47], v[168:175], v220\n"
"buffer_load_dwordx4 v2, s[48:51], s64 offen offset:3072 sc1 lds\n"
"s_add_u32 m0, s55, 49152\n"
"v_exp_f32 v146, v146\n"
"v_exp_f32 v147, v147\n"
"v_exp_f32 v148, v148\n"
"v_smfmac_f32_16x16x64_bf16 v[212:215], v[216:219], v[168:175], v220\n"
"ds_read_b128 a[40:43], v222 offset:26624\n"
"s_add_u32 s57, s88, 917504\n"
"v_exp_f32 v149, v149\n"
"v_mfma_f32_32x32x16_bf16 v[160:175], a[48:51], v[104:107], v[128:143]\n"
"v_exp_f32 v150, v150\n"
"v_exp_f32 v151, v151\n"
"v_exp_f32 v152, v152\n"
"v_mfma_f32_32x32x16_bf16 v[160:175], a[52:55], v[108:111], v[160:175]\n"
"s_waitcnt lgkmcnt(1)\n"
"v_exp_f32 v153, v153\n"
"v_exp_f32 v154, v154\n"
"v_exp_f32 v155, v155\n"
"v_smfmac_f32_32x32x32_bf16 v[176:191], a[24:27], v[144:151], v220\n"
"buffer_load_dwordx4 v33, s[28:31], s57 offen lds\n"
"s_add_u32 m0, s92, 2048\n"
"v_exp_f32 v156, v156\n"
"v_exp_f32 v157, v157\n"
"v_exp_f32 v158, v158\n"
"v_smfmac_f32_16x16x64_bf16 v[208:211], v[216:219], v[144:151], v220\n"
"ds_read_b128 a[44:47], v222 offset:27648\n"
"s_add_u32 s57, s91, 32768\n"
"v_exp_f32 v159, v159\n"
"v_exp_f32 v160, v160\n"
"v_exp_f32 v161, v161\n"
"v_smfmac_f32_32x32x32_bf16 v[176:191], a[28:31], v[152:159], v220\n"
"buffer_load_dwordx4 v44, s[12:15], s57 offen lds\n"
"s_add_u32 m0, s55, 53248\n"
"v_exp_f32 v162, v162\n"
"v_exp_f32 v163, v163\n"
"v_exp_f32 v164, v164\n"
"v_smfmac_f32_16x16x64_bf16 v[208:211], v[216:219], v[152:159], v220\n"
"ds_read_b128 a[48:51], v222 offset:28672\n"
"s_add_u32 s57, s88, 1146880\n"
"v_exp_f32 v165, v165\n"
"v_mfma_f32_32x32x16_bf16 v[144:159], a[32:35], v[96:99], v[112:127]\n"
"v_exp_f32 v166, v166\n"
"v_exp_f32 v167, v167\n"
"v_exp_f32 v168, v168\n"
"v_mfma_f32_32x32x16_bf16 v[144:159], a[36:39], v[100:103], v[144:159]\n"
"v_exp_f32 v169, v169\n"
"v_exp_f32 v170, v170\n"
"v_exp_f32 v171, v171\n"
"v_smfmac_f32_32x32x32_bf16 v[192:207], a[56:59], v[160:167], v220\n"
"buffer_load_dwordx4 v33, s[28:31], s57 offen lds\n"
"s_add_u32 m0, s92, 3072\n"
"v_exp_f32 v172, v172\n"
"v_exp_f32 v173, v173\n"
"v_exp_f32 v174, v174\n"
"v_smfmac_f32_16x16x64_bf16 v[212:215], v[216:219], v[160:167], v220\n"
"ds_read_b128 a[52:55], v222 offset:29696\n"
"s_add_u32 s57, s91, 49152\n"
"v_exp_f32 v175, v175\n"
"v_exp_f32 v144, v144\n"
"v_exp_f32 v145, v145\n"
"v_smfmac_f32_32x32x32_bf16 v[192:207], a[60:63], v[168:175], v220\n"
"buffer_load_dwordx4 v44, s[12:15], s57 offen lds\n"
"s_add_u32 m0, s55, 57344\n"
"v_exp_f32 v146, v146\n"
"v_exp_f32 v147, v147\n"
"v_exp_f32 v148, v148\n"
"v_smfmac_f32_16x16x64_bf16 v[212:215], v[216:219], v[168:175], v220\n"
"ds_read_b128 a[56:59], v222 offset:30720\n"
"ds_read_b128 a[60:63], v222 offset:31744\n"
"v_exp_f32 v149, v149\n"
"v_mfma_f32_32x32x16_bf16 v[160:175], a[64:67], v[104:107], v[128:143]\n"
"s_add_u32 s57, s88, 1376256\n"
"v_exp_f32 v150, v150\n"
"v_exp_f32 v151, v151\n"
"v_exp_f32 v152, v152\n"
"v_mfma_f32_32x32x16_bf16 v[160:175], a[68:71], v[108:111], v[160:175]\n"
"s_waitcnt lgkmcnt(2)\n"
"v_exp_f32 v153, v153\n"
"v_exp_f32 v154, v154\n"
"v_exp_f32 v155, v155\n"
"v_smfmac_f32_32x32x32_bf16 v[176:191], a[40:43], v[144:151], v220\n"
"buffer_load_dwordx4 v33, s[28:31], s57 offen lds\n"
"v_exp_f32 v156, v156\n"
"v_exp_f32 v157, v157\n"
"v_exp_f32 v158, v158\n"
"v_smfmac_f32_16x16x64_bf16 v[208:211], v[216:219], v[144:151], v220\n"
"v_exp_f32 v159, v159\n"
"v_exp_f32 v160, v160\n"
"v_exp_f32 v161, v161\n"
"v_smfmac_f32_32x32x32_bf16 v[176:191], a[44:47], v[152:159], v220\n"
"s_waitcnt vmcnt(22) lgkmcnt(0)\n"
"s_barrier\n"
"v_exp_f32 v162, v162\n"
"v_exp_f32 v163, v163\n"
"v_exp_f32 v164, v164\n"
"v_smfmac_f32_16x16x64_bf16 v[208:211], v[216:219], v[152:159], v220\n"
"ds_read_b128 a[64:67], v2\n"
"v_exp_f32 v165, v165\n"
"v_mfma_f32_32x32x16_bf16 v[144:159], a[48:51], v[96:99], v[112:127]\n"
"v_exp_f32 v166, v166\n"
"v_exp_f32 v167, v167\n"
"v_exp_f32 v168, v168\n"
"v_mfma_f32_32x32x16_bf16 v[144:159], a[52:55], v[100:103], v[144:159]\n"
"v_exp_f32 v169, v169\n"
"v_exp_f32 v170, v170\n"
"v_exp_f32 v171, v171\n"
"v_smfmac_f32_32x32x32_bf16 v[192:207], a[72:75], v[160:167], v220\n"
"v_exp_f32 v172, v172\n"
"v_exp_f32 v173, v173\n"
"v_exp_f32 v174, v174\n"
"v_smfmac_f32_16x16x64_bf16 v[212:215], v[216:219], v[160:167], v220\n"
"ds_read_b128 a[68:71], v2 offset:1024\n"
"v_exp_f32 v175, v175\n"
"v_exp_f32 v144, v144\n"
"v_exp_f32 v145, v145\n"
"v_smfmac_f32_32x32x32_bf16 v[192:207], a[76:79], v[168:175], v220\n"
"s_add_u32 m0, s61, 147456\n"
"s_add_u32 s64, s61, 1196032\n"
"v_exp_f32 v146, v146\n"
"v_exp_f32 v147, v147\n"
"v_exp_f32 v148, v148\n"
"v_smfmac_f32_16x16x64_bf16 v[212:215], v[216:219], v[168:175], v220\n"
"ds_read_b128 a[72:75], v2 offset:2048\n"
"v_exp_f32 v149, v149\n"
"v_mfma_f32_32x32x16_bf16 v[160:175], a[80:83], v[104:107], v[128:143]\n"
"v_exp_f32 v150, v150\n"
"v_exp_f32 v151, v151\n"
"v_exp_f32 v152, v152\n"
"v_mfma_f32_32x32x16_bf16 v[160:175], a[84:87], v[108:111], v[160:175]\n"
"s_waitcnt lgkmcnt(1)\n"
"v_exp_f32 v153, v153\n"
"v_exp_f32 v154, v154\n"
"v_exp_f32 v155, v155\n"
"v_smfmac_f32_32x32x32_bf16 v[176:191], a[56:59], v[144:151], v220\n"
"buffer_load_dwordx4 v2, s[48:51], s64 offen sc1 lds\n"
"v_exp_f32 v156, v156\n"
"v_exp_f32 v157, v157\n"
"v_exp_f32 v158, v158\n"
"v_smfmac_f32_16x16x64_bf16 v[208:211], v[216:219], v[144:151], v220\n"
"ds_read_b128 a[76:79], v2 offset:3072\n"
"v_exp_f32 v159, v159\n"
"v_exp_f32 v160, v160\n"
"v_exp_f32 v161, v161\n"
"v_smfmac_f32_32x32x32_bf16 v[176:191], a[60:63], v[152:159], v220\n"
"buffer_load_dwordx4 v2, s[48:51], s64 offen offset:1024 sc1 lds\n"
"v_exp_f32 v162, v162\n"
"v_exp_f32 v163, v163\n"
"v_exp_f32 v164, v164\n"
"v_smfmac_f32_16x16x64_bf16 v[208:211], v[216:219], v[152:159], v220\n"
"ds_read_b128 a[80:83], v2 offset:4096\n"
"v_exp_f32 v165, v165\n"
"v_mfma_f32_32x32x16_bf16 v[144:159], a[64:67], v[96:99], v[112:127]\n"
"v_exp_f32 v166, v166\n"
"v_exp_f32 v167, v167\n"
"v_exp_f32 v168, v168\n"
"v_mfma_f32_32x32x16_bf16 v[144:159], a[68:71], v[100:103], v[144:159]\n"
"v_exp_f32 v169, v169\n"
"v_exp_f32 v170, v170\n"
"v_exp_f32 v171, v171\n"
"v_smfmac_f32_32x32x32_bf16 v[192:207], a[88:91], v[160:167], v220\n"
"buffer_load_dwordx4 v2, s[48:51], s64 offen offset:2048 sc1 lds\n"
"v_exp_f32 v172, v172\n"
"v_exp_f32 v173, v173\n"
"v_exp_f32 v174, v174\n"
"v_smfmac_f32_16x16x64_bf16 v[212:215], v[216:219], v[160:167], v220\n"
"ds_read_b128 a[84:87], v2 offset:5120\n"
"v_exp_f32 v175, v175\n"
"v_exp_f32 v144, v144\n"
"v_exp_f32 v145, v145\n"
"v_smfmac_f32_32x32x32_bf16 v[192:207], a[92:95], v[168:175], v220\n"
"buffer_load_dwordx4 v2, s[48:51], s64 offen offset:3072 sc1 lds\n"
"s_add_u32 m0, s55, 61440\n"
"v_exp_f32 v146, v146\n"
"v_exp_f32 v147, v147\n"
"v_exp_f32 v148, v148\n"
"v_smfmac_f32_16x16x64_bf16 v[212:215], v[216:219], v[168:175], v220\n"
"ds_read_b128 a[88:91], v2 offset:6144\n"
"s_add_u32 s57, s88, 1605632\n"
"v_exp_f32 v149, v149\n"
"v_mfma_f32_32x32x16_bf16 v[160:175], a[96:99], v[104:107], v[128:143]\n"
"v_exp_f32 v150, v150\n"
"v_exp_f32 v151, v151\n"
"v_exp_f32 v152, v152\n"
"v_mfma_f32_32x32x16_bf16 v[160:175], a[100:103], v[108:111], v[160:175]\n"
"s_waitcnt lgkmcnt(1)\n"
"v_exp_f32 v153, v153\n"
"v_exp_f32 v154, v154\n"
"v_exp_f32 v155, v155\n"
"v_smfmac_f32_32x32x32_bf16 v[176:191], a[72:75], v[144:151], v220\n"
"buffer_load_dwordx4 v33, s[28:31], s57 offen lds\n"
"v_exp_f32 v156, v156\n"
"v_exp_f32 v157, v157\n"
"v_exp_f32 v158, v158\n"
"v_smfmac_f32_16x16x64_bf16 v[208:211], v[216:219], v[144:151], v220\n"
"ds_read_b128 a[92:95], v2 offset:7168\n"
"v_exp_f32 v159, v159\n"
"v_exp_f32 v160, v160\n"
"v_exp_f32 v161, v161\n"
"v_smfmac_f32_32x32x32_bf16 v[176:191], a[76:79], v[152:159], v220\n"
"v_exp_f32 v162, v162\n"
"v_exp_f32 v163, v163\n"
"v_exp_f32 v164, v164\n"
"v_smfmac_f32_16x16x64_bf16 v[208:211], v[216:219], v[152:159], v220\n"
"ds_read_b128 a[96:99], v2 offset:8192\n"
"v_exp_f32 v165, v165\n"
"v_mfma_f32_32x32x16_bf16 v[144:159], a[80:83], v[96:99], v[112:127]\n"
"v_exp_f32 v166, v166\n"
"v_exp_f32 v167, v167\n"
"v_exp_f32 v168, v168\n"
"v_mfma_f32_32x32x16_bf16 v[144:159], a[84:87], v[100:103], v[144:159]\n"
"v_exp_f32 v169, v169\n"
"v_exp_f32 v170, v170\n"
"v_exp_f32 v171, v171\n"
"v_smfmac_f32_32x32x32_bf16 v[192:207], a[104:107], v[160:167], v220\n"
"v_exp_f32 v172, v172\n"
"v_exp_f32 v173, v173\n"
"v_exp_f32 v174, v174\n"
"v_smfmac_f32_16x16x64_bf16 v[212:215], v[216:219], v[160:167], v220\n"
"ds_read_b128 a[100:103], v2 offset:9216\n"
"v_exp_f32 v175, v175\n"
"v_exp_f32 v144, v144\n"
"v_exp_f32 v145, v145\n"
"v_smfmac_f32_32x32x32_bf16 v[192:207], a[108:111], v[168:175], v220\n"
"buffer_load_dwordx4 v[92:95], v2, s[24:27], s94 offen\n"
"v_exp_f32 v146, v146\n"
"v_exp_f32 v147, v147\n"
"v_exp_f32 v148, v148\n"
"v_smfmac_f32_16x16x64_bf16 v[212:215], v[216:219], v[168:175], v220\n"
"ds_read_b128 a[104:107], v2 offset:10240\n"
"v_exp_f32 v149, v149\n"
"v_mfma_f32_32x32x16_bf16 v[160:175], a[112:115], v[104:107], v[128:143]\n"
"v_exp_f32 v150, v150\n"
"v_exp_f32 v151, v151\n"
"v_exp_f32 v152, v152\n"
"v_mfma_f32_32x32x16_bf16 v[160:175], a[116:119], v[108:111], v[160:175]\n"
"s_waitcnt lgkmcnt(1)\n"
"v_exp_f32 v153, v153\n"
"v_exp_f32 v154, v154\n"
"v_exp_f32 v155, v155\n"
"v_smfmac_f32_32x32x32_bf16 v[176:191], a[88:91], v[144:151], v220\n"
"v_exp_f32 v156, v156\n"
"v_exp_f32 v157, v157\n"
"v_exp_f32 v158, v158\n"
"v_smfmac_f32_16x16x64_bf16 v[208:211], v[216:219], v[144:151], v220\n"
"ds_read_b128 a[108:111], v2 offset:11264\n"
"v_exp_f32 v159, v159\n"
"v_exp_f32 v160, v160\n"
"v_exp_f32 v161, v161\n"
"v_smfmac_f32_32x32x32_bf16 v[176:191], a[92:95], v[152:159], v220\n"
"v_exp_f32 v162, v162\n"
"v_exp_f32 v163, v163\n"
"v_exp_f32 v164, v164\n"
"v_smfmac_f32_16x16x64_bf16 v[208:211], v[216:219], v[152:159], v220\n"
"ds_read_b128 a[112:115], v2 offset:12288\n"
"v_exp_f32 v165, v165\n"
"v_mfma_f32_32x32x16_bf16 v[144:159], a[96:99], v[96:99], v[112:127]\n"
"v_exp_f32 v166, v166\n"
"v_exp_f32 v167, v167\n"
"v_exp_f32 v168, v168\n"
"v_mfma_f32_32x32x16_bf16 v[144:159], a[100:103], v[100:103], v[144:159]\n"
"v_exp_f32 v169, v169\n"
"v_exp_f32 v170, v170\n"
"v_exp_f32 v171, v171\n"
"v_smfmac_f32_32x32x32_bf16 v[192:207], a[120:123], v[160:167], v220\n"
"v_exp_f32 v172, v172\n"
"v_exp_f32 v173, v173\n"
"v_exp_f32 v174, v174\n"
"v_smfmac_f32_16x16x64_bf16 v[212:215], v[216:219], v[160:167], v220\n"
"ds_read_b128 a[116:119], v2 offset:13312\n"
"v_exp_f32 v175, v175\n"
"v_exp_f32 v144, v144\n"
"v_exp_f32 v145, v145\n"
"v_smfmac_f32_32x32x32_bf16 v[192:207], a[124:127], v[168:175], v220\n"
"buffer_load_dwordx4 v[92:95], v2, s[24:27], s94 offen offset:2048\n"
"v_exp_f32 v146, v146\n"
"v_exp_f32 v147, v147\n"
"v_exp_f32 v148, v148\n"
"v_smfmac_f32_16x16x64_bf16 v[212:215], v[216:219], v[168:175], v220\n"
"ds_read_b128 a[120:123], v2 offset:14336\n"
"ds_read_b128 a[124:127], v2 offset:15360\n"
"v_exp_f32 v149, v149\n"
"v_mfma_f32_32x32x16_bf16 v[160:175], a[128:131], v[104:107], v[128:143]\n"
"v_exp_f32 v150, v150\n"
"v_exp_f32 v151, v151\n"
"v_exp_f32 v152, v152\n"
"v_mfma_f32_32x32x16_bf16 v[160:175], a[132:135], v[108:111], v[160:175]\n"
"s_waitcnt lgkmcnt(2)\n"
"v_exp_f32 v153, v153\n"
"v_exp_f32 v154, v154\n"
"v_exp_f32 v155, v155\n"
"v_smfmac_f32_32x32x32_bf16 v[176:191], a[104:107], v[144:151], v220\n"
"buffer_load_ushort v14, v45, s[36:39], s93 offen\n"
"v_exp_f32 v156, v156\n"
"v_exp_f32 v157, v157\n"
"v_exp_f32 v158, v158\n"
"v_smfmac_f32_16x16x64_bf16 v[208:211], v[216:219], v[144:151], v220\n"
"v_exp_f32 v159, v159\n"
"v_exp_f32 v160, v160\n"
"v_exp_f32 v161, v161\n"
"v_smfmac_f32_32x32x32_bf16 v[176:191], a[108:111], v[152:159], v220\n"
"s_waitcnt vmcnt(23) lgkmcnt(0)\n"
"s_barrier\n"
"v_exp_f32 v162, v162\n"
"v_exp_f32 v163, v163\n"
"v_exp_f32 v164, v164\n"
"v_smfmac_f32_16x16x64_bf16 v[208:211], v[216:219], v[152:159], v220\n"
"ds_read_b128 a[128:131], v2 offset:16384\n"
"v_exp_f32 v165, v165\n"
"v_mfma_f32_32x32x16_bf16 v[144:159], a[112:115], v[96:99], v[112:127]\n"
"v_exp_f32 v166, v166\n"
"v_exp_f32 v167, v167\n"
"v_exp_f32 v168, v168\n"
"v_mfma_f32_32x32x16_bf16 v[144:159], a[116:119], v[100:103], v[144:159]\n"
"v_exp_f32 v169, v169\n"
"v_exp_f32 v170, v170\n"
"v_exp_f32 v171, v171\n"
"v_smfmac_f32_32x32x32_bf16 v[192:207], a[136:139], v[160:167], v220\n"
"buffer_load_ushort v15, v45, s[36:39], s93 offen offset:32\n"
"v_exp_f32 v172, v172\n"
"v_exp_f32 v173, v173\n"
"v_exp_f32 v174, v174\n"
"v_smfmac_f32_16x16x64_bf16 v[212:215], v[216:219], v[160:167], v220\n"
"ds_read_b128 a[132:135], v2 offset:17408\n"
"v_exp_f32 v175, v175\n"
"v_exp_f32 v144, v144\n"
"v_exp_f32 v145, v145\n"
"v_smfmac_f32_32x32x32_bf16 v[192:207], a[140:143], v[168:175], v220\n"
"s_add_u32 m0, s61, 0\n"
"s_add_u32 s64, s61, 1212416\n"
"v_exp_f32 v146, v146\n"
"v_exp_f32 v147, v147\n"
"v_exp_f32 v148, v148\n"
"v_smfmac_f32_16x16x64_bf16 v[212:215], v[216:219], v[168:175], v220\n"
"ds_read_b128 a[136:139], v2 offset:18432\n"
"v_exp_f32 v149, v149\n"
"v_mfma_f32_32x32x16_bf16 v[160:175], a[144:147], v[104:107], v[128:143]\n"
"v_exp_f32 v150, v150\n"
"v_exp_f32 v151, v151\n"
"v_exp_f32 v152, v152\n"
"v_mfma_f32_32x32x16_bf16 v[160:175], a[148:151], v[108:111], v[160:175]\n"
"s_waitcnt lgkmcnt(1)\n"
"v_exp_f32 v153, v153\n"
"v_exp_f32 v154, v154\n"
"v_exp_f32 v155, v155\n"
"v_smfmac_f32_32x32x32_bf16 v[176:191], a[120:123], v[144:151], v220\n"
"buffer_load_dwordx4 v2, s[48:51], s64 offen sc1 lds\n"
"v_exp_f32 v156, v156\n"
"v_exp_f32 v157, v157\n"
"v_exp_f32 v158, v158\n"
"v_smfmac_f32_16x16x64_bf16 v[208:211], v[216:219], v[144:151], v220\n"
"ds_read_b128 a[140:143], v2 offset:19456\n"
"v_exp_f32 v159, v159\n"
"v_exp_f32 v160, v160\n"
"v_exp_f32 v161, v161\n"
"v_smfmac_f32_32x32x32_bf16 v[176:191], a[124:127], v[152:159], v220\n"
"buffer_load_dwordx4 v2, s[48:51], s64 offen offset:1024 sc1 lds\n"
"v_exp_f32 v162, v162\n"
"v_exp_f32 v163, v163\n"
"v_exp_f32 v164, v164\n"
"v_smfmac_f32_16x16x64_bf16 v[208:211], v[216:219], v[152:159], v220\n"
"ds_read_b128 a[144:147], v2 offset:20480\n"
"v_exp_f32 v165, v165\n"
"v_mfma_f32_32x32x16_bf16 v[144:159], a[128:131], v[96:99], v[112:127]\n"
"v_exp_f32 v166, v166\n"
"v_exp_f32 v167, v167\n"
"v_exp_f32 v168, v168\n"
"v_mfma_f32_32x32x16_bf16 v[144:159], a[132:135], v[100:103], v[144:159]\n"
"v_exp_f32 v169, v169\n"
"v_exp_f32 v170, v170\n"
"v_exp_f32 v171, v171\n"
"v_smfmac_f32_32x32x32_bf16 v[192:207], a[152:155], v[160:167], v220\n"
"buffer_load_dwordx4 v2, s[48:51], s64 offen offset:2048 sc1 lds\n"
"v_exp_f32 v172, v172\n"
"v_exp_f32 v173, v173\n"
"v_exp_f32 v174, v174\n"
"v_smfmac_f32_16x16x64_bf16 v[212:215], v[216:219], v[160:167], v220\n"
"ds_read_b128 a[148:151], v2 offset:21504\n"
"v_exp_f32 v175, v175\n"
"v_exp_f32 v144, v144\n"
"v_exp_f32 v145, v145\n"
"v_smfmac_f32_32x32x32_bf16 v[192:207], a[156:159], v[168:175], v220\n"
"buffer_load_dwordx4 v2, s[48:51], s64 offen offset:3072 sc1 lds\n"
"s_add_u32 m0, s90, 0\n"
"v_exp_f32 v146, v146\n"
"v_exp_f32 v147, v147\n"
"v_exp_f32 v148, v148\n"
"v_smfmac_f32_16x16x64_bf16 v[212:215], v[216:219], v[168:175], v220\n"
"ds_read_b128 a[152:155], v2 offset:22528\n"
"s_add_u32 s57, s89, 0\n"
"v_exp_f32 v149, v149\n"
"v_mfma_f32_32x32x16_bf16 v[160:175], a[160:163], v[104:107], v[128:143]\n"
"v_exp_f32 v150, v150\n"
"v_exp_f32 v151, v151\n"
"v_exp_f32 v152, v152\n"
"v_mfma_f32_32x32x16_bf16 v[160:175], a[164:167], v[108:111], v[160:175]\n"
"s_waitcnt lgkmcnt(1)\n"
"v_exp_f32 v153, v153\n"
"v_exp_f32 v154, v154\n"
"v_exp_f32 v155, v155\n"
"v_smfmac_f32_32x32x32_bf16 v[176:191], a[136:139], v[144:151], v220\n"
"buffer_load_dwordx4 v40, s[32:35], s57 offen lds\n"
"v_exp_f32 v156, v156\n"
"v_exp_f32 v157, v157\n"
"v_exp_f32 v158, v158\n"
"v_smfmac_f32_16x16x64_bf16 v[208:211], v[216:219], v[144:151], v220\n"
"ds_read_b128 a[156:159], v2 offset:23552\n"
"v_exp_f32 v159, v159\n"
"v_exp_f32 v160, v160\n"
"v_exp_f32 v161, v161\n"
"v_smfmac_f32_32x32x32_bf16 v[176:191], a[140:143], v[152:159], v220\n"
"v_exp_f32 v162, v162\n"
"v_exp_f32 v163, v163\n"
"v_exp_f32 v164, v164\n"
"v_smfmac_f32_16x16x64_bf16 v[208:211], v[216:219], v[152:159], v220\n"
"ds_read_b128 a[160:163], v2 offset:24576\n"
"v_exp_f32 v165, v165\n"
"v_mfma_f32_32x32x16_bf16 v[144:159], a[144:147], v[96:99], v[112:127]\n"
"v_exp_f32 v166, v166\n"
"v_exp_f32 v167, v167\n"
"v_exp_f32 v168, v168\n"
"v_mfma_f32_32x32x16_bf16 v[144:159], a[148:151], v[100:103], v[144:159]\n"
"v_exp_f32 v169, v169\n"
"v_exp_f32 v170, v170\n"
"v_exp_f32 v171, v171\n"
"v_smfmac_f32_32x32x32_bf16 v[192:207], a[168:171], v[160:167], v220\n"
"v_exp_f32 v172, v172\n"
"v_exp_f32 v173, v173\n"
"v_exp_f32 v174, v174\n"
"v_smfmac_f32_16x16x64_bf16 v[212:215], v[216:219], v[160:167], v220\n"
"ds_read_b128 a[164:167], v2 offset:25600\n"
"v_exp_f32 v175, v175\n"
"v_exp_f32 v144, v144\n"
"v_exp_f32 v145, v145\n"
"v_smfmac_f32_32x32x32_bf16 v[192:207], a[172:175], v[168:175], v220\n"
"s_add_u32 m0, s90, 1024\n"
"s_add_u32 s57, s89, 0\n"
"v_exp_f32 v146, v146\n"
"v_exp_f32 v147, v147\n"
"v_exp_f32 v148, v148\n"
"v_smfmac_f32_16x16x64_bf16 v[212:215], v[216:219], v[168:175], v220\n"
"ds_read_b128 a[168:171], v2 offset:26624\n"
"v_exp_f32 v149, v149\n"
"v_mfma_f32_32x32x16_bf16 v[160:175], a[176:179], v[104:107], v[128:143]\n"
"v_exp_f32 v150, v150\n"
"v_exp_f32 v151, v151\n"
"v_exp_f32 v152, v152\n"
"v_mfma_f32_32x32x16_bf16 v[160:175], a[180:183], v[108:111], v[160:175]\n"
"s_waitcnt lgkmcnt(1)\n"
"v_exp_f32 v153, v153\n"
"v_exp_f32 v154, v154\n"
"v_exp_f32 v155, v155\n"
"v_smfmac_f32_32x32x32_bf16 v[176:191], a[152:155], v[144:151], v220\n"
"buffer_load_dwordx4 v41, s[32:35], s57 offen lds\n"
"v_exp_f32 v156, v156\n"
"v_exp_f32 v157, v157\n"
"v_exp_f32 v158, v158\n"
"v_smfmac_f32_16x16x64_bf16 v[208:211], v[216:219], v[144:151], v220\n"
"ds_read_b128 a[172:175], v2 offset:27648\n"
"v_exp_f32 v159, v159\n"
"v_exp_f32 v160, v160\n"
"v_exp_f32 v161, v161\n"
"v_smfmac_f32_32x32x32_bf16 v[176:191], a[156:159], v[152:159], v220\n"
"v_exp_f32 v162, v162\n"
"v_exp_f32 v163, v163\n"
"v_exp_f32 v164, v164\n"
"v_smfmac_f32_16x16x64_bf16 v[208:211], v[216:219], v[152:159], v220\n"
"ds_read_b128 a[176:179], v2 offset:28672\n"
"v_exp_f32 v165, v165\n"
"v_mfma_f32_32x32x16_bf16 v[144:159], a[160:163], v[96:99], v[112:127]\n"
"v_exp_f32 v166, v166\n"
"v_exp_f32 v167, v167\n"
"v_exp_f32 v168, v168\n"
"v_mfma_f32_32x32x16_bf16 v[144:159], a[164:167], v[100:103], v[144:159]\n"
"v_exp_f32 v169, v169\n"
"v_exp_f32 v170, v170\n"
"v_exp_f32 v171, v171\n"
"v_smfmac_f32_32x32x32_bf16 v[192:207], a[184:187], v[160:167], v220\n"
"v_exp_f32 v172, v172\n"
"v_exp_f32 v173, v173\n"
"v_exp_f32 v174, v174\n"
"v_smfmac_f32_16x16x64_bf16 v[212:215], v[216:219], v[160:167], v220\n"
"ds_read_b128 a[180:183], v2 offset:29696\n"
"v_exp_f32 v175, v175\n"
"v_exp_f32 v144, v144\n"
"v_exp_f32 v145, v145\n"
"v_smfmac_f32_32x32x32_bf16 v[192:207], a[188:191], v[168:175], v220\n"
"s_add_u32 m0, s90, 2048\n"
"s_add_u32 s57, s89, 0\n"
"v_exp_f32 v146, v146\n"
"v_exp_f32 v147, v147\n"
"v_exp_f32 v148, v148\n"
"v_smfmac_f32_16x16x64_bf16 v[212:215], v[216:219], v[168:175], v220\n"
"ds_read_b128 a[184:187], v2 offset:30720\n"
"ds_read_b128 a[188:191], v2 offset:31744\n"
"v_exp_f32 v149, v149\n"
"v_mfma_f32_32x32x16_bf16 v[160:175], a[192:195], v[104:107], v[128:143]\n"
"v_exp_f32 v150, v150\n"
"v_exp_f32 v151, v151\n"
"v_exp_f32 v152, v152\n"
"v_mfma_f32_32x32x16_bf16 v[160:175], a[196:199], v[108:111], v[160:175]\n"
"s_waitcnt lgkmcnt(2)\n"
"v_exp_f32 v153, v153\n"
"v_exp_f32 v154, v154\n"
"v_exp_f32 v155, v155\n"
"v_smfmac_f32_32x32x32_bf16 v[176:191], a[168:171], v[144:151], v220\n"
"buffer_load_dwordx4 v42, s[32:35], s57 offen lds\n"
"v_exp_f32 v156, v156\n"
"v_exp_f32 v157, v157\n"
"v_exp_f32 v158, v158\n"
"v_smfmac_f32_16x16x64_bf16 v[208:211], v[216:219], v[144:151], v220\n"
"v_exp_f32 v159, v159\n"
"v_exp_f32 v160, v160\n"
"v_exp_f32 v161, v161\n"
"v_smfmac_f32_32x32x32_bf16 v[176:191], a[172:175], v[152:159], v220\n"
"s_waitcnt vmcnt(11) lgkmcnt(0)\n"
"s_barrier\n"
"v_exp_f32 v162, v162\n"
"v_exp_f32 v163, v163\n"
"v_exp_f32 v164, v164\n"
"v_smfmac_f32_16x16x64_bf16 v[208:211], v[216:219], v[152:159], v220\n"
"ds_read_b128 a[192:195], v221 offset:49152\n"
"v_exp_f32 v165, v165\n"
"v_mfma_f32_32x32x16_bf16 v[144:159], a[176:179], v[96:99], v[112:127]\n"
"v_exp_f32 v166, v166\n"
"v_exp_f32 v167, v167\n"
"v_exp_f32 v168, v168\n"
"v_mfma_f32_32x32x16_bf16 v[144:159], a[180:183], v[100:103], v[144:159]\n"
"v_exp_f32 v169, v169\n"
"v_exp_f32 v170, v170\n"
"v_exp_f32 v171, v171\n"
"v_smfmac_f32_32x32x32_bf16 v[192:207], a[200:203], v[160:167], v220\n"
"v_exp_f32 v172, v172\n"
"v_exp_f32 v173, v173\n"
"v_exp_f32 v174, v174\n"
"v_smfmac_f32_16x16x64_bf16 v[212:215], v[216:219], v[160:167], v220\n"
"ds_read_b128 a[196:199], v221 offset:50176\n"
"v_exp_f32 v175, v175\n"
"v_exp_f32 v144, v144\n"
"v_exp_f32 v145, v145\n"
"v_smfmac_f32_32x32x32_bf16 v[192:207], a[204:207], v[168:175], v220\n"
"s_add_u32 m0, s61, 16384\n"
"s_add_u32 s64, s61, 1228800\n"
"v_exp_f32 v146, v146\n"
"v_exp_f32 v147, v147\n"
"v_exp_f32 v148, v148\n"
"v_smfmac_f32_16x16x64_bf16 v[212:215], v[216:219], v[168:175], v220\n"
"ds_read_b128 a[200:203], v221 offset:51200\n"
"ds_read_b64_tr_b16 v[224:225], v32 offset:32768\n"
"v_exp_f32 v149, v149\n"
"v_mfma_f32_32x32x16_bf16 v[160:175], a[208:211], v[104:107], v[128:143]\n"
"v_exp_f32 v150, v150\n"
"v_exp_f32 v151, v151\n"
"v_exp_f32 v152, v152\n"
"v_mfma_f32_32x32x16_bf16 v[160:175], a[212:215], v[108:111], v[160:175]\n"
"s_waitcnt lgkmcnt(2)\n"
"v_exp_f32 v153, v153\n"
"v_exp_f32 v154, v154\n"
"v_exp_f32 v155, v155\n"
"v_smfmac_f32_32x32x32_bf16 v[176:191], a[184:187], v[144:151], v220\n"
"buffer_load_dwordx4 v2, s[48:51], s64 offen sc1 lds\n"
"v_exp_f32 v156, v156\n"
"v_exp_f32 v157, v157\n"
"v_exp_f32 v158, v158\n"
"v_smfmac_f32_16x16x64_bf16 v[208:211], v[216:219], v[144:151], v220\n"
"ds_read_b128 a[204:207], v221 offset:52224\n"
"ds_read_b64_tr_b16 v[226:227], v32 offset:33280\n"
"v_exp_f32 v159, v159\n"
"v_exp_f32 v160, v160\n"
"v_exp_f32 v161, v161\n"
"v_smfmac_f32_32x32x32_bf16 v[176:191], a[188:191], v[152:159], v220\n"
"buffer_load_dwordx4 v2, s[48:51], s64 offen offset:1024 sc1 lds\n"
"v_exp_f32 v162, v162\n"
"v_exp_f32 v163, v163\n"
"v_exp_f32 v164, v164\n"
"v_smfmac_f32_16x16x64_bf16 v[208:211], v[216:219], v[152:159], v220\n"
"ds_read_b128 a[208:211], v221 offset:53248\n"
"ds_read_b64_tr_b16 v[228:229], v32 offset:36864\n"
"v_exp_f32 v165, v165\n"
"v_mfma_f32_32x32x16_bf16 v[144:159], a[192:195], v[96:99], v[112:127]\n"
"v_exp_f32 v166, v166\n"
"v_exp_f32 v167, v167\n"
"v_exp_f32 v168, v168\n"
"v_mfma_f32_32x32x16_bf16 v[144:159], a[196:199], v[100:103], v[144:159]\n"
"ds_read_b64_tr_b16 v[230:231], v32 offset:37376\n"
"v_exp_f32 v169, v169\n"
"v_exp_f32 v170, v170\n"
"v_exp_f32 v171, v171\n"
"v_smfmac_f32_32x32x32_bf16 v[192:207], a[216:219], v[160:167], v220\n"
"buffer_load_dwordx4 v2, s[48:51], s64 offen offset:2048 sc1 lds\n"
"v_exp_f32 v172, v172\n"
"v_exp_f32 v173, v173\n"
"v_exp_f32 v174, v174\n"
"v_smfmac_f32_16x16x64_bf16 v[212:215], v[216:219], v[160:167], v220\n"
"ds_read_b128 a[212:215], v221 offset:54272\n"
"ds_read_b64_tr_b16 v[232:233], v32 offset:40960\n"
"v_exp_f32 v175, v175\n"
"v_exp_f32 v144, v144\n"
"v_exp_f32 v145, v145\n"
"v_smfmac_f32_32x32x32_bf16 v[192:207], a[220:223], v[168:175], v220\n"
"buffer_load_dwordx4 v2, s[48:51], s64 offen offset:3072 sc1 lds\n"
"s_add_u32 m0, s90, 3072\n"
"v_exp_f32 v146, v146\n"
"v_exp_f32 v147, v147\n"
"v_exp_f32 v148, v148\n"
"v_smfmac_f32_16x16x64_bf16 v[212:215], v[216:219], v[168:175], v220\n"
"ds_read_b128 a[216:219], v221 offset:55296\n"
"s_add_u32 s57, s89, 0\n"
"v_exp_f32 v149, v149\n"
"v_mfma_f32_32x32x16_bf16 v[160:175], a[224:227], v[104:107], v[128:143]\n"
"v_exp_f32 v150, v150\n"
"v_exp_f32 v151, v151\n"
"v_exp_f32 v152, v152\n"
"v_mfma_f32_32x32x16_bf16 v[160:175], a[228:231], v[108:111], v[160:175]\n"
"s_waitcnt lgkmcnt(2)\n"
"v_exp_f32 v153, v153\n"
"v_exp_f32 v154, v154\n"
"v_exp_f32 v155, v155\n"
"v_smfmac_f32_32x32x32_bf16 v[176:191], a[200:203], v[144:151], v220\n"
"buffer_load_dwordx4 v43, s[32:35], s57 offen lds\n"
"v_exp_f32 v156, v156\n"
"v_exp_f32 v157, v157\n"
"v_exp_f32 v158, v158\n"
"v_smfmac_f32_16x16x64_bf16 v[208:211], v[216:219], v[144:151], v220\n"
"ds_read_b128 a[220:223], v221 offset:56320\n"
"ds_read_b64_tr_b16 v[234:235], v32 offset:41472\n"
"v_exp_f32 v159, v159\n"
"v_exp_f32 v160, v160\n"
"v_exp_f32 v161, v161\n"
"v_smfmac_f32_32x32x32_bf16 v[176:191], a[204:207], v[152:159], v220\n"
"ds_read_b64_tr_b16 v[236:237], v32 offset:45056\n"
"ds_read_b64_tr_b16 v[238:239], v32 offset:45568\n"
"v_exp_f32 v162, v162\n"
"v_exp_f32 v163, v163\n"
"v_exp_f32 v164, v164\n"
"v_smfmac_f32_16x16x64_bf16 v[208:211], v[216:219], v[152:159], v220\n"
"ds_read_b128 a[224:227], v221 offset:57344\n"
"ds_read_b64_tr_b16 v[240:241], v32 offset:49152\n"
"v_exp_f32 v165, v165\n"
"v_mfma_f32_32x32x16_bf16 v[144:159], a[208:211], v[96:99], v[112:127]\n"
"v_exp_f32 v166, v166\n"
"v_exp_f32 v167, v167\n"
"v_exp_f32 v168, v168\n"
"v_mfma_f32_32x32x16_bf16 v[144:159], a[212:215], v[100:103], v[144:159]\n"
"ds_read_b64_tr_b16 v[242:243], v32 offset:49664\n"
"v_exp_f32 v169, v169\n"
"v_exp_f32 v170, v170\n"
"v_exp_f32 v171, v171\n"
"v_smfmac_f32_32x32x32_bf16 v[192:207], a[232:235], v[160:167], v220\n"
"ds_read_b64_tr_b16 v[244:245], v32 offset:53248\n"
"ds_read_b64_tr_b16 v[246:247], v32 offset:53760\n"
"v_exp_f32 v172, v172\n"
"v_exp_f32 v173, v173\n"
"v_exp_f32 v174, v174\n"
"v_smfmac_f32_16x16x64_bf16 v[212:215], v[216:219], v[160:167], v220\n"
"ds_read_b128 a[228:231], v221 offset:58368\n"
"ds_read_b64_tr_b16 v[248:249], v32 offset:57344\n"
"v_exp_f32 v175, v175\n"
"v_exp_f32 v144, v144\n"
"v_exp_f32 v145, v145\n"
"v_smfmac_f32_32x32x32_bf16 v[192:207], a[236:239], v[168:175], v220\n"
"s_add_u32 m0, s90, 4096\n"
"s_add_u32 s57, s89, 65536\n"
"v_exp_f32 v146, v146\n"
"v_exp_f32 v147, v147\n"
"v_exp_f32 v148, v148\n"
"v_smfmac_f32_16x16x64_bf16 v[212:215], v[216:219], v[168:175], v220\n"
"ds_read_b128 a[232:235], v221 offset:59392\n"
"ds_read_b64_tr_b16 v[250:251], v32 offset:57856\n"
"v_exp_f32 v149, v149\n"
"v_mfma_f32_32x32x16_bf16 v[160:175], a[240:243], v[104:107], v[128:143]\n"
"v_exp_f32 v150, v150\n"
"v_exp_f32 v151, v151\n"
"v_exp_f32 v152, v152\n"
"v_mfma_f32_32x32x16_bf16 v[160:175], a[244:247], v[108:111], v[160:175]\n"
"s_waitcnt lgkmcnt(3)\n"
"v_exp_f32 v153, v153\n"
"v_exp_f32 v154, v154\n"
"v_exp_f32 v155, v155\n"
"v_smfmac_f32_32x32x32_bf16 v[176:191], a[216:219], v[144:151], v220\n"
"buffer_load_dwordx4 v40, s[32:35], s57 offen lds\n"
"v_exp_f32 v156, v156\n"
"v_exp_f32 v157, v157\n"
"v_exp_f32 v158, v158\n"
"v_smfmac_f32_16x16x64_bf16 v[208:211], v[216:219], v[144:151], v220\n"
"ds_read_b128 a[236:239], v221 offset:60416\n"
"ds_read_b64_tr_b16 v[252:253], v32 offset:61440\n"
"v_exp_f32 v159, v159\n"
"v_exp_f32 v160, v160\n"
"v_exp_f32 v161, v161\n"
"v_smfmac_f32_32x32x32_bf16 v[176:191], a[220:223], v[152:159], v220\n"
"ds_read_b64_tr_b16 v[254:255], v32 offset:61952\n"
"v_xor_b32 v46, 0, v35\n"
"v_exp_f32 v162, v162\n"
"v_exp_f32 v163, v163\n"
"v_exp_f32 v164, v164\n"
"v_smfmac_f32_16x16x64_bf16 v[208:211], v[216:219], v[152:159], v220\n"
"ds_read_b128 a[240:243], v221 offset:61440\n"
"ds_read_b64 v[16:17], v46\n"
"v_exp_f32 v165, v165\n"
"v_mfma_f32_32x32x16_bf16 v[144:159], a[224:227], v[96:99], v[112:127]\n"
"v_exp_f32 v166, v166\n"
"v_exp_f32 v167, v167\n"
"v_exp_f32 v168, v168\n"
"v_mfma_f32_32x32x16_bf16 v[144:159], a[228:231], v[100:103], v[144:159]\n"
"ds_read_b64 v[24:25], v46 offset:2048\n"
"v_exp_f32 v169, v169\n"
"v_exp_f32 v170, v170\n"
"v_exp_f32 v171, v171\n"
"LPG5_%=:\n"
"s_cbranch_execz LPG9_%=\n"
"v_smfmac_f32_32x32x32_bf16 v[192:207], a[248:251], v[160:167], v220\n"
"v_xor_b32 v46, 32, v35\n"
"ds_read_b64 v[18:19], v46\n"
"v_exp_f32 v172, v172\n"
"v_exp_f32 v173, v173\n"
"v_exp_f32 v174, v174\n"
"v_smfmac_f32_16x16x64_bf16 v[212:215], v[216:219], v[160:167], v220\n"
"ds_read_b128 a[244:247], v221 offset:62464\n"
"ds_read_b64 v[26:27], v46 offset:2048\n"
"v_exp_f32 v175, v175\n"
"v_exp_f32 v144, v144\n"
"v_exp_f32 v145, v145\n"
"v_smfmac_f32_32x32x32_bf16 v[192:207], a[252:255], v[168:175], v220\n"
"v_xor_b32 v46, 64, v35\n"
"ds_read_b64 v[20:21], v46\n"
"v_exp_f32 v146, v146\n"
"v_exp_f32 v147, v147\n"
"v_exp_f32 v148, v148\n"
"v_smfmac_f32_16x16x64_bf16 v[212:215], v[216:219], v[168:175], v220\n"
"ds_read_b128 a[248:251], v221 offset:63488\n"
"ds_read_b128 a[252:255], v221 offset:64512\n"
"v_exp_f32 v149, v149\n"
"v_mfma_f32_32x32x16_bf16 v[160:175], a[0:3], v[104:107], v[128:143]\n"
"v_exp_f32 v150, v150\n"
"v_exp_f32 v151, v151\n"
"v_exp_f32 v152, v152\n"
"v_mfma_f32_32x32x16_bf16 v[160:175], a[4:7], v[108:111], v[160:175]\n"
"s_waitcnt lgkmcnt(4)\n"
"v_exp_f32 v153, v153\n"
"v_exp_f32 v154, v154\n"
"v_exp_f32 v155, v155\n"
"v_smfmac_f32_32x32x32_bf16 v[176:191], a[232:235], v[144:151], v220\n"
"ds_read_b64 v[28:29], v46 offset:2048\n"
"v_xor_b32 v46, 96, v35\n"
"v_exp_f32 v156, v156\n"
"v_exp_f32 v157, v157\n"
"v_exp_f32 v158, v158\n"
"v_smfmac_f32_16x16x64_bf16 v[208:211], v[216:219], v[144:151], v220\n"
"ds_read_b64 v[22:23], v46\n"
"ds_read_b64 v[30:31], v46 offset:2048\n"
"v_exp_f32 v159, v159\n"
"v_mfma_f32_16x16x32_bf16 v[80:83], v[84:87], v[224:227], 0\n"
"v_exp_f32 v160, v160\n"
"v_exp_f32 v161, v161\n"
"v_smfmac_f32_32x32x32_bf16 v[176:191], a[236:239], v[152:159], v220\n"
"s_waitcnt lgkmcnt(3)\n"
"s_barrier\n"
"v_exp_f32 v162, v162\n"
"v_exp_f32 v163, v163\n"
"v_exp_f32 v164, v164\n"
"v_smfmac_f32_16x16x64_bf16 v[208:211], v[216:219], v[152:159], v220\n"
"ds_read_b128 a[0:3], v222\n"
"v_exp_f32 v165, v165\n"
"v_mfma_f32_32x32x16_bf16 v[144:159], a[240:243], v[96:99], v[112:127]\n"
"v_exp_f32 v166, v166\n"
"v_exp_f32 v167, v167\n"
"v_exp_f32 v168, v168\n"
"v_mfma_f32_32x32x16_bf16 v[144:159], a[244:247], v[100:103], v[144:159]\n"
"v_exp_f32 v169, v169\n"
"v_exp_f32 v170, v170\n"
"v_exp_f32 v171, v171\n"
"v_smfmac_f32_32x32x32_bf16 v[192:207], a[8:11], v[160:167], v220\n"
"v_exp_f32 v172, v172\n"
"v_exp_f32 v173, v173\n"
"v_exp_f32 v174, v174\n"
"v_smfmac_f32_16x16x64_bf16 v[212:215], v[216:219], v[160:167], v220\n"
"ds_read_b128 a[4:7], v222 offset:1024\n"
"v_exp_f32 v175, v175\n"
"v_mfma_f32_16x16x32_bf16 v[80:83], v[84:87], v[228:231], v[80:83]\n"
"v_exp_f32 v144, v144\n"
"v_exp_f32 v145, v145\n"
"v_smfmac_f32_32x32x32_bf16 v[192:207], a[12:15], v[168:175], v220\n"
"s_add_u32 m0, s61, 114688\n"
"s_add_u32 s64, s61, 1245184\n"
"v_exp_f32 v146, v146\n"
"v_exp_f32 v147, v147\n"
"v_exp_f32 v148, v148\n"
"v_smfmac_f32_16x16x64_bf16 v[212:215], v[216:219], v[168:175], v220\n"
"ds_read_b128 a[8:11], v222 offset:2048\n"
"v_exp_f32 v149, v149\n"
"v_mfma_f32_32x32x16_bf16 v[160:175], a[16:19], v[104:107], v[128:143]\n"
"v_exp_f32 v150, v150\n"
"v_exp_f32 v151, v151\n"
"v_exp_f32 v152, v152\n"
"v_mfma_f32_32x32x16_bf16 v[160:175], a[20:23], v[108:111], v[160:175]\n"
"s_waitcnt lgkmcnt(1)\n"
"v_exp_f32 v153, v153\n"
"v_exp_f32 v154, v154\n"
"v_exp_f32 v155, v155\n"
"v_smfmac_f32_32x32x32_bf16 v[176:191], a[248:251], v[144:151], v220\n"
"buffer_load_dwordx4 v2, s[48:51], s64 offen sc1 lds\n"
"v_exp_f32 v156, v156\n"
"v_exp_f32 v157, v157\n"
"v_exp_f32 v158, v158\n"
"v_smfmac_f32_16x16x64_bf16 v[208:211], v[216:219], v[144:151], v220\n"
"ds_read_b128 a[12:15], v222 offset:3072\n"
"v_exp_f32 v159, v159\n"
"v_mfma_f32_16x16x32_bf16 v[80:83], v[84:87], v[232:235], v[80:83]\n"
"v_exp_f32 v160, v160\n"
"v_exp_f32 v161, v161\n"
"v_smfmac_f32_32x32x32_bf16 v[176:191], a[252:255], v[152:159], v220\n"
"buffer_load_dwordx4 v2, s[48:51], s64 offen offset:1024 sc1 lds\n"
"v_exp_f32 v162, v162\n"
"v_exp_f32 v163, v163\n"
"v_exp_f32 v164, v164\n"
"v_smfmac_f32_16x16x64_bf16 v[208:211], v[216:219], v[152:159], v220\n"
"ds_read_b128 a[16:19], v222 offset:4096\n"
"v_exp_f32 v165, v165\n"
"v_mfma_f32_32x32x16_bf16 v[144:159], a[0:3], v[96:99], v[112:127]\n"
"v_exp_f32 v166, v166\n"
"v_exp_f32 v167, v167\n"
"v_exp_f32 v168, v168\n"
"v_mfma_f32_32x32x16_bf16 v[144:159], a[4:7], v[100:103], v[144:159]\n"
"v_exp_f32 v169, v169\n"
"v_exp_f32 v170, v170\n"
"v_exp_f32 v171, v171\n"
"v_smfmac_f32_32x32x32_bf16 v[192:207], a[24:27], v[160:167], v220\n"
"buffer_load_dwordx4 v2, s[48:51], s64 offen offset:2048 sc1 lds\n"
"v_exp_f32 v172, v172\n"
"v_exp_f32 v173, v173\n"
"v_exp_f32 v174, v174\n"
"v_smfmac_f32_16x16x64_bf16 v[212:215], v[216:219], v[160:167], v220\n"
"ds_read_b128 a[20:23], v222 offset:5120\n"
"v_exp_f32 v175, v175\n"
"v_mfma_f32_16x16x32_bf16 v[80:83], v[84:87], v[236:239], v[80:83]\n"
"v_exp_f32 v144, v144\n"
"v_exp_f32 v145, v145\n"
"v_smfmac_f32_32x32x32_bf16 v[192:207], a[28:31], v[168:175], v220\n"
"buffer_load_dwordx4 v2, s[48:51], s64 offen offset:3072 sc1 lds\n"
"s_add_u32 m0, s90, 5120\n"
"v_exp_f32 v146, v146\n"
"v_exp_f32 v147, v147\n"
"v_exp_f32 v148, v148\n"
"v_smfmac_f32_16x16x64_bf16 v[212:215], v[216:219], v[168:175], v220\n"
"ds_read_b128 a[24:27], v222 offset:6144\n"
"s_add_u32 s57, s89, 65536\n"
"v_exp_f32 v149, v149\n"
"v_mfma_f32_32x32x16_bf16 v[160:175], a[32:35], v[104:107], v[128:143]\n"
"v_exp_f32 v150, v150\n"
"v_exp_f32 v151, v151\n"
"v_exp_f32 v152, v152\n"
"v_mfma_f32_32x32x16_bf16 v[160:175], a[36:39], v[108:111], v[160:175]\n"
"s_waitcnt lgkmcnt(1)\n"
"v_exp_f32 v153, v153\n"
"v_exp_f32 v154, v154\n"
"v_exp_f32 v155, v155\n"
"v_smfmac_f32_32x32x32_bf16 v[176:191], a[8:11], v[144:151], v220\n"
"buffer_load_dwordx4 v41, s[32:35], s57 offen lds\n"
"s_add_u32 m0, s90, 6144\n"
"v_exp_f32 v156, v156\n"
"v_exp_f32 v157, v157\n"
"v_exp_f32 v158, v158\n"
"v_smfmac_f32_16x16x64_bf16 v[208:211], v[216:219], v[144:151], v220\n"
"ds_read_b128 a[28:31], v222 offset:7168\n"
"s_add_u32 s57, s89, 65536\n"
"v_exp_f32 v159, v159\n"
"v_mfma_f32_16x16x32_bf16 v[80:83], v[84:87], v[240:243], v[80:83]\n"
"v_exp_f32 v160, v160\n"
"v_exp_f32 v161, v161\n"
"v_smfmac_f32_32x32x32_bf16 v[176:191], a[12:15], v[152:159], v220\n"
"buffer_load_dwordx4 v42, s[32:35], s57 offen lds\n"
"v_exp_f32 v162, v162\n"
"v_exp_f32 v163, v163\n"
"v_exp_f32 v164, v164\n"
"v_smfmac_f32_16x16x64_bf16 v[208:211], v[216:219], v[152:159], v220\n"
"ds_read_b128 a[32:35], v222 offset:8192\n"
"v_exp_f32 v165, v165\n"
"v_mfma_f32_32x32x16_bf16 v[144:159], a[16:19], v[96:99], v[112:127]\n"
"v_exp_f32 v166, v166\n"
"v_exp_f32 v167, v167\n"
"v_exp_f32 v168, v168\n"
"v_mfma_f32_32x32x16_bf16 v[144:159], a[20:23], v[100:103], v[144:159]\n"
"v_exp_f32 v169, v169\n"
"v_exp_f32 v170, v170\n"
"v_exp_f32 v171, v171\n"
"v_smfmac_f32_32x32x32_bf16 v[192:207], a[40:43], v[160:167], v220\n"
"v_exp_f32 v172, v172\n"
"v_exp_f32 v173, v173\n"
"v_exp_f32 v174, v174\n"
"v_smfmac_f32_16x16x64_bf16 v[212:215], v[216:219], v[160:167], v220\n"
"ds_read_b128 a[36:39], v222 offset:9216\n"
"v_exp_f32 v175, v175\n"
"v_mfma_f32_16x16x32_bf16 v[80:83], v[84:87], v[244:247], v[80:83]\n"
"v_exp_f32 v144, v144\n"
"v_exp_f32 v145, v145\n"
"v_smfmac_f32_32x32x32_bf16 v[192:207], a[44:47], v[168:175], v220\n"
"s_add_u32 m0, s90, 7168\n"
"s_add_u32 s57, s89, 65536\n"
"v_exp_f32 v146, v146\n"
"v_exp_f32 v147, v147\n"
"v_exp_f32 v148, v148\n"
"v_smfmac_f32_16x16x64_bf16 v[212:215], v[216:219], v[168:175], v220\n"
"ds_read_b128 a[40:43], v222 offset:10240\n"
"v_exp_f32 v149, v149\n"
"v_mfma_f32_32x32x16_bf16 v[160:175], a[48:51], v[104:107], v[128:143]\n"
"v_exp_f32 v150, v150\n"
"v_exp_f32 v151, v151\n"
"v_exp_f32 v152, v152\n"
"v_mfma_f32_32x32x16_bf16 v[160:175], a[52:55], v[108:111], v[160:175]\n"
"s_waitcnt lgkmcnt(1)\n"
"v_exp_f32 v153, v153\n"
"v_exp_f32 v154, v154\n"
"v_exp_f32 v155, v155\n"
"v_smfmac_f32_32x32x32_bf16 v[176:191], a[24:27], v[144:151], v220\n"
"buffer_load_dwordx4 v43, s[32:35], s57 offen lds\n"
"v_exp_f32 v156, v156\n"
"v_exp_f32 v157, v157\n"
"v_exp_f32 v158, v158\n"
"v_smfmac_f32_16x16x64_bf16 v[208:211], v[216:219], v[144:151], v220\n"
"ds_read_b128 a[44:47], v222 offset:11264\n"
"v_exp_f32 v159, v159\n"
"v_mfma_f32_16x16x32_bf16 v[80:83], v[84:87], v[248:251], v[80:83]\n"
"v_exp_f32 v160, v160\n"
"v_exp_f32 v161, v161\n"
"v_smfmac_f32_32x32x32_bf16 v[176:191], a[28:31], v[152:159], v220\n"
"v_exp_f32 v162, v162\n"
"v_exp_f32 v163, v163\n"
"v_exp_f32 v164, v164\n"
"v_smfmac_f32_16x16x64_bf16 v[208:211], v[216:219], v[152:159], v220\n"
"ds_read_b128 a[48:51], v222 offset:12288\n"
"v_exp_f32 v165, v165\n"
"v_mfma_f32_32x32x16_bf16 v[144:159], a[32:35], v[96:99], v[112:127]\n"
"v_exp_f32 v166, v166\n"
"v_exp_f32 v167, v167\n"
"v_exp_f32 v168, v168\n"
"v_mfma_f32_32x32x16_bf16 v[144:159], a[36:39], v[100:103], v[144:159]\n"
"v_exp_f32 v169, v169\n"
"v_exp_f32 v170, v170\n"
"v_exp_f32 v171, v171\n"
"v_smfmac_f32_32x32x32_bf16 v[192:207], a[56:59], v[160:167], v220\n"
"v_exp_f32 v172, v172\n"
"v_exp_f32 v173, v173\n"
"v_exp_f32 v174, v174\n"
"v_smfmac_f32_16x16x64_bf16 v[212:215], v[216:219], v[160:167], v220\n"
"ds_read_b128 a[52:55], v222 offset:13312\n"
"v_exp_f32 v175, v175\n"
"v_mfma_f32_16x16x32_bf16 v[80:83], v[84:87], v[252:255], v[80:83]\n"
"v_exp_f32 v144, v144\n"
"v_exp_f32 v145, v145\n"
"v_smfmac_f32_32x32x32_bf16 v[192:207], a[60:63], v[168:175], v220\n"
"s_nop 0\n"
"s_nop 0\n"
"v_exp_f32 v146, v146\n"
"v_exp_f32 v147, v147\n"
"v_exp_f32 v148, v148\n"
"v_smfmac_f32_16x16x64_bf16 v[212:215], v[216:219], v[168:175], v220\n"
"ds_read_b128 a[56:59], v222 offset:14336\n"
"ds_read_b128 a[60:63], v222 offset:15360\n"
"v_exp_f32 v149, v149\n"
"v_mfma_f32_32x32x16_bf16 v[160:175], a[64:67], v[104:107], v[128:143]\n"
"s_nop 0\n"
"v_exp_f32 v150, v150\n"
"v_exp_f32 v151, v151\n"
"v_exp_f32 v152, v152\n"
"v_mfma_f32_32x32x16_bf16 v[160:175], a[68:71], v[108:111], v[160:175]\n"
"s_waitcnt lgkmcnt(2)\n"
"v_exp_f32 v153, v153\n"
"v_exp_f32 v154, v154\n"
"v_exp_f32 v155, v155\n"
"v_smfmac_f32_32x32x32_bf16 v[176:191], a[40:43], v[144:151], v220\n"
"v_cvt_pk_bf16_f32 v88, v80, v80\n"
"v_and_b32 v89, 0xffff0000, v88\n"
"v_sub_f32 v89, v80, v89\n"
"v_cvt_pk_bf16_f32 v89, v89, v89\n"
"v_exp_f32 v156, v156\n"
"v_exp_f32 v157, v157\n"
"v_exp_f32 v158, v158\n"
"v_smfmac_f32_16x16x64_bf16 v[208:211], v[216:219], v[144:151], v220\n"
"v_exp_f32 v159, v159\n"
"v_exp_f32 v160, v160\n"
"v_exp_f32 v161, v161\n"
"v_smfmac_f32_32x32x32_bf16 v[176:191], a[44:47], v[152:159], v220\n"
"s_waitcnt lgkmcnt(0)\n"
"s_barrier\n"
"v_exp_f32 v162, v162\n"
"v_exp_f32 v163, v163\n"
"v_exp_f32 v164, v164\n"
"v_smfmac_f32_16x16x64_bf16 v[208:211], v[216:219], v[152:159], v220\n"
"ds_read_b128 a[64:67], v222 offset:16384\n"
"v_exp_f32 v165, v165\n"
"v_mfma_f32_32x32x16_bf16 v[144:159], a[48:51], v[96:99], v[112:127]\n"
"v_exp_f32 v166, v166\n"
"v_exp_f32 v167, v167\n"
"v_exp_f32 v168, v168\n"
"v_mfma_f32_32x32x16_bf16 v[144:159], a[52:55], v[100:103], v[144:159]\n"
"s_mov_b64 exec, 0xffff\n"
"buffer_store_dwordx4 v[88:91], v2, s[48:51], s67 offen sc1\n"
"s_mov_b64 exec, -1\n"
"v_exp_f32 v169, v169\n"
"v_exp_f32 v170, v170\n"
"v_exp_f32 v171, v171\n"
"v_smfmac_f32_32x32x32_bf16 v[192:207], a[72:75], v[160:167], v220\n"
"v_exp_f32 v172, v172\n"
"v_exp_f32 v173, v173\n"
"v_exp_f32 v174, v174\n"
"v_smfmac_f32_16x16x64_bf16 v[212:215], v[216:219], v[160:167], v220\n"
"ds_read_b128 a[68:71], v222 offset:17408\n"
"v_exp_f32 v175, v175\n"
"v_exp_f32 v144, v144\n"
"v_exp_f32 v145, v145\n"
"v_smfmac_f32_32x32x32_bf16 v[192:207], a[76:79], v[168:175], v220\n"
"s_add_u32 m0, s61, 131072\n"
"s_add_u32 s64, s61, 1261568\n"
"v_exp_f32 v146, v146\n"
"v_exp_f32 v147, v147\n"
"v_exp_f32 v148, v148\n"
"v_smfmac_f32_16x16x64_bf16 v[212:215], v[216:219], v[168:175], v220\n"
"ds_read_b128 a[72:75], v222 offset:18432\n"
"v_exp_f32 v149, v149\n"
"v_mfma_f32_32x32x16_bf16 v[160:175], a[80:83], v[104:107], v[128:143]\n"
"v_exp_f32 v150, v150\n"
"v_exp_f32 v151, v151\n"
"v_exp_f32 v152, v152\n"
"v_mfma_f32_32x32x16_bf16 v[160:175], a[84:87], v[108:111], v[160:175]\n"
"s_waitcnt lgkmcnt(1)\n"
"v_exp_f32 v153, v153\n"
"v_exp_f32 v154, v154\n"
"v_exp_f32 v155, v155\n"
"v_smfmac_f32_32x32x32_bf16 v[176:191], a[56:59], v[144:151], v220\n"
"buffer_load_dwordx4 v2, s[48:51], s64 offen sc1 lds\n"
"v_exp_f32 v156, v156\n"
"v_exp_f32 v157, v157\n"
"v_exp_f32 v158, v158\n"
"v_smfmac_f32_16x16x64_bf16 v[208:211], v[216:219], v[144:151], v220\n"
"ds_read_b128 a[76:79], v222 offset:19456\n"
"v_exp_f32 v159, v159\n"
"v_exp_f32 v160, v160\n"
"v_exp_f32 v161, v161\n"
"v_smfmac_f32_32x32x32_bf16 v[176:191], a[60:63], v[152:159], v220\n"
"buffer_load_dwordx4 v2, s[48:51], s64 offen offset:1024 sc1 lds\n"
"v_exp_f32 v162, v162\n"
"v_exp_f32 v163, v163\n"
"v_exp_f32 v164, v164\n"
"v_smfmac_f32_16x16x64_bf16 v[208:211], v[216:219], v[152:159], v220\n"
"ds_read_b128 a[80:83], v222 offset:20480\n"
"v_exp_f32 v165, v165\n"
"v_mfma_f32_32x32x16_bf16 v[144:159], a[64:67], v[96:99], v[112:127]\n"
"v_exp_f32 v166, v166\n"
"v_exp_f32 v167, v167\n"
"v_exp_f32 v168, v168\n"
"v_mfma_f32_32x32x16_bf16 v[144:159], a[68:71], v[100:103], v[144:159]\n"
"v_exp_f32 v169, v169\n"
"v_exp_f32 v170, v170\n"
"v_exp_f32 v171, v171\n"
"v_smfmac_f32_32x32x32_bf16 v[192:207], a[88:91], v[160:167], v220\n"
"buffer_load_dwordx4 v2, s[48:51], s64 offen offset:2048 sc1 lds\n"
"v_exp_f32 v172, v172\n"
"v_exp_f32 v173, v173\n"
"v_exp_f32 v174, v174\n"
"v_smfmac_f32_16x16x64_bf16 v[212:215], v[216:219], v[160:167], v220\n"
"ds_read_b128 a[84:87], v222 offset:21504\n"
"v_exp_f32 v175, v175\n"
"v_exp_f32 v144, v144\n"
"v_exp_f32 v145, v145\n"
"v_smfmac_f32_32x32x32_bf16 v[192:207], a[92:95], v[168:175], v220\n"
"buffer_load_dwordx4 v2, s[48:51], s64 offen offset:3072 sc1 lds\n"
"s_add_u32 s57, s66, 0\n"
"v_exp_f32 v146, v146\n"
"v_exp_f32 v147, v147\n"
"v_exp_f32 v148, v148\n"
"v_smfmac_f32_16x16x64_bf16 v[212:215], v[216:219], v[168:175], v220\n"
"ds_read_b128 a[88:91], v222 offset:22528\n"
"v_exp_f32 v149, v149\n"
"v_mfma_f32_32x32x16_bf16 v[160:175], a[96:99], v[104:107], v[128:143]\n"
"v_exp_f32 v150, v150\n"
"v_exp_f32 v151, v151\n"
"v_exp_f32 v152, v152\n"
"v_mfma_f32_32x32x16_bf16 v[160:175], a[100:103], v[108:111], v[160:175]\n"
"s_waitcnt lgkmcnt(1)\n"
"v_exp_f32 v153, v153\n"
"v_exp_f32 v154, v154\n"
"v_exp_f32 v155, v155\n"
"v_smfmac_f32_32x32x32_bf16 v[176:191], a[72:75], v[144:151], v220\n"
"buffer_store_dwordx4 v[224:227], v2, s[48:51], s57 offen sc1\n"
"s_add_u32 s57, s66, 114688\n"
"v_exp_f32 v156, v156\n"
"v_exp_f32 v157, v157\n"
"v_exp_f32 v158, v158\n"
"v_smfmac_f32_16x16x64_bf16 v[208:211], v[216:219], v[144:151], v220\n"
"ds_read_b128 a[92:95], v222 offset:23552\n"
"v_exp_f32 v159, v159\n"
"v_exp_f32 v160, v160\n"
"v_exp_f32 v161, v161\n"
"v_smfmac_f32_32x32x32_bf16 v[176:191], a[76:79], v[152:159], v220\n"
"buffer_store_dwordx4 v[228:231], v2, s[48:51], s57 offen sc1\n"
"v_exp_f32 v162, v162\n"
"v_exp_f32 v163, v163\n"
"v_exp_f32 v164, v164\n"
"v_smfmac_f32_16x16x64_bf16 v[208:211], v[216:219], v[152:159], v220\n"
"ds_read_b128 a[96:99], v222 offset:24576\n"
"v_exp_f32 v165, v165\n"
"v_mfma_f32_32x32x16_bf16 v[144:159], a[80:83], v[96:99], v[112:127]\n"
"v_exp_f32 v166, v166\n"
"v_exp_f32 v167, v167\n"
"v_exp_f32 v168, v168\n"
"v_mfma_f32_32x32x16_bf16 v[144:159], a[84:87], v[100:103], v[144:159]\n"
"v_exp_f32 v169, v169\n"
"v_exp_f32 v170, v170\n"
"v_exp_f32 v171, v171\n"
"v_smfmac_f32_32x32x32_bf16 v[192:207], a[104:107], v[160:167], v220\n"
"v_exp_f32 v172, v172\n"
"v_exp_f32 v173, v173\n"
"v_exp_f32 v174, v174\n"
"v_smfmac_f32_16x16x64_bf16 v[212:215], v[216:219], v[160:167], v220\n"
"ds_read_b128 a[100:103], v222 offset:25600\n"
"v_exp_f32 v175, v175\n"
"v_exp_f32 v144, v144\n"
"v_exp_f32 v145, v145\n"
"v_smfmac_f32_32x32x32_bf16 v[192:207], a[108:111], v[168:175], v220\n"
"s_waitcnt vmcnt(7)\n"
"v_xor_b32 v46, 0, v34\n"
"v_exp_f32 v146, v146\n"
"v_exp_f32 v147, v147\n"
"v_exp_f32 v148, v148\n"
"v_smfmac_f32_16x16x64_bf16 v[212:215], v[216:219], v[168:175], v220\n"
"ds_read_b128 a[104:107], v222 offset:26624\n"
"ds_read_b64 v[48:49], v46\n"
"v_exp_f32 v149, v149\n"
"v_mfma_f32_32x32x16_bf16 v[160:175], a[112:115], v[104:107], v[128:143]\n"
"v_exp_f32 v150, v150\n"
"v_exp_f32 v151, v151\n"
"v_exp_f32 v152, v152\n"
"v_mfma_f32_32x32x16_bf16 v[160:175], a[116:119], v[108:111], v[160:175]\n"
"s_waitcnt lgkmcnt(2)\n"
"v_exp_f32 v153, v153\n"
"v_exp_f32 v154, v154\n"
"v_exp_f32 v155, v155\n"
"v_smfmac_f32_32x32x32_bf16 v[176:191], a[88:91], v[144:151], v220\n"
"ds_read_b64 v[52:53], v46 offset:4096\n"
"v_xor_b32 v46, 32, v34\n"
"v_exp_f32 v156, v156\n"
"v_exp_f32 v157, v157\n"
"v_exp_f32 v158, v158\n"
"v_smfmac_f32_16x16x64_bf16 v[208:211], v[216:219], v[144:151], v220\n"
"ds_read_b128 a[108:111], v222 offset:27648\n"
"ds_read_b64 v[50:51], v46\n"
"v_exp_f32 v159, v159\n"
"v_exp_f32 v160, v160\n"
"v_exp_f32 v161, v161\n"
"v_smfmac_f32_32x32x32_bf16 v[176:191], a[92:95], v[152:159], v220\n"
"ds_read_b64 v[54:55], v46 offset:4096\n"
"v_xor_b32 v46, 64, v34\n"
"v_exp_f32 v162, v162\n"
"v_exp_f32 v163, v163\n"
"v_exp_f32 v164, v164\n"
"v_smfmac_f32_16x16x64_bf16 v[208:211], v[216:219], v[152:159], v220\n"
"ds_read_b128 a[112:115], v222 offset:28672\n"
"ds_read_b64 v[56:57], v46\n"
"v_exp_f32 v165, v165\n"
"v_mfma_f32_32x32x16_bf16 v[144:159], a[96:99], v[96:99], v[112:127]\n"
"v_exp_f32 v166, v166\n"
"v_exp_f32 v167, v167\n"
"v_exp_f32 v168, v168\n"
"v_mfma_f32_32x32x16_bf16 v[144:159], a[100:103], v[100:103], v[144:159]\n"
"ds_read_b64 v[60:61], v46 offset:4096\n"
"v_exp_f32 v169, v169\n"
"v_exp_f32 v170, v170\n"
"v_exp_f32 v171, v171\n"
"v_smfmac_f32_32x32x32_bf16 v[192:207], a[120:123], v[160:167], v220\n"
"v_xor_b32 v46, 96, v34\n"
"ds_read_b64 v[58:59], v46\n"
"v_exp_f32 v172, v172\n"
"v_exp_f32 v173, v173\n"
"v_exp_f32 v174, v174\n"
"v_smfmac_f32_16x16x64_bf16 v[212:215], v[216:219], v[160:167], v220\n"
"ds_read_b128 a[116:119], v222 offset:29696\n"
"ds_read_b64 v[62:63], v46 offset:4096\n"
"v_exp_f32 v175, v175\n"
"v_exp_f32 v144, v144\n"
"v_exp_f32 v145, v145\n"
"v_smfmac_f32_32x32x32_bf16 v[192:207], a[124:127], v[168:175], v220\n"
"s_add_u32 s57, s66, 229376\n"
"v_exp_f32 v146, v146\n"
"v_exp_f32 v147, v147\n"
"v_exp_f32 v148, v148\n"
"v_smfmac_f32_16x16x64_bf16 v[212:215], v[216:219], v[168:175], v220\n"
"ds_read_b128 a[120:123], v222 offset:30720\n"
"ds_read_b128 a[124:127], v222 offset:31744\n"
"v_exp_f32 v149, v149\n"
"v_mfma_f32_32x32x16_bf16 v[160:175], a[128:131], v[104:107], v[128:143]\n"
"v_xor_b32 v46, 128, v34\n"
"v_exp_f32 v150, v150\n"
"v_exp_f32 v151, v151\n"
"v_exp_f32 v152, v152\n"
"v_mfma_f32_32x32x16_bf16 v[160:175], a[132:135], v[108:111], v[160:175]\n"
"s_waitcnt lgkmcnt(3)\n"
"v_exp_f32 v153, v153\n"
"v_exp_f32 v154, v154\n"
"v_exp_f32 v155, v155\n"
"v_smfmac_f32_32x32x32_bf16 v[176:191], a[104:107], v[144:151], v220\n"
"buffer_store_dwordx4 v[232:235], v2, s[48:51], s57 offen sc1\n"
"v_exp_f32 v156, v156\n"
"v_exp_f32 v157, v157\n"
"v_exp_f32 v158, v158\n"
"v_smfmac_f32_16x16x64_bf16 v[208:211], v[216:219], v[144:151], v220\n"
"ds_read_b64 v[64:65], v46\n"
"ds_read_b64 v[68:69], v46 offset:4096\n"
"v_exp_f32 v159, v159\n"
"v_exp_f32 v160, v160\n"
"v_exp_f32 v161, v161\n"
"v_smfmac_f32_32x32x32_bf16 v[176:191], a[108:111], v[152:159], v220\n"
"s_waitcnt lgkmcnt(2)\n"
"s_barrier\n"
"v_exp_f32 v162, v162\n"
"v_exp_f32 v163, v163\n"
"v_exp_f32 v164, v164\n"
"v_smfmac_f32_16x16x64_bf16 v[208:211], v[216:219], v[152:159], v220\n"
"ds_read_b128 a[128:131], v2\n"
"v_exp_f32 v165, v165\n"
"v_mfma_f32_32x32x16_bf16 v[144:159], a[112:115], v[96:99], v[112:127]\n"
"v_xor_b32 v46, 160, v34\n"
"v_exp_f32 v166, v166\n"
"v_exp_f32 v167, v167\n"
"v_exp_f32 v168, v168\n"
"v_mfma_f32_32x32x16_bf16 v[144:159], a[116:119], v[100:103], v[144:159]\n"
"ds_read_b64 v[66:67], v46\n"
"v_exp_f32 v169, v169\n"
"v_exp_f32 v170, v170\n"
"v_exp_f32 v171, v171\n"
"v_smfmac_f32_32x32x32_bf16 v[192:207], a[136:139], v[160:167], v220\n"
"ds_read_b64 v[70:71], v46 offset:4096\n"
"v_xor_b32 v46, 192, v34\n"
"v_exp_f32 v172, v172\n"
"v_exp_f32 v173, v173\n"
"v_exp_f32 v174, v174\n"
"v_smfmac_f32_16x16x64_bf16 v[212:215], v[216:219], v[160:167], v220\n"
"ds_read_b128 a[132:135], v2 offset:1024\n"
"ds_read_b64 v[72:73], v46\n"
"v_exp_f32 v175, v175\n"
"v_exp_f32 v144, v144\n"
"v_exp_f32 v145, v145\n"
"v_smfmac_f32_32x32x32_bf16 v[192:207], a[140:143], v[168:175], v220\n"
"s_add_u32 m0, s61, 147456\n"
"s_add_u32 s64, s61, 1277952\n"
"v_exp_f32 v146, v146\n"
"v_exp_f32 v147, v147\n"
"v_exp_f32 v148, v148\n"
"v_smfmac_f32_16x16x64_bf16 v[212:215], v[216:219], v[168:175], v220\n"
"ds_read_b128 a[136:139], v2 offset:2048\n"
"ds_read_b64 v[76:77], v46 offset:4096\n"
"v_exp_f32 v149, v149\n"
"v_mfma_f32_32x32x16_bf16 v[160:175], a[144:147], v[104:107], v[128:143]\n"
"v_xor_b32 v46, 224, v34\n"
"v_exp_f32 v150, v150\n"
"v_exp_f32 v151, v151\n"
"v_exp_f32 v152, v152\n"
"v_mfma_f32_32x32x16_bf16 v[160:175], a[148:151], v[108:111], v[160:175]\n"
"s_waitcnt lgkmcnt(3)\n"
"v_exp_f32 v153, v153\n"
"v_exp_f32 v154, v154\n"
"v_exp_f32 v155, v155\n"
"v_smfmac_f32_32x32x32_bf16 v[176:191], a[120:123], v[144:151], v220\n"
"buffer_load_dwordx4 v2, s[48:51], s64 offen sc1 lds\n"
"v_exp_f32 v156, v156\n"
"v_exp_f32 v157, v157\n"
"v_exp_f32 v158, v158\n"
"v_smfmac_f32_16x16x64_bf16 v[208:211], v[216:219], v[144:151], v220\n"
"ds_read_b128 a[140:143], v2 offset:3072\n"
"ds_read_b64 v[74:75], v46\n"
"v_exp_f32 v159, v159\n"
"v_exp_f32 v160, v160\n"
"v_exp_f32 v161, v161\n"
"v_smfmac_f32_32x32x32_bf16 v[176:191], a[124:127], v[152:159], v220\n"
"buffer_load_dwordx4 v2, s[48:51], s64 offen offset:1024 sc1 lds\n"
"v_exp_f32 v162, v162\n"
"v_exp_f32 v163, v163\n"
"v_exp_f32 v164, v164\n"
"v_smfmac_f32_16x16x64_bf16 v[208:211], v[216:219], v[152:159], v220\n"
"ds_read_b128 a[144:147], v2 offset:4096\n"
"ds_read_b64 v[78:79], v46 offset:4096\n"
"v_exp_f32 v165, v165\n"
"v_mfma_f32_32x32x16_bf16 v[144:159], a[128:131], v[96:99], v[112:127]\n"
"v_exp_f32 v166, v166\n"
"v_exp_f32 v167, v167\n"
"v_exp_f32 v168, v168\n"
"v_mfma_f32_32x32x16_bf16 v[144:159], a[132:135], v[100:103], v[144:159]\n"
"v_exp_f32 v169, v169\n"
"v_exp_f32 v170, v170\n"
"v_exp_f32 v171, v171\n"
"v_smfmac_f32_32x32x32_bf16 v[192:207], a[152:155], v[160:167], v220\n"
"buffer_load_dwordx4 v2, s[48:51], s64 offen offset:2048 sc1 lds\n"
"v_exp_f32 v172, v172\n"
"v_exp_f32 v173, v173\n"
"v_exp_f32 v174, v174\n"
"v_smfmac_f32_16x16x64_bf16 v[212:215], v[216:219], v[160:167], v220\n"
"ds_read_b128 a[148:151], v2 offset:5120\n"
"v_exp_f32 v175, v175\n"
"v_exp_f32 v144, v144\n"
"v_exp_f32 v145, v145\n"
"v_smfmac_f32_32x32x32_bf16 v[192:207], a[156:159], v[168:175], v220\n"
"buffer_load_dwordx4 v2, s[48:51], s64 offen offset:3072 sc1 lds\n"
"s_add_u32 s57, s66, 344064\n"
"v_exp_f32 v146, v146\n"
"v_exp_f32 v147, v147\n"
"v_exp_f32 v148, v148\n"
"v_smfmac_f32_16x16x64_bf16 v[212:215], v[216:219], v[168:175], v220\n"
"ds_read_b128 a[152:155], v2 offset:6144\n"
"v_exp_f32 v149, v149\n"
"v_mfma_f32_32x32x16_bf16 v[160:175], a[160:163], v[104:107], v[128:143]\n"
"v_exp_f32 v150, v150\n"
"v_exp_f32 v151, v151\n"
"v_exp_f32 v152, v152\n"
"v_mfma_f32_32x32x16_bf16 v[160:175], a[164:167], v[108:111], v[160:175]\n"
"s_waitcnt lgkmcnt(1)\n"
"v_exp_f32 v153, v153\n"
"v_exp_f32 v154, v154\n"
"v_exp_f32 v155, v155\n"
"v_smfmac_f32_32x32x32_bf16 v[176:191], a[136:139], v[144:151], v220\n"
"buffer_store_dwordx4 v[236:239], v2, s[48:51], s57 offen sc1\n"
"s_add_u32 m0, s55, 32768\n"
"v_exp_f32 v156, v156\n"
"v_exp_f32 v157, v157\n"
"v_exp_f32 v158, v158\n"
"v_smfmac_f32_16x16x64_bf16 v[208:211], v[216:219], v[144:151], v220\n"
"ds_read_b128 a[156:159], v2 offset:7168\n"
"s_add_u32 s64, s56, 0\n"
"v_exp_f32 v159, v159\n"
"v_exp_f32 v160, v160\n"
"v_exp_f32 v161, v161\n"
"v_smfmac_f32_32x32x32_bf16 v[176:191], a[140:143], v[152:159], v220\n"
"buffer_load_dwordx4 v11, s[24:27], s64 offen lds\n"
"s_add_u32 m0, s55, 36864\n"
"v_exp_f32 v162, v162\n"
"v_exp_f32 v163, v163\n"
"v_exp_f32 v164, v164\n"
"v_smfmac_f32_16x16x64_bf16 v[208:211], v[216:219], v[152:159], v220\n"
"ds_read_b128 a[160:163], v2 offset:8192\n"
"s_add_u32 s64, s56, 65536\n"
"v_exp_f32 v165, v165\n"
"v_mfma_f32_32x32x16_bf16 v[144:159], a[144:147], v[96:99], v[112:127]\n"
"v_exp_f32 v166, v166\n"
"v_exp_f32 v167, v167\n"
"v_exp_f32 v168, v168\n"
"v_mfma_f32_32x32x16_bf16 v[144:159], a[148:151], v[100:103], v[144:159]\n"
"v_exp_f32 v169, v169\n"
"v_exp_f32 v170, v170\n"
"v_exp_f32 v171, v171\n"
"v_smfmac_f32_32x32x32_bf16 v[192:207], a[168:171], v[160:167], v220\n"
"buffer_load_dwordx4 v11, s[24:27], s64 offen lds\n"
"s_add_u32 s57, s66, 458752\n"
"v_exp_f32 v172, v172\n"
"v_exp_f32 v173, v173\n"
"v_exp_f32 v174, v174\n"
"v_smfmac_f32_16x16x64_bf16 v[212:215], v[216:219], v[160:167], v220\n"
"ds_read_b128 a[164:167], v2 offset:9216\n"
"v_exp_f32 v175, v175\n"
"v_exp_f32 v144, v144\n"
"v_exp_f32 v145, v145\n"
"v_smfmac_f32_32x32x32_bf16 v[192:207], a[172:175], v[168:175], v220\n"
"buffer_store_dwordx4 v[240:243], v2, s[48:51], s57 offen sc1\n"
"s_add_u32 m0, s55, 40960\n"
"v_exp_f32 v146, v146\n"
"v_exp_f32 v147, v147\n"
"v_exp_f32 v148, v148\n"
"v_smfmac_f32_16x16x64_bf16 v[212:215], v[216:219], v[168:175], v220\n"
"ds_read_b128 a[168:171], v2 offset:10240\n"
"s_add_u32 s64, s56, 131072\n"
"v_exp_f32 v149, v149\n"
"v_mfma_f32_32x32x16_bf16 v[160:175], a[176:179], v[104:107], v[128:143]\n"
"v_exp_f32 v150, v150\n"
"v_exp_f32 v151, v151\n"
"v_exp_f32 v152, v152\n"
"v_mfma_f32_32x32x16_bf16 v[160:175], a[180:183], v[108:111], v[160:175]\n"
"s_waitcnt lgkmcnt(1)\n"
"v_exp_f32 v153, v153\n"
"v_exp_f32 v154, v154\n"
"v_exp_f32 v155, v155\n"
"v_smfmac_f32_32x32x32_bf16 v[176:191], a[152:155], v[144:151], v220\n"
"buffer_load_dwordx4 v11, s[24:27], s64 offen lds\n"
"s_add_u32 m0, s55, 45056\n"
"v_exp_f32 v156, v156\n"
"v_exp_f32 v157, v157\n"
"v_exp_f32 v158, v158\n"
"v_smfmac_f32_16x16x64_bf16 v[208:211], v[216:219], v[144:151], v220\n"
"ds_read_b128 a[172:175], v2 offset:11264\n"
"s_add_u32 s64, s56, 196608\n"
"v_exp_f32 v159, v159\n"
"v_exp_f32 v160, v160\n"
"v_exp_f32 v161, v161\n"
"v_smfmac_f32_32x32x32_bf16 v[176:191], a[156:159], v[152:159], v220\n"
"buffer_load_dwordx4 v11, s[24:27], s64 offen lds\n"
"v_exp_f32 v162, v162\n"
"v_exp_f32 v163, v163\n"
"v_exp_f32 v164, v164\n"
"v_smfmac_f32_16x16x64_bf16 v[208:211], v[216:219], v[152:159], v220\n"
"ds_read_b128 a[176:179], v2 offset:12288\n"
"v_exp_f32 v165, v165\n"
"v_mfma_f32_32x32x16_bf16 v[144:159], a[160:163], v[96:99], v[112:127]\n"
"v_exp_f32 v166, v166\n"
"v_exp_f32 v167, v167\n"
"v_exp_f32 v168, v168\n"
"v_mfma_f32_32x32x16_bf16 v[144:159], a[164:167], v[100:103], v[144:159]\n"
"v_exp_f32 v169, v169\n"
"v_exp_f32 v170, v170\n"
"v_exp_f32 v171, v171\n"
"v_smfmac_f32_32x32x32_bf16 v[192:207], a[184:187], v[160:167], v220\n"
"v_exp_f32 v172, v172\n"
"v_exp_f32 v173, v173\n"
"v_exp_f32 v174, v174\n"
"v_smfmac_f32_16x16x64_bf16 v[212:215], v[216:219], v[160:167], v220\n"
"ds_read_b128 a[180:183], v2 offset:13312\n"
"v_exp_f32 v175, v175\n"
"v_exp_f32 v144, v144\n"
"v_exp_f32 v145, v145\n"
"v_smfmac_f32_32x32x32_bf16 v[192:207], a[188:191], v[168:175], v220\n"
"s_add_u32 s57, s66, 573440\n"
"v_exp_f32 v146, v146\n"
"v_exp_f32 v147, v147\n"
"v_exp_f32 v148, v148\n"
"v_smfmac_f32_16x16x64_bf16 v[212:215], v[216:219], v[168:175], v220\n"
"ds_read_b128 a[184:187], v2 offset:14336\n"
"ds_read_b128 a[188:191], v2 offset:15360\n"
"v_exp_f32 v149, v149\n"
"v_mfma_f32_32x32x16_bf16 v[160:175], a[192:195], v[104:107], v[128:143]\n"
"v_exp_f32 v150, v150\n"
"v_exp_f32 v151, v151\n"
"v_exp_f32 v152, v152\n"
"v_mfma_f32_32x32x16_bf16 v[160:175], a[196:199], v[108:111], v[160:175]\n"
"s_waitcnt lgkmcnt(2)\n"
"v_exp_f32 v153, v153\n"
"v_exp_f32 v154, v154\n"
"v_exp_f32 v155, v155\n"
"v_smfmac_f32_32x32x32_bf16 v[176:191], a[168:171], v[144:151], v220\n"
"buffer_store_dwordx4 v[244:247], v2, s[48:51], s57 offen sc1\n"
"s_add_u32 m0, s55, 49152\n"
"v_exp_f32 v156, v156\n"
"v_exp_f32 v157, v157\n"
"v_exp_f32 v158, v158\n"
"v_smfmac_f32_16x16x64_bf16 v[208:211], v[216:219], v[144:151], v220\n"
"LPG6_%=:\n"
"s_cbranch_execz LWRa_%=\n"
"s_add_u32 s64, s56, 262144\n"
"v_exp_f32 v159, v159\n"
"v_exp_f32 v160, v160\n"
"v_exp_f32 v161, v161\n"
"v_smfmac_f32_32x32x32_bf16 v[176:191], a[172:175], v[152:159], v220\n"
"s_waitcnt lgkmcnt(0)\n"
"s_barrier\n"
"v_exp_f32 v162, v162\n"
"v_exp_f32 v163, v163\n"
"v_exp_f32 v164, v164\n"
"v_smfmac_f32_16x16x64_bf16 v[208:211], v[216:219], v[152:159], v220\n"
"ds_read_b128 a[192:195], v2 offset:16384\n"
"v_exp_f32 v165, v165\n"
"v_mfma_f32_32x32x16_bf16 v[144:159], a[176:179], v[96:99], v[112:127]\n"
"v_exp_f32 v166, v166\n"
"v_exp_f32 v167, v167\n"
"v_exp_f32 v168, v168\n"
"v_mfma_f32_32x32x16_bf16 v[144:159], a[180:183], v[100:103], v[144:159]\n"
"v_exp_f32 v169, v169\n"
"v_exp_f32 v170, v170\n"
"v_exp_f32 v171, v171\n"
"v_smfmac_f32_32x32x32_bf16 v[192:207], a[200:203], v[160:167], v220\n"
"buffer_load_dwordx4 v11, s[24:27], s64 offen lds\n"
"s_add_u32 m0, s55, 53248\n"
"v_exp_f32 v172, v172\n"
"v_exp_f32 v173, v173\n"
"v_exp_f32 v174, v174\n"
"v_smfmac_f32_16x16x64_bf16 v[212:215], v[216:219], v[160:167], v220\n"
"ds_read_b128 a[196:199], v2 offset:17408\n"
"s_add_u32 s64, s56, 327680\n"
"v_exp_f32 v175, v175\n"
"v_exp_f32 v144, v144\n"
"v_exp_f32 v145, v145\n"
"v_smfmac_f32_32x32x32_bf16 v[192:207], a[204:207], v[168:175], v220\n"
"buffer_load_dwordx4 v11, s[24:27], s64 offen lds\n"
"s_add_u32 m0, s61, 0\n"
"v_exp_f32 v146, v146\n"
"v_exp_f32 v147, v147\n"
"v_exp_f32 v148, v148\n"
"v_smfmac_f32_16x16x64_bf16 v[212:215], v[216:219], v[168:175], v220\n"
"ds_read_b128 a[200:203], v2 offset:18432\n"
"s_add_u32 s64, s61, 1294336\n"
"v_exp_f32 v149, v149\n"
"v_mfma_f32_32x32x16_bf16 v[160:175], a[208:211], v[104:107], v[128:143]\n"
"v_exp_f32 v150, v150\n"
"v_exp_f32 v151, v151\n"
"v_exp_f32 v152, v152\n"
"v_mfma_f32_32x32x16_bf16 v[160:175], a[212:215], v[108:111], v[160:175]\n"
"s_waitcnt lgkmcnt(1)\n"
"v_exp_f32 v153, v153\n"
"v_exp_f32 v154, v154\n"
"v_exp_f32 v155, v155\n"
"v_smfmac_f32_32x32x32_bf16 v[176:191], a[184:187], v[144:151], v220\n"
"buffer_load_dwordx4 v2, s[48:51], s64 offen sc1 lds\n"
"v_exp_f32 v156, v156\n"
"v_exp_f32 v157, v157\n"
"v_exp_f32 v158, v158\n"
"v_smfmac_f32_16x16x64_bf16 v[208:211], v[216:219], v[144:151], v220\n"
"ds_read_b128 a[204:207], v2 offset:19456\n"
"v_exp_f32 v159, v159\n"
"v_exp_f32 v160, v160\n"
"v_exp_f32 v161, v161\n"
"v_smfmac_f32_32x32x32_bf16 v[176:191], a[188:191], v[152:159], v220\n"
"buffer_load_dwordx4 v2, s[48:51], s64 offen offset:1024 sc1 lds\n"
"v_exp_f32 v162, v162\n"
"v_exp_f32 v163, v163\n"
"v_exp_f32 v164, v164\n"
"v_smfmac_f32_16x16x64_bf16 v[208:211], v[216:219], v[152:159], v220\n"
"ds_read_b128 a[208:211], v2 offset:20480\n"
"v_exp_f32 v165, v165\n"
"v_mfma_f32_32x32x16_bf16 v[144:159], a[192:195], v[96:99], v[112:127]\n"
"v_exp_f32 v166, v166\n"
"v_exp_f32 v167, v167\n"
"v_exp_f32 v168, v168\n"
"v_mfma_f32_32x32x16_bf16 v[144:159], a[196:199], v[100:103], v[144:159]\n"
"v_exp_f32 v169, v169\n"
"v_exp_f32 v170, v170\n"
"v_exp_f32 v171, v171\n"
"v_smfmac_f32_32x32x32_bf16 v[192:207], a[216:219], v[160:167], v220\n"
"buffer_load_dwordx4 v2, s[48:51], s64 offen offset:2048 sc1 lds\n"
"v_exp_f32 v172, v172\n"
"v_exp_f32 v173, v173\n"
"v_exp_f32 v174, v174\n"
"v_smfmac_f32_16x16x64_bf16 v[212:215], v[216:219], v[160:167], v220\n"
"ds_read_b128 a[212:215], v2 offset:21504\n"
"v_exp_f32 v175, v175\n"
"v_exp_f32 v144, v144\n"
"v_exp_f32 v145, v145\n"
"v_smfmac_f32_32x32x32_bf16 v[192:207], a[220:223], v[168:175], v220\n"
"buffer_load_dwordx4 v2, s[48:51], s64 offen offset:3072 sc1 lds\n"
"s_add_u32 s57, s66, 688128\n"
"v_exp_f32 v146, v146\n"
"v_exp_f32 v147, v147\n"
"v_exp_f32 v148, v148\n"
"v_smfmac_f32_16x16x64_bf16 v[212:215], v[216:219], v[168:175], v220\n"
"ds_read_b128 a[216:219], v2 offset:22528\n"
"v_exp_f32 v149, v149\n"
"v_mfma_f32_32x32x16_bf16 v[160:175], a[224:227], v[104:107], v[128:143]\n"
"v_exp_f32 v150, v150\n"
"v_exp_f32 v151, v151\n"
"v_exp_f32 v152, v152\n"
"v_mfma_f32_32x32x16_bf16 v[160:175], a[228:231], v[108:111], v[160:175]\n"
"s_waitcnt lgkmcnt(1)\n"
"v_exp_f32 v153, v153\n"
"v_exp_f32 v154, v154\n"
"v_exp_f32 v155, v155\n"
"v_smfmac_f32_32x32x32_bf16 v[176:191], a[200:203], v[144:151], v220\n"
"buffer_store_dwordx4 v[248:251], v2, s[48:51], s57 offen sc1\n"
"s_add_u32 m0, s55, 57344\n"
"v_exp_f32 v156, v156\n"
"v_exp_f32 v157, v157\n"
"v_exp_f32 v158, v158\n"
"v_smfmac_f32_16x16x64_bf16 v[208:211], v[216:219], v[144:151], v220\n"
"ds_read_b128 a[220:223], v2 offset:23552\n"
"s_add_u32 s64, s56, 393216\n"
"v_exp_f32 v159, v159\n"
"v_exp_f32 v160, v160\n"
"v_exp_f32 v161, v161\n"
"v_smfmac_f32_32x32x32_bf16 v[176:191], a[204:207], v[152:159], v220\n"
"buffer_load_dwordx4 v11, s[24:27], s64 offen lds\n"
"s_add_u32 m0, s55, 61440\n"
"v_exp_f32 v162, v162\n"
"v_exp_f32 v163, v163\n"
"v_exp_f32 v164, v164\n"
"v_smfmac_f32_16x16x64_bf16 v[208:211], v[216:219], v[152:159], v220\n"
"ds_read_b128 a[224:227], v2 offset:24576\n"
"s_add_u32 s64, s56, 458752\n"
"v_exp_f32 v165, v165\n"
"v_mfma_f32_32x32x16_bf16 v[144:159], a[208:211], v[96:99], v[112:127]\n"
"v_exp_f32 v166, v166\n"
"v_exp_f32 v167, v167\n"
"v_exp_f32 v168, v168\n"
"v_mfma_f32_32x32x16_bf16 v[144:159], a[212:215], v[100:103], v[144:159]\n"
"v_exp_f32 v169, v169\n"
"v_exp_f32 v170, v170\n"
"v_exp_f32 v171, v171\n"
"v_smfmac_f32_32x32x32_bf16 v[192:207], a[232:235], v[160:167], v220\n"
"buffer_load_dwordx4 v11, s[24:27], s64 offen lds\n"
"v_exp_f32 v172, v172\n"
"v_exp_f32 v173, v173\n"
"v_exp_f32 v174, v174\n"
"v_smfmac_f32_16x16x64_bf16 v[212:215], v[216:219], v[160:167], v220\n"
"ds_read_b128 a[228:231], v2 offset:25600\n"
"v_exp_f32 v175, v175\n"
"v_exp_f32 v144, v144\n"
"v_exp_f32 v145, v145\n"
"v_smfmac_f32_32x32x32_bf16 v[192:207], a[236:239], v[168:175], v220\n"
"s_add_u32 s57, s66, 802816\n"
"v_exp_f32 v146, v146\n"
"v_exp_f32 v147, v147\n"
"v_exp_f32 v148, v148\n"
"v_smfmac_f32_16x16x64_bf16 v[212:215], v[216:219], v[168:175], v220\n"
"ds_read_b128 a[232:235], v2 offset:26624\n"
"v_exp_f32 v149, v149\n"
"v_mfma_f32_32x32x16_bf16 v[160:175], a[240:243], v[104:107], v[128:143]\n"
"v_exp_f32 v150, v150\n"
"v_exp_f32 v151, v151\n"
"v_exp_f32 v152, v152\n"
"v_mfma_f32_32x32x16_bf16 v[160:175], a[244:247], v[108:111], v[160:175]\n"
"s_waitcnt lgkmcnt(1)\n"
"v_exp_f32 v153, v153\n"
"v_exp_f32 v154, v154\n"
"v_exp_f32 v155, v155\n"
"v_smfmac_f32_32x32x32_bf16 v[176:191], a[216:219], v[144:151], v220\n"
"buffer_store_dwordx4 v[252:255], v2, s[48:51], s57 offen sc1\n"
"s_add_u32 m0, s55, 98304\n"
"v_exp_f32 v156, v156\n"
"v_exp_f32 v157, v157\n"
"v_exp_f32 v158, v158\n"
"v_smfmac_f32_16x16x64_bf16 v[208:211], v[216:219], v[144:151], v220\n"
"ds_read_b128 a[236:239], v2 offset:27648\n"
"s_add_u32 s64, s56, 1048576\n"
"v_exp_f32 v159, v159\n"
"v_exp_f32 v160, v160\n"
"v_exp_f32 v161, v161\n"
"v_smfmac_f32_32x32x32_bf16 v[176:191], a[220:223], v[152:159], v220\n"
"buffer_load_dwordx4 v11, s[24:27], s64 offen lds\n"
"s_add_u32 m0, s55, 102400\n"
"v_exp_f32 v162, v162\n"
"v_exp_f32 v163, v163\n"
"v_exp_f32 v164, v164\n"
"v_smfmac_f32_16x16x64_bf16 v[208:211], v[216:219], v[152:159], v220\n"
"ds_read_b128 a[240:243], v2 offset:28672\n"
"s_add_u32 s64, s56, 1114112\n"
"v_exp_f32 v165, v165\n"
"v_mfma_f32_32x32x16_bf16 v[144:159], a[224:227], v[96:99], v[112:127]\n"
"v_exp_f32 v166, v166\n"
"v_exp_f32 v167, v167\n"
"v_exp_f32 v168, v168\n"
"v_mfma_f32_32x32x16_bf16 v[144:159], a[228:231], v[100:103], v[144:159]\n"
"v_exp_f32 v169, v169\n"
"v_exp_f32 v170, v170\n"
"v_exp_f32 v171, v171\n"
"v_smfmac_f32_32x32x32_bf16 v[192:207], a[248:251], v[160:167], v220\n"
"buffer_load_dwordx4 v11, s[24:27], s64 offen lds\n"
"v_exp_f32 v172, v172\n"
"v_exp_f32 v173, v173\n"
"v_exp_f32 v174, v174\n"
"v_smfmac_f32_16x16x64_bf16 v[212:215], v[216:219], v[160:167], v220\n"
"ds_read_b128 a[244:247], v2 offset:29696\n"
"v_exp_f32 v175, v175\n"
"v_exp_f32 v144, v144\n"
"v_exp_f32 v145, v145\n"
"v_smfmac_f32_32x32x32_bf16 v[192:207], a[252:255], v[168:175], v220\n"
"s_add_u32 s57, s81, 22020096\n"
"v_exp_f32 v146, v146\n"
"v_exp_f32 v147, v147\n"
"v_exp_f32 v148, v148\n"
"v_smfmac_f32_16x16x64_bf16 v[212:215], v[216:219], v[168:175], v220\n"
"ds_read_b128 a[248:251], v2 offset:30720\n"
"ds_read_b128 a[252:255], v2 offset:31744\n"
"v_exp_f32 v149, v149\n"
"v_mfma_f32_32x32x16_bf16 v[160:175], a[0:3], v[104:107], v[128:143]\n"
"v_exp_f32 v150, v150\n"
"v_exp_f32 v151, v151\n"
"v_exp_f32 v152, v152\n"
"v_mfma_f32_32x32x16_bf16 v[160:175], a[4:7], v[108:111], v[160:175]\n"
"s_waitcnt lgkmcnt(2)\n"
"v_exp_f32 v153, v153\n"
"v_exp_f32 v154, v154\n"
"v_exp_f32 v155, v155\n"
"v_smfmac_f32_32x32x32_bf16 v[176:191], a[232:235], v[144:151], v220\n"
"buffer_store_dwordx4 v[48:51], v2, s[48:51], s57 offen sc1\n"
"s_add_u32 m0, s55, 106496\n"
"v_exp_f32 v156, v156\n"
"v_exp_f32 v157, v157\n"
"v_exp_f32 v158, v158\n"
"v_smfmac_f32_16x16x64_bf16 v[208:211], v[216:219], v[144:151], v220\n"
"s_add_u32 s64, s56, 1179648\n"
"v_exp_f32 v159, v159\n"
"v_exp_f32 v160, v160\n"
"v_exp_f32 v161, v161\n"
"v_smfmac_f32_32x32x32_bf16 v[176:191], a[236:239], v[152:159], v220\n"
"s_waitcnt lgkmcnt(0)\n"
"s_barrier\n"
"v_exp_f32 v162, v162\n"
"v_exp_f32 v163, v163\n"
"v_exp_f32 v164, v164\n"
"v_smfmac_f32_16x16x64_bf16 v[208:211], v[216:219], v[152:159], v220\n"
"ds_read_b128 a[0:3], v221 offset:49152\n"
"v_exp_f32 v165, v165\n"
"v_mfma_f32_32x32x16_bf16 v[144:159], a[240:243], v[96:99], v[112:127]\n"
"v_exp_f32 v166, v166\n"
"v_exp_f32 v167, v167\n"
"v_exp_f32 v168, v168\n"
"v_mfma_f32_32x32x16_bf16 v[144:159], a[244:247], v[100:103], v[144:159]\n"
"v_exp_f32 v169, v169\n"
"v_exp_f32 v170, v170\n"
"v_exp_f32 v171, v171\n"
"v_smfmac_f32_32x32x32_bf16 v[192:207], a[8:11], v[160:167], v220\n"
"buffer_load_dwordx4 v11, s[24:27], s64 offen lds\n"
"v_exp_f32 v172, v172\n"
"v_exp_f32 v173, v173\n"
"v_exp_f32 v174, v174\n"
"v_smfmac_f32_16x16x64_bf16 v[212:215], v[216:219], v[160:167], v220\n"
"ds_read_b128 a[4:7], v221 offset:50176\n"
"v_exp_f32 v175, v175\n"
"v_exp_f32 v144, v144\n"
"v_exp_f32 v145, v145\n"
"v_smfmac_f32_32x32x32_bf16 v[192:207], a[12:15], v[168:175], v220\n"
"s_add_u32 s57, s81, 22021120\n"
"v_exp_f32 v146, v146\n"
"v_exp_f32 v147, v147\n"
"v_exp_f32 v148, v148\n"
"v_smfmac_f32_16x16x64_bf16 v[212:215], v[216:219], v[168:175], v220\n"
"ds_read_b128 a[8:11], v221 offset:51200\n"
"v_exp_f32 v149, v149\n"
"v_mfma_f32_32x32x16_bf16 v[160:175], a[16:19], v[104:107], v[128:143]\n"
"v_exp_f32 v150, v150\n"
"v_exp_f32 v151, v151\n"
"v_exp_f32 v152, v152\n"
"v_mfma_f32_32x32x16_bf16 v[160:175], a[20:23], v[108:111], v[160:175]\n"
"s_waitcnt lgkmcnt(1)\n"
"v_exp_f32 v153, v153\n"
"v_exp_f32 v154, v154\n"
"v_exp_f32 v155, v155\n"
"v_smfmac_f32_32x32x32_bf16 v[176:191], a[248:251], v[144:151], v220\n"
"buffer_store_dwordx4 v[52:55], v2, s[48:51], s57 offen sc1\n"
"s_add_u32 m0, s55, 110592\n"
"v_exp_f32 v156, v156\n"
"v_exp_f32 v157, v157\n"
"v_exp_f32 v158, v158\n"
"v_smfmac_f32_16x16x64_bf16 v[208:211], v[216:219], v[144:151], v220\n"
"ds_read_b128 a[12:15], v221 offset:52224\n"
"s_add_u32 s64, s56, 1245184\n"
"v_exp_f32 v159, v159\n"
"v_exp_f32 v160, v160\n"
"v_exp_f32 v161, v161\n"
"v_smfmac_f32_32x32x32_bf16 v[176:191], a[252:255], v[152:159], v220\n"
"buffer_load_dwordx4 v11, s[24:27], s64 offen lds\n"
"v_exp_f32 v162, v162\n"
"v_exp_f32 v163, v163\n"
"v_exp_f32 v164, v164\n"
"v_smfmac_f32_16x16x64_bf16 v[208:211], v[216:219], v[152:159], v220\n"
"ds_read_b128 a[16:19], v221 offset:53248\n"
"v_exp_f32 v165, v165\n"
"v_mfma_f32_32x32x16_bf16 v[144:159], a[0:3], v[96:99], v[112:127]\n"
"v_exp_f32 v166, v166\n"
"v_exp_f32 v167, v167\n"
"v_exp_f32 v168, v168\n"
"v_mfma_f32_32x32x16_bf16 v[144:159], a[4:7], v[100:103], v[144:159]\n"
"v_exp_f32 v169, v169\n"
"v_exp_f32 v170, v170\n"
"v_exp_f32 v171, v171\n"
"v_smfmac_f32_32x32x32_bf16 v[192:207], a[24:27], v[160:167], v220\n"
"v_exp_f32 v172, v172\n"
"v_exp_f32 v173, v173\n"
"v_exp_f32 v174, v174\n"
"v_smfmac_f32_16x16x64_bf16 v[212:215], v[216:219], v[160:167], v220\n"
"ds_read_b128 a[20:23], v221 offset:54272\n"
"v_exp_f32 v175, v175\n"
"v_exp_f32 v144, v144\n"
"v_exp_f32 v145, v145\n"
"v_smfmac_f32_32x32x32_bf16 v[192:207], a[28:31], v[168:175], v220\n"
"s_add_u32 m0, s55, 65536\n"
"s_add_u32 s64, s56, 524288\n"
"v_exp_f32 v146, v146\n"
"v_exp_f32 v147, v147\n"
"v_exp_f32 v148, v148\n"
"v_smfmac_f32_16x16x64_bf16 v[212:215], v[216:219], v[168:175], v220\n"
"ds_read_b128 a[24:27], v221 offset:55296\n"
"v_exp_f32 v149, v149\n"
"v_mfma_f32_32x32x16_bf16 v[160:175], a[32:35], v[104:107], v[128:143]\n"
"v_exp_f32 v150, v150\n"
"v_exp_f32 v151, v151\n"
"v_exp_f32 v152, v152\n"
"v_mfma_f32_32x32x16_bf16 v[160:175], a[36:39], v[108:111], v[160:175]\n"
"s_waitcnt lgkmcnt(1)\n"
"v_exp_f32 v153, v153\n"
"v_exp_f32 v154, v154\n"
"v_exp_f32 v155, v155\n"
"v_smfmac_f32_32x32x32_bf16 v[176:191], a[8:11], v[144:151], v220\n"
"buffer_load_dwordx4 v11, s[24:27], s64 offen lds\n"
"s_add_u32 m0, s55, 69632\n"
"v_exp_f32 v156, v156\n"
"v_exp_f32 v157, v157\n"
"v_exp_f32 v158, v158\n"
"v_smfmac_f32_16x16x64_bf16 v[208:211], v[216:219], v[144:151], v220\n"
"ds_read_b128 a[28:31], v221 offset:56320\n"
"s_add_u32 s64, s56, 589824\n"
"v_exp_f32 v159, v159\n"
"v_exp_f32 v160, v160\n"
"v_exp_f32 v161, v161\n"
"v_smfmac_f32_32x32x32_bf16 v[176:191], a[12:15], v[152:159], v220\n"
"buffer_load_dwordx4 v11, s[24:27], s64 offen lds\n"
"v_exp_f32 v162, v162\n"
"v_exp_f32 v163, v163\n"
"v_exp_f32 v164, v164\n"
"v_smfmac_f32_16x16x64_bf16 v[208:211], v[216:219], v[152:159], v220\n"
"ds_read_b128 a[32:35], v221 offset:57344\n"
"v_exp_f32 v165, v165\n"
"v_mfma_f32_32x32x16_bf16 v[144:159], a[16:19], v[96:99], v[112:127]\n"
"v_exp_f32 v166, v166\n"
"v_exp_f32 v167, v167\n"
"v_exp_f32 v168, v168\n"
"v_mfma_f32_32x32x16_bf16 v[144:159], a[20:23], v[100:103], v[144:159]\n"
"v_exp_f32 v169, v169\n"
"v_exp_f32 v170, v170\n"
"v_exp_f32 v171, v171\n"
"v_smfmac_f32_32x32x32_bf16 v[192:207], a[40:43], v[160:167], v220\n"
"v_exp_f32 v172, v172\n"
"v_exp_f32 v173, v173\n"
"v_exp_f32 v174, v174\n"
"v_smfmac_f32_16x16x64_bf16 v[212:215], v[216:219], v[160:167], v220\n"
"ds_read_b128 a[36:39], v221 offset:58368\n"
"v_exp_f32 v175, v175\n"
"v_exp_f32 v144, v144\n"
"v_exp_f32 v145, v145\n"
"v_smfmac_f32_32x32x32_bf16 v[192:207], a[44:47], v[168:175], v220\n"
"s_add_u32 s57, s81, 22151168\n"
"v_exp_f32 v146, v146\n"
"v_exp_f32 v147, v147\n"
"v_exp_f32 v148, v148\n"
"v_smfmac_f32_16x16x64_bf16 v[212:215], v[216:219], v[168:175], v220\n"
"ds_read_b128 a[40:43], v221 offset:59392\n"
"v_exp_f32 v149, v149\n"
"v_mfma_f32_32x32x16_bf16 v[160:175], a[48:51], v[104:107], v[128:143]\n"
"v_exp_f32 v150, v150\n"
"v_exp_f32 v151, v151\n"
"v_exp_f32 v152, v152\n"
"v_mfma_f32_32x32x16_bf16 v[160:175], a[52:55], v[108:111], v[160:175]\n"
"s_waitcnt lgkmcnt(1)\n"
"v_exp_f32 v153, v153\n"
"v_exp_f32 v154, v154\n"
"v_exp_f32 v155, v155\n"
"v_smfmac_f32_32x32x32_bf16 v[176:191], a[24:27], v[144:151], v220\n"
"buffer_store_dwordx4 v[56:59], v2, s[48:51], s57 offen sc1\n"
"s_add_u32 m0, s55, 73728\n"
"v_exp_f32 v156, v156\n"
"v_exp_f32 v157, v157\n"
"v_exp_f32 v158, v158\n"
"v_smfmac_f32_16x16x64_bf16 v[208:211], v[216:219], v[144:151], v220\n"
"ds_read_b128 a[44:47], v221 offset:60416\n"
"s_add_u32 s64, s56, 655360\n"
"v_exp_f32 v159, v159\n"
"v_exp_f32 v160, v160\n"
"v_exp_f32 v161, v161\n"
"v_smfmac_f32_32x32x32_bf16 v[176:191], a[28:31], v[152:159], v220\n"
"buffer_load_dwordx4 v11, s[24:27], s64 offen lds\n"
"v_exp_f32 v162, v162\n"
"v_exp_f32 v163, v163\n"
"v_exp_f32 v164, v164\n"
"v_smfmac_f32_16x16x64_bf16 v[208:211], v[216:219], v[152:159], v220\n"
"ds_read_b128 a[48:51], v221 offset:61440\n"
"v_exp_f32 v165, v165\n"
"v_mfma_f32_32x32x16_bf16 v[144:159], a[32:35], v[96:99], v[112:127]\n"
"v_exp_f32 v166, v166\n"
"v_exp_f32 v167, v167\n"
"v_exp_f32 v168, v168\n"
"v_mfma_f32_32x32x16_bf16 v[144:159], a[36:39], v[100:103], v[144:159]\n"
"v_exp_f32 v169, v169\n"
"v_exp_f32 v170, v170\n"
"v_exp_f32 v171, v171\n"
"v_smfmac_f32_32x32x32_bf16 v[192:207], a[56:59], v[160:167], v220\n"
"v_exp_f32 v172, v172\n"
"v_exp_f32 v173, v173\n"
"v_exp_f32 v174, v174\n"
"v_smfmac_f32_16x16x64_bf16 v[212:215], v[216:219], v[160:167], v220\n"
"ds_read_b128 a[52:55], v221 offset:62464\n"
"v_exp_f32 v175, v175\n"
"v_exp_f32 v144, v144\n"
"v_exp_f32 v145, v145\n"
"v_smfmac_f32_32x32x32_bf16 v[192:207], a[60:63], v[168:175], v220\n"
"s_add_u32 s57, s81, 22152192\n"
"v_exp_f32 v146, v146\n"
"v_exp_f32 v147, v147\n"
"v_exp_f32 v148, v148\n"
"v_smfmac_f32_16x16x64_bf16 v[212:215], v[216:219], v[168:175], v220\n"
"ds_read_b128 a[56:59], v221 offset:63488\n"
"ds_read_b128 a[60:63], v221 offset:64512\n"
"v_exp_f32 v149, v149\n"
"v_mfma_f32_32x32x16_bf16 v[160:175], a[64:67], v[104:107], v[128:143]\n"
"v_exp_f32 v150, v150\n"
"v_exp_f32 v151, v151\n"
"v_exp_f32 v152, v152\n"
"v_mfma_f32_32x32x16_bf16 v[160:175], a[68:71], v[108:111], v[160:175]\n"
"s_waitcnt lgkmcnt(2)\n"
"v_exp_f32 v153, v153\n"
"v_exp_f32 v154, v154\n"
"v_exp_f32 v155, v155\n"
"v_smfmac_f32_32x32x32_bf16 v[176:191], a[40:43], v[144:151], v220\n"
"buffer_store_dwordx4 v[60:63], v2, s[48:51], s57 offen sc1\n"
"s_add_u32 m0, s55, 77824\n"
"v_exp_f32 v156, v156\n"
"v_exp_f32 v157, v157\n"
"v_exp_f32 v158, v158\n"
"v_smfmac_f32_16x16x64_bf16 v[208:211], v[216:219], v[144:151], v220\n"
"s_add_u32 s64, s56, 720896\n"
"v_exp_f32 v159, v159\n"
"v_exp_f32 v160, v160\n"
"v_exp_f32 v161, v161\n"
"v_smfmac_f32_32x32x32_bf16 v[176:191], a[44:47], v[152:159], v220\n"
"s_waitcnt vmcnt(11) lgkmcnt(0)\n"
"s_barrier\n"
"v_exp_f32 v162, v162\n"
"v_exp_f32 v163, v163\n"
"v_exp_f32 v164, v164\n"
"v_smfmac_f32_16x16x64_bf16 v[208:211], v[216:219], v[152:159], v220\n"
"ds_read_b128 a[64:67], v222\n"
"v_exp_f32 v165, v165\n"
"v_mfma_f32_32x32x16_bf16 v[144:159], a[48:51], v[96:99], v[112:127]\n"
"v_exp_f32 v166, v166\n"
"v_exp_f32 v167, v167\n"
"v_exp_f32 v168, v168\n"
"v_mfma_f32_32x32x16_bf16 v[144:159], a[52:55], v[100:103], v[144:159]\n"
"v_exp_f32 v169, v169\n"
"v_exp_f32 v170, v170\n"
"v_exp_f32 v171, v171\n"
"v_smfmac_f32_32x32x32_bf16 v[192:207], a[72:75], v[160:167], v220\n"
"buffer_load_dwordx4 v11, s[24:27], s64 offen lds\n"
"v_exp_f32 v172, v172\n"
"v_exp_f32 v173, v173\n"
"v_exp_f32 v174, v174\n"
"v_smfmac_f32_16x16x64_bf16 v[212:215], v[216:219], v[160:167], v220\n"
"ds_read_b128 a[68:71], v222 offset:1024\n"
"v_exp_f32 v175, v175\n"
"v_exp_f32 v144, v144\n"
"v_exp_f32 v145, v145\n"
"v_smfmac_f32_32x32x32_bf16 v[192:207], a[76:79], v[168:175], v220\n"
"s_mov_b32 s57, 57671680\n"
"s_cmp_eq_u32 s7, 0\n"
"s_cselect_b32 s57, 1757184, s57\n"
"s_mov_b64 exec, 2\n"
"buffer_atomic_add v1, off, s[48:51], s57 sc1\n"
"s_mov_b64 exec, -1\n"
"v_exp_f32 v146, v146\n"
"v_exp_f32 v147, v147\n"
"v_exp_f32 v148, v148\n"
"v_smfmac_f32_16x16x64_bf16 v[212:215], v[216:219], v[168:175], v220\n"
"ds_read_b128 a[72:75], v222 offset:2048\n"
"s_add_u32 m0, s55, 81920\n"
"v_exp_f32 v149, v149\n"
"v_mfma_f32_32x32x16_bf16 v[160:175], a[80:83], v[104:107], v[128:143]\n"
"s_add_u32 s64, s56, 786432\n"
"v_exp_f32 v150, v150\n"
"v_exp_f32 v151, v151\n"
"v_exp_f32 v152, v152\n"
"v_mfma_f32_32x32x16_bf16 v[160:175], a[84:87], v[108:111], v[160:175]\n"
"s_waitcnt lgkmcnt(1)\n"
"v_exp_f32 v153, v153\n"
"v_exp_f32 v154, v154\n"
"v_exp_f32 v155, v155\n"
"v_smfmac_f32_32x32x32_bf16 v[176:191], a[56:59], v[144:151], v220\n"
"buffer_load_dwordx4 v11, s[24:27], s64 offen lds\n"
"s_add_u32 m0, s55, 86016\n"
"v_exp_f32 v156, v156\n"
"v_exp_f32 v157, v157\n"
"v_exp_f32 v158, v158\n"
"v_smfmac_f32_16x16x64_bf16 v[208:211], v[216:219], v[144:151], v220\n"
"ds_read_b128 a[76:79], v222 offset:3072\n"
"s_add_u32 s64, s56, 851968\n"
"v_exp_f32 v159, v159\n"
"v_exp_f32 v160, v160\n"
"v_exp_f32 v161, v161\n"
"v_smfmac_f32_32x32x32_bf16 v[176:191], a[60:63], v[152:159], v220\n"
"buffer_load_dwordx4 v11, s[24:27], s64 offen lds\n"
"v_exp_f32 v162, v162\n"
"v_exp_f32 v163, v163\n"
"v_exp_f32 v164, v164\n"
"v_smfmac_f32_16x16x64_bf16 v[208:211], v[216:219], v[152:159], v220\n"
"ds_read_b128 a[80:83], v222 offset:4096\n"
"v_exp_f32 v165, v165\n"
"v_mfma_f32_32x32x16_bf16 v[144:159], a[64:67], v[96:99], v[112:127]\n"
"v_exp_f32 v166, v166\n"
"v_exp_f32 v167, v167\n"
"v_exp_f32 v168, v168\n"
"v_mfma_f32_32x32x16_bf16 v[144:159], a[68:71], v[100:103], v[144:159]\n"
"v_exp_f32 v169, v169\n"
"v_exp_f32 v170, v170\n"
"v_exp_f32 v171, v171\n"
"v_smfmac_f32_32x32x32_bf16 v[192:207], a[88:91], v[160:167], v220\n"
"v_exp_f32 v172, v172\n"
"v_exp_f32 v173, v173\n"
"v_exp_f32 v174, v174\n"
"v_smfmac_f32_16x16x64_bf16 v[212:215], v[216:219], v[160:167], v220\n"
"ds_read_b128 a[84:87], v222 offset:5120\n"
"v_exp_f32 v175, v175\n"
"v_exp_f32 v144, v144\n"
"v_exp_f32 v145, v145\n"
"v_smfmac_f32_32x32x32_bf16 v[192:207], a[92:95], v[168:175], v220\n"
"s_add_u32 s57, s81, 22282240\n"
"v_exp_f32 v146, v146\n"
"v_exp_f32 v147, v147\n"
"v_exp_f32 v148, v148\n"
"v_smfmac_f32_16x16x64_bf16 v[212:215], v[216:219], v[168:175], v220\n"
"ds_read_b128 a[88:91], v222 offset:6144\n"
"v_exp_f32 v149, v149\n"
"v_mfma_f32_32x32x16_bf16 v[160:175], a[96:99], v[104:107], v[128:143]\n"
"v_exp_f32 v150, v150\n"
"v_exp_f32 v151, v151\n"
"v_exp_f32 v152, v152\n"
"v_mfma_f32_32x32x16_bf16 v[160:175], a[100:103], v[108:111], v[160:175]\n"
"s_waitcnt lgkmcnt(1)\n"
"v_exp_f32 v153, v153\n"
"v_exp_f32 v154, v154\n"
"v_exp_f32 v155, v155\n"
"v_smfmac_f32_32x32x32_bf16 v[176:191], a[72:75], v[144:151], v220\n"
"buffer_store_dwordx4 v[64:67], v2, s[48:51], s57 offen sc1\n"
"s_add_u32 m0, s55, 90112\n"
"v_exp_f32 v156, v156\n"
"v_exp_f32 v157, v157\n"
"v_exp_f32 v158, v158\n"
"v_smfmac_f32_16x16x64_bf16 v[208:211], v[216:219], v[144:151], v220\n"
"ds_read_b128 a[92:95], v222 offset:7168\n"
"s_add_u32 s64, s56, 917504\n"
"v_exp_f32 v159, v159\n"
"v_exp_f32 v160, v160\n"
"v_exp_f32 v161, v161\n"
"v_smfmac_f32_32x32x32_bf16 v[176:191], a[76:79], v[152:159], v220\n"
"buffer_load_dwordx4 v11, s[24:27], s64 offen lds\n"
"v_exp_f32 v162, v162\n"
"v_exp_f32 v163, v163\n"
"v_exp_f32 v164, v164\n"
"v_smfmac_f32_16x16x64_bf16 v[208:211], v[216:219], v[152:159], v220\n"
"ds_read_b128 a[96:99], v222 offset:8192\n"
"v_exp_f32 v165, v165\n"
"v_mfma_f32_32x32x16_bf16 v[144:159], a[80:83], v[96:99], v[112:127]\n"
"v_exp_f32 v166, v166\n"
"v_exp_f32 v167, v167\n"
"v_exp_f32 v168, v168\n"
"v_mfma_f32_32x32x16_bf16 v[144:159], a[84:87], v[100:103], v[144:159]\n"
"v_exp_f32 v169, v169\n"
"v_exp_f32 v170, v170\n"
"v_exp_f32 v171, v171\n"
"v_smfmac_f32_32x32x32_bf16 v[192:207], a[104:107], v[160:167], v220\n"
"v_exp_f32 v172, v172\n"
"v_exp_f32 v173, v173\n"
"v_exp_f32 v174, v174\n"
"v_smfmac_f32_16x16x64_bf16 v[212:215], v[216:219], v[160:167], v220\n"
"ds_read_b128 a[100:103], v222 offset:9216\n"
"v_exp_f32 v175, v175\n"
"v_exp_f32 v144, v144\n"
"v_exp_f32 v145, v145\n"
"v_smfmac_f32_32x32x32_bf16 v[192:207], a[108:111], v[168:175], v220\n"
"s_add_u32 s57, s81, 22283264\n"
"v_exp_f32 v146, v146\n"
"v_exp_f32 v147, v147\n"
"v_exp_f32 v148, v148\n"
"v_smfmac_f32_16x16x64_bf16 v[212:215], v[216:219], v[168:175], v220\n"
"ds_read_b128 a[104:107], v222 offset:10240\n"
"v_exp_f32 v149, v149\n"
"v_mfma_f32_32x32x16_bf16 v[160:175], a[112:115], v[104:107], v[128:143]\n"
"v_exp_f32 v150, v150\n"
"v_exp_f32 v151, v151\n"
"v_exp_f32 v152, v152\n"
"v_mfma_f32_32x32x16_bf16 v[160:175], a[116:119], v[108:111], v[160:175]\n"
"s_waitcnt lgkmcnt(1)\n"
"v_exp_f32 v153, v153\n"
"v_exp_f32 v154, v154\n"
"v_exp_f32 v155, v155\n"
"v_smfmac_f32_32x32x32_bf16 v[176:191], a[88:91], v[144:151], v220\n"
"buffer_store_dwordx4 v[68:71], v2, s[48:51], s57 offen sc1\n"
"s_add_u32 m0, s55, 94208\n"
"v_exp_f32 v156, v156\n"
"v_exp_f32 v157, v157\n"
"v_exp_f32 v158, v158\n"
"v_smfmac_f32_16x16x64_bf16 v[208:211], v[216:219], v[144:151], v220\n"
"ds_read_b128 a[108:111], v222 offset:11264\n"
"s_add_u32 s64, s56, 983040\n"
"v_exp_f32 v159, v159\n"
"v_exp_f32 v160, v160\n"
"v_exp_f32 v161, v161\n"
"v_smfmac_f32_32x32x32_bf16 v[176:191], a[92:95], v[152:159], v220\n"
"buffer_load_dwordx4 v11, s[24:27], s64 offen lds\n"
"v_exp_f32 v162, v162\n"
"v_exp_f32 v163, v163\n"
"v_exp_f32 v164, v164\n"
"v_smfmac_f32_16x16x64_bf16 v[208:211], v[216:219], v[152:159], v220\n"
"ds_read_b128 a[112:115], v222 offset:12288\n"
"v_exp_f32 v165, v165\n"
"v_mfma_f32_32x32x16_bf16 v[144:159], a[96:99], v[96:99], v[112:127]\n"
"v_exp_f32 v166, v166\n"
"v_exp_f32 v167, v167\n"
"v_exp_f32 v168, v168\n"
"v_mfma_f32_32x32x16_bf16 v[144:159], a[100:103], v[100:103], v[144:159]\n"
"v_exp_f32 v169, v169\n"
"v_exp_f32 v170, v170\n"
"v_exp_f32 v171, v171\n"
"v_smfmac_f32_32x32x32_bf16 v[192:207], a[120:123], v[160:167], v220\n"
"v_exp_f32 v172, v172\n"
"v_exp_f32 v173, v173\n"
"v_exp_f32 v174, v174\n"
"v_smfmac_f32_16x16x64_bf16 v[212:215], v[216:219], v[160:167], v220\n"
"ds_read_b128 a[116:119], v222 offset:13312\n"
"v_exp_f32 v175, v175\n"
"v_exp_f32 v144, v144\n"
"v_exp_f32 v145, v145\n"
"v_smfmac_f32_32x32x32_bf16 v[192:207], a[124:127], v[168:175], v220\n"
"s_add_u32 s57, s81, 22413312\n"
"v_exp_f32 v146, v146\n"
"v_exp_f32 v147, v147\n"
"v_exp_f32 v148, v148\n"
"v_smfmac_f32_16x16x64_bf16 v[212:215], v[216:219], v[168:175], v220\n"
"ds_read_b128 a[120:123], v222 offset:14336\n"
"ds_read_b128 a[124:127], v222 offset:15360\n"
"v_exp_f32 v149, v149\n"
"v_mfma_f32_32x32x16_bf16 v[160:175], a[128:131], v[104:107], v[128:143]\n"
"v_exp_f32 v150, v150\n"
"v_exp_f32 v151, v151\n"
"v_exp_f32 v152, v152\n"
"v_mfma_f32_32x32x16_bf16 v[160:175], a[132:135], v[108:111], v[160:175]\n"
"s_waitcnt lgkmcnt(2)\n"
"v_exp_f32 v153, v153\n"
"v_exp_f32 v154, v154\n"
"v_exp_f32 v155, v155\n"
"v_smfmac_f32_32x32x32_bf16 v[176:191], a[104:107], v[144:151], v220\n"
"buffer_store_dwordx4 v[72:75], v2, s[48:51], s57 offen sc1\n"
"s_add_u32 m0, s55, 114688\n"
"v_exp_f32 v156, v156\n"
"v_exp_f32 v157, v157\n"
"v_exp_f32 v158, v158\n"
"v_smfmac_f32_16x16x64_bf16 v[208:211], v[216:219], v[144:151], v220\n"
"s_add_u32 s64, s56, 1310720\n"
"v_exp_f32 v159, v159\n"
"v_exp_f32 v160, v160\n"
"v_exp_f32 v161, v161\n"
"v_smfmac_f32_32x32x32_bf16 v[176:191], a[108:111], v[152:159], v220\n"
"s_waitcnt lgkmcnt(0)\n"
"s_barrier\n"
"v_exp_f32 v162, v162\n"
"v_exp_f32 v163, v163\n"
"v_exp_f32 v164, v164\n"
"v_smfmac_f32_16x16x64_bf16 v[208:211], v[216:219], v[152:159], v220\n"
"ds_read_b128 a[128:131], v222 offset:16384\n"
"v_exp_f32 v165, v165\n"
"v_mfma_f32_32x32x16_bf16 v[144:159], a[112:115], v[96:99], v[112:127]\n"
"v_exp_f32 v166, v166\n"
"v_exp_f32 v167, v167\n"
"v_exp_f32 v168, v168\n"
"v_mfma_f32_32x32x16_bf16 v[144:159], a[116:119], v[100:103], v[144:159]\n"
"v_exp_f32 v169, v169\n"
"v_exp_f32 v170, v170\n"
"v_exp_f32 v171, v171\n"
"v_smfmac_f32_32x32x32_bf16 v[192:207], a[136:139], v[160:167], v220\n"
"buffer_load_dwordx4 v11, s[24:27], s64 offen lds\n"
"s_add_u32 m0, s55, 118784\n"
"v_exp_f32 v172, v172\n"
"v_exp_f32 v173, v173\n"
"v_exp_f32 v174, v174\n"
"v_smfmac_f32_16x16x64_bf16 v[212:215], v[216:219], v[160:167], v220\n"
"ds_read_b128 a[132:135], v222 offset:17408\n"
"s_add_u32 s64, s56, 1376256\n"
"v_exp_f32 v175, v175\n"
"v_exp_f32 v144, v144\n"
"v_exp_f32 v145, v145\n"
"v_smfmac_f32_32x32x32_bf16 v[192:207], a[140:143], v[168:175], v220\n"
"buffer_load_dwordx4 v11, s[24:27], s64 offen lds\n"
"s_add_u32 m0, s55, 122880\n"
"v_exp_f32 v146, v146\n"
"v_exp_f32 v147, v147\n"
"v_exp_f32 v148, v148\n"
"v_smfmac_f32_16x16x64_bf16 v[212:215], v[216:219], v[168:175], v220\n"
"ds_read_b128 a[136:139], v222 offset:18432\n"
"s_add_u32 s64, s56, 1441792\n"
"v_exp_f32 v149, v149\n"
"v_mfma_f32_32x32x16_bf16 v[160:175], a[144:147], v[104:107], v[128:143]\n"
"v_add_u32 v46, LP3_%=-LPC_%=, v0\n"
"v_exp_f32 v150, v150\n"
"v_exp_f32 v151, v151\n"
"v_exp_f32 v152, v152\n"
"v_mfma_f32_32x32x16_bf16 v[160:175], a[148:151], v[108:111], v[160:175]\n"
"s_waitcnt lgkmcnt(1)\n"
"v_exp_f32 v153, v153\n"
"v_exp_f32 v154, v154\n"
"v_exp_f32 v155, v155\n"
"v_smfmac_f32_32x32x32_bf16 v[176:191], a[120:123], v[144:151], v220\n"
"buffer_load_dwordx4 v11, s[24:27], s64 offen lds\n"
"v_exp_f32 v156, v156\n"
"v_exp_f32 v157, v157\n"
"v_exp_f32 v158, v158\n"
"v_smfmac_f32_16x16x64_bf16 v[208:211], v[216:219], v[144:151], v220\n"
"ds_read_b128 a[140:143], v222 offset:19456\n"
"LPG7_%=:\n"
"s_cbranch_execz LWRa_%=\n"
"v_min_u32 v46, LEND_%=-LPC_%=-4, v46\n"
"v_exp_f32 v159, v159\n"
"v_exp_f32 v160, v160\n"
"v_exp_f32 v161, v161\n"
"v_smfmac_f32_32x32x32_bf16 v[176:191], a[124:127], v[152:159], v220\n"
"global_load_dword v92, v46, s[70:71]\n"
"v_exp_f32 v162, v162\n"
"v_exp_f32 v163, v163\n"
"v_exp_f32 v164, v164\n"
"v_smfmac_f32_16x16x64_bf16 v[208:211], v[216:219], v[152:159], v220\n"
"ds_read_b128 a[144:147], v222 offset:20480\n"
"v_exp_f32 v165, v165\n"
"v_mfma_f32_32x32x16_bf16 v[144:159], a[128:131], v[96:99], v[112:127]\n"
"v_exp_f32 v166, v166\n"
"v_exp_f32 v167, v167\n"
"v_exp_f32 v168, v168\n"
"v_mfma_f32_32x32x16_bf16 v[144:159], a[132:135], v[100:103], v[144:159]\n"
"v_exp_f32 v169, v169\n"
"v_exp_f32 v170, v170\n"
"v_exp_f32 v171, v171\n"
"v_smfmac_f32_32x32x32_bf16 v[192:207], a[152:155], v[160:167], v220\n"
"v_exp_f32 v172, v172\n"
"v_exp_f32 v173, v173\n"
"v_exp_f32 v174, v174\n"
"v_smfmac_f32_16x16x64_bf16 v[212:215], v[216:219], v[160:167], v220\n"
"ds_read_b128 a[148:151], v222 offset:21504\n"
"v_exp_f32 v175, v175\n"
"v_exp_f32 v144, v144\n"
"v_exp_f32 v145, v145\n"
"v_smfmac_f32_32x32x32_bf16 v[192:207], a[156:159], v[168:175], v220\n"
"s_add_u32 s57, s81, 22414336\n"
"v_exp_f32 v146, v146\n"
"v_exp_f32 v147, v147\n"
"v_exp_f32 v148, v148\n"
"v_smfmac_f32_16x16x64_bf16 v[212:215], v[216:219], v[168:175], v220\n"
"ds_read_b128 a[152:155], v222 offset:22528\n"
"v_exp_f32 v149, v149\n"
"v_mfma_f32_32x32x16_bf16 v[160:175], a[160:163], v[104:107], v[128:143]\n"
"v_exp_f32 v150, v150\n"
"v_exp_f32 v151, v151\n"
"v_exp_f32 v152, v152\n"
"v_mfma_f32_32x32x16_bf16 v[160:175], a[164:167], v[108:111], v[160:175]\n"
"s_waitcnt lgkmcnt(1)\n"
"v_exp_f32 v153, v153\n"
"v_exp_f32 v154, v154\n"
"v_exp_f32 v155, v155\n"
"v_smfmac_f32_32x32x32_bf16 v[176:191], a[136:139], v[144:151], v220\n"
"buffer_store_dwordx4 v[76:79], v2, s[48:51], s57 offen sc1\n"
"s_add_u32 m0, s55, 126976\n"
"v_exp_f32 v156, v156\n"
"v_exp_f32 v157, v157\n"
"v_exp_f32 v158, v158\n"
"v_smfmac_f32_16x16x64_bf16 v[208:211], v[216:219], v[144:151], v220\n"
"ds_read_b128 a[156:159], v222 offset:23552\n"
"s_add_u32 s64, s56, 1507328\n"
"v_exp_f32 v159, v159\n"
"v_exp_f32 v160, v160\n"
"v_exp_f32 v161, v161\n"
"v_smfmac_f32_32x32x32_bf16 v[176:191], a[140:143], v[152:159], v220\n"
"buffer_load_dwordx4 v11, s[24:27], s64 offen lds\n"
"v_exp_f32 v162, v162\n"
"v_exp_f32 v163, v163\n"
"v_exp_f32 v164, v164\n"
"v_smfmac_f32_16x16x64_bf16 v[208:211], v[216:219], v[152:159], v220\n"
"ds_read_b128 a[160:163], v222 offset:24576\n"
"v_exp_f32 v165, v165\n"
"v_mfma_f32_32x32x16_bf16 v[144:159], a[144:147], v[96:99], v[112:127]\n"
"v_exp_f32 v166, v166\n"
"v_exp_f32 v167, v167\n"
"v_exp_f32 v168, v168\n"
"v_mfma_f32_32x32x16_bf16 v[144:159], a[148:151], v[100:103], v[144:159]\n"
"v_exp_f32 v169, v169\n"
"v_exp_f32 v170, v170\n"
"v_exp_f32 v171, v171\n"
"v_smfmac_f32_32x32x32_bf16 v[192:207], a[168:171], v[160:167], v220\n"
"v_exp_f32 v172, v172\n"
"v_exp_f32 v173, v173\n"
"v_exp_f32 v174, v174\n"
"v_smfmac_f32_16x16x64_bf16 v[212:215], v[216:219], v[160:167], v220\n"
"ds_read_b128 a[164:167], v222 offset:25600\n"
"v_exp_f32 v175, v175\n"
"v_exp_f32 v144, v144\n"
"v_exp_f32 v145, v145\n"
"v_smfmac_f32_32x32x32_bf16 v[192:207], a[172:175], v[168:175], v220\n"
"buffer_load_dwordx4 v[92:95], v2, s[40:43], s80 offen\n"
"s_add_u32 m0, s55, 131072\n"
"v_exp_f32 v146, v146\n"
"v_exp_f32 v147, v147\n"
"v_exp_f32 v148, v148\n"
"v_smfmac_f32_16x16x64_bf16 v[212:215], v[216:219], v[168:175], v220\n"
"ds_read_b128 a[168:171], v222 offset:26624\n"
"s_add_u32 s64, s56, 1572864\n"
"v_exp_f32 v149, v149\n"
"v_mfma_f32_32x32x16_bf16 v[160:175], a[176:179], v[104:107], v[128:143]\n"
"v_exp_f32 v150, v150\n"
"v_exp_f32 v151, v151\n"
"v_exp_f32 v152, v152\n"
"v_mfma_f32_32x32x16_bf16 v[160:175], a[180:183], v[108:111], v[160:175]\n"
"s_waitcnt lgkmcnt(1)\n"
"v_exp_f32 v153, v153\n"
"v_exp_f32 v154, v154\n"
"v_exp_f32 v155, v155\n"
"v_smfmac_f32_32x32x32_bf16 v[176:191], a[152:155], v[144:151], v220\n"
"buffer_load_dwordx4 v11, s[24:27], s64 offen lds\n"
"s_add_u32 m0, s55, 135168\n"
"v_exp_f32 v156, v156\n"
"v_exp_f32 v157, v157\n"
"v_exp_f32 v158, v158\n"
"v_smfmac_f32_16x16x64_bf16 v[208:211], v[216:219], v[144:151], v220\n"
"ds_read_b128 a[172:175], v222 offset:27648\n"
"s_add_u32 s64, s56, 1638400\n"
"v_exp_f32 v159, v159\n"
"v_exp_f32 v160, v160\n"
"v_exp_f32 v161, v161\n"
"v_smfmac_f32_32x32x32_bf16 v[176:191], a[156:159], v[152:159], v220\n"
"buffer_load_dwordx4 v11, s[24:27], s64 offen lds\n"
"v_exp_f32 v162, v162\n"
"v_exp_f32 v163, v163\n"
"v_exp_f32 v164, v164\n"
"v_smfmac_f32_16x16x64_bf16 v[208:211], v[216:219], v[152:159], v220\n"
"ds_read_b128 a[176:179], v222 offset:28672\n"
"v_exp_f32 v165, v165\n"
"v_mfma_f32_32x32x16_bf16 v[144:159], a[160:163], v[96:99], v[112:127]\n"
"v_exp_f32 v166, v166\n"
"v_exp_f32 v167, v167\n"
"v_exp_f32 v168, v168\n"
"v_mfma_f32_32x32x16_bf16 v[144:159], a[164:167], v[100:103], v[144:159]\n"
"v_exp_f32 v169, v169\n"
"v_exp_f32 v170, v170\n"
"v_exp_f32 v171, v171\n"
"v_smfmac_f32_32x32x32_bf16 v[192:207], a[184:187], v[160:167], v220\n"
"v_exp_f32 v172, v172\n"
"v_exp_f32 v173, v173\n"
"v_exp_f32 v174, v174\n"
"v_smfmac_f32_16x16x64_bf16 v[212:215], v[216:219], v[160:167], v220\n"
"ds_read_b128 a[180:183], v222 offset:29696\n"
"v_exp_f32 v175, v175\n"
"v_exp_f32 v144, v144\n"
"v_exp_f32 v145, v145\n"
"v_smfmac_f32_32x32x32_bf16 v[192:207], a[188:191], v[168:175], v220\n"
"s_add_u32 m0, s55, 139264\n"
"s_add_u32 s64, s56, 1703936\n"
"v_exp_f32 v146, v146\n"
"v_exp_f32 v147, v147\n"
"v_exp_f32 v148, v148\n"
"v_smfmac_f32_16x16x64_bf16 v[212:215], v[216:219], v[168:175], v220\n"
"ds_read_b128 a[184:187], v222 offset:30720\n"
"ds_read_b128 a[188:191], v222 offset:31744\n"
"v_exp_f32 v149, v149\n"
"v_mfma_f32_32x32x16_bf16 v[160:175], a[192:195], v[104:107], v[128:143]\n"
"v_exp_f32 v150, v150\n"
"v_exp_f32 v151, v151\n"
"v_exp_f32 v152, v152\n"
"v_mfma_f32_32x32x16_bf16 v[160:175], a[196:199], v[108:111], v[160:175]\n"
"s_waitcnt lgkmcnt(2)\n"
"v_exp_f32 v153, v153\n"
"v_exp_f32 v154, v154\n"
"v_exp_f32 v155, v155\n"
"v_smfmac_f32_32x32x32_bf16 v[176:191], a[168:171], v[144:151], v220\n"
"buffer_load_dwordx4 v11, s[24:27], s64 offen lds\n"
"v_exp_f32 v156, v156\n"
"v_exp_f32 v157, v157\n"
"v_exp_f32 v158, v158\n"
"v_smfmac_f32_16x16x64_bf16 v[208:211], v[216:219], v[144:151], v220\n"
"v_exp_f32 v159, v159\n"
"v_exp_f32 v160, v160\n"
"v_exp_f32 v161, v161\n"
"v_smfmac_f32_32x32x32_bf16 v[176:191], a[172:175], v[152:159], v220\n"
"s_waitcnt lgkmcnt(0)\n"
"s_barrier\n"
"v_exp_f32 v162, v162\n"
"v_exp_f32 v163, v163\n"
"v_exp_f32 v164, v164\n"
"v_smfmac_f32_16x16x64_bf16 v[208:211], v[216:219], v[152:159], v220\n"
"ds_read_b128 a[192:195], v2\n"
"v_exp_f32 v165, v165\n"
"v_mfma_f32_32x32x16_bf16 v[144:159], a[176:179], v[96:99], v[112:127]\n"
"v_exp_f32 v166, v166\n"
"v_exp_f32 v167, v167\n"
"v_exp_f32 v168, v168\n"
"v_mfma_f32_32x32x16_bf16 v[144:159], a[180:183], v[100:103], v[144:159]\n"
"v_exp_f32 v169, v169\n"
"v_exp_f32 v170, v170\n"
"v_exp_f32 v171, v171\n"
"v_smfmac_f32_32x32x32_bf16 v[192:207], a[200:203], v[160:167], v220\n"
"v_exp_f32 v172, v172\n"
"v_exp_f32 v173, v173\n"
"v_exp_f32 v174, v174\n"
"v_smfmac_f32_16x16x64_bf16 v[212:215], v[216:219], v[160:167], v220\n"
"ds_read_b128 a[196:199], v2 offset:1024\n"
"v_exp_f32 v175, v175\n"
"v_exp_f32 v144, v144\n"
"v_exp_f32 v145, v145\n"
"v_smfmac_f32_32x32x32_bf16 v[192:207], a[204:207], v[168:175], v220\n"
"buffer_load_dwordx4 v[92:95], v2, s[40:43], s80 offen offset:1024\n"
"s_add_u32 m0, s55, 143360\n"
"v_exp_f32 v146, v146\n"
"v_exp_f32 v147, v147\n"
"v_exp_f32 v148, v148\n"
"v_smfmac_f32_16x16x64_bf16 v[212:215], v[216:219], v[168:175], v220\n"
"ds_read_b128 a[200:203], v2 offset:2048\n"
"s_add_u32 s64, s56, 1769472\n"
"v_exp_f32 v149, v149\n"
"v_mfma_f32_32x32x16_bf16 v[160:175], a[208:211], v[104:107], v[128:143]\n"
"v_exp_f32 v150, v150\n"
"v_exp_f32 v151, v151\n"
"v_exp_f32 v152, v152\n"
"v_mfma_f32_32x32x16_bf16 v[160:175], a[212:215], v[108:111], v[160:175]\n"
"s_waitcnt lgkmcnt(1)\n"
"v_exp_f32 v153, v153\n"
"v_exp_f32 v154, v154\n"
"v_exp_f32 v155, v155\n"
"v_smfmac_f32_32x32x32_bf16 v[176:191], a[184:187], v[144:151], v220\n"
"buffer_load_dwordx4 v11, s[24:27], s64 offen lds\n"
"v_exp_f32 v156, v156\n"
"v_exp_f32 v157, v157\n"
"v_exp_f32 v158, v158\n"
"v_smfmac_f32_16x16x64_bf16 v[208:211], v[216:219], v[144:151], v220\n"
"ds_read_b128 a[204:207], v2 offset:3072\n"
"v_exp_f32 v159, v159\n"
"v_exp_f32 v160, v160\n"
"v_exp_f32 v161, v161\n"
"v_smfmac_f32_32x32x32_bf16 v[176:191], a[188:191], v[152:159], v220\n"
"v_exp_f32 v162, v162\n"
"v_exp_f32 v163, v163\n"
"v_exp_f32 v164, v164\n"
"v_smfmac_f32_16x16x64_bf16 v[208:211], v[216:219], v[152:159], v220\n"
"ds_read_b128 a[208:211], v2 offset:4096\n"
"v_exp_f32 v165, v165\n"
"v_mfma_f32_32x32x16_bf16 v[144:159], a[192:195], v[96:99], v[112:127]\n"
"v_exp_f32 v166, v166\n"
"v_exp_f32 v167, v167\n"
"v_exp_f32 v168, v168\n"
"v_mfma_f32_32x32x16_bf16 v[144:159], a[196:199], v[100:103], v[144:159]\n"
"v_exp_f32 v169, v169\n"
"v_exp_f32 v170, v170\n"
"v_exp_f32 v171, v171\n"
"v_smfmac_f32_32x32x32_bf16 v[192:207], a[216:219], v[160:167], v220\n"
"v_exp_f32 v172, v172\n"
"v_exp_f32 v173, v173\n"
"v_exp_f32 v174, v174\n"
"v_smfmac_f32_16x16x64_bf16 v[212:215], v[216:219], v[160:167], v220\n"
"ds_read_b128 a[212:215], v2 offset:5120\n"
"v_exp_f32 v175, v175\n"
"v_exp_f32 v144, v144\n"
"v_exp_f32 v145, v145\n"
"v_smfmac_f32_32x32x32_bf16 v[192:207], a[220:223], v[168:175], v220\n"
"buffer_load_dwordx4 v[92:95], v2, s[40:43], s80 offen offset:2048\n"
"s_add_u32 m0, s55, 147456\n"
"v_exp_f32 v146, v146\n"
"v_exp_f32 v147, v147\n"
"v_exp_f32 v148, v148\n"
"v_smfmac_f32_16x16x64_bf16 v[212:215], v[216:219], v[168:175], v220\n"
"ds_read_b128 a[216:219], v2 offset:6144\n"
"s_add_u32 s64, s56, 1835008\n"
"v_exp_f32 v149, v149\n"
"v_mfma_f32_32x32x16_bf16 v[160:175], a[224:227], v[104:107], v[128:143]\n"
"v_exp_f32 v150, v150\n"
"v_exp_f32 v151, v151\n"
"v_exp_f32 v152, v152\n"
"v_mfma_f32_32x32x16_bf16 v[160:175], a[228:231], v[108:111], v[160:175]\n"
"s_waitcnt lgkmcnt(1)\n"
"v_exp_f32 v153, v153\n"
"v_exp_f32 v154, v154\n"
"v_exp_f32 v155, v155\n"
"v_smfmac_f32_32x32x32_bf16 v[176:191], a[200:203], v[144:151], v220\n"
"buffer_load_dwordx4 v11, s[24:27], s64 offen lds\n"
"s_add_u32 m0, s55, 151552\n"
"v_exp_f32 v156, v156\n"
"v_exp_f32 v157, v157\n"
"v_exp_f32 v158, v158\n"
"v_smfmac_f32_16x16x64_bf16 v[208:211], v[216:219], v[144:151], v220\n"
"ds_read_b128 a[220:223], v2 offset:7168\n"
"s_add_u32 s64, s56, 1900544\n"
"v_exp_f32 v159, v159\n"
"v_exp_f32 v160, v160\n"
"v_exp_f32 v161, v161\n"
"v_smfmac_f32_32x32x32_bf16 v[176:191], a[204:207], v[152:159], v220\n"
"buffer_load_dwordx4 v11, s[24:27], s64 offen lds\n"
"v_exp_f32 v162, v162\n"
"v_exp_f32 v163, v163\n"
"v_exp_f32 v164, v164\n"
"v_smfmac_f32_16x16x64_bf16 v[208:211], v[216:219], v[152:159], v220\n"
"ds_read_b128 a[224:227], v2 offset:8192\n"
"v_exp_f32 v165, v165\n"
"v_mfma_f32_32x32x16_bf16 v[144:159], a[208:211], v[96:99], v[112:127]\n"
"v_exp_f32 v166, v166\n"
"v_exp_f32 v167, v167\n"
"v_exp_f32 v168, v168\n"
"v_mfma_f32_32x32x16_bf16 v[144:159], a[212:215], v[100:103], v[144:159]\n"
"v_exp_f32 v169, v169\n"
"v_exp_f32 v170, v170\n"
"v_exp_f32 v171, v171\n"
"v_smfmac_f32_32x32x32_bf16 v[192:207], a[232:235], v[160:167], v220\n"
"v_exp_f32 v172, v172\n"
"v_exp_f32 v173, v173\n"
"v_exp_f32 v174, v174\n"
"v_smfmac_f32_16x16x64_bf16 v[212:215], v[216:219], v[160:167], v220\n"
"ds_read_b128 a[228:231], v2 offset:9216\n"
"v_exp_f32 v175, v175\n"
"v_exp_f32 v144, v144\n"
"v_exp_f32 v145, v145\n"
"v_smfmac_f32_32x32x32_bf16 v[192:207], a[236:239], v[168:175], v220\n"
"s_mul_i32 s66, s9, 262144\n"
"s_add_u32 s66, s66, s52\n"
"v_exp_f32 v146, v146\n"
"v_exp_f32 v147, v147\n"
"v_exp_f32 v148, v148\n"
"v_smfmac_f32_16x16x64_bf16 v[212:215], v[216:219], v[168:175], v220\n"
"ds_read_b128 a[232:235], v2 offset:10240\n"
"v_exp_f32 v149, v149\n"
"v_mfma_f32_32x32x16_bf16 v[160:175], a[240:243], v[104:107], v[128:143]\n"
"v_bfe_u32 v38, v1, 4, 1\n"
"v_exp_f32 v150, v150\n"
"v_exp_f32 v151, v151\n"
"v_exp_f32 v152, v152\n"
"v_mfma_f32_32x32x16_bf16 v[160:175], a[244:247], v[108:111], v[160:175]\n"
"s_waitcnt lgkmcnt(1)\n"
"v_exp_f32 v153, v153\n"
"v_exp_f32 v154, v154\n"
"v_exp_f32 v155, v155\n"
"v_smfmac_f32_32x32x32_bf16 v[176:191], a[216:219], v[144:151], v220\n"
"v_lshlrev_b32 v38, 10, v38\n"
"v_lshrrev_b32 v39, 5, v1\n"
"v_exp_f32 v156, v156\n"
"v_exp_f32 v157, v157\n"
"v_exp_f32 v158, v158\n"
"v_smfmac_f32_16x16x64_bf16 v[208:211], v[216:219], v[144:151], v220\n"
"ds_read_b128 a[236:239], v2 offset:11264\n"
"v_lshl_add_u32 v38, v39, 8, v38\n"
"v_exp_f32 v159, v159\n"
"v_exp_f32 v160, v160\n"
"v_exp_f32 v161, v161\n"
"v_smfmac_f32_32x32x32_bf16 v[176:191], a[220:223], v[152:159], v220\n"
"v_lshl_add_u32 v38, v3, 4, v38\n"
"buffer_load_dwordx4 v[92:95], v2, s[40:43], s80 offen offset:3072\n"
"v_exp_f32 v162, v162\n"
"v_exp_f32 v163, v163\n"
"v_exp_f32 v164, v164\n"
"v_smfmac_f32_16x16x64_bf16 v[208:211], v[216:219], v[152:159], v220\n"
"ds_read_b128 a[240:243], v2 offset:12288\n"
"s_add_u32 m0, s55, 155648\n"
"v_exp_f32 v165, v165\n"
"v_mfma_f32_32x32x16_bf16 v[144:159], a[224:227], v[96:99], v[112:127]\n"
"s_add_u32 s64, s56, 1966080\n"
"v_exp_f32 v166, v166\n"
"v_exp_f32 v167, v167\n"
"v_exp_f32 v168, v168\n"
"v_mfma_f32_32x32x16_bf16 v[144:159], a[228:231], v[100:103], v[144:159]\n"
"v_exp_f32 v169, v169\n"
"v_exp_f32 v170, v170\n"
"v_exp_f32 v171, v171\n"
"v_smfmac_f32_32x32x32_bf16 v[192:207], a[248:251], v[160:167], v220\n"
"buffer_load_dwordx4 v11, s[24:27], s64 offen lds\n"
"v_exp_f32 v172, v172\n"
"v_exp_f32 v173, v173\n"
"v_exp_f32 v174, v174\n"
"v_smfmac_f32_16x16x64_bf16 v[212:215], v[216:219], v[160:167], v220\n"
"ds_read_b128 a[244:247], v2 offset:13312\n"
"v_exp_f32 v175, v175\n"
"v_exp_f32 v144, v144\n"
"v_exp_f32 v145, v145\n"
"v_smfmac_f32_32x32x32_bf16 v[192:207], a[252:255], v[168:175], v220\n"
"s_add_u32 m0, s55, 159744\n"
"s_add_u32 s64, s56, 2031616\n"
"v_exp_f32 v146, v146\n"
"v_exp_f32 v147, v147\n"
"v_exp_f32 v148, v148\n"
"v_smfmac_f32_16x16x64_bf16 v[212:215], v[216:219], v[168:175], v220\n"
"ds_read_b128 a[248:251], v2 offset:14336\n"
"ds_read_b128 a[252:255], v2 offset:15360\n"
"v_exp_f32 v149, v149\n"
"v_mfma_f32_32x32x16_bf16 v[160:175], a[0:3], v[104:107], v[128:143]\n"
"v_exp_f32 v150, v150\n"
"v_exp_f32 v151, v151\n"
"v_exp_f32 v152, v152\n"
"v_mfma_f32_32x32x16_bf16 v[160:175], a[4:7], v[108:111], v[160:175]\n"
"s_waitcnt lgkmcnt(2)\n"
"v_exp_f32 v153, v153\n"
"v_exp_f32 v154, v154\n"
"v_exp_f32 v155, v155\n"
"v_smfmac_f32_32x32x32_bf16 v[176:191], a[232:235], v[144:151], v220\n"
"buffer_load_dwordx4 v11, s[24:27], s64 offen lds\n"
"v_exp_f32 v156, v156\n"
"v_exp_f32 v157, v157\n"
"v_exp_f32 v158, v158\n"
"v_smfmac_f32_16x16x64_bf16 v[208:211], v[216:219], v[144:151], v220\n"
"v_exp_f32 v159, v159\n"
"v_exp_f32 v160, v160\n"
"v_exp_f32 v161, v161\n"
"v_smfmac_f32_32x32x32_bf16 v[176:191], a[236:239], v[152:159], v220\n"
"v_exp_f32 v162, v162\n"
"v_exp_f32 v163, v163\n"
"v_exp_f32 v164, v164\n"
"v_smfmac_f32_16x16x64_bf16 v[208:211], v[216:219], v[152:159], v220\n"
"v_exp_f32 v165, v165\n"
"v_mfma_f32_32x32x16_bf16 v[144:159], a[240:243], v[96:99], v[112:127]\n"
"v_exp_f32 v166, v166\n"
"v_exp_f32 v167, v167\n"
"v_exp_f32 v168, v168\n"
"v_mfma_f32_32x32x16_bf16 v[144:159], a[244:247], v[100:103], v[144:159]\n"
"v_exp_f32 v169, v169\n"
"v_exp_f32 v170, v170\n"
"v_exp_f32 v171, v171\n"
"v_smfmac_f32_32x32x32_bf16 v[192:207], a[8:11], v[160:167], v220\n"
"v_exp_f32 v172, v172\n"
"v_exp_f32 v173, v173\n"
"v_exp_f32 v174, v174\n"
"v_smfmac_f32_16x16x64_bf16 v[212:215], v[216:219], v[160:167], v220\n"
"v_exp_f32 v175, v175\n"
"v_exp_f32 v144, v144\n"
"v_exp_f32 v145, v145\n"
"v_smfmac_f32_32x32x32_bf16 v[192:207], a[12:15], v[168:175], v220\n"
"v_exp_f32 v146, v146\n"
"v_exp_f32 v147, v147\n"
"v_exp_f32 v148, v148\n"
"v_smfmac_f32_16x16x64_bf16 v[212:215], v[216:219], v[168:175], v220\n"
"v_exp_f32 v149, v149\n"
"v_mfma_f32_32x32x16_bf16 v[160:175], a[16:19], v[104:107], v[128:143]\n"
"v_exp_f32 v150, v150\n"
"v_exp_f32 v151, v151\n"
"v_exp_f32 v152, v152\n"
"v_mfma_f32_32x32x16_bf16 v[160:175], a[20:23], v[108:111], v[160:175]\n"
"s_waitcnt lgkmcnt(0)\n"
"v_exp_f32 v153, v153\n"
"v_exp_f32 v154, v154\n"
"v_exp_f32 v155, v155\n"
"v_smfmac_f32_32x32x32_bf16 v[176:191], a[248:251], v[144:151], v220\n"
"v_exp_f32 v156, v156\n"
"v_exp_f32 v157, v157\n"
"v_exp_f32 v158, v158\n"
"v_smfmac_f32_16x16x64_bf16 v[208:211], v[216:219], v[144:151], v220\n"
"v_exp_f32 v159, v159\n"
"v_exp_f32 v160, v160\n"
"v_exp_f32 v161, v161\n"
"v_smfmac_f32_32x32x32_bf16 v[176:191], a[252:255], v[152:159], v220\n"
"v_exp_f32 v162, v162\n"
"v_exp_f32 v163, v163\n"
"v_exp_f32 v164, v164\n"
"v_smfmac_f32_16x16x64_bf16 v[208:211], v[216:219], v[152:159], v220\n"
"v_exp_f32 v165, v165\n"
"v_mfma_f32_32x32x16_bf16 v[144:159], a[32:35], v[104:107], v[128:143]\n"
"v_exp_f32 v166, v166\n"
"v_exp_f32 v167, v167\n"
"v_exp_f32 v168, v168\n"
"v_mfma_f32_32x32x16_bf16 v[144:159], a[36:39], v[108:111], v[144:159]\n"
"v_exp_f32 v169, v169\n"
"v_exp_f32 v170, v170\n"
"v_exp_f32 v171, v171\n"
"v_smfmac_f32_32x32x32_bf16 v[192:207], a[24:27], v[160:167], v220\n"
"v_exp_f32 v172, v172\n"
"v_exp_f32 v173, v173\n"
"v_exp_f32 v174, v174\n"
"v_smfmac_f32_16x16x64_bf16 v[212:215], v[216:219], v[160:167], v220\n"
"v_exp_f32 v175, v175\n"
"v_exp_f32 v144, v144\n"
"v_exp_f32 v145, v145\n"
"v_smfmac_f32_32x32x32_bf16 v[192:207], a[28:31], v[168:175], v220\n"
"v_rcp_f32 v36, v208\n"
"s_add_u32 s60, s66, 6291456\n"
"v_pk_mul_f32 v[176:177], v[176:177], v[36:37] op_sel_hi:[1,0]\n"
"v_cvt_pk_bf16_f32 v240, v176, v177\n"
"v_pk_mul_f32 v[178:179], v[178:179], v[36:37] op_sel_hi:[1,0]\n"
"v_cvt_pk_bf16_f32 v241, v178, v179\n"
"v_pk_mul_f32 v[180:181], v[180:181], v[36:37] op_sel_hi:[1,0]\n"
"v_cvt_pk_bf16_f32 v242, v180, v181\n"
"v_pk_mul_f32 v[182:183], v[182:183], v[36:37] op_sel_hi:[1,0]\n"
"v_cvt_pk_bf16_f32 v243, v182, v183\n"
"v_exp_f32 v146, v146\n"
"v_exp_f32 v147, v147\n"
"v_exp_f32 v148, v148\n"
"v_smfmac_f32_16x16x64_bf16 v[212:215], v[216:219], v[168:175], v220\n"
"v_exp_f32 v149, v149\n"
"v_mfma_f32_32x32x16_bf16 v[160:175], a[48:51], v[104:107], v[128:143]\n"
"v_exp_f32 v150, v150\n"
"v_exp_f32 v151, v151\n"
"v_exp_f32 v152, v152\n"
"v_mfma_f32_32x32x16_bf16 v[160:175], a[52:55], v[108:111], v[160:175]\n"
"v_exp_f32 v153, v153\n"
"v_exp_f32 v154, v154\n"
"v_exp_f32 v155, v155\n"
"v_smfmac_f32_32x32x32_bf16 v[192:207], a[40:43], v[144:151], v220\n"
"buffer_store_dwordx4 v[240:243], v38, s[48:51], s60 offen sc1\n"
"v_exp_f32 v156, v156\n"
"v_exp_f32 v157, v157\n"
"v_exp_f32 v158, v158\n"
"v_smfmac_f32_16x16x64_bf16 v[212:215], v[216:219], v[144:151], v220\n"
"v_exp_f32 v159, v159\n"
"v_exp_f32 v160, v160\n"
"v_exp_f32 v161, v161\n"
"v_smfmac_f32_32x32x32_bf16 v[192:207], a[44:47], v[152:159], v220\n"
"v_pk_mul_f32 v[184:185], v[184:185], v[36:37] op_sel_hi:[1,0]\n"
"v_cvt_pk_bf16_f32 v244, v184, v185\n"
"v_pk_mul_f32 v[186:187], v[186:187], v[36:37] op_sel_hi:[1,0]\n"
"v_cvt_pk_bf16_f32 v245, v186, v187\n"
"v_pk_mul_f32 v[188:189], v[188:189], v[36:37] op_sel_hi:[1,0]\n"
"v_cvt_pk_bf16_f32 v246, v188, v189\n"
"v_pk_mul_f32 v[190:191], v[190:191], v[36:37] op_sel_hi:[1,0]\n"
"v_cvt_pk_bf16_f32 v247, v190, v191\n"
"v_exp_f32 v162, v162\n"
"v_exp_f32 v163, v163\n"
"v_exp_f32 v164, v164\n"
"v_smfmac_f32_16x16x64_bf16 v[212:215], v[216:219], v[152:159], v220\n"
"v_exp_f32 v165, v165\n"
"v_mfma_f32_32x32x16_bf16 v[144:159], a[64:67], v[104:107], v[128:143]\n"
"v_exp_f32 v166, v166\n"
"v_exp_f32 v167, v167\n"
"v_exp_f32 v168, v168\n"
"v_mfma_f32_32x32x16_bf16 v[144:159], a[68:71], v[108:111], v[144:159]\n"
"v_exp_f32 v169, v169\n"
"v_exp_f32 v170, v170\n"
"v_exp_f32 v171, v171\n"
"v_smfmac_f32_32x32x32_bf16 v[192:207], a[56:59], v[160:167], v220\n"
"buffer_store_dwordx4 v[244:247], v38, s[48:51], s60 offen offset:512 sc1\n"
"v_exp_f32 v172, v172\n"
"v_exp_f32 v173, v173\n"
"v_exp_f32 v174, v174\n"
"v_smfmac_f32_16x16x64_bf16 v[212:215], v[216:219], v[160:167], v220\n"
"v_exp_f32 v175, v175\n"
"v_exp_f32 v144, v144\n"
"v_exp_f32 v145, v145\n"
"v_smfmac_f32_32x32x32_bf16 v[192:207], a[60:63], v[168:175], v220\n"
"v_exp_f32 v146, v146\n"
"v_exp_f32 v147, v147\n"
"v_exp_f32 v148, v148\n"
"v_smfmac_f32_16x16x64_bf16 v[212:215], v[216:219], v[168:175], v220\n"
"v_exp_f32 v149, v149\n"
"v_mfma_f32_32x32x16_bf16 v[160:175], a[80:83], v[104:107], v[128:143]\n"
"v_exp_f32 v150, v150\n"
"v_exp_f32 v151, v151\n"
"v_exp_f32 v152, v152\n"
"v_mfma_f32_32x32x16_bf16 v[160:175], a[84:87], v[108:111], v[160:175]\n"
"v_exp_f32 v153, v153\n"
"v_exp_f32 v154, v154\n"
"v_exp_f32 v155, v155\n"
"v_smfmac_f32_32x32x32_bf16 v[192:207], a[72:75], v[144:151], v220\n"
"v_exp_f32 v156, v156\n"
"v_exp_f32 v157, v157\n"
"v_exp_f32 v158, v158\n"
"v_smfmac_f32_16x16x64_bf16 v[212:215], v[216:219], v[144:151], v220\n"
"v_exp_f32 v159, v159\n"
"v_exp_f32 v160, v160\n"
"v_exp_f32 v161, v161\n"
"v_smfmac_f32_32x32x32_bf16 v[192:207], a[76:79], v[152:159], v220\n"
"v_exp_f32 v162, v162\n"
"v_exp_f32 v163, v163\n"
"v_exp_f32 v164, v164\n"
"v_smfmac_f32_16x16x64_bf16 v[212:215], v[216:219], v[152:159], v220\n"
"v_exp_f32 v165, v165\n"
"v_mfma_f32_32x32x16_bf16 v[144:159], a[96:99], v[104:107], v[128:143]\n"
"v_exp_f32 v166, v166\n"
"v_exp_f32 v167, v167\n"
"v_exp_f32 v168, v168\n"
"v_mfma_f32_32x32x16_bf16 v[144:159], a[100:103], v[108:111], v[144:159]\n"
"v_exp_f32 v169, v169\n"
"v_exp_f32 v170, v170\n"
"v_exp_f32 v171, v171\n"
"v_smfmac_f32_32x32x32_bf16 v[192:207], a[88:91], v[160:167], v220\n"
"v_exp_f32 v172, v172\n"
"v_exp_f32 v173, v173\n"
"v_exp_f32 v174, v174\n"
"v_smfmac_f32_16x16x64_bf16 v[212:215], v[216:219], v[160:167], v220\n"
"v_exp_f32 v175, v175\n"
"v_exp_f32 v144, v144\n"
"v_exp_f32 v145, v145\n"
"v_smfmac_f32_32x32x32_bf16 v[192:207], a[92:95], v[168:175], v220\n"
"v_exp_f32 v146, v146\n"
"v_exp_f32 v147, v147\n"
"v_exp_f32 v148, v148\n"
"v_smfmac_f32_16x16x64_bf16 v[212:215], v[216:219], v[168:175], v220\n"
"v_exp_f32 v149, v149\n"
"v_mfma_f32_32x32x16_bf16 v[160:175], a[112:115], v[104:107], v[128:143]\n"
"v_exp_f32 v150, v150\n"
"v_exp_f32 v151, v151\n"
"v_exp_f32 v152, v152\n"
"v_mfma_f32_32x32x16_bf16 v[160:175], a[116:119], v[108:111], v[160:175]\n"
"v_exp_f32 v153, v153\n"
"v_exp_f32 v154, v154\n"
"v_exp_f32 v155, v155\n"
"v_smfmac_f32_32x32x32_bf16 v[192:207], a[104:107], v[144:151], v220\n"
"v_exp_f32 v156, v156\n"
"v_exp_f32 v157, v157\n"
"v_exp_f32 v158, v158\n"
"v_smfmac_f32_16x16x64_bf16 v[212:215], v[216:219], v[144:151], v220\n"
"v_exp_f32 v159, v159\n"
"v_exp_f32 v160, v160\n"
"v_exp_f32 v161, v161\n"
"v_smfmac_f32_32x32x32_bf16 v[192:207], a[108:111], v[152:159], v220\n"
"s_waitcnt vmcnt(0)\n"
"s_add_u32 s62, s0, 1572864\n"
"s_mov_b64 exec, 2\n"
"buffer_atomic_add v1, off, s[48:51], s62 sc1\n"
"s_mov_b64 exec, -1\n"
"v_exp_f32 v162, v162\n"
"v_exp_f32 v163, v163\n"
"v_exp_f32 v164, v164\n"
"v_smfmac_f32_16x16x64_bf16 v[212:215], v[216:219], v[152:159], v220\n"
"v_exp_f32 v165, v165\n"
"v_mfma_f32_32x32x16_bf16 v[144:159], a[128:131], v[104:107], v[128:143]\n"
"v_exp_f32 v166, v166\n"
"v_exp_f32 v167, v167\n"
"v_exp_f32 v168, v168\n"
"v_mfma_f32_32x32x16_bf16 v[144:159], a[132:135], v[108:111], v[144:159]\n"
"v_exp_f32 v169, v169\n"
"v_exp_f32 v170, v170\n"
"v_exp_f32 v171, v171\n"
"v_smfmac_f32_32x32x32_bf16 v[192:207], a[120:123], v[160:167], v220\n"
"v_exp_f32 v172, v172\n"
"v_exp_f32 v173, v173\n"
"v_exp_f32 v174, v174\n"
"v_smfmac_f32_16x16x64_bf16 v[212:215], v[216:219], v[160:167], v220\n"
"v_exp_f32 v175, v175\n"
"v_exp_f32 v144, v144\n"
"v_exp_f32 v145, v145\n"
"v_smfmac_f32_32x32x32_bf16 v[192:207], a[124:127], v[168:175], v220\n"
"v_exp_f32 v146, v146\n"
"v_exp_f32 v147, v147\n"
"v_exp_f32 v148, v148\n"
"v_smfmac_f32_16x16x64_bf16 v[212:215], v[216:219], v[168:175], v220\n"
"v_exp_f32 v149, v149\n"
"v_mfma_f32_32x32x16_bf16 v[160:175], a[144:147], v[104:107], v[128:143]\n"
"v_exp_f32 v150, v150\n"
"v_exp_f32 v151, v151\n"
"v_exp_f32 v152, v152\n"
"v_mfma_f32_32x32x16_bf16 v[160:175], a[148:151], v[108:111], v[160:175]\n"
"v_exp_f32 v153, v153\n"
"v_exp_f32 v154, v154\n"
"v_exp_f32 v155, v155\n"
"v_smfmac_f32_32x32x32_bf16 v[192:207], a[136:139], v[144:151], v220\n"
"v_exp_f32 v156, v156\n"
"v_exp_f32 v157, v157\n"
"v_exp_f32 v158, v158\n"
"v_smfmac_f32_16x16x64_bf16 v[212:215], v[216:219], v[144:151], v220\n"
"v_exp_f32 v159, v159\n"
"v_exp_f32 v160, v160\n"
"v_exp_f32 v161, v161\n"
"v_smfmac_f32_32x32x32_bf16 v[192:207], a[140:143], v[152:159], v220\n"
"s_barrier\n"
"v_exp_f32 v162, v162\n"
"v_exp_f32 v163, v163\n"
"v_exp_f32 v164, v164\n"
"v_smfmac_f32_16x16x64_bf16 v[212:215], v[216:219], v[152:159], v220\n"
"v_exp_f32 v165, v165\n"
"v_mfma_f32_32x32x16_bf16 v[144:159], a[160:163], v[104:107], v[128:143]\n"
"v_exp_f32 v166, v166\n"
"v_exp_f32 v167, v167\n"
"v_exp_f32 v168, v168\n"
"v_mfma_f32_32x32x16_bf16 v[144:159], a[164:167], v[108:111], v[144:159]\n"
"v_exp_f32 v169, v169\n"
"v_exp_f32 v170, v170\n"
"v_exp_f32 v171, v171\n"
"v_smfmac_f32_32x32x32_bf16 v[192:207], a[152:155], v[160:167], v220\n"
"v_exp_f32 v172, v172\n"
"v_exp_f32 v173, v173\n"
"v_exp_f32 v174, v174\n"
"v_smfmac_f32_16x16x64_bf16 v[212:215], v[216:219], v[160:167], v220\n"
"v_exp_f32 v175, v175\n"
"v_exp_f32 v144, v144\n"
"v_exp_f32 v145, v145\n"
"v_smfmac_f32_32x32x32_bf16 v[192:207], a[156:159], v[168:175], v220\n"
"v_exp_f32 v146, v146\n"
"v_exp_f32 v147, v147\n"
"v_exp_f32 v148, v148\n"
"v_smfmac_f32_16x16x64_bf16 v[212:215], v[216:219], v[168:175], v220\n"
"v_exp_f32 v149, v149\n"
"v_mfma_f32_32x32x16_bf16 v[160:175], a[176:179], v[104:107], v[128:143]\n"
"v_exp_f32 v150, v150\n"
"v_exp_f32 v151, v151\n"
"v_exp_f32 v152, v152\n"
"v_mfma_f32_32x32x16_bf16 v[160:175], a[180:183], v[108:111], v[160:175]\n"
"v_exp_f32 v153, v153\n"
"v_exp_f32 v154, v154\n"
"v_exp_f32 v155, v155\n"
"v_smfmac_f32_32x32x32_bf16 v[192:207], a[168:171], v[144:151], v220\n"
"v_exp_f32 v156, v156\n"
"v_exp_f32 v157, v157\n"
"v_exp_f32 v158, v158\n"
"v_smfmac_f32_16x16x64_bf16 v[212:215], v[216:219], v[144:151], v220\n"
"v_exp_f32 v159, v159\n"
"v_exp_f32 v160, v160\n"
"v_exp_f32 v161, v161\n"
"v_smfmac_f32_32x32x32_bf16 v[192:207], a[172:175], v[152:159], v220\n"
"s_add_u32 s62, s0, 1572864\n"
"v_mov_b32 v236, 0\n"
"s_mov_b64 exec, 1\n"
"buffer_atomic_or v236, off, s[48:51], s62 sc0 sc1\n"
"s_mov_b64 exec, -1\n"
"v_exp_f32 v162, v162\n"
"v_exp_f32 v163, v163\n"
"v_exp_f32 v164, v164\n"
"v_smfmac_f32_16x16x64_bf16 v[212:215], v[216:219], v[152:159], v220\n"
"v_exp_f32 v165, v165\n"
"v_mfma_f32_32x32x16_bf16 v[144:159], a[192:195], v[104:107], v[128:143]\n"
"v_exp_f32 v166, v166\n"
"v_exp_f32 v167, v167\n"
"v_exp_f32 v168, v168\n"
"v_mfma_f32_32x32x16_bf16 v[144:159], a[196:199], v[108:111], v[144:159]\n"
"v_exp_f32 v169, v169\n"
"v_exp_f32 v170, v170\n"
"v_exp_f32 v171, v171\n"
"v_smfmac_f32_32x32x32_bf16 v[192:207], a[184:187], v[160:167], v220\n"
"v_exp_f32 v172, v172\n"
"v_exp_f32 v173, v173\n"
"v_exp_f32 v174, v174\n"
"v_smfmac_f32_16x16x64_bf16 v[212:215], v[216:219], v[160:167], v220\n"
"v_exp_f32 v175, v175\n"
"v_exp_f32 v144, v144\n"
"v_exp_f32 v145, v145\n"
"v_smfmac_f32_32x32x32_bf16 v[192:207], a[188:191], v[168:175], v220\n"
"v_exp_f32 v146, v146\n"
"v_exp_f32 v147, v147\n"
"v_exp_f32 v148, v148\n"
"v_smfmac_f32_16x16x64_bf16 v[212:215], v[216:219], v[168:175], v220\n"
"v_exp_f32 v149, v149\n"
"v_mfma_f32_32x32x16_bf16 v[160:175], a[208:211], v[104:107], v[128:143]\n"
"v_exp_f32 v150, v150\n"
"v_exp_f32 v151, v151\n"
"v_exp_f32 v152, v152\n"
"v_mfma_f32_32x32x16_bf16 v[160:175], a[212:215], v[108:111], v[160:175]\n"
"v_exp_f32 v153, v153\n"
"v_exp_f32 v154, v154\n"
"v_exp_f32 v155, v155\n"
"v_smfmac_f32_32x32x32_bf16 v[192:207], a[200:203], v[144:151], v220\n"
"v_exp_f32 v156, v156\n"
"v_exp_f32 v157, v157\n"
"v_exp_f32 v158, v158\n"
"v_smfmac_f32_16x16x64_bf16 v[212:215], v[216:219], v[144:151], v220\n"
"v_exp_f32 v159, v159\n"
"v_exp_f32 v160, v160\n"
"v_exp_f32 v161, v161\n"
"v_smfmac_f32_32x32x32_bf16 v[192:207], a[204:207], v[152:159], v220\n"
"v_exp_f32 v162, v162\n"
"v_exp_f32 v163, v163\n"
"v_exp_f32 v164, v164\n"
"v_smfmac_f32_16x16x64_bf16 v[212:215], v[216:219], v[152:159], v220\n"
"v_exp_f32 v165, v165\n"
"v_mfma_f32_32x32x16_bf16 v[144:159], a[224:227], v[104:107], v[128:143]\n"
"v_exp_f32 v166, v166\n"
"v_exp_f32 v167, v167\n"
"v_exp_f32 v168, v168\n"
"v_mfma_f32_32x32x16_bf16 v[144:159], a[228:231], v[108:111], v[144:159]\n"
"v_exp_f32 v169, v169\n"
"v_exp_f32 v170, v170\n"
"v_exp_f32 v171, v171\n"
"v_smfmac_f32_32x32x32_bf16 v[192:207], a[216:219], v[160:167], v220\n"
"v_exp_f32 v172, v172\n"
"v_exp_f32 v173, v173\n"
"v_exp_f32 v174, v174\n"
"v_smfmac_f32_16x16x64_bf16 v[212:215], v[216:219], v[160:167], v220\n"
"v_exp_f32 v175, v175\n"
"v_exp_f32 v144, v144\n"
"v_exp_f32 v145, v145\n"
"v_smfmac_f32_32x32x32_bf16 v[192:207], a[220:223], v[168:175], v220\n"
"v_exp_f32 v146, v146\n"
"v_exp_f32 v147, v147\n"
"v_exp_f32 v148, v148\n"
"v_smfmac_f32_16x16x64_bf16 v[212:215], v[216:219], v[168:175], v220\n"
"LPG8_%=:\n"
"s_cbranch_execz LWRa_%=\n"
"v_exp_f32 v149, v149\n"
"v_mfma_f32_32x32x16_bf16 v[160:175], a[240:243], v[104:107], v[128:143]\n"
"v_exp_f32 v150, v150\n"
"v_exp_f32 v151, v151\n"
"v_exp_f32 v152, v152\n"
"v_mfma_f32_32x32x16_bf16 v[160:175], a[244:247], v[108:111], v[160:175]\n"
"v_exp_f32 v153, v153\n"
"v_exp_f32 v154, v154\n"
"v_exp_f32 v155, v155\n"
"v_smfmac_f32_32x32x32_bf16 v[192:207], a[232:235], v[144:151], v220\n"
"v_exp_f32 v156, v156\n"
"v_exp_f32 v157, v157\n"
"v_exp_f32 v158, v158\n"
"v_smfmac_f32_16x16x64_bf16 v[212:215], v[216:219], v[144:151], v220\n"
"v_exp_f32 v159, v159\n"
"v_exp_f32 v160, v160\n"
"v_exp_f32 v161, v161\n"
"v_smfmac_f32_32x32x32_bf16 v[192:207], a[236:239], v[152:159], v220\n"
"s_mov_b64 exec, 1\n"
"s_waitcnt vmcnt(0)\n"
"v_cmp_ne_u32 vcc, 16, v236\n"
"s_mov_b64 exec, -1\n"
"s_nop 3\n"
"s_cbranch_vccz Ld10_%=\n"
"s_mov_b32 s65, 0\n"
"Lp9_%=:\n"
"s_add_u32 s62, s0, 1572864\n"
"v_mov_b32 v236, 0\n"
"s_mov_b64 exec, 1\n"
"buffer_atomic_or v236, off, s[48:51], s62 sc0 sc1\n"
"s_mov_b64 exec, -1\n"
"s_add_u32 s65, s65, 1\n"
"s_cmp_gt_u32 s65, 131072\n"
"s_waitcnt vmcnt(0)\n"
"s_cbranch_scc1 Ld10_%=\n"
"s_mov_b64 exec, 1\n"
"v_cmp_ne_u32 vcc, 16, v236\n"
"s_mov_b64 exec, -1\n"
"s_nop 3\n"
"s_cbranch_vccnz Lp9_%=\n"
"Ld10_%=:\n"
"v_exp_f32 v162, v162\n"
"v_exp_f32 v163, v163\n"
"v_exp_f32 v164, v164\n"
"v_smfmac_f32_16x16x64_bf16 v[212:215], v[216:219], v[152:159], v220\n"
"v_exp_f32 v165, v165\n"
"v_exp_f32 v166, v166\n"
"v_exp_f32 v167, v167\n"
"v_exp_f32 v168, v168\n"
"v_exp_f32 v169, v169\n"
"v_exp_f32 v170, v170\n"
"v_exp_f32 v171, v171\n"
"v_smfmac_f32_32x32x32_bf16 v[192:207], a[248:251], v[160:167], v220\n"
"v_exp_f32 v172, v172\n"
"v_exp_f32 v173, v173\n"
"v_exp_f32 v174, v174\n"
"v_smfmac_f32_16x16x64_bf16 v[212:215], v[216:219], v[160:167], v220\n"
"v_exp_f32 v175, v175\n"
"s_nop 1\n"
"v_smfmac_f32_32x32x32_bf16 v[192:207], a[252:255], v[168:175], v220\n"
"v_smfmac_f32_16x16x64_bf16 v[212:215], v[216:219], v[168:175], v220\n"
"s_nop 7\n"
"v_rcp_f32 v36, v212\n"
"s_add_u32 s60, s66, 6422528\n"
"v_pk_mul_f32 v[192:193], v[192:193], v[36:37] op_sel_hi:[1,0]\n"
"v_cvt_pk_bf16_f32 v248, v192, v193\n"
"v_pk_mul_f32 v[194:195], v[194:195], v[36:37] op_sel_hi:[1,0]\n"
"v_cvt_pk_bf16_f32 v249, v194, v195\n"
"v_pk_mul_f32 v[196:197], v[196:197], v[36:37] op_sel_hi:[1,0]\n"
"v_cvt_pk_bf16_f32 v250, v196, v197\n"
"v_pk_mul_f32 v[198:199], v[198:199], v[36:37] op_sel_hi:[1,0]\n"
"v_cvt_pk_bf16_f32 v251, v198, v199\n"
"buffer_store_dwordx4 v[248:251], v38, s[48:51], s60 offen sc1\n"
"v_pk_mul_f32 v[200:201], v[200:201], v[36:37] op_sel_hi:[1,0]\n"
"v_cvt_pk_bf16_f32 v252, v200, v201\n"
"v_pk_mul_f32 v[202:203], v[202:203], v[36:37] op_sel_hi:[1,0]\n"
"v_cvt_pk_bf16_f32 v253, v202, v203\n"
"v_pk_mul_f32 v[204:205], v[204:205], v[36:37] op_sel_hi:[1,0]\n"
"v_cvt_pk_bf16_f32 v254, v204, v205\n"
"v_pk_mul_f32 v[206:207], v[206:207], v[36:37] op_sel_hi:[1,0]\n"
"v_cvt_pk_bf16_f32 v255, v206, v207\n"
"buffer_store_dwordx4 v[252:255], v38, s[48:51], s60 offen offset:512 sc1\n"
"LP3_%=:\n"
"s_add_u32 s63, s52, 6291456\n"
"buffer_load_dwordx4 a[0:3], v2, s[48:51], s63 offen sc1\n"
"buffer_load_dwordx4 a[4:7], v2, s[48:51], s63 offen offset:1024 sc1\n"
"s_add_u32 s63, s52, 6553600\n"
"buffer_load_dwordx4 a[8:11], v2, s[48:51], s63 offen sc1\n"
"buffer_load_dwordx4 a[12:15], v2, s[48:51], s63 offen offset:1024 sc1\n"
"s_add_u32 s63, s52, 6815744\n"
"buffer_load_dwordx4 a[16:19], v2, s[48:51], s63 offen sc1\n"
"buffer_load_dwordx4 a[20:23], v2, s[48:51], s63 offen offset:1024 sc1\n"
"s_add_u32 s63, s52, 7077888\n"
"buffer_load_dwordx4 a[24:27], v2, s[48:51], s63 offen sc1\n"
"buffer_load_dwordx4 a[28:31], v2, s[48:51], s63 offen offset:1024 sc1\n"
"v_lshlrev_b32 v64, 16, v16\n"
"v_and_b32 v65, 0xffff0000, v16\n"
"v_lshlrev_b32 v66, 16, v17\n"
"v_and_b32 v67, 0xffff0000, v17\n"
"v_lshlrev_b32 v68, 16, v24\n"
"v_and_b32 v69, 0xffff0000, v24\n"
"v_lshlrev_b32 v70, 16, v25\n"
"v_and_b32 v71, 0xffff0000, v25\n"
"v_lshlrev_b32 v72, 16, v18\n"
"v_and_b32 v73, 0xffff0000, v18\n"
"v_lshlrev_b32 v74, 16, v19\n"
"v_and_b32 v75, 0xffff0000, v19\n"
"v_lshlrev_b32 v76, 16, v26\n"
"v_and_b32 v77, 0xffff0000, v26\n"
"v_lshlrev_b32 v78, 16, v27\n"
"v_and_b32 v79, 0xffff0000, v27\n"
"v_lshlrev_b32 v80, 16, v20\n"
"v_and_b32 v81, 0xffff0000, v20\n"
"v_lshlrev_b32 v82, 16, v21\n"
"v_and_b32 v83, 0xffff0000, v21\n"
"v_lshlrev_b32 v84, 16, v28\n"
"v_and_b32 v85, 0xffff0000, v28\n"
"v_lshlrev_b32 v86, 16, v29\n"
"v_and_b32 v87, 0xffff0000, v29\n"
"v_lshlrev_b32 v88, 16, v22\n"
"v_and_b32 v89, 0xffff0000, v22\n"
"v_lshlrev_b32 v90, 16, v23\n"
"v_and_b32 v91, 0xffff0000, v23\n"
"v_lshlrev_b32 v92, 16, v30\n"
"v_and_b32 v93, 0xffff0000, v30\n"
"v_lshlrev_b32_e64 v94, 16, v31\n"
"v_and_b32 v95, 0xffff0000, v31\n"
"v_add_u32 v5, 4294868992, v5\n"
"v_add_u32 v6, 4294868992, v6\n"
"v_add_u32 v7, 4294868992, v7\n"
"v_add_u32 v8, 4294868992, v8\n"
"ds_read_b64_tr_b16 v[224:225], v5\n"
"ds_read_b64_tr_b16 v[226:227], v5 offset:512\n"
"ds_read_b64_tr_b16 v[228:229], v6\n"
"ds_read_b64_tr_b16 v[230:231], v6 offset:512\n"
"ds_read_b64_tr_b16 v[232:233], v7\n"
"ds_read_b64_tr_b16 v[234:235], v7 offset:512\n"
"ds_read_b64_tr_b16 v[236:237], v8\n"
"ds_read_b64_tr_b16 v[238:239], v8 offset:512\n"
"s_waitcnt vmcnt(7) lgkmcnt(6)\n"
"v_mfma_f32_16x16x32_bf16 v[64:67], v[224:227], a[0:3], v[64:67]\n"
"ds_read_b64_tr_b16 v[240:241], v5 offset:8192\n"
"s_add_u32 s63, s52, 7340032\n"
"s_waitcnt vmcnt(6)\n"
"v_mfma_f32_16x16x32_bf16 v[68:71], v[224:227], a[4:7], v[68:71]\n"
"ds_read_b64_tr_b16 v[242:243], v5 offset:8704\n"
"s_waitcnt lgkmcnt(6)\n"
"v_mfma_f32_16x16x32_bf16 v[72:75], v[228:231], a[0:3], v[72:75]\n"
"ds_read_b64_tr_b16 v[224:225], v6 offset:8192\n"
"buffer_load_dwordx4 a[32:35], v2, s[48:51], s63 offen sc1\n"
"v_mfma_f32_16x16x32_bf16 v[76:79], v[228:231], a[4:7], v[76:79]\n"
"ds_read_b64_tr_b16 v[226:227], v6 offset:8704\n"
"s_waitcnt lgkmcnt(4)\n"
"v_mfma_f32_16x16x32_bf16 v[80:83], v[232:235], a[0:3], v[80:83]\n"
"ds_read_b64_tr_b16 v[228:229], v7 offset:8192\n"
"buffer_load_dwordx4 a[36:39], v2, s[48:51], s63 offen offset:1024 sc1\n"
"v_mfma_f32_16x16x32_bf16 v[84:87], v[232:235], a[4:7], v[84:87]\n"
"ds_read_b64_tr_b16 v[230:231], v7 offset:8704\n"
"v_mfma_f32_16x16x32_bf16 v[88:91], v[236:239], a[0:3], v[88:91]\n"
"ds_read_b64_tr_b16 v[232:233], v8 offset:8192\n"
"s_add_u32 s63, s52, 7602176\n"
"v_mfma_f32_16x16x32_bf16 v[92:95], v[236:239], a[4:7], v[92:95]\n"
"ds_read_b64_tr_b16 v[234:235], v8 offset:8704\n"
"s_waitcnt vmcnt(6) lgkmcnt(4)\n"
"s_add_u32 s62, s0, 1589248\n"
"s_mov_b64 exec, 2\n"
"buffer_atomic_add v1, off, s[48:51], s62 sc1\n"
"s_mov_b64 exec, -1\n"
"v_mfma_f32_16x16x32_bf16 v[64:67], v[240:243], a[8:11], v[64:67]\n"
"ds_read_b64_tr_b16 v[236:237], v5 offset:16384\n"
"buffer_load_dwordx4 a[40:43], v2, s[48:51], s63 offen sc1\n"
"v_mfma_f32_16x16x32_bf16 v[68:71], v[240:243], a[12:15], v[68:71]\n"
"ds_read_b64_tr_b16 v[238:239], v5 offset:16896\n"
"v_mov_b32 v48, 0x3f803f80\n"
"v_mfma_f32_16x16x32_bf16 v[72:75], v[224:227], a[8:11], v[72:75]\n"
"ds_read_b64_tr_b16 v[240:241], v6 offset:16384\n"
"buffer_load_dwordx4 a[44:47], v2, s[48:51], s63 offen offset:1024 sc1\n"
"v_mfma_f32_16x16x32_bf16 v[76:79], v[224:227], a[12:15], v[76:79]\n"
"ds_read_b64_tr_b16 v[242:243], v6 offset:16896\n"
"s_waitcnt lgkmcnt(4)\n"
"v_mfma_f32_16x16x32_bf16 v[80:83], v[228:231], a[8:11], v[80:83]\n"
"ds_read_b64_tr_b16 v[224:225], v7 offset:16384\n"
"s_add_u32 s63, s52, 7864320\n"
"v_mfma_f32_16x16x32_bf16 v[84:87], v[228:231], a[12:15], v[84:87]\n"
"ds_read_b64_tr_b16 v[226:227], v7 offset:16896\n"
"v_mov_b32 v49, 0x3f803f80\n"
"v_mfma_f32_16x16x32_bf16 v[88:91], v[232:235], a[8:11], v[88:91]\n"
"ds_read_b64_tr_b16 v[228:229], v8 offset:16384\n"
"buffer_load_dwordx4 a[48:51], v2, s[48:51], s63 offen sc1\n"
"v_mfma_f32_16x16x32_bf16 v[92:95], v[232:235], a[12:15], v[92:95]\n"
"ds_read_b64_tr_b16 v[230:231], v8 offset:16896\n"
"s_waitcnt vmcnt(8) lgkmcnt(4)\n"
"v_mfma_f32_16x16x32_bf16 v[64:67], v[236:239], a[16:19], v[64:67]\n"
"ds_read_b64_tr_b16 v[232:233], v5 offset:24576\n"
"buffer_load_dwordx4 a[52:55], v2, s[48:51], s63 offen offset:1024 sc1\n"
"v_mfma_f32_16x16x32_bf16 v[68:71], v[236:239], a[20:23], v[68:71]\n"
"ds_read_b64_tr_b16 v[234:235], v5 offset:25088\n"
"v_mov_b32 v50, 0x3f803f80\n"
"v_mfma_f32_16x16x32_bf16 v[72:75], v[240:243], a[16:19], v[72:75]\n"
"ds_read_b64_tr_b16 v[236:237], v6 offset:24576\n"
"s_add_u32 s63, s52, 8126464\n"
"v_mfma_f32_16x16x32_bf16 v[76:79], v[240:243], a[20:23], v[76:79]\n"
"ds_read_b64_tr_b16 v[238:239], v6 offset:25088\n"
"s_waitcnt lgkmcnt(4)\n"
"v_mfma_f32_16x16x32_bf16 v[80:83], v[224:227], a[16:19], v[80:83]\n"
"ds_read_b64_tr_b16 v[240:241], v7 offset:24576\n"
"buffer_load_dwordx4 a[56:59], v2, s[48:51], s63 offen sc1\n"
"v_mfma_f32_16x16x32_bf16 v[84:87], v[224:227], a[20:23], v[84:87]\n"
"ds_read_b64_tr_b16 v[242:243], v7 offset:25088\n"
"v_mov_b32 v51, 0x3f803f80\n"
"v_mfma_f32_16x16x32_bf16 v[88:91], v[228:231], a[16:19], v[88:91]\n"
"ds_read_b64_tr_b16 v[224:225], v8 offset:24576\n"
"buffer_load_dwordx4 a[60:63], v2, s[48:51], s63 offen offset:1024 sc1\n"
"v_mfma_f32_16x16x32_bf16 v[92:95], v[228:231], a[20:23], v[92:95]\n"
"ds_read_b64_tr_b16 v[226:227], v8 offset:25088\n"
"s_waitcnt vmcnt(9) lgkmcnt(4)\n"
"s_mul_i32 s68, s7, 114688\n"
"s_mul_i32 s58, s21, 7168\n"
"s_add_u32 s68, s68, s58\n"
"s_mul_i32 s57, s9, 262144\n"
"s_add_u32 s57, s57, s52\n"
"s_add_u32 s57, s57, 10485760\n"
"s_add_u32 s58, s57, 131072\n"
"s_lshl_b32 s59, s53, 5\n"
"s_add_u32 s59, s59, 262144\n"
"s_lshr_b32 s60, s9, 3\n"
"s_mul_i32 s90, s60, 1835008\n"
"s_xor_b32 s60, s60, 1\n"
"s_mul_i32 s85, s60, 4194304\n"
"s_add_u32 s85, s85, s22\n"
"s_add_u32 s90, s90, s56\n"
"s_xor_b32 s89, s90, 1024\n"
"s_lshl_b32 s84, s7, 13\n"
"s_lshl_b32 s60, s60, 17\n"
"s_lshl_b32 s80, s7, 11\n"
"s_add_u32 s60, s60, s80\n"
"s_lshl_b32 s81, s21, 13\n"
"s_add_u32 s81, s81, s60\n"
"s_xor_b32 s80, s81, 0x10000\n"
"s_lshr_b32 s60, s7, 1\n"
"s_mul_i32 s82, s60, 114688\n"
"s_mul_i32 s83, s60, 7168\n"
"s_and_b32 s60, s7, 1\n"
"s_mul_i32 s60, s60, 3072\n"
"s_add_u32 s83, s83, s60\n"
"s_add_u32 s82, s82, s60\n"
"s_mul_i32 s60, s21, 7168\n"
"s_add_u32 s82, s82, s60\n"
"s_xor_b32 s88, s53, 64\n"
"v_mfma_f32_16x16x32_bf16 v[64:67], v[232:235], a[24:27], v[64:67]\n"
"ds_read_b64_tr_b16 v[228:229], v5 offset:32768\n"
"s_add_u32 s63, s52, 8388608\n"
"v_mfma_f32_16x16x32_bf16 v[68:71], v[232:235], a[28:31], v[68:71]\n"
"ds_read_b64_tr_b16 v[230:231], v5 offset:33280\n"
"v_lshlrev_b32 v52, 4, v3\n"
"v_mfma_f32_16x16x32_bf16 v[72:75], v[236:239], a[24:27], v[72:75]\n"
"ds_read_b64_tr_b16 v[232:233], v6 offset:32768\n"
"buffer_load_dwordx4 a[64:67], v2, s[48:51], s63 offen sc1\n"
"v_mfma_f32_16x16x32_bf16 v[76:79], v[236:239], a[28:31], v[76:79]\n"
"ds_read_b64_tr_b16 v[234:235], v6 offset:33280\n"
"s_waitcnt lgkmcnt(4)\n"
"v_mfma_f32_16x16x32_bf16 v[80:83], v[240:243], a[24:27], v[80:83]\n"
"ds_read_b64_tr_b16 v[236:237], v7 offset:32768\n"
"buffer_load_dwordx4 a[68:71], v2, s[48:51], s63 offen offset:1024 sc1\n"
"v_mfma_f32_16x16x32_bf16 v[84:87], v[240:243], a[28:31], v[84:87]\n"
"ds_read_b64_tr_b16 v[238:239], v7 offset:33280\n"
"v_add_u32 v221, 57344, v2\n"
"v_mfma_f32_16x16x32_bf16 v[88:91], v[224:227], a[24:27], v[88:91]\n"
"ds_read_b64_tr_b16 v[240:241], v8 offset:32768\n"
"s_add_u32 s63, s52, 8650752\n"
"v_mfma_f32_16x16x32_bf16 v[92:95], v[224:227], a[28:31], v[92:95]\n"
"ds_read_b64_tr_b16 v[242:243], v8 offset:33280\n"
"s_waitcnt vmcnt(9) lgkmcnt(4)\n"
"v_mfma_f32_16x16x32_bf16 v[64:67], v[228:231], a[32:35], v[64:67]\n"
"ds_read_b64_tr_b16 v[224:225], v5 offset:40960\n"
"buffer_load_dwordx4 a[72:75], v2, s[48:51], s63 offen sc1\n"
"v_mfma_f32_16x16x32_bf16 v[68:71], v[228:231], a[36:39], v[68:71]\n"
"ds_read_b64_tr_b16 v[226:227], v5 offset:41472\n"
"v_add_u32 v222, 114688, v2\n"
"v_mfma_f32_16x16x32_bf16 v[72:75], v[232:235], a[32:35], v[72:75]\n"
"ds_read_b64_tr_b16 v[228:229], v6 offset:40960\n"
"buffer_load_dwordx4 a[76:79], v2, s[48:51], s63 offen offset:1024 sc1\n"
"v_mfma_f32_16x16x32_bf16 v[76:79], v[232:235], a[36:39], v[76:79]\n"
"ds_read_b64_tr_b16 v[230:231], v6 offset:41472\n"
"s_waitcnt lgkmcnt(4)\n"
"v_mfma_f32_16x16x32_bf16 v[80:83], v[236:239], a[32:35], v[80:83]\n"
"ds_read_b64_tr_b16 v[232:233], v7 offset:40960\n"
"s_add_u32 s63, s52, 8912896\n"
"v_mfma_f32_16x16x32_bf16 v[84:87], v[236:239], a[36:39], v[84:87]\n"
"ds_read_b64_tr_b16 v[234:235], v7 offset:41472\n"
"v_mov_b32 v162, 0xbdd2d3e8\n"
"v_mfma_f32_16x16x32_bf16 v[88:91], v[240:243], a[32:35], v[88:91]\n"
"ds_read_b64_tr_b16 v[236:237], v8 offset:40960\n"
"buffer_load_dwordx4 a[80:83], v2, s[48:51], s63 offen sc1\n"
"v_mfma_f32_16x16x32_bf16 v[92:95], v[240:243], a[36:39], v[92:95]\n"
"ds_read_b64_tr_b16 v[238:239], v8 offset:41472\n"
"s_waitcnt vmcnt(9) lgkmcnt(4)\n"
"s_mov_b32 s62, 1757184\n"
"buffer_load_dword v128, off, s[48:51], s62 sc1\n"
"v_mfma_f32_16x16x32_bf16 v[64:67], v[224:227], a[40:43], v[64:67]\n"
"ds_read_b64_tr_b16 v[240:241], v5 offset:49152\n"
"buffer_load_dwordx4 a[84:87], v2, s[48:51], s63 offen offset:1024 sc1\n"
"v_mfma_f32_16x16x32_bf16 v[68:71], v[224:227], a[44:47], v[68:71]\n"
"ds_read_b64_tr_b16 v[242:243], v5 offset:49664\n"
"v_mov_b32 v163, 0xc0135761\n"
"v_mfma_f32_16x16x32_bf16 v[72:75], v[228:231], a[40:43], v[72:75]\n"
"ds_read_b64_tr_b16 v[224:225], v6 offset:49152\n"
"s_add_u32 s63, s52, 9175040\n"
"v_mfma_f32_16x16x32_bf16 v[76:79], v[228:231], a[44:47], v[76:79]\n"
"ds_read_b64_tr_b16 v[226:227], v6 offset:49664\n"
"s_waitcnt lgkmcnt(4)\n"
"v_mfma_f32_16x16x32_bf16 v[80:83], v[232:235], a[40:43], v[80:83]\n"
"ds_read_b64_tr_b16 v[228:229], v7 offset:49152\n"
"buffer_load_dwordx4 a[88:91], v2, s[48:51], s63 offen sc1\n"
"v_mfma_f32_16x16x32_bf16 v[84:87], v[232:235], a[44:47], v[84:87]\n"
"ds_read_b64_tr_b16 v[230:231], v7 offset:49664\n"
"v_mfma_f32_16x16x32_bf16 v[88:91], v[236:239], a[40:43], v[88:91]\n"
"ds_read_b64_tr_b16 v[232:233], v8 offset:49152\n"
"buffer_load_dwordx4 a[92:95], v2, s[48:51], s63 offen offset:1024 sc1\n"
"v_mfma_f32_16x16x32_bf16 v[92:95], v[236:239], a[44:47], v[92:95]\n"
"ds_read_b64_tr_b16 v[234:235], v8 offset:49664\n"
"s_waitcnt vmcnt(11) lgkmcnt(4)\n"
"v_mfma_f32_16x16x32_bf16 v[64:67], v[240:243], a[48:51], v[64:67]\n"
"ds_read_b64_tr_b16 v[236:237], v5 offset:57344\n"
"s_add_u32 s63, s52, 9437184\n"
"v_mfma_f32_16x16x32_bf16 v[68:71], v[240:243], a[52:55], v[68:71]\n"
"ds_read_b64_tr_b16 v[238:239], v5 offset:57856\n"
"v_mfma_f32_16x16x32_bf16 v[72:75], v[224:227], a[48:51], v[72:75]\n"
"ds_read_b64_tr_b16 v[240:241], v6 offset:57344\n"
"buffer_load_dwordx4 a[96:99], v2, s[48:51], s63 offen sc1\n"
"v_mfma_f32_16x16x32_bf16 v[76:79], v[224:227], a[52:55], v[76:79]\n"
"ds_read_b64_tr_b16 v[242:243], v6 offset:57856\n"
"s_waitcnt lgkmcnt(4)\n"
"v_mfma_f32_16x16x32_bf16 v[80:83], v[228:231], a[48:51], v[80:83]\n"
"ds_read_b64_tr_b16 v[224:225], v7 offset:57344\n"
"buffer_load_dwordx4 a[100:103], v2, s[48:51], s63 offen offset:1024 sc1\n"
"v_mfma_f32_16x16x32_bf16 v[84:87], v[228:231], a[52:55], v[84:87]\n"
"ds_read_b64_tr_b16 v[226:227], v7 offset:57856\n"
"v_mfma_f32_16x16x32_bf16 v[88:91], v[232:235], a[48:51], v[88:91]\n"
"ds_read_b64_tr_b16 v[228:229], v8 offset:57344\n"
"s_add_u32 s63, s52, 9699328\n"
"v_mfma_f32_16x16x32_bf16 v[92:95], v[232:235], a[52:55], v[92:95]\n"
"ds_read_b64_tr_b16 v[230:231], v8 offset:57856\n"
"s_waitcnt vmcnt(11) lgkmcnt(4)\n"
"v_mfma_f32_16x16x32_bf16 v[64:67], v[236:239], a[56:59], v[64:67]\n"
"v_add_u32 v5, 65536, v5\n"
"v_add_u32 v6, 65536, v6\n"
"v_add_u32 v7, 65536, v7\n"
"v_add_u32 v8, 65536, v8\n"
"ds_read_b64_tr_b16 v[232:233], v5\n"
"v_mfma_f32_16x16x32_bf16 v[68:71], v[236:239], a[60:63], v[68:71]\n"
"ds_read_b64_tr_b16 v[234:235], v5 offset:512\n"
"v_mfma_f32_16x16x32_bf16 v[72:75], v[240:243], a[56:59], v[72:75]\n"
"ds_read_b64_tr_b16 v[236:237], v6\n"
"buffer_load_dwordx4 a[104:107], v2, s[48:51], s63 offen sc1\n"
"v_mfma_f32_16x16x32_bf16 v[76:79], v[240:243], a[60:63], v[76:79]\n"
"ds_read_b64_tr_b16 v[238:239], v6 offset:512\n"
"s_waitcnt lgkmcnt(4)\n"
"v_mfma_f32_16x16x32_bf16 v[80:83], v[224:227], a[56:59], v[80:83]\n"
"ds_read_b64_tr_b16 v[240:241], v7\n"
"buffer_load_dwordx4 a[108:111], v2, s[48:51], s63 offen offset:1024 sc1\n"
"v_mfma_f32_16x16x32_bf16 v[84:87], v[224:227], a[60:63], v[84:87]\n"
"ds_read_b64_tr_b16 v[242:243], v7 offset:512\n"
"s_add_u32 s63, s52, 9961472\n"
"v_mfma_f32_16x16x32_bf16 v[88:91], v[228:231], a[56:59], v[88:91]\n"
"ds_read_b64_tr_b16 v[224:225], v8\n"
"buffer_load_dwordx4 a[112:115], v2, s[48:51], s63 offen sc1\n"
"v_mfma_f32_16x16x32_bf16 v[92:95], v[228:231], a[60:63], v[92:95]\n"
"ds_read_b64_tr_b16 v[226:227], v8 offset:512\n"
"s_waitcnt vmcnt(12) lgkmcnt(4)\n"
"s_add_u32 s62, s0, 1589248\n"
"v_mov_b32 v60, 0\n"
"s_mov_b64 exec, 1\n"
"buffer_atomic_or v60, off, s[48:51], s62 sc0 sc1\n"
"s_mov_b64 exec, -1\n"
"v_mfma_f32_16x16x32_bf16 v[64:67], v[232:235], a[64:67], v[64:67]\n"
"ds_read_b64_tr_b16 v[228:229], v5 offset:8192\n"
"buffer_load_dwordx4 a[116:119], v2, s[48:51], s63 offen offset:1024 sc1\n"
"v_mfma_f32_16x16x32_bf16 v[68:71], v[232:235], a[68:71], v[68:71]\n"
"ds_read_b64_tr_b16 v[230:231], v5 offset:8704\n"
"v_mfma_f32_16x16x32_bf16 v[72:75], v[236:239], a[64:67], v[72:75]\n"
"ds_read_b64_tr_b16 v[232:233], v6 offset:8192\n"
"s_add_u32 s63, s52, 10223616\n"
"v_mfma_f32_16x16x32_bf16 v[76:79], v[236:239], a[68:71], v[76:79]\n"
"ds_read_b64_tr_b16 v[234:235], v6 offset:8704\n"
"s_waitcnt lgkmcnt(4)\n"
"v_mfma_f32_16x16x32_bf16 v[80:83], v[240:243], a[64:67], v[80:83]\n"
"ds_read_b64_tr_b16 v[236:237], v7 offset:8192\n"
"buffer_load_dwordx4 a[120:123], v2, s[48:51], s63 offen sc1\n"
"v_mfma_f32_16x16x32_bf16 v[84:87], v[240:243], a[68:71], v[84:87]\n"
"ds_read_b64_tr_b16 v[238:239], v7 offset:8704\n"
"v_mfma_f32_16x16x32_bf16 v[88:91], v[224:227], a[64:67], v[88:91]\n"
"ds_read_b64_tr_b16 v[240:241], v8 offset:8192\n"
"buffer_load_dwordx4 a[124:127], v2, s[48:51], s63 offen offset:1024 sc1\n"
"v_mfma_f32_16x16x32_bf16 v[92:95], v[224:227], a[68:71], v[92:95]\n"
"ds_read_b64_tr_b16 v[242:243], v8 offset:8704\n"
"s_waitcnt vmcnt(14) lgkmcnt(4)\n"
"v_mfma_f32_16x16x32_bf16 v[64:67], v[228:231], a[72:75], v[64:67]\n"
"ds_read_b64_tr_b16 v[224:225], v5 offset:16384\n"
"v_mfma_f32_16x16x32_bf16 v[68:71], v[228:231], a[76:79], v[68:71]\n"
"ds_read_b64_tr_b16 v[226:227], v5 offset:16896\n"
"v_mfma_f32_16x16x32_bf16 v[72:75], v[232:235], a[72:75], v[72:75]\n"
"ds_read_b64_tr_b16 v[228:229], v6 offset:16384\n"
"v_mfma_f32_16x16x32_bf16 v[76:79], v[232:235], a[76:79], v[76:79]\n"
"ds_read_b64_tr_b16 v[230:231], v6 offset:16896\n"
"s_waitcnt lgkmcnt(4)\n"
"v_mfma_f32_16x16x32_bf16 v[80:83], v[236:239], a[72:75], v[80:83]\n"
"ds_read_b64_tr_b16 v[232:233], v7 offset:16384\n"
"v_mfma_f32_16x16x32_bf16 v[84:87], v[236:239], a[76:79], v[84:87]\n"
"ds_read_b64_tr_b16 v[234:235], v7 offset:16896\n"
"v_mfma_f32_16x16x32_bf16 v[88:91], v[240:243], a[72:75], v[88:91]\n"
"ds_read_b64_tr_b16 v[236:237], v8 offset:16384\n"
"v_mfma_f32_16x16x32_bf16 v[92:95], v[240:243], a[76:79], v[92:95]\n"
"ds_read_b64_tr_b16 v[238:239], v8 offset:16896\n"
"s_waitcnt vmcnt(11) lgkmcnt(4)\n"
"v_mfma_f32_16x16x32_bf16 v[64:67], v[224:227], a[80:83], v[64:67]\n"
"ds_read_b64_tr_b16 v[240:241], v5 offset:24576\n"
"v_mfma_f32_16x16x32_bf16 v[68:71], v[224:227], a[84:87], v[68:71]\n"
"ds_read_b64_tr_b16 v[242:243], v5 offset:25088\n"
"v_mfma_f32_16x16x32_bf16 v[72:75], v[228:231], a[80:83], v[72:75]\n"
"ds_read_b64_tr_b16 v[224:225], v6 offset:24576\n"
"v_mfma_f32_16x16x32_bf16 v[76:79], v[228:231], a[84:87], v[76:79]\n"
"ds_read_b64_tr_b16 v[226:227], v6 offset:25088\n"
"s_waitcnt lgkmcnt(4)\n"
"v_mfma_f32_16x16x32_bf16 v[80:83], v[232:235], a[80:83], v[80:83]\n"
"ds_read_b64_tr_b16 v[228:229], v7 offset:24576\n"
"v_mfma_f32_16x16x32_bf16 v[84:87], v[232:235], a[84:87], v[84:87]\n"
"ds_read_b64_tr_b16 v[230:231], v7 offset:25088\n"
"v_mfma_f32_16x16x32_bf16 v[88:91], v[236:239], a[80:83], v[88:91]\n"
"ds_read_b64_tr_b16 v[232:233], v8 offset:24576\n"
"v_mfma_f32_16x16x32_bf16 v[92:95], v[236:239], a[84:87], v[92:95]\n"
"ds_read_b64_tr_b16 v[234:235], v8 offset:25088\n"
"s_waitcnt vmcnt(9) lgkmcnt(4)\n"
"v_mfma_f32_16x16x32_bf16 v[64:67], v[240:243], a[88:91], v[64:67]\n"
"ds_read_b64_tr_b16 v[236:237], v5 offset:32768\n"
"v_mfma_f32_16x16x32_bf16 v[68:71], v[240:243], a[92:95], v[68:71]\n"
"ds_read_b64_tr_b16 v[238:239], v5 offset:33280\n"
"v_mfma_f32_16x16x32_bf16 v[72:75], v[224:227], a[88:91], v[72:75]\n"
"ds_read_b64_tr_b16 v[240:241], v6 offset:32768\n"
"v_mfma_f32_16x16x32_bf16 v[76:79], v[224:227], a[92:95], v[76:79]\n"
"ds_read_b64_tr_b16 v[242:243], v6 offset:33280\n"
"s_waitcnt lgkmcnt(4)\n"
"v_cmp_ne_u32 vcc, 0x100, v128\n"
"s_nop 4\n"
"s_cbranch_vccz Ld12_%=\n"
"s_mov_b32 s65, 0\n"
"Lp11_%=:\n"
"s_mov_b32 s62, 1757184\n"
"buffer_load_dword v128, off, s[48:51], s62 sc1\n"
"s_add_u32 s65, s65, 1\n"
"s_cmp_gt_u32 s65, 131072\n"
"s_waitcnt vmcnt(0)\n"
"s_cbranch_scc1 Ld12_%=\n"
"v_cmp_ne_u32 vcc, 0x100, v128\n"
"s_nop 4\n"
"s_cbranch_vccnz Lp11_%=\n"
"Ld12_%=:\n"
"v_mfma_f32_16x16x32_bf16 v[80:83], v[228:231], a[88:91], v[80:83]\n"
"ds_read_b64_tr_b16 v[224:225], v7 offset:32768\n"
"v_mfma_f32_16x16x32_bf16 v[84:87], v[228:231], a[92:95], v[84:87]\n"
"ds_read_b64_tr_b16 v[226:227], v7 offset:33280\n"
"v_mfma_f32_16x16x32_bf16 v[88:91], v[232:235], a[88:91], v[88:91]\n"
"ds_read_b64_tr_b16 v[228:229], v8 offset:32768\n"
"v_mfma_f32_16x16x32_bf16 v[92:95], v[232:235], a[92:95], v[92:95]\n"
"ds_read_b64_tr_b16 v[230:231], v8 offset:33280\n"
"s_waitcnt vmcnt(7) lgkmcnt(4)\n"
"s_mov_b64 exec, 1\n"
"s_waitcnt vmcnt(3)\n"
"v_cmp_ne_u32 vcc, 16, v60\n"
"s_mov_b64 exec, -1\n"
"s_nop 3\n"
"s_cbranch_vccz Ld14_%=\n"
"s_mov_b32 s65, 0\n"
"Lp13_%=:\n"
"s_add_u32 s62, s0, 1589248\n"
"v_mov_b32 v60, 0\n"
"s_mov_b64 exec, 1\n"
"buffer_atomic_or v60, off, s[48:51], s62 sc0 sc1\n"
"s_mov_b64 exec, -1\n"
"s_add_u32 s65, s65, 1\n"
"s_cmp_gt_u32 s65, 131072\n"
"s_waitcnt vmcnt(0)\n"
"s_cbranch_scc1 Ld14_%=\n"
"s_mov_b64 exec, 1\n"
"v_cmp_ne_u32 vcc, 16, v60\n"
"s_mov_b64 exec, -1\n"
"s_nop 3\n"
"s_cbranch_vccnz Lp13_%=\n"
"Ld14_%=:\n"
"s_add_u32 s63, s52, 6422528\n"
"buffer_load_dwordx4 a[128:131], v2, s[48:51], s63 offen sc1\n"
"buffer_load_dwordx4 a[132:135], v2, s[48:51], s63 offen offset:1024 sc1\n"
"s_add_u32 s63, s52, 6684672\n"
"buffer_load_dwordx4 a[136:139], v2, s[48:51], s63 offen sc1\n"
"buffer_load_dwordx4 a[140:143], v2, s[48:51], s63 offen offset:1024 sc1\n"
"s_add_u32 s63, s52, 6946816\n"
"buffer_load_dwordx4 a[144:147], v2, s[48:51], s63 offen sc1\n"
"buffer_load_dwordx4 a[148:151], v2, s[48:51], s63 offen offset:1024 sc1\n"
"s_add_u32 s63, s52, 7208960\n"
"buffer_load_dwordx4 a[152:155], v2, s[48:51], s63 offen sc1\n"
"buffer_load_dwordx4 a[156:159], v2, s[48:51], s63 offen offset:1024 sc1\n"
"s_add_u32 s63, s52, 7471104\n"
"buffer_load_dwordx4 a[160:163], v2, s[48:51], s63 offen sc1\n"
"buffer_load_dwordx4 a[164:167], v2, s[48:51], s63 offen offset:1024 sc1\n"
"s_add_u32 s63, s52, 7733248\n"
"buffer_load_dwordx4 a[168:171], v2, s[48:51], s63 offen sc1\n"
"buffer_load_dwordx4 a[172:175], v2, s[48:51], s63 offen offset:1024 sc1\n"
"v_mfma_f32_16x16x32_bf16 v[64:67], v[236:239], a[96:99], v[64:67]\n"
"ds_read_b64_tr_b16 v[232:233], v5 offset:40960\n"
"v_mfma_f32_16x16x32_bf16 v[68:71], v[236:239], a[100:103], v[68:71]\n"
"ds_read_b64_tr_b16 v[234:235], v5 offset:41472\n"
"v_mfma_f32_16x16x32_bf16 v[72:75], v[240:243], a[96:99], v[72:75]\n"
"ds_read_b64_tr_b16 v[236:237], v6 offset:40960\n"
"v_mfma_f32_16x16x32_bf16 v[76:79], v[240:243], a[100:103], v[76:79]\n"
"ds_read_b64_tr_b16 v[238:239], v6 offset:41472\n"
"s_waitcnt lgkmcnt(4)\n"
"v_mfma_f32_16x16x32_bf16 v[80:83], v[224:227], a[96:99], v[80:83]\n"
"ds_read_b64_tr_b16 v[240:241], v7 offset:40960\n"
"v_mfma_f32_16x16x32_bf16 v[84:87], v[224:227], a[100:103], v[84:87]\n"
"ds_read_b64_tr_b16 v[242:243], v7 offset:41472\n"
"v_mfma_f32_16x16x32_bf16 v[88:91], v[228:231], a[96:99], v[88:91]\n"
"ds_read_b64_tr_b16 v[224:225], v8 offset:40960\n"
"v_mfma_f32_16x16x32_bf16 v[92:95], v[228:231], a[100:103], v[92:95]\n"
"ds_read_b64_tr_b16 v[226:227], v8 offset:41472\n"
"s_waitcnt lgkmcnt(4)\n"
"v_mfma_f32_16x16x32_bf16 v[64:67], v[232:235], a[104:107], v[64:67]\n"
"ds_read_b64_tr_b16 v[228:229], v5 offset:49152\n"
"v_mfma_f32_16x16x32_bf16 v[68:71], v[232:235], a[108:111], v[68:71]\n"
"ds_read_b64_tr_b16 v[230:231], v5 offset:49664\n"
"v_mfma_f32_16x16x32_bf16 v[72:75], v[236:239], a[104:107], v[72:75]\n"
"ds_read_b64_tr_b16 v[232:233], v6 offset:49152\n"
"v_mfma_f32_16x16x32_bf16 v[76:79], v[236:239], a[108:111], v[76:79]\n"
"ds_read_b64_tr_b16 v[234:235], v6 offset:49664\n"
"s_waitcnt lgkmcnt(4)\n"
"v_mfma_f32_16x16x32_bf16 v[80:83], v[240:243], a[104:107], v[80:83]\n"
"ds_read_b64_tr_b16 v[236:237], v7 offset:49152\n"
"v_mfma_f32_16x16x32_bf16 v[84:87], v[240:243], a[108:111], v[84:87]\n"
"ds_read_b64_tr_b16 v[238:239], v7 offset:49664\n"
"v_mfma_f32_16x16x32_bf16 v[88:91], v[224:227], a[104:107], v[88:91]\n"
"ds_read_b64_tr_b16 v[240:241], v8 offset:49152\n"
"v_mfma_f32_16x16x32_bf16 v[92:95], v[224:227], a[108:111], v[92:95]\n"
"ds_read_b64_tr_b16 v[242:243], v8 offset:49664\n"
"s_waitcnt vmcnt(14) lgkmcnt(4)\n"
"v_mfma_f32_16x16x32_bf16 v[64:67], v[228:231], a[112:115], v[64:67]\n"
"ds_read_b64_tr_b16 v[224:225], v5 offset:57344\n"
"v_mfma_f32_16x16x32_bf16 v[68:71], v[228:231], a[116:119], v[68:71]\n"
"ds_read_b64_tr_b16 v[226:227], v5 offset:57856\n"
"v_mfma_f32_16x16x32_bf16 v[72:75], v[232:235], a[112:115], v[72:75]\n"
"ds_read_b64_tr_b16 v[228:229], v6 offset:57344\n"
"s_add_u32 m0, s69, 0\n"
"s_add_u32 s64, s68, 30408704\n"
"v_mfma_f32_16x16x32_bf16 v[76:79], v[232:235], a[116:119], v[76:79]\n"
"ds_read_b64_tr_b16 v[230:231], v6 offset:57856\n"
"s_waitcnt lgkmcnt(4)\n"
"v_mfma_f32_16x16x32_bf16 v[80:83], v[236:239], a[112:115], v[80:83]\n"
"ds_read_b64_tr_b16 v[232:233], v7 offset:57344\n"
"v_mfma_f32_16x16x32_bf16 v[84:87], v[236:239], a[116:119], v[84:87]\n"
"ds_read_b64_tr_b16 v[234:235], v7 offset:57856\n"
"v_mfma_f32_16x16x32_bf16 v[88:91], v[240:243], a[112:115], v[88:91]\n"
"ds_read_b64_tr_b16 v[236:237], v8 offset:57344\n"
"v_mfma_f32_16x16x32_bf16 v[92:95], v[240:243], a[116:119], v[92:95]\n"
"ds_read_b64_tr_b16 v[238:239], v8 offset:57856\n"
"s_waitcnt vmcnt(12) lgkmcnt(4)\n"
"v_mfma_f32_16x16x32_bf16 v[64:67], v[224:227], a[120:123], v[64:67]\n"
"v_add_u32 v5, 4294905856, v5\n"
"v_add_u32 v6, 4294905856, v6\n"
"v_add_u32 v7, 4294905856, v7\n"
"v_add_u32 v8, 4294905856, v8\n"
"ds_read_b64_tr_b16 v[240:241], v5\n"
"v_mfma_f32_16x16x32_bf16 v[68:71], v[224:227], a[124:127], v[68:71]\n"
"ds_read_b64_tr_b16 v[242:243], v5 offset:512\n"
"v_mfma_f32_16x16x32_bf16 v[72:75], v[228:231], a[120:123], v[72:75]\n"
"ds_read_b64_tr_b16 v[224:225], v6\n"
"v_mfma_f32_16x16x32_bf16 v[76:79], v[228:231], a[124:127], v[76:79]\n"
"ds_read_b64_tr_b16 v[226:227], v6 offset:512\n"
"s_waitcnt lgkmcnt(4)\n"
"v_mfma_f32_16x16x32_bf16 v[80:83], v[232:235], a[120:123], v[80:83]\n"
"ds_read_b64_tr_b16 v[228:229], v7\n"
"v_mfma_f32_16x16x32_bf16 v[84:87], v[232:235], a[124:127], v[84:87]\n"
"ds_read_b64_tr_b16 v[230:231], v7 offset:512\n"
"v_mfma_f32_16x16x32_bf16 v[88:91], v[236:239], a[120:123], v[88:91]\n"
"ds_read_b64_tr_b16 v[232:233], v8\n"
"v_mfma_f32_16x16x32_bf16 v[92:95], v[236:239], a[124:127], v[92:95]\n"
"ds_read_b64_tr_b16 v[234:235], v8 offset:512\n"
"s_waitcnt vmcnt(10) lgkmcnt(4)\n"
"v_mfma_f32_16x16x32_bf16 v[64:67], v[240:243], a[128:131], v[64:67]\n"
"ds_read_b64_tr_b16 v[236:237], v5 offset:8192\n"
"s_add_u32 s63, s52, 7995392\n"
"v_mfma_f32_16x16x32_bf16 v[68:71], v[240:243], a[132:135], v[68:71]\n"
"ds_read_b64_tr_b16 v[238:239], v5 offset:8704\n"
"v_mfma_f32_16x16x32_bf16 v[72:75], v[224:227], a[128:131], v[72:75]\n"
"ds_read_b64_tr_b16 v[240:241], v6 offset:8192\n"
"buffer_load_dwordx4 a[176:179], v2, s[48:51], s63 offen sc1\n"
"v_mfma_f32_16x16x32_bf16 v[76:79], v[224:227], a[132:135], v[76:79]\n"
"LPG9_%=:\n"
"s_cbranch_execz LWRa_%=\n"
"ds_read_b64_tr_b16 v[242:243], v6 offset:8704\n"
"s_waitcnt lgkmcnt(4)\n"
"v_mfma_f32_16x16x32_bf16 v[80:83], v[228:231], a[128:131], v[80:83]\n"
"ds_read_b64_tr_b16 v[224:225], v7 offset:8192\n"
"buffer_load_dwordx4 a[180:183], v2, s[48:51], s63 offen offset:1024 sc1\n"
"v_mfma_f32_16x16x32_bf16 v[84:87], v[228:231], a[132:135], v[84:87]\n"
"ds_read_b64_tr_b16 v[226:227], v7 offset:8704\n"
"v_mfma_f32_16x16x32_bf16 v[88:91], v[232:235], a[128:131], v[88:91]\n"
"ds_read_b64_tr_b16 v[228:229], v8 offset:8192\n"
"v_mfma_f32_16x16x32_bf16 v[92:95], v[232:235], a[132:135], v[92:95]\n"
"ds_read_b64_tr_b16 v[230:231], v8 offset:8704\n"
"s_waitcnt vmcnt(10) lgkmcnt(4)\n"
"v_mfma_f32_16x16x32_bf16 v[64:67], v[236:239], a[136:139], v[64:67]\n"
"ds_read_b64_tr_b16 v[232:233], v5 offset:16384\n"
"s_add_u32 s63, s52, 8257536\n"
"v_mfma_f32_16x16x32_bf16 v[68:71], v[236:239], a[140:143], v[68:71]\n"
"ds_read_b64_tr_b16 v[234:235], v5 offset:16896\n"
"v_mfma_f32_16x16x32_bf16 v[72:75], v[240:243], a[136:139], v[72:75]\n"
"ds_read_b64_tr_b16 v[236:237], v6 offset:16384\n"
"buffer_load_dwordx4 a[184:187], v2, s[48:51], s63 offen sc1\n"
"v_mfma_f32_16x16x32_bf16 v[76:79], v[240:243], a[140:143], v[76:79]\n"
"ds_read_b64_tr_b16 v[238:239], v6 offset:16896\n"
"s_waitcnt lgkmcnt(4)\n"
"v_mfma_f32_16x16x32_bf16 v[80:83], v[224:227], a[136:139], v[80:83]\n"
"ds_read_b64_tr_b16 v[240:241], v7 offset:16384\n"
"buffer_load_dwordx4 a[188:191], v2, s[48:51], s63 offen offset:1024 sc1\n"
"v_mfma_f32_16x16x32_bf16 v[84:87], v[224:227], a[140:143], v[84:87]\n"
"ds_read_b64_tr_b16 v[242:243], v7 offset:16896\n"
"v_mfma_f32_16x16x32_bf16 v[88:91], v[228:231], a[136:139], v[88:91]\n"
"ds_read_b64_tr_b16 v[224:225], v8 offset:16384\n"
"v_mfma_f32_16x16x32_bf16 v[92:95], v[228:231], a[140:143], v[92:95]\n"
"ds_read_b64_tr_b16 v[226:227], v8 offset:16896\n"
"s_waitcnt vmcnt(10) lgkmcnt(4)\n"
"v_mfma_f32_16x16x32_bf16 v[64:67], v[232:235], a[144:147], v[64:67]\n"
"ds_read_b64_tr_b16 v[228:229], v5 offset:24576\n"
"s_add_u32 s63, s52, 8519680\n"
"v_mfma_f32_16x16x32_bf16 v[68:71], v[232:235], a[148:151], v[68:71]\n"
"ds_read_b64_tr_b16 v[230:231], v5 offset:25088\n"
"v_mfma_f32_16x16x32_bf16 v[72:75], v[236:239], a[144:147], v[72:75]\n"
"ds_read_b64_tr_b16 v[232:233], v6 offset:24576\n"
"buffer_load_dwordx4 a[192:195], v2, s[48:51], s63 offen sc1\n"
"v_mfma_f32_16x16x32_bf16 v[76:79], v[236:239], a[148:151], v[76:79]\n"
"ds_read_b64_tr_b16 v[234:235], v6 offset:25088\n"
"s_waitcnt lgkmcnt(4)\n"
"v_mfma_f32_16x16x32_bf16 v[80:83], v[240:243], a[144:147], v[80:83]\n"
"ds_read_b64_tr_b16 v[236:237], v7 offset:24576\n"
"buffer_load_dwordx4 a[196:199], v2, s[48:51], s63 offen offset:1024 sc1\n"
"v_mfma_f32_16x16x32_bf16 v[84:87], v[240:243], a[148:151], v[84:87]\n"
"ds_read_b64_tr_b16 v[238:239], v7 offset:25088\n"
"v_mfma_f32_16x16x32_bf16 v[88:91], v[224:227], a[144:147], v[88:91]\n"
"ds_read_b64_tr_b16 v[240:241], v8 offset:24576\n"
"v_mfma_f32_16x16x32_bf16 v[92:95], v[224:227], a[148:151], v[92:95]\n"
"ds_read_b64_tr_b16 v[242:243], v8 offset:25088\n"
"s_waitcnt vmcnt(10) lgkmcnt(4)\n"
"v_mfma_f32_16x16x32_bf16 v[64:67], v[228:231], a[152:155], v[64:67]\n"
"ds_read_b64_tr_b16 v[224:225], v5 offset:32768\n"
"s_add_u32 s63, s52, 8781824\n"
"v_mfma_f32_16x16x32_bf16 v[68:71], v[228:231], a[156:159], v[68:71]\n"
"ds_read_b64_tr_b16 v[226:227], v5 offset:33280\n"
"v_mfma_f32_16x16x32_bf16 v[72:75], v[232:235], a[152:155], v[72:75]\n"
"ds_read_b64_tr_b16 v[228:229], v6 offset:32768\n"
"buffer_load_dwordx4 a[200:203], v2, s[48:51], s63 offen sc1\n"
"v_mfma_f32_16x16x32_bf16 v[76:79], v[232:235], a[156:159], v[76:79]\n"
"ds_read_b64_tr_b16 v[230:231], v6 offset:33280\n"
"s_waitcnt lgkmcnt(4)\n"
"v_mfma_f32_16x16x32_bf16 v[80:83], v[236:239], a[152:155], v[80:83]\n"
"ds_read_b64_tr_b16 v[232:233], v7 offset:32768\n"
"buffer_load_dwordx4 a[204:207], v2, s[48:51], s63 offen offset:1024 sc1\n"
"v_mfma_f32_16x16x32_bf16 v[84:87], v[236:239], a[156:159], v[84:87]\n"
"ds_read_b64_tr_b16 v[234:235], v7 offset:33280\n"
"v_mfma_f32_16x16x32_bf16 v[88:91], v[240:243], a[152:155], v[88:91]\n"
"ds_read_b64_tr_b16 v[236:237], v8 offset:32768\n"
"v_mfma_f32_16x16x32_bf16 v[92:95], v[240:243], a[156:159], v[92:95]\n"
"ds_read_b64_tr_b16 v[238:239], v8 offset:33280\n"
"s_waitcnt vmcnt(10) lgkmcnt(4)\n"
"v_mfma_f32_16x16x32_bf16 v[64:67], v[224:227], a[160:163], v[64:67]\n"
"ds_read_b64_tr_b16 v[240:241], v5 offset:40960\n"
"s_add_u32 s63, s52, 9043968\n"
"v_mfma_f32_16x16x32_bf16 v[68:71], v[224:227], a[164:167], v[68:71]\n"
"ds_read_b64_tr_b16 v[242:243], v5 offset:41472\n"
"v_mfma_f32_16x16x32_bf16 v[72:75], v[228:231], a[160:163], v[72:75]\n"
"ds_read_b64_tr_b16 v[224:225], v6 offset:40960\n"
"buffer_load_dwordx4 a[208:211], v2, s[48:51], s63 offen sc1\n"
"v_mfma_f32_16x16x32_bf16 v[76:79], v[228:231], a[164:167], v[76:79]\n"
"ds_read_b64_tr_b16 v[226:227], v6 offset:41472\n"
"s_waitcnt lgkmcnt(4)\n"
"v_mfma_f32_16x16x32_bf16 v[80:83], v[232:235], a[160:163], v[80:83]\n"
"ds_read_b64_tr_b16 v[228:229], v7 offset:40960\n"
"buffer_load_dwordx4 a[212:215], v2, s[48:51], s63 offen offset:1024 sc1\n"
"v_mfma_f32_16x16x32_bf16 v[84:87], v[232:235], a[164:167], v[84:87]\n"
"ds_read_b64_tr_b16 v[230:231], v7 offset:41472\n"
"v_mfma_f32_16x16x32_bf16 v[88:91], v[236:239], a[160:163], v[88:91]\n"
"ds_read_b64_tr_b16 v[232:233], v8 offset:40960\n"
"v_mfma_f32_16x16x32_bf16 v[92:95], v[236:239], a[164:167], v[92:95]\n"
"ds_read_b64_tr_b16 v[234:235], v8 offset:41472\n"
"s_waitcnt vmcnt(10) lgkmcnt(4)\n"
"v_mfma_f32_16x16x32_bf16 v[64:67], v[240:243], a[168:171], v[64:67]\n"
"ds_read_b64_tr_b16 v[236:237], v5 offset:49152\n"
"s_add_u32 s63, s52, 9306112\n"
"v_mfma_f32_16x16x32_bf16 v[68:71], v[240:243], a[172:175], v[68:71]\n"
"ds_read_b64_tr_b16 v[238:239], v5 offset:49664\n"
"v_mfma_f32_16x16x32_bf16 v[72:75], v[224:227], a[168:171], v[72:75]\n"
"ds_read_b64_tr_b16 v[240:241], v6 offset:49152\n"
"buffer_load_dwordx4 a[216:219], v2, s[48:51], s63 offen sc1\n"
"v_mfma_f32_16x16x32_bf16 v[76:79], v[224:227], a[172:175], v[76:79]\n"
"ds_read_b64_tr_b16 v[242:243], v6 offset:49664\n"
"s_waitcnt lgkmcnt(4)\n"
"v_mfma_f32_16x16x32_bf16 v[80:83], v[228:231], a[168:171], v[80:83]\n"
"ds_read_b64_tr_b16 v[224:225], v7 offset:49152\n"
"buffer_load_dwordx4 a[220:223], v2, s[48:51], s63 offen offset:1024 sc1\n"
"v_mfma_f32_16x16x32_bf16 v[84:87], v[228:231], a[172:175], v[84:87]\n"
"ds_read_b64_tr_b16 v[226:227], v7 offset:49664\n"
"v_mfma_f32_16x16x32_bf16 v[88:91], v[232:235], a[168:171], v[88:91]\n"
"ds_read_b64_tr_b16 v[228:229], v8 offset:49152\n"
"v_mfma_f32_16x16x32_bf16 v[92:95], v[232:235], a[172:175], v[92:95]\n"
"ds_read_b64_tr_b16 v[230:231], v8 offset:49664\n"
"s_waitcnt vmcnt(10) lgkmcnt(4)\n"
"v_mfma_f32_16x16x32_bf16 v[64:67], v[236:239], a[176:179], v[64:67]\n"
"ds_read_b64_tr_b16 v[232:233], v5 offset:57344\n"
"s_add_u32 s63, s52, 9568256\n"
"v_mfma_f32_16x16x32_bf16 v[68:71], v[236:239], a[180:183], v[68:71]\n"
"ds_read_b64_tr_b16 v[234:235], v5 offset:57856\n"
"v_mfma_f32_16x16x32_bf16 v[72:75], v[240:243], a[176:179], v[72:75]\n"
"ds_read_b64_tr_b16 v[236:237], v6 offset:57344\n"
"buffer_load_dwordx4 a[224:227], v2, s[48:51], s63 offen sc1\n"
"v_mfma_f32_16x16x32_bf16 v[76:79], v[240:243], a[180:183], v[76:79]\n"
"ds_read_b64_tr_b16 v[238:239], v6 offset:57856\n"
"s_waitcnt lgkmcnt(4)\n"
"v_mfma_f32_16x16x32_bf16 v[80:83], v[224:227], a[176:179], v[80:83]\n"
"ds_read_b64_tr_b16 v[240:241], v7 offset:57344\n"
"buffer_load_dwordx4 a[228:231], v2, s[48:51], s63 offen offset:1024 sc1\n"
"v_mfma_f32_16x16x32_bf16 v[84:87], v[224:227], a[180:183], v[84:87]\n"
"ds_read_b64_tr_b16 v[242:243], v7 offset:57856\n"
"v_mfma_f32_16x16x32_bf16 v[88:91], v[228:231], a[176:179], v[88:91]\n"
"ds_read_b64_tr_b16 v[224:225], v8 offset:57344\n"
"v_mfma_f32_16x16x32_bf16 v[92:95], v[228:231], a[180:183], v[92:95]\n"
"ds_read_b64_tr_b16 v[226:227], v8 offset:57856\n"
"s_waitcnt vmcnt(10) lgkmcnt(4)\n"
"v_mfma_f32_16x16x32_bf16 v[64:67], v[232:235], a[184:187], v[64:67]\n"
"v_add_u32 v5, 65536, v5\n"
"v_add_u32 v6, 65536, v6\n"
"v_add_u32 v7, 65536, v7\n"
"v_add_u32 v8, 65536, v8\n"
"ds_read_b64_tr_b16 v[228:229], v5\n"
"v_mfma_f32_16x16x32_bf16 v[68:71], v[232:235], a[188:191], v[68:71]\n"
"ds_read_b64_tr_b16 v[230:231], v5 offset:512\n"
"s_add_u32 s63, s52, 9830400\n"
"v_mfma_f32_16x16x32_bf16 v[72:75], v[236:239], a[184:187], v[72:75]\n"
"ds_read_b64_tr_b16 v[232:233], v6\n"
"buffer_load_dwordx4 a[232:235], v2, s[48:51], s63 offen sc1\n"
"v_mfma_f32_16x16x32_bf16 v[76:79], v[236:239], a[188:191], v[76:79]\n"
"ds_read_b64_tr_b16 v[234:235], v6 offset:512\n"
"s_waitcnt lgkmcnt(4)\n"
"v_mfma_f32_16x16x32_bf16 v[80:83], v[240:243], a[184:187], v[80:83]\n"
"ds_read_b64_tr_b16 v[236:237], v7\n"
"buffer_load_dwordx4 a[236:239], v2, s[48:51], s63 offen offset:1024 sc1\n"
"v_mfma_f32_16x16x32_bf16 v[84:87], v[240:243], a[188:191], v[84:87]\n"
"ds_read_b64_tr_b16 v[238:239], v7 offset:512\n"
"v_mfma_f32_16x16x32_bf16 v[88:91], v[224:227], a[184:187], v[88:91]\n"
"ds_read_b64_tr_b16 v[240:241], v8\n"
"v_mfma_f32_16x16x32_bf16 v[92:95], v[224:227], a[188:191], v[92:95]\n"
"ds_read_b64_tr_b16 v[242:243], v8 offset:512\n"
"s_waitcnt vmcnt(10) lgkmcnt(4)\n"
"v_mfma_f32_16x16x32_bf16 v[64:67], v[228:231], a[192:195], v[64:67]\n"
"ds_read_b64_tr_b16 v[224:225], v5 offset:8192\n"
"s_add_u32 s63, s52, 10092544\n"
"v_mfma_f32_16x16x32_bf16 v[68:71], v[228:231], a[196:199], v[68:71]\n"
"ds_read_b64_tr_b16 v[226:227], v5 offset:8704\n"
"v_mfma_f32_16x16x32_bf16 v[72:75], v[232:235], a[192:195], v[72:75]\n"
"ds_read_b64_tr_b16 v[228:229], v6 offset:8192\n"
"buffer_load_dwordx4 a[240:243], v2, s[48:51], s63 offen sc1\n"
"v_mfma_f32_16x16x32_bf16 v[76:79], v[232:235], a[196:199], v[76:79]\n"
"ds_read_b64_tr_b16 v[230:231], v6 offset:8704\n"
"s_waitcnt lgkmcnt(4)\n"
"v_mfma_f32_16x16x32_bf16 v[80:83], v[236:239], a[192:195], v[80:83]\n"
"ds_read_b64_tr_b16 v[232:233], v7 offset:8192\n"
"buffer_load_dwordx4 a[244:247], v2, s[48:51], s63 offen offset:1024 sc1\n"
"v_mfma_f32_16x16x32_bf16 v[84:87], v[236:239], a[196:199], v[84:87]\n"
"ds_read_b64_tr_b16 v[234:235], v7 offset:8704\n"
"v_mfma_f32_16x16x32_bf16 v[88:91], v[240:243], a[192:195], v[88:91]\n"
"ds_read_b64_tr_b16 v[236:237], v8 offset:8192\n"
"v_mfma_f32_16x16x32_bf16 v[92:95], v[240:243], a[196:199], v[92:95]\n"
"ds_read_b64_tr_b16 v[238:239], v8 offset:8704\n"
"s_waitcnt vmcnt(10) lgkmcnt(4)\n"
"v_mfma_f32_16x16x32_bf16 v[64:67], v[224:227], a[200:203], v[64:67]\n"
"ds_read_b64_tr_b16 v[240:241], v5 offset:16384\n"
"s_add_u32 s63, s52, 10354688\n"
"v_mfma_f32_16x16x32_bf16 v[68:71], v[224:227], a[204:207], v[68:71]\n"
"ds_read_b64_tr_b16 v[242:243], v5 offset:16896\n"
"v_mfma_f32_16x16x32_bf16 v[72:75], v[228:231], a[200:203], v[72:75]\n"
"ds_read_b64_tr_b16 v[224:225], v6 offset:16384\n"
"buffer_load_dwordx4 a[248:251], v2, s[48:51], s63 offen sc1\n"
"v_mfma_f32_16x16x32_bf16 v[76:79], v[228:231], a[204:207], v[76:79]\n"
"ds_read_b64_tr_b16 v[226:227], v6 offset:16896\n"
"s_waitcnt lgkmcnt(4)\n"
"v_mfma_f32_16x16x32_bf16 v[80:83], v[232:235], a[200:203], v[80:83]\n"
"ds_read_b64_tr_b16 v[228:229], v7 offset:16384\n"
"buffer_load_dwordx4 a[252:255], v2, s[48:51], s63 offen offset:1024 sc1\n"
"v_mfma_f32_16x16x32_bf16 v[84:87], v[232:235], a[204:207], v[84:87]\n"
"ds_read_b64_tr_b16 v[230:231], v7 offset:16896\n"
"v_mfma_f32_16x16x32_bf16 v[88:91], v[236:239], a[200:203], v[88:91]\n"
"ds_read_b64_tr_b16 v[232:233], v8 offset:16384\n"
"v_mfma_f32_16x16x32_bf16 v[92:95], v[236:239], a[204:207], v[92:95]\n"
"ds_read_b64_tr_b16 v[234:235], v8 offset:16896\n"
"s_waitcnt vmcnt(10) lgkmcnt(4)\n"
"v_mfma_f32_16x16x32_bf16 v[64:67], v[240:243], a[208:211], v[64:67]\n"
"ds_read_b64_tr_b16 v[236:237], v5 offset:24576\n"
"v_mfma_f32_16x16x32_bf16 v[68:71], v[240:243], a[212:215], v[68:71]\n"
"ds_read_b64_tr_b16 v[238:239], v5 offset:25088\n"
"v_mfma_f32_16x16x32_bf16 v[72:75], v[224:227], a[208:211], v[72:75]\n"
"ds_read_b64_tr_b16 v[240:241], v6 offset:24576\n"
"v_mfma_f32_16x16x32_bf16 v[76:79], v[224:227], a[212:215], v[76:79]\n"
"ds_read_b64_tr_b16 v[242:243], v6 offset:25088\n"
"s_waitcnt lgkmcnt(4)\n"
"v_mfma_f32_16x16x32_bf16 v[80:83], v[228:231], a[208:211], v[80:83]\n"
"ds_read_b64_tr_b16 v[224:225], v7 offset:24576\n"
"v_mfma_f32_16x16x32_bf16 v[84:87], v[228:231], a[212:215], v[84:87]\n"
"ds_read_b64_tr_b16 v[226:227], v7 offset:25088\n"
"v_mfma_f32_16x16x32_bf16 v[88:91], v[232:235], a[208:211], v[88:91]\n"
"ds_read_b64_tr_b16 v[228:229], v8 offset:24576\n"
"v_mfma_f32_16x16x32_bf16 v[92:95], v[232:235], a[212:215], v[92:95]\n"
"ds_read_b64_tr_b16 v[230:231], v8 offset:25088\n"
"s_waitcnt vmcnt(8) lgkmcnt(4)\n"
"v_mfma_f32_16x16x32_bf16 v[64:67], v[236:239], a[216:219], v[64:67]\n"
"ds_read_b64_tr_b16 v[232:233], v5 offset:32768\n"
"buffer_load_dwordx4 v2, s[48:51], s64 offen sc1 lds\n"
"v_mfma_f32_16x16x32_bf16 v[68:71], v[236:239], a[220:223], v[68:71]\n"
"ds_read_b64_tr_b16 v[234:235], v5 offset:33280\n"
"v_mfma_f32_16x16x32_bf16 v[72:75], v[240:243], a[216:219], v[72:75]\n"
"ds_read_b64_tr_b16 v[236:237], v6 offset:32768\n"
"v_mfma_f32_16x16x32_bf16 v[76:79], v[240:243], a[220:223], v[76:79]\n"
"ds_read_b64_tr_b16 v[238:239], v6 offset:33280\n"
"s_waitcnt lgkmcnt(4)\n"
"v_mfma_f32_16x16x32_bf16 v[80:83], v[224:227], a[216:219], v[80:83]\n"
"ds_read_b64_tr_b16 v[240:241], v7 offset:32768\n"
"buffer_load_dwordx4 v2, s[48:51], s64 offen offset:1024 sc1 lds\n"
"v_mfma_f32_16x16x32_bf16 v[84:87], v[224:227], a[220:223], v[84:87]\n"
"ds_read_b64_tr_b16 v[242:243], v7 offset:33280\n"
"v_mfma_f32_16x16x32_bf16 v[88:91], v[228:231], a[216:219], v[88:91]\n"
"ds_read_b64_tr_b16 v[224:225], v8 offset:32768\n"
"v_mfma_f32_16x16x32_bf16 v[92:95], v[228:231], a[220:223], v[92:95]\n"
"ds_read_b64_tr_b16 v[226:227], v8 offset:33280\n"
"s_waitcnt vmcnt(8) lgkmcnt(4)\n"
"v_mfma_f32_16x16x32_bf16 v[64:67], v[232:235], a[224:227], v[64:67]\n"
"ds_read_b64_tr_b16 v[228:229], v5 offset:40960\n"
"buffer_load_dwordx4 v2, s[48:51], s64 offen offset:2048 sc1 lds\n"
"v_mfma_f32_16x16x32_bf16 v[68:71], v[232:235], a[228:231], v[68:71]\n"
"ds_read_b64_tr_b16 v[230:231], v5 offset:41472\n"
"v_mfma_f32_16x16x32_bf16 v[72:75], v[236:239], a[224:227], v[72:75]\n"
"ds_read_b64_tr_b16 v[232:233], v6 offset:40960\n"
"v_mfma_f32_16x16x32_bf16 v[76:79], v[236:239], a[228:231], v[76:79]\n"
"ds_read_b64_tr_b16 v[234:235], v6 offset:41472\n"
"s_waitcnt lgkmcnt(4)\n"
"v_mfma_f32_16x16x32_bf16 v[80:83], v[240:243], a[224:227], v[80:83]\n"
"ds_read_b64_tr_b16 v[236:237], v7 offset:40960\n"
"buffer_load_dwordx4 v2, s[48:51], s64 offen offset:3072 sc1 lds\n"
"v_mfma_f32_16x16x32_bf16 v[84:87], v[240:243], a[228:231], v[84:87]\n"
"ds_read_b64_tr_b16 v[238:239], v7 offset:41472\n"
"s_add_u32 m0, s69, 4096\n"
"v_mfma_f32_16x16x32_bf16 v[88:91], v[224:227], a[224:227], v[88:91]\n"
"ds_read_b64_tr_b16 v[240:241], v8 offset:40960\n"
"s_add_u32 s64, s68, 30412800\n"
"v_mfma_f32_16x16x32_bf16 v[92:95], v[224:227], a[228:231], v[92:95]\n"
"ds_read_b64_tr_b16 v[242:243], v8 offset:41472\n"
"s_waitcnt vmcnt(8) lgkmcnt(4)\n"
"v_mfma_f32_16x16x32_bf16 v[64:67], v[228:231], a[232:235], v[64:67]\n"
"ds_read_b64_tr_b16 v[224:225], v5 offset:49152\n"
"buffer_load_dwordx4 v2, s[48:51], s64 offen sc1 lds\n"
"v_mfma_f32_16x16x32_bf16 v[68:71], v[228:231], a[236:239], v[68:71]\n"
"ds_read_b64_tr_b16 v[226:227], v5 offset:49664\n"
"v_mfma_f32_16x16x32_bf16 v[72:75], v[232:235], a[232:235], v[72:75]\n"
"ds_read_b64_tr_b16 v[228:229], v6 offset:49152\n"
"v_mfma_f32_16x16x32_bf16 v[76:79], v[232:235], a[236:239], v[76:79]\n"
"ds_read_b64_tr_b16 v[230:231], v6 offset:49664\n"
"s_waitcnt lgkmcnt(4)\n"
"v_mfma_f32_16x16x32_bf16 v[80:83], v[236:239], a[232:235], v[80:83]\n"
"ds_read_b64_tr_b16 v[232:233], v7 offset:49152\n"
"buffer_load_dwordx4 v2, s[48:51], s64 offen offset:1024 sc1 lds\n"
"v_mfma_f32_16x16x32_bf16 v[84:87], v[236:239], a[236:239], v[84:87]\n"
"ds_read_b64_tr_b16 v[234:235], v7 offset:49664\n"
"v_mfma_f32_16x16x32_bf16 v[88:91], v[240:243], a[232:235], v[88:91]\n"
"ds_read_b64_tr_b16 v[236:237], v8 offset:49152\n"
"v_mfma_f32_16x16x32_bf16 v[92:95], v[240:243], a[236:239], v[92:95]\n"
"ds_read_b64_tr_b16 v[238:239], v8 offset:49664\n"
"s_waitcnt vmcnt(8) lgkmcnt(4)\n"
"v_mfma_f32_16x16x32_bf16 v[64:67], v[224:227], a[240:243], v[64:67]\n"
"ds_read_b64_tr_b16 v[240:241], v5 offset:57344\n"
"buffer_load_dwordx4 v2, s[48:51], s64 offen offset:2048 sc1 lds\n"
"v_mfma_f32_16x16x32_bf16 v[68:71], v[224:227], a[244:247], v[68:71]\n"
"ds_read_b64_tr_b16 v[242:243], v5 offset:57856\n"
"v_mfma_f32_16x16x32_bf16 v[72:75], v[228:231], a[240:243], v[72:75]\n"
"ds_read_b64_tr_b16 v[224:225], v6 offset:57344\n"
"v_mfma_f32_16x16x32_bf16 v[76:79], v[228:231], a[244:247], v[76:79]\n"
"ds_read_b64_tr_b16 v[226:227], v6 offset:57856\n"
"s_waitcnt lgkmcnt(4)\n"
"v_mfma_f32_16x16x32_bf16 v[80:83], v[232:235], a[240:243], v[80:83]\n"
"ds_read_b64_tr_b16 v[228:229], v7 offset:57344\n"
"v_mfma_f32_16x16x32_bf16 v[84:87], v[232:235], a[244:247], v[84:87]\n"
"ds_read_b64_tr_b16 v[230:231], v7 offset:57856\n"
"v_mfma_f32_16x16x32_bf16 v[88:91], v[236:239], a[240:243], v[88:91]\n"
"ds_read_b64_tr_b16 v[232:233], v8 offset:57344\n"
"v_mfma_f32_16x16x32_bf16 v[92:95], v[236:239], a[244:247], v[92:95]\n"
"ds_read_b64_tr_b16 v[234:235], v8 offset:57856\n"
"s_waitcnt vmcnt(7) lgkmcnt(4)\n"
"v_mfma_f32_16x16x32_bf16 v[64:67], v[240:243], a[248:251], v[64:67]\n"
"v_mfma_f32_16x16x32_bf16 v[68:71], v[240:243], a[252:255], v[68:71]\n"
"v_mfma_f32_16x16x32_bf16 v[72:75], v[224:227], a[248:251], v[72:75]\n"
"v_mfma_f32_16x16x32_bf16 v[76:79], v[224:227], a[252:255], v[76:79]\n"
"s_waitcnt lgkmcnt(2)\n"
"v_mfma_f32_16x16x32_bf16 v[80:83], v[228:231], a[248:251], v[80:83]\n"
"v_mfma_f32_16x16x32_bf16 v[84:87], v[228:231], a[252:255], v[84:87]\n"
"s_waitcnt lgkmcnt(0)\n"
"v_mfma_f32_16x16x32_bf16 v[88:91], v[232:235], a[248:251], v[88:91]\n"
"v_mfma_f32_16x16x32_bf16 v[92:95], v[232:235], a[252:255], v[92:95]\n"
"s_barrier\n"
"s_nop 0\n"
"v_cvt_pk_bf16_f32 v16, v64, v65\n"
"v_cvt_pk_bf16_f32 v17, v66, v67\n"
"v_cvt_pk_bf16_f32 v18, v72, v73\n"
"v_cvt_pk_bf16_f32 v19, v74, v75\n"
"buffer_store_dwordx4 v[16:19], v2, s[48:51], s57 offen sc1\n"
"s_nop 0\n"
"v_mfma_f32_16x16x32_bf16 v[32:35], v[48:51], v[16:19], 0\n"
"v_mfma_f32_16x16x32_bf16 v[36:39], v[16:19], v[16:19], 0\n"
"v_cvt_pk_bf16_f32 v24, v68, v69\n"
"v_cvt_pk_bf16_f32 v25, v70, v71\n"
"v_cvt_pk_bf16_f32 v26, v76, v77\n"
"v_cvt_pk_bf16_f32 v27, v78, v79\n"
"buffer_store_dwordx4 v[24:27], v2, s[48:51], s57 offen offset:1024 sc1\n"
"s_nop 0\n"
"v_mfma_f32_16x16x32_bf16 v[40:43], v[48:51], v[24:27], 0\n"
"v_mfma_f32_16x16x32_bf16 v[44:47], v[24:27], v[24:27], 0\n"
"v_cvt_pk_bf16_f32 v20, v80, v81\n"
"v_cvt_pk_bf16_f32 v21, v82, v83\n"
"v_cvt_pk_bf16_f32 v22, v88, v89\n"
"v_cvt_pk_bf16_f32 v23, v90, v91\n"
"buffer_store_dwordx4 v[20:23], v2, s[48:51], s58 offen sc1\n"
"s_nop 0\n"
"v_mfma_f32_16x16x32_bf16 v[32:35], v[48:51], v[20:23], v[32:35]\n"
"v_mfma_f32_16x16x32_bf16 v[36:39], v[20:23], v[20:23], v[36:39]\n"
"v_cvt_pk_bf16_f32 v28, v84, v85\n"
"v_cvt_pk_bf16_f32 v29, v86, v87\n"
"v_cvt_pk_bf16_f32 v30, v92, v93\n"
"v_cvt_pk_bf16_f32 v31, v94, v95\n"
"buffer_store_dwordx4 v[28:31], v2, s[48:51], s58 offen offset:1024 sc1\n"
"s_nop 0\n"
"v_mfma_f32_16x16x32_bf16 v[40:43], v[48:51], v[28:31], v[40:43]\n"
"v_mfma_f32_16x16x32_bf16 v[44:47], v[28:31], v[28:31], v[44:47]\n"
"v_cndmask_b32 v33, v36, v37, s[72:73]\n"
"v_cndmask_b32 v33, v33, v38, s[74:75]\n"
"v_cndmask_b32 v33, v33, v39, s[76:77]\n"
"s_nop 3\n"
"v_mov_b32 v34, v40\n"
"v_cndmask_b32 v35, v44, v45, s[72:73]\n"
"v_cndmask_b32 v35, v35, v46, s[74:75]\n"
"v_cndmask_b32 v35, v35, v47, s[76:77]\n"
"s_mov_b64 exec, s[78:79]\n"
"buffer_store_dwordx4 v[32:35], v52, s[48:51], s59 offen sc1\n"
"s_mov_b64 exec, -1\n"
"s_lshl_b32 s24, s4, 5\n"
"s_add_u32 s24, s24, 524288\n"
"s_lshl_b32 s25, s23, 5\n"
"s_add_u32 s25, s25, 524288\n"
"s_and_b32 s26, s81, 0x1ffff\n"
"s_lshr_b32 s60, s20, 3\n"
"s_lshl_b32 s60, s60, 17\n"
"s_add_u32 s26, s26, s60\n"
"s_and_b32 s60, s20, 1\n"
"s_lshl_b32 s60, s60, 10\n"
"s_add_u32 s26, s26, s60\n"
"v_lshlrev_b32 v14, 16, v14\n"
"s_add_u32 m0, s69, 28672\n"
"s_add_u32 s64, s68, 30867456\n"
"s_bfe_u32 s60, s20, 0x20001\n"
"s_mul_i32 s60, s60, 1892352\n"
"s_add_u32 s26, s26, s60\n"
"s_lshl2_add_u32 s27, s21, s7\n"
"s_and_b32 s28, s27, 31\n"
"s_lshl_b32 s28, s28, 7\n"
"v_lshlrev_b32 v15, 16, v15\n"
"buffer_load_dwordx4 v2, s[48:51], s64 offen sc1 lds\n"
"s_lshr_b32 s91, s21, 1\n"
"s_mul_i32 s91, s91, 7\n"
"s_and_b32 s60, s21, 1\n"
"s_add_u32 s92, s91, 3\n"
"s_mul_i32 s92, s92, 131072\n"
"s_lshl2_add_u32 s91, s60, s91\n"
"s_lshl_b32 s60, s60, 9\n"
"s_add_u32 s92, s92, s60\n"
"s_add_u32 s92, s92, s22\n"
"s_add_u32 s92, s92, 14680064\n"
"v_and_b32 v220, 3, v1\n"
"buffer_load_dwordx4 v2, s[48:51], s64 offen offset:1024 sc1 lds\n"
"s_mul_i32 s91, s91, 131072\n"
"s_add_u32 s91, s91, s22\n"
"s_lshr_b32 s60, s9, 3\n"
"s_mul_i32 s29, s60, 3670016\n"
"s_add_u32 s29, s29, s52\n"
"s_mul_i32 s30, s5, 896\n"
"s_mul_i32 s31, s4, 896\n"
"s_lshl_b32 s32, s88, 9\n"
"s_lshl_b32 s33, s53, 9\n"
"s_lshr_b32 s38, s6, 3\n"
"s_lshl2_add_u32 s38, s38, s7\n"
"s_mul_i32 s38, s38, 14336\n"
"s_mul_i32 s39, s60, 1835008\n"
"s_add_u32 s38, s38, s39\n"
"s_sub_u32 s39, 0x180, s40\n"
"s_and_b32 s39, s39, 0x380\n"
"s_add_u32 s38, s38, s39\n"
"v_lshrrev_b32 v251, 1, v1\n"
"buffer_load_dwordx4 v2, s[48:51], s64 offen offset:2048 sc1 lds\n"
"v_min_u32 v251, 13, v251\n"
"v_lshlrev_b32 v251, 10, v251\n"
"v_and_b32 v250, 1, v1\n"
"v_lshl_add_u32 v251, v250, 6, v251\n"
"s_lshl_b32 s2, s28, 1\n"
"s_lshl_b32 s35, s60, 13\n"
"s_xor_b32 s35, s35, 8192\n"
"s_add_u32 s35, s35, s2\n"
"s_lshr_b32 s36, s20, 3\n"
"s_lshl_b32 s36, s36, 13\n"
"s_add_u32 s2, s2, s36\n"
"s_lshl_b32 s60, s60, 8\n"
"s_lshl2_add_u32 s36, s23, s60\n"
"s_lshl2_add_u32 s37, s54, s60\n"
"s_lshl_b32 s16, s5, 5\n"
"s_lshl_b32 s17, s4, 5\n"
"s_lshl_b32 s18, s88, 5\n"
"s_lshl_b32 s19, s53, 5\n"
"s_lshl_b32 s34, s52, 5\n"
"s_add_u32 s34, s34, s56\n"
"v_lshlrev_b32 v220, 8, v220\n"
"buffer_load_dwordx4 v2, s[48:51], s64 offen offset:3072 sc1 lds\n"
"v_and_b32 v248, 60, v1\n"
"s_add_u32 m0, s69, 32768\n"
"s_add_u32 s64, s68, 30871552\n"
"v_lshl_add_u32 v220, v248, 2, v220\n"
"v_lshlrev_b32 v248, 2, v1\n"
"v_bfe_u32 v249, v1, 4, 1\n"
"v_lshlrev_b32 v249, 10, v249\n"
"buffer_load_dwordx4 v2, s[48:51], s64 offen sc1 lds\n"
"v_lshrrev_b32 v250, 5, v1\n"
"v_lshl_add_u32 v249, v250, 8, v249\n"
"v_lshl_add_u32 v249, v3, 4, v249\n"
"buffer_load_dwordx4 v2, s[48:51], s64 offen offset:1024 sc1 lds\n"
"buffer_load_dwordx4 v2, s[48:51], s64 offen offset:2048 sc1 lds\n"
"s_waitcnt vmcnt(7)\n"
"s_add_u32 s62, s0, 1605632\n"
"s_mov_b64 exec, 2\n"
"buffer_atomic_add v1, off, s[48:51], s62 sc1\n"
"s_mov_b64 exec, -1\n"
"s_add_u32 m0, s69, 57344\n"
"s_add_u32 s64, s68, 31326208\n"
"buffer_load_dwordx4 v2, s[48:51], s64 offen sc1 lds\n"
"buffer_load_dwordx4 v2, s[48:51], s64 offen offset:1024 sc1 lds\n"
"buffer_load_dwordx4 v2, s[48:51], s64 offen offset:2048 sc1 lds\n"
"buffer_load_dwordx4 v2, s[48:51], s64 offen offset:3072 sc1 lds\n"
"s_add_u32 m0, s69, 61440\n"
"s_add_u32 s64, s68, 31330304\n"
"buffer_load_dwordx4 v2, s[48:51], s64 offen sc1 lds\n"
"buffer_load_dwordx4 v2, s[48:51], s64 offen offset:1024 sc1 lds\n"
"buffer_load_dwordx4 v2, s[48:51], s64 offen offset:2048 sc1 lds\n"
"s_add_u32 s62, s1, 1605632\n"
"v_mov_b32 v164, 0\n"
"s_mov_b64 exec, 1\n"
"buffer_atomic_or v164, off, s[48:51], s62 sc0 sc1\n"
"s_mov_b64 exec, -1\n"
"s_mov_b64 exec, 0\n"
"s_cmp_eq_u32 s7, 0\n"
"s_cbranch_scc1 LPG10_%=\n"
"s_cmp_eq_u32 s7, 1\n"
"s_cbranch_scc1 LPG11_%=\n"
"s_cmp_eq_u32 s7, 2\n"
"s_cbranch_scc1 LPG12_%=\n"
"s_branch LPG13_%=\n"
"LWRc_%=:\n"
"s_mov_b64 exec, -1\n"
"s_mov_b64 exec, 1\n"
"s_waitcnt vmcnt(0)\n"
"v_cmp_ne_u32 vcc, 16, v164\n"
"s_mov_b64 exec, -1\n"
"s_nop 3\n"
"s_cbranch_vccz Ld16_%=\n"
"s_mov_b32 s65, 0\n"
"Lp15_%=:\n"
"s_add_u32 s62, s1, 1605632\n"
"v_mov_b32 v164, 0\n"
"s_mov_b64 exec, 1\n"
"buffer_atomic_or v164, off, s[48:51], s62 sc0 sc1\n"
"s_mov_b64 exec, -1\n"
"s_add_u32 s65, s65, 1\n"
"s_cmp_gt_u32 s65, 131072\n"
"s_waitcnt vmcnt(0)\n"
"s_cbranch_scc1 Ld16_%=\n"
"s_mov_b64 exec, 1\n"
"v_cmp_ne_u32 vcc, 16, v164\n"
"s_mov_b64 exec, -1\n"
"s_nop 3\n"
"s_cbranch_vccnz Lp15_%=\n"
"Ld16_%=:\n"
"s_add_u32 s63, s22, 10485760\n"
"buffer_load_dwordx4 a[0:3], v2, s[48:51], s63 offen sc1\n"
"buffer_load_dwordx4 a[4:7], v2, s[48:51], s63 offen offset:1024 sc1\n"
"s_add_u32 s63, s22, 10616832\n"
"buffer_load_dwordx4 a[8:11], v2, s[48:51], s63 offen sc1\n"
"buffer_load_dwordx4 a[12:15], v2, s[48:51], s63 offen offset:1024 sc1\n"
"s_add_u32 s63, s22, 10747904\n"
"buffer_load_dwordx4 a[16:19], v2, s[48:51], s63 offen sc1\n"
"buffer_load_dwordx4 a[20:23], v2, s[48:51], s63 offen offset:1024 sc1\n"
"s_add_u32 s63, s22, 10878976\n"
"buffer_load_dwordx4 a[24:27], v2, s[48:51], s63 offen sc1\n"
"buffer_load_dwordx4 a[28:31], v2, s[48:51], s63 offen offset:1024 sc1\n"
"s_add_u32 s63, s22, 11010048\n"
"buffer_load_dwordx4 a[32:35], v2, s[48:51], s63 offen sc1\n"
"buffer_load_dwordx4 a[36:39], v2, s[48:51], s63 offen offset:1024 sc1\n"
"s_lshl_b32 s57, s23, 5\n"
"s_add_u32 s57, s57, 262144\n"
"s_barrier\n"
"ds_read_b128 v[224:227], v2\n"
"ds_read_b128 v[228:231], v2 offset:7168\n"
"ds_read_b128 v[232:235], v2 offset:1024\n"
"ds_read_b128 v[236:239], v2 offset:8192\n"
"ds_read_b128 v[240:243], v2 offset:2048\n"
"LZ0_%=:\n"
"v_add_u32 v11, LZ1_%=-LPC_%=, v0\n"
"s_add_u32 s63, s22, 11141120\n"
"s_add_u32 m0, s69, 86016\n"
"v_min_u32 v11, LZ3_%=-LPC_%=-4, v11\n"
"buffer_load_dwordx4 a[40:43], v2, s[48:51], s63 offen sc1\n"
"global_load_dword v11, v11, s[70:71]\n"
"s_waitcnt vmcnt(11) lgkmcnt(4)\n"
"v_mfma_f32_16x16x32_bf16 v[32:35], a[0:3], v[224:227], 0\n"
"ds_read_b128 v[244:247], v2 offset:9216\n"
"s_add_u32 s64, s68, 31784960\n"
"s_waitcnt vmcnt(9) lgkmcnt(4)\n"
"v_mfma_f32_16x16x32_bf16 v[32:35], a[8:11], v[228:231], v[32:35]\n"
"v_mfma_f32_16x16x32_bf16 v[36:39], a[4:7], v[224:227], 0\n"
"ds_read_b128 v[224:227], v2 offset:3072\n"
"s_waitcnt vmcnt(8)\n"
"v_mfma_f32_16x16x32_bf16 v[36:39], a[12:15], v[228:231], v[36:39]\n"
"s_waitcnt lgkmcnt(3)\n"
"buffer_load_dwordx4 a[44:47], v2, s[48:51], s63 offen offset:1024 sc1\n"
"v_mfma_f32_16x16x32_bf16 v[40:43], a[0:3], v[232:235], 0\n"
"ds_read_b128 v[228:231], v2 offset:10240\n"
"s_add_u32 s63, s22, 11272192\n"
"v_mfma_f32_16x16x32_bf16 v[40:43], a[8:11], v[236:239], v[40:43]\n"
"v_mfma_f32_16x16x32_bf16 v[44:47], a[4:7], v[232:235], 0\n"
"ds_read_b128 v[232:235], v2 offset:4096\n"
"v_mfma_f32_16x16x32_bf16 v[44:47], a[12:15], v[236:239], v[44:47]\n"
"s_waitcnt lgkmcnt(3)\n"
"buffer_load_dwordx4 a[48:51], v2, s[48:51], s63 offen sc1\n"
"v_mfma_f32_16x16x32_bf16 v[48:51], a[0:3], v[240:243], 0\n"
"ds_read_b128 v[236:239], v2 offset:11264\n"
"v_mfma_f32_16x16x32_bf16 v[48:51], a[8:11], v[244:247], v[48:51]\n"
"v_mfma_f32_16x16x32_bf16 v[52:55], a[4:7], v[240:243], 0\n"
"ds_read_b128 v[240:243], v2 offset:5120\n"
"v_mfma_f32_16x16x32_bf16 v[52:55], a[12:15], v[244:247], v[52:55]\n"
"s_waitcnt lgkmcnt(3)\n"
"buffer_load_dwordx4 a[52:55], v2, s[48:51], s63 offen offset:1024 sc1\n"
"v_mfma_f32_16x16x32_bf16 v[56:59], a[0:3], v[224:227], 0\n"
"ds_read_b128 v[244:247], v2 offset:12288\n"
"s_add_u32 s63, s22, 11403264\n"
"v_mfma_f32_16x16x32_bf16 v[56:59], a[8:11], v[228:231], v[56:59]\n"
"v_mfma_f32_16x16x32_bf16 v[60:63], a[4:7], v[224:227], 0\n"
"ds_read_b128 v[224:227], v2 offset:6144\n"
"v_mfma_f32_16x16x32_bf16 v[60:63], a[12:15], v[228:231], v[60:63]\n"
"s_waitcnt lgkmcnt(3)\n"
"buffer_load_dwordx4 a[56:59], v2, s[48:51], s63 offen sc1\n"
"v_mfma_f32_16x16x32_bf16 v[64:67], a[0:3], v[232:235], 0\n"
"ds_read_b128 v[228:231], v2 offset:13312\n"
"v_mfma_f32_16x16x32_bf16 v[64:67], a[8:11], v[236:239], v[64:67]\n"
"v_mfma_f32_16x16x32_bf16 v[68:71], a[4:7], v[232:235], 0\n"
"ds_read_b128 v[232:235], v2 offset:14336\n"
"v_mfma_f32_16x16x32_bf16 v[68:71], a[12:15], v[236:239], v[68:71]\n"
"s_waitcnt lgkmcnt(3)\n"
"buffer_load_dwordx4 a[60:63], v2, s[48:51], s63 offen offset:1024 sc1\n"
"v_mfma_f32_16x16x32_bf16 v[72:75], a[0:3], v[240:243], 0\n"
"ds_read_b128 v[236:239], v2 offset:21504\n"
"s_add_u32 s63, s22, 11534336\n"
"v_mfma_f32_16x16x32_bf16 v[72:75], a[8:11], v[244:247], v[72:75]\n"
"v_mfma_f32_16x16x32_bf16 v[76:79], a[4:7], v[240:243], 0\n"
"ds_read_b128 v[240:243], v2 offset:15360\n"
"v_mfma_f32_16x16x32_bf16 v[76:79], a[12:15], v[244:247], v[76:79]\n"
"s_waitcnt lgkmcnt(3)\n"
"v_mfma_f32_16x16x32_bf16 v[80:83], a[0:3], v[224:227], 0\n"
"LPG10_%=:\n"
"s_cbranch_execz LPG14_%=\n"
"ds_read_b128 v[244:247], v2 offset:22528\n"
"v_mfma_f32_16x16x32_bf16 v[80:83], a[8:11], v[228:231], v[80:83]\n"
"v_mfma_f32_16x16x32_bf16 v[84:87], a[4:7], v[224:227], 0\n"
"ds_read_b128 v[224:227], v2 offset:16384\n"
"v_mfma_f32_16x16x32_bf16 v[84:87], a[12:15], v[228:231], v[84:87]\n"
"s_waitcnt vmcnt(9) lgkmcnt(3)\n"
"v_mfma_f32_16x16x32_bf16 v[32:35], a[16:19], v[232:235], v[32:35]\n"
"ds_read_b128 v[228:231], v2 offset:23552\n"
"v_mfma_f32_16x16x32_bf16 v[32:35], a[24:27], v[236:239], v[32:35]\n"
"buffer_load_dwordx4 a[64:67], v2, s[48:51], s63 offen sc1\n"
"v_mfma_f32_16x16x32_bf16 v[36:39], a[20:23], v[232:235], v[36:39]\n"
"ds_read_b128 v[232:235], v2 offset:17408\n"
"v_mfma_f32_16x16x32_bf16 v[36:39], a[28:31], v[236:239], v[36:39]\n"
"s_waitcnt lgkmcnt(3)\n"
"v_mfma_f32_16x16x32_bf16 v[40:43], a[16:19], v[240:243], v[40:43]\n"
"ds_read_b128 v[236:239], v2 offset:24576\n"
"v_mfma_f32_16x16x32_bf16 v[40:43], a[24:27], v[244:247], v[40:43]\n"
"buffer_load_dwordx4 a[68:71], v2, s[48:51], s63 offen offset:1024 sc1\n"
"v_mfma_f32_16x16x32_bf16 v[44:47], a[20:23], v[240:243], v[44:47]\n"
"ds_read_b128 v[240:243], v2 offset:18432\n"
"s_add_u32 s63, s22, 11665408\n"
"v_mfma_f32_16x16x32_bf16 v[44:47], a[28:31], v[244:247], v[44:47]\n"
"s_waitcnt lgkmcnt(3)\n"
"v_mfma_f32_16x16x32_bf16 v[48:51], a[16:19], v[224:227], v[48:51]\n"
"ds_read_b128 v[244:247], v2 offset:25600\n"
"v_mfma_f32_16x16x32_bf16 v[48:51], a[24:27], v[228:231], v[48:51]\n"
"buffer_load_dwordx4 a[72:75], v2, s[48:51], s63 offen sc1\n"
"v_mfma_f32_16x16x32_bf16 v[52:55], a[20:23], v[224:227], v[52:55]\n"
"ds_read_b128 v[224:227], v2 offset:19456\n"
"v_mfma_f32_16x16x32_bf16 v[52:55], a[28:31], v[228:231], v[52:55]\n"
"s_waitcnt lgkmcnt(3)\n"
"v_mfma_f32_16x16x32_bf16 v[56:59], a[16:19], v[232:235], v[56:59]\n"
"ds_read_b128 v[228:231], v2 offset:26624\n"
"v_mfma_f32_16x16x32_bf16 v[56:59], a[24:27], v[236:239], v[56:59]\n"
"v_mfma_f32_16x16x32_bf16 v[60:63], a[20:23], v[232:235], v[60:63]\n"
"ds_read_b128 v[232:235], v2 offset:20480\n"
"v_mfma_f32_16x16x32_bf16 v[60:63], a[28:31], v[236:239], v[60:63]\n"
"s_waitcnt lgkmcnt(3)\n"
"buffer_load_dwordx4 a[76:79], v2, s[48:51], s63 offen offset:1024 sc1\n"
"v_mfma_f32_16x16x32_bf16 v[64:67], a[16:19], v[240:243], v[64:67]\n"
"ds_read_b128 v[236:239], v2 offset:27648\n"
"v_mfma_f32_16x16x32_bf16 v[64:67], a[24:27], v[244:247], v[64:67]\n"
"v_mfma_f32_16x16x32_bf16 v[68:71], a[20:23], v[240:243], v[68:71]\n"
"s_barrier\n"
"ds_read_b128 v[240:243], v2 offset:28672\n"
"v_mfma_f32_16x16x32_bf16 v[68:71], a[28:31], v[244:247], v[68:71]\n"
"s_waitcnt lgkmcnt(3)\n"
"buffer_load_dwordx4 v[164:167], v2, s[48:51], s57 offen sc1\n"
"v_mfma_f32_16x16x32_bf16 v[72:75], a[16:19], v[224:227], v[72:75]\n"
"ds_read_b128 v[244:247], v2 offset:35840\n"
"v_mfma_f32_16x16x32_bf16 v[72:75], a[24:27], v[228:231], v[72:75]\n"
"v_mfma_f32_16x16x32_bf16 v[76:79], a[20:23], v[224:227], v[76:79]\n"
"ds_read_b128 v[224:227], v2 offset:29696\n"
"v_mfma_f32_16x16x32_bf16 v[76:79], a[28:31], v[228:231], v[76:79]\n"
"s_waitcnt lgkmcnt(3)\n"
"buffer_load_dwordx4 v[168:171], v2, s[48:51], s57 offen offset:1024 sc1\n"
"v_mfma_f32_16x16x32_bf16 v[80:83], a[16:19], v[232:235], v[80:83]\n"
"ds_read_b128 v[228:231], v2 offset:36864\n"
"v_mfma_f32_16x16x32_bf16 v[80:83], a[24:27], v[236:239], v[80:83]\n"
"v_mfma_f32_16x16x32_bf16 v[84:87], a[20:23], v[232:235], v[84:87]\n"
"ds_read_b128 v[232:235], v2 offset:30720\n"
"v_mfma_f32_16x16x32_bf16 v[84:87], a[28:31], v[236:239], v[84:87]\n"
"s_waitcnt vmcnt(10) lgkmcnt(3)\n"
"buffer_load_dwordx4 v[172:175], v2, s[48:51], s57 offen offset:2048 sc1\n"
"v_mfma_f32_16x16x32_bf16 v[32:35], a[32:35], v[240:243], v[32:35]\n"
"ds_read_b128 v[236:239], v2 offset:37888\n"
"v_mfma_f32_16x16x32_bf16 v[32:35], a[40:43], v[244:247], v[32:35]\n"
"buffer_load_dwordx4 v[176:179], v2, s[48:51], s57 offen offset:3072 sc1\n"
"v_mfma_f32_16x16x32_bf16 v[36:39], a[36:39], v[240:243], v[36:39]\n"
"ds_read_b128 v[240:243], v2 offset:31744\n"
"s_add_u32 s63, s22, 11796480\n"
"v_mfma_f32_16x16x32_bf16 v[36:39], a[44:47], v[244:247], v[36:39]\n"
"s_waitcnt lgkmcnt(3)\n"
"v_mfma_f32_16x16x32_bf16 v[40:43], a[32:35], v[224:227], v[40:43]\n"
"ds_read_b128 v[244:247], v2 offset:38912\n"
"v_mfma_f32_16x16x32_bf16 v[40:43], a[40:43], v[228:231], v[40:43]\n"
"buffer_load_dwordx4 v2, s[48:51], s64 offen sc1 lds\n"
"v_mfma_f32_16x16x32_bf16 v[44:47], a[36:39], v[224:227], v[44:47]\n"
"ds_read_b128 v[224:227], v2 offset:32768\n"
"v_mfma_f32_16x16x32_bf16 v[44:47], a[44:47], v[228:231], v[44:47]\n"
"s_waitcnt lgkmcnt(3)\n"
"v_mfma_f32_16x16x32_bf16 v[48:51], a[32:35], v[232:235], v[48:51]\n"
"ds_read_b128 v[228:231], v2 offset:39936\n"
"v_mfma_f32_16x16x32_bf16 v[48:51], a[40:43], v[236:239], v[48:51]\n"
"buffer_load_dwordx4 a[80:83], v2, s[48:51], s63 offen sc1\n"
"v_mfma_f32_16x16x32_bf16 v[52:55], a[36:39], v[232:235], v[52:55]\n"
"ds_read_b128 v[232:235], v2 offset:33792\n"
"v_mfma_f32_16x16x32_bf16 v[52:55], a[44:47], v[236:239], v[52:55]\n"
"s_waitcnt lgkmcnt(3)\n"
"v_mfma_f32_16x16x32_bf16 v[56:59], a[32:35], v[240:243], v[56:59]\n"
"ds_read_b128 v[236:239], v2 offset:40960\n"
"v_mfma_f32_16x16x32_bf16 v[56:59], a[40:43], v[244:247], v[56:59]\n"
"buffer_load_dwordx4 a[84:87], v2, s[48:51], s63 offen offset:1024 sc1\n"
"v_mfma_f32_16x16x32_bf16 v[60:63], a[36:39], v[240:243], v[60:63]\n"
"ds_read_b128 v[240:243], v2 offset:34816\n"
"s_add_u32 s63, s22, 11927552\n"
"v_mfma_f32_16x16x32_bf16 v[60:63], a[44:47], v[244:247], v[60:63]\n"
"s_waitcnt lgkmcnt(3)\n"
"v_mfma_f32_16x16x32_bf16 v[64:67], a[32:35], v[224:227], v[64:67]\n"
"ds_read_b128 v[244:247], v2 offset:41984\n"
"v_mfma_f32_16x16x32_bf16 v[64:67], a[40:43], v[228:231], v[64:67]\n"
"buffer_load_dwordx4 v2, s[48:51], s64 offen offset:1024 sc1 lds\n"
"v_mfma_f32_16x16x32_bf16 v[68:71], a[36:39], v[224:227], v[68:71]\n"
"ds_read_b128 v[224:227], v2 offset:43008\n"
"v_mfma_f32_16x16x32_bf16 v[68:71], a[44:47], v[228:231], v[68:71]\n"
"s_waitcnt lgkmcnt(3)\n"
"v_mfma_f32_16x16x32_bf16 v[72:75], a[32:35], v[232:235], v[72:75]\n"
"ds_read_b128 v[228:231], v2 offset:50176\n"
"v_mfma_f32_16x16x32_bf16 v[72:75], a[40:43], v[236:239], v[72:75]\n"
"buffer_load_dwordx4 v2, s[48:51], s64 offen offset:2048 sc1 lds\n"
"v_mfma_f32_16x16x32_bf16 v[76:79], a[36:39], v[232:235], v[76:79]\n"
"ds_read_b128 v[232:235], v2 offset:44032\n"
"v_mfma_f32_16x16x32_bf16 v[76:79], a[44:47], v[236:239], v[76:79]\n"
"s_waitcnt lgkmcnt(3)\n"
"v_mfma_f32_16x16x32_bf16 v[80:83], a[32:35], v[240:243], v[80:83]\n"
"ds_read_b128 v[236:239], v2 offset:51200\n"
"v_mfma_f32_16x16x32_bf16 v[80:83], a[40:43], v[244:247], v[80:83]\n"
"buffer_load_dwordx4 v2, s[48:51], s64 offen offset:3072 sc1 lds\n"
"v_mfma_f32_16x16x32_bf16 v[84:87], a[36:39], v[240:243], v[84:87]\n"
"ds_read_b128 v[240:243], v2 offset:45056\n"
"s_add_u32 m0, s69, 90112\n"
"v_mfma_f32_16x16x32_bf16 v[84:87], a[44:47], v[244:247], v[84:87]\n"
"s_waitcnt vmcnt(14) lgkmcnt(3)\n"
"buffer_load_dwordx4 a[88:91], v2, s[48:51], s63 offen sc1\n"
"v_mfma_f32_16x16x32_bf16 v[32:35], a[48:51], v[224:227], v[32:35]\n"
"ds_read_b128 v[244:247], v2 offset:52224\n"
"s_add_u32 s64, s68, 31789056\n"
"v_mfma_f32_16x16x32_bf16 v[32:35], a[56:59], v[228:231], v[32:35]\n"
"buffer_load_dwordx4 a[92:95], v2, s[48:51], s63 offen offset:1024 sc1\n"
"v_mfma_f32_16x16x32_bf16 v[36:39], a[52:55], v[224:227], v[36:39]\n"
"ds_read_b128 v[224:227], v2 offset:46080\n"
"s_add_u32 s63, s22, 12058624\n"
"v_mfma_f32_16x16x32_bf16 v[36:39], a[60:63], v[228:231], v[36:39]\n"
"s_waitcnt lgkmcnt(3)\n"
"v_mfma_f32_16x16x32_bf16 v[40:43], a[48:51], v[232:235], v[40:43]\n"
"ds_read_b128 v[228:231], v2 offset:53248\n"
"v_mfma_f32_16x16x32_bf16 v[40:43], a[56:59], v[236:239], v[40:43]\n"
"buffer_load_dwordx4 v2, s[48:51], s64 offen sc1 lds\n"
"v_mfma_f32_16x16x32_bf16 v[44:47], a[52:55], v[232:235], v[44:47]\n"
"ds_read_b128 v[232:235], v2 offset:47104\n"
"v_mfma_f32_16x16x32_bf16 v[44:47], a[60:63], v[236:239], v[44:47]\n"
"s_waitcnt lgkmcnt(3)\n"
"v_mfma_f32_16x16x32_bf16 v[48:51], a[48:51], v[240:243], v[48:51]\n"
"ds_read_b128 v[236:239], v2 offset:54272\n"
"v_mfma_f32_16x16x32_bf16 v[48:51], a[56:59], v[244:247], v[48:51]\n"
"buffer_load_dwordx4 v2, s[48:51], s64 offen offset:1024 sc1 lds\n"
"v_mfma_f32_16x16x32_bf16 v[52:55], a[52:55], v[240:243], v[52:55]\n"
"ds_read_b128 v[240:243], v2 offset:48128\n"
"v_mfma_f32_16x16x32_bf16 v[52:55], a[60:63], v[244:247], v[52:55]\n"
"s_waitcnt lgkmcnt(3)\n"
"v_mfma_f32_16x16x32_bf16 v[56:59], a[48:51], v[224:227], v[56:59]\n"
"ds_read_b128 v[244:247], v2 offset:55296\n"
"v_mfma_f32_16x16x32_bf16 v[56:59], a[56:59], v[228:231], v[56:59]\n"
"buffer_load_dwordx4 v2, s[48:51], s64 offen offset:2048 sc1 lds\n"
"v_mfma_f32_16x16x32_bf16 v[60:63], a[52:55], v[224:227], v[60:63]\n"
"ds_read_b128 v[224:227], v2 offset:49152\n"
"s_add_u32 m0, s69, 114688\n"
"v_mfma_f32_16x16x32_bf16 v[60:63], a[60:63], v[228:231], v[60:63]\n"
"s_waitcnt lgkmcnt(3)\n"
"buffer_load_dwordx4 a[96:99], v2, s[48:51], s63 offen sc1\n"
"v_mfma_f32_16x16x32_bf16 v[64:67], a[48:51], v[232:235], v[64:67]\n"
"ds_read_b128 v[228:231], v2 offset:56320\n"
"s_add_u32 s64, s68, 32243712\n"
"v_mfma_f32_16x16x32_bf16 v[64:67], a[56:59], v[236:239], v[64:67]\n"
"buffer_load_dwordx4 a[100:103], v2, s[48:51], s63 offen offset:1024 sc1\n"
"v_mfma_f32_16x16x32_bf16 v[68:71], a[52:55], v[232:235], v[68:71]\n"
"s_barrier\n"
"ds_read_b128 v[232:235], v221\n"
"v_mfma_f32_16x16x32_bf16 v[68:71], a[60:63], v[236:239], v[68:71]\n"
"s_waitcnt lgkmcnt(3)\n"
"s_add_u32 s63, s22, 12189696\n"
"v_mfma_f32_16x16x32_bf16 v[72:75], a[48:51], v[240:243], v[72:75]\n"
"ds_read_b128 v[236:239], v221 offset:7168\n"
"v_mfma_f32_16x16x32_bf16 v[72:75], a[56:59], v[244:247], v[72:75]\n"
"buffer_load_dwordx4 a[104:107], v2, s[48:51], s63 offen sc1\n"
"v_mfma_f32_16x16x32_bf16 v[76:79], a[52:55], v[240:243], v[76:79]\n"
"ds_read_b128 v[240:243], v221 offset:1024\n"
"v_mfma_f32_16x16x32_bf16 v[76:79], a[60:63], v[244:247], v[76:79]\n"
"s_waitcnt lgkmcnt(3)\n"
"v_mfma_f32_16x16x32_bf16 v[80:83], a[48:51], v[224:227], v[80:83]\n"
"ds_read_b128 v[244:247], v221 offset:8192\n"
"v_mfma_f32_16x16x32_bf16 v[80:83], a[56:59], v[228:231], v[80:83]\n"
"buffer_load_dwordx4 a[108:111], v2, s[48:51], s63 offen offset:1024 sc1\n"
"v_mfma_f32_16x16x32_bf16 v[84:87], a[52:55], v[224:227], v[84:87]\n"
"ds_read_b128 v[224:227], v221 offset:2048\n"
"s_add_u32 s63, s22, 12320768\n"
"v_mfma_f32_16x16x32_bf16 v[84:87], a[60:63], v[228:231], v[84:87]\n"
"s_waitcnt vmcnt(19) lgkmcnt(3)\n"
"v_mfma_f32_16x16x32_bf16 v[32:35], a[64:67], v[232:235], v[32:35]\n"
"ds_read_b128 v[228:231], v221 offset:9216\n"
"v_mfma_f32_16x16x32_bf16 v[32:35], a[72:75], v[236:239], v[32:35]\n"
"buffer_load_dwordx4 v2, s[48:51], s64 offen sc1 lds\n"
"v_mfma_f32_16x16x32_bf16 v[36:39], a[68:71], v[232:235], v[36:39]\n"
"ds_read_b128 v[232:235], v221 offset:3072\n"
"v_mfma_f32_16x16x32_bf16 v[36:39], a[76:79], v[236:239], v[36:39]\n"
"s_waitcnt lgkmcnt(3)\n"
"v_mfma_f32_16x16x32_bf16 v[40:43], a[64:67], v[240:243], v[40:43]\n"
"ds_read_b128 v[236:239], v221 offset:10240\n"
"v_mfma_f32_16x16x32_bf16 v[40:43], a[72:75], v[244:247], v[40:43]\n"
"buffer_load_dwordx4 a[112:115], v2, s[48:51], s63 offen sc1\n"
"v_mfma_f32_16x16x32_bf16 v[44:47], a[68:71], v[240:243], v[44:47]\n"
"ds_read_b128 v[240:243], v221 offset:4096\n"
"v_mfma_f32_16x16x32_bf16 v[44:47], a[76:79], v[244:247], v[44:47]\n"
"s_waitcnt lgkmcnt(3)\n"
"v_mfma_f32_16x16x32_bf16 v[48:51], a[64:67], v[224:227], v[48:51]\n"
"ds_read_b128 v[244:247], v221 offset:11264\n"
"v_mfma_f32_16x16x32_bf16 v[48:51], a[72:75], v[228:231], v[48:51]\n"
"buffer_load_dwordx4 a[116:119], v2, s[48:51], s63 offen offset:1024 sc1\n"
"v_mfma_f32_16x16x32_bf16 v[52:55], a[68:71], v[224:227], v[52:55]\n"
"ds_read_b128 v[224:227], v221 offset:5120\n"
"s_add_u32 s63, s22, 12451840\n"
"v_mfma_f32_16x16x32_bf16 v[52:55], a[76:79], v[228:231], v[52:55]\n"
"s_waitcnt lgkmcnt(3)\n"
"v_mfma_f32_16x16x32_bf16 v[56:59], a[64:67], v[232:235], v[56:59]\n"
"ds_read_b128 v[228:231], v221 offset:12288\n"
"v_mfma_f32_16x16x32_bf16 v[56:59], a[72:75], v[236:239], v[56:59]\n"
"buffer_load_dwordx4 v2, s[48:51], s64 offen offset:1024 sc1 lds\n"
"v_mfma_f32_16x16x32_bf16 v[60:63], a[68:71], v[232:235], v[60:63]\n"
"ds_read_b128 v[232:235], v221 offset:6144\n"
"v_mfma_f32_16x16x32_bf16 v[60:63], a[76:79], v[236:239], v[60:63]\n"
"s_waitcnt lgkmcnt(3)\n"
"v_mfma_f32_16x16x32_bf16 v[64:67], a[64:67], v[240:243], v[64:67]\n"
"ds_read_b128 v[236:239], v221 offset:13312\n"
"v_mfma_f32_16x16x32_bf16 v[64:67], a[72:75], v[244:247], v[64:67]\n"
"buffer_load_dwordx4 v2, s[48:51], s64 offen offset:2048 sc1 lds\n"
"v_mfma_f32_16x16x32_bf16 v[68:71], a[68:71], v[240:243], v[68:71]\n"
"ds_read_b128 v[240:243], v221 offset:14336\n"
"v_mfma_f32_16x16x32_bf16 v[68:71], a[76:79], v[244:247], v[68:71]\n"
"s_waitcnt lgkmcnt(3)\n"
"v_mfma_f32_16x16x32_bf16 v[72:75], a[64:67], v[224:227], v[72:75]\n"
"ds_read_b128 v[244:247], v221 offset:21504\n"
"v_mfma_f32_16x16x32_bf16 v[72:75], a[72:75], v[228:231], v[72:75]\n"
"buffer_load_dwordx4 v2, s[48:51], s64 offen offset:3072 sc1 lds\n"
"v_mfma_f32_16x16x32_bf16 v[76:79], a[68:71], v[224:227], v[76:79]\n"
"ds_read_b128 v[224:227], v221 offset:15360\n"
"s_add_u32 m0, s69, 118784\n"
"v_mfma_f32_16x16x32_bf16 v[76:79], a[76:79], v[228:231], v[76:79]\n"
"s_waitcnt lgkmcnt(3)\n"
"s_add_u32 s64, s68, 32247808\n"
"v_mfma_f32_16x16x32_bf16 v[80:83], a[64:67], v[232:235], v[80:83]\n"
"ds_read_b128 v[228:231], v221 offset:22528\n"
"v_mfma_f32_16x16x32_bf16 v[80:83], a[72:75], v[236:239], v[80:83]\n"
"buffer_load_dwordx4 a[120:123], v2, s[48:51], s63 offen sc1\n"
"v_mfma_f32_16x16x32_bf16 v[84:87], a[68:71], v[232:235], v[84:87]\n"
"ds_read_b128 v[232:235], v221 offset:16384\n"
"v_mfma_f32_16x16x32_bf16 v[84:87], a[76:79], v[236:239], v[84:87]\n"
"s_waitcnt vmcnt(14) lgkmcnt(3)\n"
"buffer_load_dwordx4 a[124:127], v2, s[48:51], s63 offen offset:1024 sc1\n"
"v_mfma_f32_16x16x32_bf16 v[32:35], a[80:83], v[240:243], v[32:35]\n"
"ds_read_b128 v[236:239], v221 offset:23552\n"
"s_add_u32 s63, s22, 12582912\n"
"v_mfma_f32_16x16x32_bf16 v[32:35], a[88:91], v[244:247], v[32:35]\n"
"v_mfma_f32_16x16x32_bf16 v[36:39], a[84:87], v[240:243], v[36:39]\n"
"ds_read_b128 v[240:243], v221 offset:17408\n"
"v_mfma_f32_16x16x32_bf16 v[36:39], a[92:95], v[244:247], v[36:39]\n"
"s_waitcnt lgkmcnt(3)\n"
"buffer_load_dwordx4 v2, s[48:51], s64 offen sc1 lds\n"
"v_mfma_f32_16x16x32_bf16 v[40:43], a[80:83], v[224:227], v[40:43]\n"
"ds_read_b128 v[244:247], v221 offset:24576\n"
"v_mfma_f32_16x16x32_bf16 v[40:43], a[88:91], v[228:231], v[40:43]\n"
"v_mfma_f32_16x16x32_bf16 v[44:47], a[84:87], v[224:227], v[44:47]\n"
"ds_read_b128 v[224:227], v221 offset:18432\n"
"v_mfma_f32_16x16x32_bf16 v[44:47], a[92:95], v[228:231], v[44:47]\n"
"s_waitcnt lgkmcnt(3)\n"
"buffer_load_dwordx4 v2, s[48:51], s64 offen offset:1024 sc1 lds\n"
"v_mfma_f32_16x16x32_bf16 v[48:51], a[80:83], v[232:235], v[48:51]\n"
"ds_read_b128 v[228:231], v221 offset:25600\n"
"v_mfma_f32_16x16x32_bf16 v[48:51], a[88:91], v[236:239], v[48:51]\n"
"v_mfma_f32_16x16x32_bf16 v[52:55], a[84:87], v[232:235], v[52:55]\n"
"ds_read_b128 v[232:235], v221 offset:19456\n"
"v_mfma_f32_16x16x32_bf16 v[52:55], a[92:95], v[236:239], v[52:55]\n"
"s_waitcnt lgkmcnt(3)\n"
"buffer_load_dwordx4 v2, s[48:51], s64 offen offset:2048 sc1 lds\n"
"v_mfma_f32_16x16x32_bf16 v[56:59], a[80:83], v[240:243], v[56:59]\n"
"ds_read_b128 v[236:239], v221 offset:26624\n"
"s_add_u32 m0, s69, 0\n"
"v_mfma_f32_16x16x32_bf16 v[56:59], a[88:91], v[244:247], v[56:59]\n"
"buffer_load_dwordx4 a[128:131], v2, s[48:51], s63 offen sc1\n"
"v_mfma_f32_16x16x32_bf16 v[60:63], a[84:87], v[240:243], v[60:63]\n"
"ds_read_b128 v[240:243], v221 offset:20480\n"
"s_add_u32 s64, s68, 32702464\n"
"v_mfma_f32_16x16x32_bf16 v[60:63], a[92:95], v[244:247], v[60:63]\n"
"s_waitcnt lgkmcnt(3)\n"
"buffer_load_dwordx4 a[132:135], v2, s[48:51], s63 offen offset:1024 sc1\n"
"v_mfma_f32_16x16x32_bf16 v[64:67], a[80:83], v[224:227], v[64:67]\n"
"ds_read_b128 v[244:247], v221 offset:27648\n"
"s_add_u32 s63, s22, 12713984\n"
"v_mfma_f32_16x16x32_bf16 v[64:67], a[88:91], v[228:231], v[64:67]\n"
"v_mfma_f32_16x16x32_bf16 v[68:71], a[84:87], v[224:227], v[68:71]\n"
"s_waitcnt vmcnt(17)\n"
"s_barrier\n"
"ds_read_b128 v[224:227], v221 offset:28672\n"
"v_mfma_f32_16x16x32_bf16 v[68:71], a[92:95], v[228:231], v[68:71]\n"
"s_waitcnt lgkmcnt(3)\n"
"buffer_load_dwordx4 a[136:139], v2, s[48:51], s63 offen sc1\n"
"v_mfma_f32_16x16x32_bf16 v[72:75], a[80:83], v[232:235], v[72:75]\n"
"ds_read_b128 v[228:231], v221 offset:35840\n"
"v_mfma_f32_16x16x32_bf16 v[72:75], a[88:91], v[236:239], v[72:75]\n"
"v_mfma_f32_16x16x32_bf16 v[76:79], a[84:87], v[232:235], v[76:79]\n"
"ds_read_b128 v[232:235], v221 offset:29696\n"
"v_mfma_f32_16x16x32_bf16 v[76:79], a[92:95], v[236:239], v[76:79]\n"
"s_waitcnt lgkmcnt(3)\n"
"buffer_load_dwordx4 a[140:143], v2, s[48:51], s63 offen offset:1024 sc1\n"
"v_mfma_f32_16x16x32_bf16 v[80:83], a[80:83], v[240:243], v[80:83]\n"
"ds_read_b128 v[236:239], v221 offset:36864\n"
"s_add_u32 s63, s22, 12845056\n"
"v_mfma_f32_16x16x32_bf16 v[80:83], a[88:91], v[244:247], v[80:83]\n"
"v_mfma_f32_16x16x32_bf16 v[84:87], a[84:87], v[240:243], v[84:87]\n"
"ds_read_b128 v[240:243], v221 offset:30720\n"
"v_mfma_f32_16x16x32_bf16 v[84:87], a[92:95], v[244:247], v[84:87]\n"
"s_waitcnt vmcnt(15) lgkmcnt(3)\n"
"v_mfma_f32_16x16x32_bf16 v[32:35], a[96:99], v[224:227], v[32:35]\n"
"ds_read_b128 v[244:247], v221 offset:37888\n"
"v_mfma_f32_16x16x32_bf16 v[32:35], a[104:107], v[228:231], v[32:35]\n"
"buffer_load_dwordx4 v2, s[48:51], s64 offen sc1 lds\n"
"v_mfma_f32_16x16x32_bf16 v[36:39], a[100:103], v[224:227], v[36:39]\n"
"ds_read_b128 v[224:227], v221 offset:31744\n"
"v_mfma_f32_16x16x32_bf16 v[36:39], a[108:111], v[228:231], v[36:39]\n"
"s_waitcnt lgkmcnt(3)\n"
"v_mfma_f32_16x16x32_bf16 v[40:43], a[96:99], v[232:235], v[40:43]\n"
"ds_read_b128 v[228:231], v221 offset:38912\n"
"v_mfma_f32_16x16x32_bf16 v[40:43], a[104:107], v[236:239], v[40:43]\n"
"buffer_load_dwordx4 a[144:147], v2, s[48:51], s63 offen sc1\n"
"v_mfma_f32_16x16x32_bf16 v[44:47], a[100:103], v[232:235], v[44:47]\n"
"ds_read_b128 v[232:235], v221 offset:32768\n"
"v_mfma_f32_16x16x32_bf16 v[44:47], a[108:111], v[236:239], v[44:47]\n"
"s_waitcnt lgkmcnt(3)\n"
"v_mfma_f32_16x16x32_bf16 v[48:51], a[96:99], v[240:243], v[48:51]\n"
"ds_read_b128 v[236:239], v221 offset:39936\n"
"v_mfma_f32_16x16x32_bf16 v[48:51], a[104:107], v[244:247], v[48:51]\n"
"buffer_load_dwordx4 a[148:151], v2, s[48:51], s63 offen offset:1024 sc1\n"
"v_mfma_f32_16x16x32_bf16 v[52:55], a[100:103], v[240:243], v[52:55]\n"
"ds_read_b128 v[240:243], v221 offset:33792\n"
"s_add_u32 s63, s22, 12976128\n"
"v_mfma_f32_16x16x32_bf16 v[52:55], a[108:111], v[244:247], v[52:55]\n"
"s_waitcnt lgkmcnt(3)\n"
"v_mfma_f32_16x16x32_bf16 v[56:59], a[96:99], v[224:227], v[56:59]\n"
"ds_read_b128 v[244:247], v221 offset:40960\n"
"v_mfma_f32_16x16x32_bf16 v[56:59], a[104:107], v[228:231], v[56:59]\n"
"buffer_load_dwordx4 v2, s[48:51], s64 offen offset:1024 sc1 lds\n"
"v_mfma_f32_16x16x32_bf16 v[60:63], a[100:103], v[224:227], v[60:63]\n"
"ds_read_b128 v[224:227], v221 offset:34816\n"
"v_mfma_f32_16x16x32_bf16 v[60:63], a[108:111], v[228:231], v[60:63]\n"
"s_waitcnt lgkmcnt(3)\n"
"v_mfma_f32_16x16x32_bf16 v[64:67], a[96:99], v[232:235], v[64:67]\n"
"ds_read_b128 v[228:231], v221 offset:41984\n"
"v_mfma_f32_16x16x32_bf16 v[64:67], a[104:107], v[236:239], v[64:67]\n"
"buffer_load_dwordx4 v2, s[48:51], s64 offen offset:2048 sc1 lds\n"
"v_mfma_f32_16x16x32_bf16 v[68:71], a[100:103], v[232:235], v[68:71]\n"
"ds_read_b128 v[232:235], v221 offset:43008\n"
"v_mfma_f32_16x16x32_bf16 v[68:71], a[108:111], v[236:239], v[68:71]\n"
"s_waitcnt lgkmcnt(3)\n"
"v_mfma_f32_16x16x32_bf16 v[72:75], a[96:99], v[240:243], v[72:75]\n"
"ds_read_b128 v[236:239], v221 offset:50176\n"
"v_mfma_f32_16x16x32_bf16 v[72:75], a[104:107], v[244:247], v[72:75]\n"
"buffer_load_dwordx4 v2, s[48:51], s64 offen offset:3072 sc1 lds\n"
"v_mfma_f32_16x16x32_bf16 v[76:79], a[100:103], v[240:243], v[76:79]\n"
"ds_read_b128 v[240:243], v221 offset:44032\n"
"s_add_u32 m0, s69, 4096\n"
"v_mfma_f32_16x16x32_bf16 v[76:79], a[108:111], v[244:247], v[76:79]\n"
"s_waitcnt lgkmcnt(3)\n"
"s_add_u32 s64, s68, 32706560\n"
"v_mfma_f32_16x16x32_bf16 v[80:83], a[96:99], v[224:227], v[80:83]\n"
"ds_read_b128 v[244:247], v221 offset:51200\n"
"v_mfma_f32_16x16x32_bf16 v[80:83], a[104:107], v[228:231], v[80:83]\n"
"buffer_load_dwordx4 a[152:155], v2, s[48:51], s63 offen sc1\n"
"v_mfma_f32_16x16x32_bf16 v[84:87], a[100:103], v[224:227], v[84:87]\n"
"ds_read_b128 v[224:227], v221 offset:45056\n"
"v_mfma_f32_16x16x32_bf16 v[84:87], a[108:111], v[228:231], v[84:87]\n"
"s_waitcnt vmcnt(14) lgkmcnt(3)\n"
"buffer_load_dwordx4 a[156:159], v2, s[48:51], s63 offen offset:1024 sc1\n"
"v_mfma_f32_16x16x32_bf16 v[32:35], a[112:115], v[232:235], v[32:35]\n"
"ds_read_b128 v[228:231], v221 offset:52224\n"
"s_add_u32 s63, s22, 13107200\n"
"v_mfma_f32_16x16x32_bf16 v[32:35], a[120:123], v[236:239], v[32:35]\n"
"v_add_f32 v164, v164, v168\n"
"v_add_f32 v172, v172, v176\n"
"v_mfma_f32_16x16x32_bf16 v[36:39], a[116:119], v[232:235], v[36:39]\n"
"ds_read_b128 v[232:235], v221 offset:46080\n"
"v_add_f32 v165, v165, v169\n"
"v_mfma_f32_16x16x32_bf16 v[36:39], a[124:127], v[236:239], v[36:39]\n"
"s_waitcnt lgkmcnt(3)\n"
"buffer_load_dwordx4 v2, s[48:51], s64 offen sc1 lds\n"
"v_mfma_f32_16x16x32_bf16 v[40:43], a[112:115], v[240:243], v[40:43]\n"
"ds_read_b128 v[236:239], v221 offset:53248\n"
"v_add_f32 v173, v173, v177\n"
"v_mfma_f32_16x16x32_bf16 v[40:43], a[120:123], v[244:247], v[40:43]\n"
"v_add_f32 v166, v166, v170\n"
"v_add_f32 v174, v174, v178\n"
"v_mfma_f32_16x16x32_bf16 v[44:47], a[116:119], v[240:243], v[44:47]\n"
"ds_read_b128 v[240:243], v221 offset:47104\n"
"v_add_f32 v167, v167, v171\n"
"v_mfma_f32_16x16x32_bf16 v[44:47], a[124:127], v[244:247], v[44:47]\n"
"s_waitcnt lgkmcnt(3)\n"
"buffer_load_dwordx4 v2, s[48:51], s64 offen offset:1024 sc1 lds\n"
"v_mfma_f32_16x16x32_bf16 v[48:51], a[112:115], v[224:227], v[48:51]\n"
"ds_read_b128 v[244:247], v221 offset:54272\n"
"v_add_f32 v175, v175, v179\n"
"v_mfma_f32_16x16x32_bf16 v[48:51], a[120:123], v[228:231], v[48:51]\n"
"v_add_f32 v164, v164, v172\n"
"v_add_f32 v165, v165, v173\n"
"v_mfma_f32_16x16x32_bf16 v[52:55], a[116:119], v[224:227], v[52:55]\n"
"ds_read_b128 v[224:227], v221 offset:48128\n"
"v_add_f32 v166, v166, v174\n"
"v_mfma_f32_16x16x32_bf16 v[52:55], a[124:127], v[228:231], v[52:55]\n"
"s_waitcnt lgkmcnt(3)\n"
"buffer_load_dwordx4 v2, s[48:51], s64 offen offset:2048 sc1 lds\n"
"v_mfma_f32_16x16x32_bf16 v[56:59], a[112:115], v[232:235], v[56:59]\n"
"ds_read_b128 v[228:231], v221 offset:55296\n"
"s_add_u32 m0, s69, 28672\n"
"v_mfma_f32_16x16x32_bf16 v[56:59], a[120:123], v[236:239], v[56:59]\n"
"buffer_load_dwordx4 a[160:163], v2, s[48:51], s63 offen sc1\n"
"v_mfma_f32_16x16x32_bf16 v[60:63], a[116:119], v[232:235], v[60:63]\n"
"ds_read_b128 v[232:235], v221 offset:49152\n"
"s_add_u32 s64, s68, 33161216\n"
"v_mfma_f32_16x16x32_bf16 v[60:63], a[124:127], v[236:239], v[60:63]\n"
"s_waitcnt lgkmcnt(3)\n"
"buffer_load_dwordx4 a[164:167], v2, s[48:51], s63 offen offset:1024 sc1\n"
"v_mfma_f32_16x16x32_bf16 v[64:67], a[112:115], v[240:243], v[64:67]\n"
"ds_read_b128 v[236:239], v221 offset:56320\n"
"s_add_u32 s63, s22, 13238272\n"
"v_mfma_f32_16x16x32_bf16 v[64:67], a[120:123], v[244:247], v[64:67]\n"
"v_add_f32 v167, v167, v175\n"
"v_mov_b32 v168, v164\n"
"v_mfma_f32_16x16x32_bf16 v[68:71], a[116:119], v[240:243], v[68:71]\n"
"s_waitcnt vmcnt(17)\n"
"s_barrier\n"
"ds_read_b128 v[240:243], v222\n"
"v_mfma_f32_16x16x32_bf16 v[68:71], a[124:127], v[244:247], v[68:71]\n"
"s_waitcnt lgkmcnt(3)\n"
"buffer_load_dwordx4 a[168:171], v2, s[48:51], s63 offen sc1\n"
"v_mfma_f32_16x16x32_bf16 v[72:75], a[112:115], v[224:227], v[72:75]\n"
"ds_read_b128 v[244:247], v222 offset:7168\n"
"v_mov_b32 v169, v165\n"
"v_mfma_f32_16x16x32_bf16 v[72:75], a[120:123], v[228:231], v[72:75]\n"
"v_mov_b32 v170, v166\n"
"v_mov_b32 v171, v167\n"
"v_mfma_f32_16x16x32_bf16 v[76:79], a[116:119], v[224:227], v[76:79]\n"
"ds_read_b128 v[224:227], v222 offset:1024\n"
"v_permlane16_swap_b32 v164, v168\n"
"v_mfma_f32_16x16x32_bf16 v[76:79], a[124:127], v[228:231], v[76:79]\n"
"s_waitcnt lgkmcnt(3)\n"
"buffer_load_dwordx4 a[172:175], v2, s[48:51], s63 offen offset:1024 sc1\n"
"v_mfma_f32_16x16x32_bf16 v[80:83], a[112:115], v[232:235], v[80:83]\n"
"ds_read_b128 v[228:231], v222 offset:8192\n"
"s_add_u32 s63, s22, 13369344\n"
"v_mfma_f32_16x16x32_bf16 v[80:83], a[120:123], v[236:239], v[80:83]\n"
"v_permlane16_swap_b32 v165, v169\n"
"v_permlane16_swap_b32 v166, v170\n"
"v_mfma_f32_16x16x32_bf16 v[84:87], a[116:119], v[232:235], v[84:87]\n"
"ds_read_b128 v[232:235], v222 offset:2048\n"
"v_permlane16_swap_b32 v167, v171\n"
"v_mfma_f32_16x16x32_bf16 v[84:87], a[124:127], v[236:239], v[84:87]\n"
"s_waitcnt vmcnt(15) lgkmcnt(3)\n"
"v_add_f32 v164, v164, v168\n"
"v_add_f32 v165, v165, v169\n"
"LZ1_%=:\n"
"v_add_u32 v11, LZ2_%=-LPC_%=, v0\n"
"buffer_load_dwordx4 v2, s[48:51], s64 offen sc1 lds\n"
"v_min_u32 v11, LZ4_%=-LPC_%=-4, v11\n"
"global_load_dword v11, v11, s[70:71]\n"
"v_mfma_f32_16x16x32_bf16 v[32:35], a[128:131], v[240:243], v[32:35]\n"
"ds_read_b128 v[236:239], v222 offset:9216\n"
"v_add_f32 v166, v166, v170\n"
"v_mfma_f32_16x16x32_bf16 v[32:35], a[136:139], v[244:247], v[32:35]\n"
"v_add_f32 v167, v167, v171\n"
"v_mov_b32 v168, v164\n"
"v_mfma_f32_16x16x32_bf16 v[36:39], a[132:135], v[240:243], v[36:39]\n"
"ds_read_b128 v[240:243], v222 offset:3072\n"
"v_mov_b32 v169, v165\n"
"v_mfma_f32_16x16x32_bf16 v[36:39], a[140:143], v[244:247], v[36:39]\n"
"s_waitcnt lgkmcnt(3)\n"
"buffer_load_dwordx4 a[176:179], v2, s[48:51], s63 offen sc1\n"
"v_mfma_f32_16x16x32_bf16 v[40:43], a[128:131], v[224:227], v[40:43]\n"
"ds_read_b128 v[244:247], v222 offset:10240\n"
"v_mov_b32 v170, v166\n"
"v_mfma_f32_16x16x32_bf16 v[40:43], a[136:139], v[228:231], v[40:43]\n"
"v_mov_b32 v171, v167\n"
"v_permlane32_swap_b32 v164, v168\n"
"v_mfma_f32_16x16x32_bf16 v[44:47], a[132:135], v[224:227], v[44:47]\n"
"ds_read_b128 v[224:227], v222 offset:4096\n"
"v_permlane32_swap_b32 v165, v169\n"
"v_mfma_f32_16x16x32_bf16 v[44:47], a[140:143], v[228:231], v[44:47]\n"
"s_waitcnt lgkmcnt(3)\n"
"buffer_load_dwordx4 a[180:183], v2, s[48:51], s63 offen offset:1024 sc1\n"
"v_mfma_f32_16x16x32_bf16 v[48:51], a[128:131], v[232:235], v[48:51]\n"
"ds_read_b128 v[228:231], v222 offset:11264\n"
"s_add_u32 s63, s22, 13500416\n"
"v_mfma_f32_16x16x32_bf16 v[48:51], a[136:139], v[236:239], v[48:51]\n"
"v_permlane32_swap_b32 v166, v170\n"
"v_permlane32_swap_b32 v167, v171\n"
"v_mfma_f32_16x16x32_bf16 v[52:55], a[132:135], v[232:235], v[52:55]\n"
"ds_read_b128 v[232:235], v222 offset:5120\n"
"v_add_f32 v164, v164, v168\n"
"v_mfma_f32_16x16x32_bf16 v[52:55], a[140:143], v[236:239], v[52:55]\n"
"s_waitcnt lgkmcnt(3)\n"
"buffer_load_dwordx4 v2, s[48:51], s64 offen offset:1024 sc1 lds\n"
"v_mfma_f32_16x16x32_bf16 v[56:59], a[128:131], v[240:243], v[56:59]\n"
"ds_read_b128 v[236:239], v222 offset:12288\n"
"v_add_f32 v165, v165, v169\n"
"v_mfma_f32_16x16x32_bf16 v[56:59], a[136:139], v[244:247], v[56:59]\n"
"v_add_f32 v166, v166, v170\n"
"v_add_f32 v167, v167, v171\n"
"v_mfma_f32_16x16x32_bf16 v[60:63], a[132:135], v[240:243], v[60:63]\n"
"ds_read_b128 v[240:243], v222 offset:6144\n"
"v_mul_f32 v164, 0xba800000, v164\n"
"v_mfma_f32_16x16x32_bf16 v[60:63], a[140:143], v[244:247], v[60:63]\n"
"s_waitcnt lgkmcnt(3)\n"
"buffer_load_dwordx4 v2, s[48:51], s64 offen offset:2048 sc1 lds\n"
"v_mfma_f32_16x16x32_bf16 v[64:67], a[128:131], v[224:227], v[64:67]\n"
"ds_read_b128 v[244:247], v222 offset:13312\n"
"v_mul_f32 v165, 0x3a800000, v165\n"
"v_mfma_f32_16x16x32_bf16 v[64:67], a[136:139], v[228:231], v[64:67]\n"
"v_fma_f32 v165, -v164, v164, v165\n"
"v_max_f32 v165, 0, v165\n"
"v_mfma_f32_16x16x32_bf16 v[68:71], a[132:135], v[224:227], v[68:71]\n"
"ds_read_b128 v[224:227], v222 offset:14336\n"
"v_add_f32 v165, 0x3727c5ac, v165\n"
"v_mfma_f32_16x16x32_bf16 v[68:71], a[140:143], v[228:231], v[68:71]\n"
"s_waitcnt lgkmcnt(3)\n"
"buffer_load_dwordx4 v2, s[48:51], s64 offen offset:3072 sc1 lds\n"
"v_mfma_f32_16x16x32_bf16 v[72:75], a[128:131], v[232:235], v[72:75]\n"
"ds_read_b128 v[228:231], v222 offset:21504\n"
"s_add_u32 m0, s69, 32768\n"
"v_mfma_f32_16x16x32_bf16 v[72:75], a[136:139], v[236:239], v[72:75]\n"
"s_add_u32 s64, s68, 33165312\n"
"v_mfma_f32_16x16x32_bf16 v[76:79], a[132:135], v[232:235], v[76:79]\n"
"ds_read_b128 v[232:235], v222 offset:15360\n"
"v_mfma_f32_16x16x32_bf16 v[76:79], a[140:143], v[236:239], v[76:79]\n"
"s_waitcnt lgkmcnt(3)\n"
"buffer_load_dwordx4 a[184:187], v2, s[48:51], s63 offen sc1\n"
"v_mfma_f32_16x16x32_bf16 v[80:83], a[128:131], v[240:243], v[80:83]\n"
"ds_read_b128 v[236:239], v222 offset:22528\n"
"v_mfma_f32_16x16x32_bf16 v[80:83], a[136:139], v[244:247], v[80:83]\n"
"v_rsq_f32 v160, v165\n"
"v_mfma_f32_16x16x32_bf16 v[84:87], a[132:135], v[240:243], v[84:87]\n"
"ds_read_b128 v[240:243], v222 offset:16384\n"
"v_cvt_pk_bf16_f32 v168, v164, v164\n"
"v_mfma_f32_16x16x32_bf16 v[84:87], a[140:143], v[244:247], v[84:87]\n"
"s_waitcnt vmcnt(15) lgkmcnt(3)\n"
"buffer_load_dwordx4 a[188:191], v2, s[48:51], s63 offen offset:1024 sc1\n"
"v_mfma_f32_16x16x32_bf16 v[32:35], a[144:147], v[224:227], v[32:35]\n"
"ds_read_b128 v[244:247], v222 offset:23552\n"
"s_add_u32 s63, s22, 13631488\n"
"v_mfma_f32_16x16x32_bf16 v[32:35], a[152:155], v[228:231], v[32:35]\n"
"v_and_b32 v168, 0xffff0000, v168\n"
"v_sub_f32 v169, v164, v168\n"
"v_mfma_f32_16x16x32_bf16 v[36:39], a[148:151], v[224:227], v[36:39]\n"
"ds_read_b128 v[224:227], v222 offset:17408\n"
"v_cvt_pk_bf16_f32 v144, v168, v169\n"
"v_mfma_f32_16x16x32_bf16 v[36:39], a[156:159], v[228:231], v[36:39]\n"
"s_waitcnt lgkmcnt(3)\n"
"buffer_load_dwordx4 v2, s[48:51], s64 offen sc1 lds\n"
"v_mfma_f32_16x16x32_bf16 v[40:43], a[144:147], v[232:235], v[40:43]\n"
"ds_read_b128 v[228:231], v222 offset:24576\n"
"v_mov_b32 v145, v144\n"
"v_mfma_f32_16x16x32_bf16 v[40:43], a[152:155], v[236:239], v[40:43]\n"
"v_mov_b32 v146, 0\n"
"v_mov_b32 v147, 0\n"
"v_mfma_f32_16x16x32_bf16 v[44:47], a[148:151], v[232:235], v[44:47]\n"
"ds_read_b128 v[232:235], v222 offset:18432\n"
"v_mul_f32 v166, 0xba800000, v166\n"
"v_mfma_f32_16x16x32_bf16 v[44:47], a[156:159], v[236:239], v[44:47]\n"
"s_waitcnt lgkmcnt(3)\n"
"buffer_load_dwordx4 v2, s[48:51], s64 offen offset:1024 sc1 lds\n"
"v_mfma_f32_16x16x32_bf16 v[48:51], a[144:147], v[240:243], v[48:51]\n"
"ds_read_b128 v[236:239], v222 offset:25600\n"
"LPG11_%=:\n"
"s_cbranch_execz LPG15_%=\n"
"v_mul_f32 v167, 0x3a800000, v167\n"
"v_mfma_f32_16x16x32_bf16 v[48:51], a[152:155], v[244:247], v[48:51]\n"
"v_fma_f32 v167, -v166, v166, v167\n"
"v_max_f32 v167, 0, v167\n"
"v_mfma_f32_16x16x32_bf16 v[52:55], a[148:151], v[240:243], v[52:55]\n"
"ds_read_b128 v[240:243], v222 offset:19456\n"
"v_add_f32 v167, 0x3727c5ac, v167\n"
"v_mfma_f32_16x16x32_bf16 v[52:55], a[156:159], v[244:247], v[52:55]\n"
"s_waitcnt lgkmcnt(3)\n"
"buffer_load_dwordx4 v2, s[48:51], s64 offen offset:2048 sc1 lds\n"
"v_mfma_f32_16x16x32_bf16 v[56:59], a[144:147], v[224:227], v[56:59]\n"
"ds_read_b128 v[244:247], v222 offset:26624\n"
"s_add_u32 m0, s69, 57344\n"
"v_mfma_f32_16x16x32_bf16 v[56:59], a[152:155], v[228:231], v[56:59]\n"
"buffer_load_dwordx4 a[192:195], v2, s[48:51], s63 offen sc1\n"
"v_mfma_f32_16x16x32_bf16 v[60:63], a[148:151], v[224:227], v[60:63]\n"
"ds_read_b128 v[224:227], v222 offset:20480\n"
"s_add_u32 s64, s68, 33619968\n"
"v_mfma_f32_16x16x32_bf16 v[60:63], a[156:159], v[228:231], v[60:63]\n"
"s_waitcnt lgkmcnt(3)\n"
"buffer_load_dwordx4 a[196:199], v2, s[48:51], s63 offen offset:1024 sc1\n"
"v_mfma_f32_16x16x32_bf16 v[64:67], a[144:147], v[232:235], v[64:67]\n"
"ds_read_b128 v[228:231], v222 offset:27648\n"
"s_add_u32 s63, s22, 13762560\n"
"v_mfma_f32_16x16x32_bf16 v[64:67], a[152:155], v[236:239], v[64:67]\n"
"v_rsq_f32 v161, v167\n"
"v_mfma_f32_16x16x32_bf16 v[68:71], a[148:151], v[232:235], v[68:71]\n"
"s_waitcnt vmcnt(18)\n"
"s_barrier\n"
"ds_read_b128 v[232:235], v2\n"
"v_mfma_f32_16x16x32_bf16 v[68:71], a[156:159], v[236:239], v[68:71]\n"
"s_waitcnt lgkmcnt(3)\n"
"buffer_load_dwordx4 a[200:203], v2, s[48:51], s63 offen sc1\n"
"v_mfma_f32_16x16x32_bf16 v[72:75], a[144:147], v[240:243], v[72:75]\n"
"ds_read_b128 v[236:239], v2 offset:7168\n"
"v_cvt_pk_bf16_f32 v168, v166, v166\n"
"v_mfma_f32_16x16x32_bf16 v[72:75], a[152:155], v[244:247], v[72:75]\n"
"v_and_b32 v168, 0xffff0000, v168\n"
"v_sub_f32 v169, v166, v168\n"
"v_mfma_f32_16x16x32_bf16 v[76:79], a[148:151], v[240:243], v[76:79]\n"
"ds_read_b128 v[240:243], v2 offset:1024\n"
"v_cvt_pk_bf16_f32 v148, v168, v169\n"
"v_mfma_f32_16x16x32_bf16 v[76:79], a[156:159], v[244:247], v[76:79]\n"
"s_waitcnt lgkmcnt(3)\n"
"buffer_load_dwordx4 a[204:207], v2, s[48:51], s63 offen offset:1024 sc1\n"
"v_mfma_f32_16x16x32_bf16 v[80:83], a[144:147], v[224:227], v[80:83]\n"
"ds_read_b128 v[244:247], v2 offset:8192\n"
"s_add_u32 s63, s22, 13893632\n"
"v_mfma_f32_16x16x32_bf16 v[80:83], a[152:155], v[228:231], v[80:83]\n"
"v_mov_b32 v149, v148\n"
"v_mov_b32 v150, 0\n"
"v_mfma_f32_16x16x32_bf16 v[84:87], a[148:151], v[224:227], v[84:87]\n"
"ds_read_b128 v[224:227], v2 offset:2048\n"
"v_mov_b32 v151, 0\n"
"v_mfma_f32_16x16x32_bf16 v[84:87], a[156:159], v[228:231], v[84:87]\n"
"s_waitcnt vmcnt(16) lgkmcnt(3)\n"
"v_lshlrev_b32 v170, 4, v4\n"
"ds_bpermute_b32 v152, v170, v160\n"
"v_mfma_f32_16x16x32_bf16 v[32:35], a[160:163], v[232:235], v[32:35]\n"
"ds_read_b128 v[228:231], v2 offset:9216\n"
"ds_bpermute_b32 v153, v170, v160 offset:4\n"
"v_mfma_f32_16x16x32_bf16 v[32:35], a[168:171], v[236:239], v[32:35]\n"
"buffer_load_dwordx4 v2, s[48:51], s64 offen sc1 lds\n"
"v_mfma_f32_16x16x32_bf16 v[36:39], a[164:167], v[232:235], v[36:39]\n"
"ds_read_b128 v[232:235], v2 offset:3072\n"
"ds_bpermute_b32 v154, v170, v160 offset:8\n"
"v_mfma_f32_16x16x32_bf16 v[36:39], a[172:175], v[236:239], v[36:39]\n"
"s_waitcnt lgkmcnt(6)\n"
"ds_bpermute_b32 v155, v170, v160 offset:12\n"
"ds_bpermute_b32 v156, v170, v161\n"
"v_mfma_f32_16x16x32_bf16 v[40:43], a[160:163], v[240:243], v[40:43]\n"
"ds_read_b128 v[236:239], v2 offset:10240\n"
"ds_bpermute_b32 v157, v170, v161 offset:4\n"
"v_mfma_f32_16x16x32_bf16 v[40:43], a[168:171], v[244:247], v[40:43]\n"
"buffer_load_dwordx4 a[208:211], v2, s[48:51], s63 offen sc1\n"
"v_mfma_f32_16x16x32_bf16 v[44:47], a[164:167], v[240:243], v[44:47]\n"
"ds_read_b128 v[240:243], v2 offset:4096\n"
"ds_bpermute_b32 v158, v170, v161 offset:8\n"
"v_mfma_f32_16x16x32_bf16 v[44:47], a[172:175], v[244:247], v[44:47]\n"
"s_waitcnt lgkmcnt(9)\n"
"ds_bpermute_b32 v159, v170, v161 offset:12\n"
"v_mfma_f32_16x16x32_bf16 v[48:51], a[160:163], v[224:227], v[48:51]\n"
"ds_read_b128 v[244:247], v2 offset:11264\n"
"v_mfma_f32_16x16x32_bf16 v[48:51], a[168:171], v[228:231], v[48:51]\n"
"buffer_load_dwordx4 a[212:215], v2, s[48:51], s63 offen offset:1024 sc1\n"
"v_mfma_f32_16x16x32_bf16 v[52:55], a[164:167], v[224:227], v[52:55]\n"
"ds_read_b128 v[224:227], v2 offset:5120\n"
"s_add_u32 s63, s22, 14024704\n"
"v_mfma_f32_16x16x32_bf16 v[52:55], a[172:175], v[228:231], v[52:55]\n"
"s_waitcnt lgkmcnt(6)\n"
"v_mfma_f32_16x16x32_bf16 v[56:59], a[160:163], v[232:235], v[56:59]\n"
"ds_read_b128 v[228:231], v2 offset:12288\n"
"v_mfma_f32_16x16x32_bf16 v[56:59], a[168:171], v[236:239], v[56:59]\n"
"buffer_load_dwordx4 v2, s[48:51], s64 offen offset:1024 sc1 lds\n"
"v_mfma_f32_16x16x32_bf16 v[60:63], a[164:167], v[232:235], v[60:63]\n"
"ds_read_b128 v[232:235], v2 offset:6144\n"
"v_mfma_f32_16x16x32_bf16 v[60:63], a[172:175], v[236:239], v[60:63]\n"
"s_waitcnt lgkmcnt(3)\n"
"v_mfma_f32_16x16x32_bf16 v[64:67], a[160:163], v[240:243], v[64:67]\n"
"ds_read_b128 v[236:239], v2 offset:13312\n"
"v_mfma_f32_16x16x32_bf16 v[64:67], a[168:171], v[244:247], v[64:67]\n"
"buffer_load_dwordx4 v2, s[48:51], s64 offen offset:2048 sc1 lds\n"
"v_mfma_f32_16x16x32_bf16 v[68:71], a[164:167], v[240:243], v[68:71]\n"
"ds_read_b128 v[240:243], v2 offset:14336\n"
"v_mfma_f32_16x16x32_bf16 v[68:71], a[172:175], v[244:247], v[68:71]\n"
"s_waitcnt lgkmcnt(3)\n"
"v_mfma_f32_16x16x32_bf16 v[72:75], a[160:163], v[224:227], v[72:75]\n"
"ds_read_b128 v[244:247], v2 offset:21504\n"
"v_mfma_f32_16x16x32_bf16 v[72:75], a[168:171], v[228:231], v[72:75]\n"
"buffer_load_dwordx4 v2, s[48:51], s64 offen offset:3072 sc1 lds\n"
"v_mfma_f32_16x16x32_bf16 v[76:79], a[164:167], v[224:227], v[76:79]\n"
"ds_read_b128 v[224:227], v2 offset:15360\n"
"s_add_u32 m0, s69, 61440\n"
"v_mfma_f32_16x16x32_bf16 v[76:79], a[172:175], v[228:231], v[76:79]\n"
"s_waitcnt lgkmcnt(3)\n"
"s_add_u32 s64, s68, 33624064\n"
"v_mfma_f32_16x16x32_bf16 v[80:83], a[160:163], v[232:235], v[80:83]\n"
"ds_read_b128 v[228:231], v2 offset:22528\n"
"v_mfma_f32_16x16x32_bf16 v[80:83], a[168:171], v[236:239], v[80:83]\n"
"buffer_load_dwordx4 a[216:219], v2, s[48:51], s63 offen sc1\n"
"v_mfma_f32_16x16x32_bf16 v[84:87], a[164:167], v[232:235], v[84:87]\n"
"ds_read_b128 v[232:235], v2 offset:16384\n"
"v_mfma_f32_16x16x32_bf16 v[84:87], a[172:175], v[236:239], v[84:87]\n"
"s_waitcnt vmcnt(14) lgkmcnt(3)\n"
"buffer_load_dwordx4 a[220:223], v2, s[48:51], s63 offen offset:1024 sc1\n"
"v_mfma_f32_16x16x32_bf16 v[32:35], a[176:179], v[240:243], v[32:35]\n"
"ds_read_b128 v[236:239], v2 offset:23552\n"
"s_add_u32 s63, s22, 14155776\n"
"v_mfma_f32_16x16x32_bf16 v[32:35], a[184:187], v[244:247], v[32:35]\n"
"v_mfma_f32_16x16x32_bf16 v[36:39], a[180:183], v[240:243], v[36:39]\n"
"ds_read_b128 v[240:243], v2 offset:17408\n"
"v_mfma_f32_16x16x32_bf16 v[36:39], a[188:191], v[244:247], v[36:39]\n"
"s_waitcnt lgkmcnt(3)\n"
"buffer_load_dwordx4 v2, s[48:51], s64 offen sc1 lds\n"
"v_mfma_f32_16x16x32_bf16 v[40:43], a[176:179], v[224:227], v[40:43]\n"
"ds_read_b128 v[244:247], v2 offset:24576\n"
"v_mfma_f32_16x16x32_bf16 v[40:43], a[184:187], v[228:231], v[40:43]\n"
"v_mfma_f32_16x16x32_bf16 v[44:47], a[180:183], v[224:227], v[44:47]\n"
"ds_read_b128 v[224:227], v2 offset:18432\n"
"v_mfma_f32_16x16x32_bf16 v[44:47], a[188:191], v[228:231], v[44:47]\n"
"s_waitcnt lgkmcnt(3)\n"
"buffer_load_dwordx4 v2, s[48:51], s64 offen offset:1024 sc1 lds\n"
"v_mfma_f32_16x16x32_bf16 v[48:51], a[176:179], v[232:235], v[48:51]\n"
"ds_read_b128 v[228:231], v2 offset:25600\n"
"v_mfma_f32_16x16x32_bf16 v[48:51], a[184:187], v[236:239], v[48:51]\n"
"v_mfma_f32_16x16x32_bf16 v[52:55], a[180:183], v[232:235], v[52:55]\n"
"ds_read_b128 v[232:235], v2 offset:19456\n"
"v_mfma_f32_16x16x32_bf16 v[52:55], a[188:191], v[236:239], v[52:55]\n"
"s_waitcnt lgkmcnt(3)\n"
"buffer_load_dwordx4 v2, s[48:51], s64 offen offset:2048 sc1 lds\n"
"v_mfma_f32_16x16x32_bf16 v[56:59], a[176:179], v[240:243], v[56:59]\n"
"ds_read_b128 v[236:239], v2 offset:26624\n"
"s_add_u32 m0, s69, 86016\n"
"v_mfma_f32_16x16x32_bf16 v[56:59], a[184:187], v[244:247], v[56:59]\n"
"buffer_load_dwordx4 a[224:227], v2, s[48:51], s63 offen sc1\n"
"v_mfma_f32_16x16x32_bf16 v[60:63], a[180:183], v[240:243], v[60:63]\n"
"ds_read_b128 v[240:243], v2 offset:20480\n"
"s_add_u32 s64, s68, 34078720\n"
"v_mfma_f32_16x16x32_bf16 v[60:63], a[188:191], v[244:247], v[60:63]\n"
"s_waitcnt lgkmcnt(3)\n"
"buffer_load_dwordx4 a[228:231], v2, s[48:51], s63 offen offset:1024 sc1\n"
"v_mfma_f32_16x16x32_bf16 v[64:67], a[176:179], v[224:227], v[64:67]\n"
"ds_read_b128 v[244:247], v2 offset:27648\n"
"s_add_u32 s63, s22, 14286848\n"
"v_mfma_f32_16x16x32_bf16 v[64:67], a[184:187], v[228:231], v[64:67]\n"
"v_mfma_f32_16x16x32_bf16 v[68:71], a[180:183], v[224:227], v[68:71]\n"
"s_waitcnt vmcnt(17)\n"
"s_barrier\n"
"ds_read_b128 v[224:227], v2 offset:28672\n"
"v_mfma_f32_16x16x32_bf16 v[68:71], a[188:191], v[228:231], v[68:71]\n"
"s_waitcnt lgkmcnt(3)\n"
"buffer_load_dwordx4 a[232:235], v2, s[48:51], s63 offen sc1\n"
"v_mfma_f32_16x16x32_bf16 v[72:75], a[176:179], v[232:235], v[72:75]\n"
"ds_read_b128 v[228:231], v2 offset:35840\n"
"v_mfma_f32_16x16x32_bf16 v[72:75], a[184:187], v[236:239], v[72:75]\n"
"v_mfma_f32_16x16x32_bf16 v[76:79], a[180:183], v[232:235], v[76:79]\n"
"ds_read_b128 v[232:235], v2 offset:29696\n"
"v_mfma_f32_16x16x32_bf16 v[76:79], a[188:191], v[236:239], v[76:79]\n"
"s_waitcnt lgkmcnt(3)\n"
"buffer_load_dwordx4 a[236:239], v2, s[48:51], s63 offen offset:1024 sc1\n"
"v_mfma_f32_16x16x32_bf16 v[80:83], a[176:179], v[240:243], v[80:83]\n"
"ds_read_b128 v[236:239], v2 offset:36864\n"
"s_add_u32 s63, s22, 14417920\n"
"v_mfma_f32_16x16x32_bf16 v[80:83], a[184:187], v[244:247], v[80:83]\n"
"v_mfma_f32_16x16x32_bf16 v[84:87], a[180:183], v[240:243], v[84:87]\n"
"ds_read_b128 v[240:243], v2 offset:30720\n"
"v_mfma_f32_16x16x32_bf16 v[84:87], a[188:191], v[244:247], v[84:87]\n"
"s_waitcnt vmcnt(15) lgkmcnt(3)\n"
"v_mfma_f32_16x16x32_bf16 v[32:35], a[192:195], v[224:227], v[32:35]\n"
"ds_read_b128 v[244:247], v2 offset:37888\n"
"v_mfma_f32_16x16x32_bf16 v[32:35], a[200:203], v[228:231], v[32:35]\n"
"buffer_load_dwordx4 v2, s[48:51], s64 offen sc1 lds\n"
"v_mfma_f32_16x16x32_bf16 v[36:39], a[196:199], v[224:227], v[36:39]\n"
"ds_read_b128 v[224:227], v2 offset:31744\n"
"v_mfma_f32_16x16x32_bf16 v[36:39], a[204:207], v[228:231], v[36:39]\n"
"s_waitcnt lgkmcnt(3)\n"
"v_mfma_f32_16x16x32_bf16 v[40:43], a[192:195], v[232:235], v[40:43]\n"
"ds_read_b128 v[228:231], v2 offset:38912\n"
"v_mfma_f32_16x16x32_bf16 v[40:43], a[200:203], v[236:239], v[40:43]\n"
"buffer_load_dwordx4 a[240:243], v2, s[48:51], s63 offen sc1\n"
"v_mfma_f32_16x16x32_bf16 v[44:47], a[196:199], v[232:235], v[44:47]\n"
"ds_read_b128 v[232:235], v2 offset:32768\n"
"v_mfma_f32_16x16x32_bf16 v[44:47], a[204:207], v[236:239], v[44:47]\n"
"s_waitcnt lgkmcnt(3)\n"
"v_mfma_f32_16x16x32_bf16 v[48:51], a[192:195], v[240:243], v[48:51]\n"
"ds_read_b128 v[236:239], v2 offset:39936\n"
"v_mfma_f32_16x16x32_bf16 v[48:51], a[200:203], v[244:247], v[48:51]\n"
"buffer_load_dwordx4 a[244:247], v2, s[48:51], s63 offen offset:1024 sc1\n"
"v_mfma_f32_16x16x32_bf16 v[52:55], a[196:199], v[240:243], v[52:55]\n"
"ds_read_b128 v[240:243], v2 offset:33792\n"
"s_add_u32 s63, s22, 14548992\n"
"v_mfma_f32_16x16x32_bf16 v[52:55], a[204:207], v[244:247], v[52:55]\n"
"s_waitcnt lgkmcnt(3)\n"
"v_mfma_f32_16x16x32_bf16 v[56:59], a[192:195], v[224:227], v[56:59]\n"
"ds_read_b128 v[244:247], v2 offset:40960\n"
"v_mfma_f32_16x16x32_bf16 v[56:59], a[200:203], v[228:231], v[56:59]\n"
"buffer_load_dwordx4 v2, s[48:51], s64 offen offset:1024 sc1 lds\n"
"v_mfma_f32_16x16x32_bf16 v[60:63], a[196:199], v[224:227], v[60:63]\n"
"ds_read_b128 v[224:227], v2 offset:34816\n"
"v_mfma_f32_16x16x32_bf16 v[60:63], a[204:207], v[228:231], v[60:63]\n"
"s_waitcnt lgkmcnt(3)\n"
"v_mfma_f32_16x16x32_bf16 v[64:67], a[192:195], v[232:235], v[64:67]\n"
"ds_read_b128 v[228:231], v2 offset:41984\n"
"v_mfma_f32_16x16x32_bf16 v[64:67], a[200:203], v[236:239], v[64:67]\n"
"buffer_load_dwordx4 v2, s[48:51], s64 offen offset:2048 sc1 lds\n"
"v_mfma_f32_16x16x32_bf16 v[68:71], a[196:199], v[232:235], v[68:71]\n"
"ds_read_b128 v[232:235], v2 offset:43008\n"
"v_mfma_f32_16x16x32_bf16 v[68:71], a[204:207], v[236:239], v[68:71]\n"
"s_waitcnt lgkmcnt(3)\n"
"v_mfma_f32_16x16x32_bf16 v[72:75], a[192:195], v[240:243], v[72:75]\n"
"ds_read_b128 v[236:239], v2 offset:50176\n"
"v_mfma_f32_16x16x32_bf16 v[72:75], a[200:203], v[244:247], v[72:75]\n"
"buffer_load_dwordx4 v2, s[48:51], s64 offen offset:3072 sc1 lds\n"
"v_mfma_f32_16x16x32_bf16 v[76:79], a[196:199], v[240:243], v[76:79]\n"
"ds_read_b128 v[240:243], v2 offset:44032\n"
"s_add_u32 m0, s69, 90112\n"
"v_mfma_f32_16x16x32_bf16 v[76:79], a[204:207], v[244:247], v[76:79]\n"
"s_waitcnt lgkmcnt(3)\n"
"s_add_u32 s64, s68, 34082816\n"
"v_mfma_f32_16x16x32_bf16 v[80:83], a[192:195], v[224:227], v[80:83]\n"
"ds_read_b128 v[244:247], v2 offset:51200\n"
"v_mfma_f32_16x16x32_bf16 v[80:83], a[200:203], v[228:231], v[80:83]\n"
"buffer_load_dwordx4 a[248:251], v2, s[48:51], s63 offen sc1\n"
"v_mfma_f32_16x16x32_bf16 v[84:87], a[196:199], v[224:227], v[84:87]\n"
"ds_read_b128 v[224:227], v2 offset:45056\n"
"v_mfma_f32_16x16x32_bf16 v[84:87], a[204:207], v[228:231], v[84:87]\n"
"s_waitcnt vmcnt(14) lgkmcnt(3)\n"
"buffer_load_dwordx4 a[252:255], v2, s[48:51], s63 offen offset:1024 sc1\n"
"v_mfma_f32_16x16x32_bf16 v[32:35], a[208:211], v[232:235], v[32:35]\n"
"ds_read_b128 v[228:231], v2 offset:52224\n"
"v_mfma_f32_16x16x32_bf16 v[32:35], a[216:219], v[236:239], v[32:35]\n"
"v_mfma_f32_16x16x32_bf16 v[36:39], a[212:215], v[232:235], v[36:39]\n"
"ds_read_b128 v[232:235], v2 offset:46080\n"
"v_mfma_f32_16x16x32_bf16 v[36:39], a[220:223], v[236:239], v[36:39]\n"
"s_waitcnt lgkmcnt(3)\n"
"buffer_load_dwordx4 v2, s[48:51], s64 offen sc1 lds\n"
"v_mfma_f32_16x16x32_bf16 v[40:43], a[208:211], v[240:243], v[40:43]\n"
"ds_read_b128 v[236:239], v2 offset:53248\n"
"v_mfma_f32_16x16x32_bf16 v[40:43], a[216:219], v[244:247], v[40:43]\n"
"v_mfma_f32_16x16x32_bf16 v[44:47], a[212:215], v[240:243], v[44:47]\n"
"ds_read_b128 v[240:243], v2 offset:47104\n"
"v_mfma_f32_16x16x32_bf16 v[44:47], a[220:223], v[244:247], v[44:47]\n"
"s_waitcnt lgkmcnt(3)\n"
"buffer_load_dwordx4 v2, s[48:51], s64 offen offset:1024 sc1 lds\n"
"v_mfma_f32_16x16x32_bf16 v[48:51], a[208:211], v[224:227], v[48:51]\n"
"ds_read_b128 v[244:247], v2 offset:54272\n"
"v_mfma_f32_16x16x32_bf16 v[48:51], a[216:219], v[228:231], v[48:51]\n"
"v_mfma_f32_16x16x32_bf16 v[52:55], a[212:215], v[224:227], v[52:55]\n"
"ds_read_b128 v[224:227], v2 offset:48128\n"
"v_mfma_f32_16x16x32_bf16 v[52:55], a[220:223], v[228:231], v[52:55]\n"
"s_waitcnt lgkmcnt(3)\n"
"buffer_load_dwordx4 v2, s[48:51], s64 offen offset:2048 sc1 lds\n"
"v_mfma_f32_16x16x32_bf16 v[56:59], a[208:211], v[232:235], v[56:59]\n"
"ds_read_b128 v[228:231], v2 offset:55296\n"
"s_add_u32 m0, s69, 114688\n"
"v_mfma_f32_16x16x32_bf16 v[56:59], a[216:219], v[236:239], v[56:59]\n"
"s_add_u32 s64, s68, 34537472\n"
"v_mfma_f32_16x16x32_bf16 v[60:63], a[212:215], v[232:235], v[60:63]\n"
"ds_read_b128 v[232:235], v2 offset:49152\n"
"v_mfma_f32_16x16x32_bf16 v[60:63], a[220:223], v[236:239], v[60:63]\n"
"s_waitcnt lgkmcnt(3)\n"
"v_mfma_f32_16x16x32_bf16 v[64:67], a[208:211], v[240:243], v[64:67]\n"
"ds_read_b128 v[236:239], v2 offset:56320\n"
"v_mfma_f32_16x16x32_bf16 v[64:67], a[216:219], v[244:247], v[64:67]\n"
"v_mfma_f32_16x16x32_bf16 v[68:71], a[212:215], v[240:243], v[68:71]\n"
"s_waitcnt vmcnt(15)\n"
"s_barrier\n"
"ds_read_b128 v[240:243], v221\n"
"v_mfma_f32_16x16x32_bf16 v[68:71], a[220:223], v[244:247], v[68:71]\n"
"s_waitcnt lgkmcnt(3)\n"
"v_mfma_f32_16x16x32_bf16 v[72:75], a[208:211], v[224:227], v[72:75]\n"
"ds_read_b128 v[244:247], v221 offset:7168\n"
"v_mfma_f32_16x16x32_bf16 v[72:75], a[216:219], v[228:231], v[72:75]\n"
"v_mfma_f32_16x16x32_bf16 v[76:79], a[212:215], v[224:227], v[76:79]\n"
"ds_read_b128 v[224:227], v221 offset:1024\n"
"v_mfma_f32_16x16x32_bf16 v[76:79], a[220:223], v[228:231], v[76:79]\n"
"s_waitcnt lgkmcnt(3)\n"
"v_mfma_f32_16x16x32_bf16 v[80:83], a[208:211], v[232:235], v[80:83]\n"
"ds_read_b128 v[228:231], v221 offset:8192\n"
"v_mfma_f32_16x16x32_bf16 v[80:83], a[216:219], v[236:239], v[80:83]\n"
"v_mfma_f32_16x16x32_bf16 v[84:87], a[212:215], v[232:235], v[84:87]\n"
"ds_read_b128 v[232:235], v221 offset:2048\n"
"v_mfma_f32_16x16x32_bf16 v[84:87], a[220:223], v[236:239], v[84:87]\n"
"s_waitcnt vmcnt(11) lgkmcnt(3)\n"
"v_mfma_f32_16x16x32_bf16 v[32:35], a[224:227], v[240:243], v[32:35]\n"
"ds_read_b128 v[236:239], v221 offset:9216\n"
"v_mfma_f32_16x16x32_bf16 v[32:35], a[232:235], v[244:247], v[32:35]\n"
"buffer_load_dwordx4 v2, s[48:51], s64 offen sc1 lds\n"
"v_mfma_f32_16x16x32_bf16 v[36:39], a[228:231], v[240:243], v[36:39]\n"
"ds_read_b128 v[240:243], v221 offset:3072\n"
"v_mfma_f32_16x16x32_bf16 v[36:39], a[236:239], v[244:247], v[36:39]\n"
"s_waitcnt lgkmcnt(3)\n"
"v_mfma_f32_16x16x32_bf16 v[40:43], a[224:227], v[224:227], v[40:43]\n"
"ds_read_b128 v[244:247], v221 offset:10240\n"
"v_mfma_f32_16x16x32_bf16 v[40:43], a[232:235], v[228:231], v[40:43]\n"
"buffer_load_dwordx4 v2, s[48:51], s64 offen offset:1024 sc1 lds\n"
"v_mfma_f32_16x16x32_bf16 v[44:47], a[228:231], v[224:227], v[44:47]\n"
"ds_read_b128 v[224:227], v221 offset:4096\n"
"v_mfma_f32_16x16x32_bf16 v[44:47], a[236:239], v[228:231], v[44:47]\n"
"s_waitcnt lgkmcnt(3)\n"
"v_mfma_f32_16x16x32_bf16 v[48:51], a[224:227], v[232:235], v[48:51]\n"
"ds_read_b128 v[228:231], v221 offset:11264\n"
"v_mfma_f32_16x16x32_bf16 v[48:51], a[232:235], v[236:239], v[48:51]\n"
"buffer_load_dwordx4 v2, s[48:51], s64 offen offset:2048 sc1 lds\n"
"v_mfma_f32_16x16x32_bf16 v[52:55], a[228:231], v[232:235], v[52:55]\n"
"ds_read_b128 v[232:235], v221 offset:5120\n"
"v_mfma_f32_16x16x32_bf16 v[52:55], a[236:239], v[236:239], v[52:55]\n"
"s_waitcnt lgkmcnt(3)\n"
"v_mfma_f32_16x16x32_bf16 v[56:59], a[224:227], v[240:243], v[56:59]\n"
"ds_read_b128 v[236:239], v221 offset:12288\n"
"v_mfma_f32_16x16x32_bf16 v[56:59], a[232:235], v[244:247], v[56:59]\n"
"buffer_load_dwordx4 v2, s[48:51], s64 offen offset:3072 sc1 lds\n"
"v_mfma_f32_16x16x32_bf16 v[60:63], a[228:231], v[240:243], v[60:63]\n"
"ds_read_b128 v[240:243], v221 offset:6144\n"
"s_add_u32 m0, s69, 118784\n"
"v_mfma_f32_16x16x32_bf16 v[60:63], a[236:239], v[244:247], v[60:63]\n"
"s_waitcnt lgkmcnt(3)\n"
"s_add_u32 s64, s68, 34541568\n"
"v_mfma_f32_16x16x32_bf16 v[64:67], a[224:227], v[224:227], v[64:67]\n"
"ds_read_b128 v[244:247], v221 offset:13312\n"
"v_mfma_f32_16x16x32_bf16 v[64:67], a[232:235], v[228:231], v[64:67]\n"
"v_mfma_f32_16x16x32_bf16 v[68:71], a[228:231], v[224:227], v[68:71]\n"
"ds_read_b128 v[224:227], v221 offset:14336\n"
"v_mfma_f32_16x16x32_bf16 v[68:71], a[236:239], v[228:231], v[68:71]\n"
"s_waitcnt lgkmcnt(3)\n"
"v_mfma_f32_16x16x32_bf16 v[72:75], a[224:227], v[232:235], v[72:75]\n"
"ds_read_b128 v[228:231], v221 offset:21504\n"
"v_mfma_f32_16x16x32_bf16 v[72:75], a[232:235], v[236:239], v[72:75]\n"
"buffer_load_dwordx4 v2, s[48:51], s64 offen sc1 lds\n"
"v_mfma_f32_16x16x32_bf16 v[76:79], a[228:231], v[232:235], v[76:79]\n"
"ds_read_b128 v[232:235], v221 offset:15360\n"
"v_mfma_f32_16x16x32_bf16 v[76:79], a[236:239], v[236:239], v[76:79]\n"
"s_waitcnt lgkmcnt(3)\n"
"v_mfma_f32_16x16x32_bf16 v[80:83], a[224:227], v[240:243], v[80:83]\n"
"ds_read_b128 v[236:239], v221 offset:22528\n"
"v_mfma_f32_16x16x32_bf16 v[80:83], a[232:235], v[244:247], v[80:83]\n"
"buffer_load_dwordx4 v2, s[48:51], s64 offen offset:1024 sc1 lds\n"
"v_mfma_f32_16x16x32_bf16 v[84:87], a[228:231], v[240:243], v[84:87]\n"
"ds_read_b128 v[240:243], v221 offset:16384\n"
"v_mfma_f32_16x16x32_bf16 v[84:87], a[236:239], v[244:247], v[84:87]\n"
"s_waitcnt vmcnt(9) lgkmcnt(3)\n"
"v_mfma_f32_16x16x32_bf16 v[32:35], a[240:243], v[224:227], v[32:35]\n"
"ds_read_b128 v[244:247], v221 offset:23552\n"
"v_mfma_f32_16x16x32_bf16 v[32:35], a[248:251], v[228:231], v[32:35]\n"
"buffer_load_dwordx4 v2, s[48:51], s64 offen offset:2048 sc1 lds\n"
"v_mfma_f32_16x16x32_bf16 v[36:39], a[244:247], v[224:227], v[36:39]\n"
"ds_read_b128 v[224:227], v221 offset:17408\n"
"s_add_u32 m0, s69, 0\n"
"v_mfma_f32_16x16x32_bf16 v[36:39], a[252:255], v[228:231], v[36:39]\n"
"s_waitcnt lgkmcnt(3)\n"
"s_add_u32 s64, s68, 34996224\n"
"v_mfma_f32_16x16x32_bf16 v[40:43], a[240:243], v[232:235], v[40:43]\n"
"ds_read_b128 v[228:231], v221 offset:24576\n"
"v_mfma_f32_16x16x32_bf16 v[40:43], a[248:251], v[236:239], v[40:43]\n"
"v_mfma_f32_16x16x32_bf16 v[44:47], a[244:247], v[232:235], v[44:47]\n"
"ds_read_b128 v[232:235], v221 offset:18432\n"
"v_mfma_f32_16x16x32_bf16 v[44:47], a[252:255], v[236:239], v[44:47]\n"
"s_waitcnt lgkmcnt(3)\n"
"v_mfma_f32_16x16x32_bf16 v[48:51], a[240:243], v[240:243], v[48:51]\n"
"ds_read_b128 v[236:239], v221 offset:25600\n"
"v_mfma_f32_16x16x32_bf16 v[48:51], a[248:251], v[244:247], v[48:51]\n"
"v_mfma_f32_16x16x32_bf16 v[52:55], a[244:247], v[240:243], v[52:55]\n"
"ds_read_b128 v[240:243], v221 offset:19456\n"
"v_mfma_f32_16x16x32_bf16 v[52:55], a[252:255], v[244:247], v[52:55]\n"
"s_waitcnt lgkmcnt(3)\n"
"v_mfma_f32_16x16x32_bf16 v[56:59], a[240:243], v[224:227], v[56:59]\n"
"ds_read_b128 v[244:247], v221 offset:26624\n"
"v_mfma_f32_16x16x32_bf16 v[56:59], a[248:251], v[228:231], v[56:59]\n"
"v_mfma_f32_16x16x32_bf16 v[60:63], a[244:247], v[224:227], v[60:63]\n"
"ds_read_b128 v[224:227], v221 offset:20480\n"
"v_mfma_f32_16x16x32_bf16 v[60:63], a[252:255], v[228:231], v[60:63]\n"
"s_waitcnt lgkmcnt(3)\n"
"v_mfma_f32_16x16x32_bf16 v[64:67], a[240:243], v[232:235], v[64:67]\n"
"ds_read_b128 v[228:231], v221 offset:27648\n"
"v_mfma_f32_16x16x32_bf16 v[64:67], a[248:251], v[236:239], v[64:67]\n"
"v_mfma_f32_16x16x32_bf16 v[68:71], a[244:247], v[232:235], v[68:71]\n"
"s_waitcnt vmcnt(7)\n"
"s_barrier\n"
"ds_read_b128 v[232:235], v221 offset:28672\n"
"v_mfma_f32_16x16x32_bf16 v[68:71], a[252:255], v[236:239], v[68:71]\n"
"s_waitcnt lgkmcnt(3)\n"
"v_mfma_f32_16x16x32_bf16 v[72:75], a[240:243], v[240:243], v[72:75]\n"
"ds_read_b128 v[236:239], v221 offset:29696\n"
"v_mfma_f32_16x16x32_bf16 v[72:75], a[248:251], v[244:247], v[72:75]\n"
"v_mfma_f32_16x16x32_bf16 v[76:79], a[244:247], v[240:243], v[76:79]\n"
"ds_read_b128 v[240:243], v221 offset:30720\n"
"v_mfma_f32_16x16x32_bf16 v[76:79], a[252:255], v[244:247], v[76:79]\n"
"s_waitcnt lgkmcnt(3)\n"
"v_mfma_f32_16x16x32_bf16 v[80:83], a[240:243], v[224:227], v[80:83]\n"
"ds_read_b128 v[244:247], v221 offset:31744\n"
"v_mfma_f32_16x16x32_bf16 v[80:83], a[248:251], v[228:231], v[80:83]\n"
"v_mfma_f32_16x16x32_bf16 v[84:87], a[244:247], v[224:227], v[84:87]\n"
"ds_read_b128 v[224:227], v221 offset:32768\n"
"v_mfma_f32_16x16x32_bf16 v[84:87], a[252:255], v[228:231], v[84:87]\n"
"s_waitcnt lgkmcnt(3)\n"
"LZ2_%=:\n"
"v_add_u32 v11, LZ3_%=-LPC_%=, v0\n"
"buffer_load_dwordx4 v2, s[48:51], s64 offen sc1 lds\n"
"v_min_u32 v11, LZ5_%=-LPC_%=-4, v11\n"
"global_load_dword v11, v11, s[70:71]\n"
"v_mfma_f32_16x16x32_bf16 v[32:35], v[144:147], v[232:235], v[32:35]\n"
"ds_read_b128 v[228:231], v221 offset:33792\n"
"v_mfma_f32_16x16x32_bf16 v[36:39], v[148:151], v[232:235], v[36:39]\n"
"v_mfma_f32_16x16x32_bf16 v[40:43], v[144:147], v[236:239], v[40:43]\n"
"ds_read_b128 v[232:235], v221 offset:34816\n"
"v_mfma_f32_16x16x32_bf16 v[44:47], v[148:151], v[236:239], v[44:47]\n"
"s_waitcnt lgkmcnt(4)\n"
"v_mfma_f32_16x16x32_bf16 v[48:51], v[144:147], v[240:243], v[48:51]\n"
"ds_read_b128 v[236:239], v221 offset:35840\n"
"v_mfma_f32_16x16x32_bf16 v[52:55], v[148:151], v[240:243], v[52:55]\n"
"s_waitcnt lgkmcnt(4)\n"
"buffer_load_dwordx4 v2, s[48:51], s64 offen offset:1024 sc1 lds\n"
"v_mfma_f32_16x16x32_bf16 v[56:59], v[144:147], v[244:247], v[56:59]\n"
"ds_read_b128 v[240:243], v221 offset:36864\n"
"v_mfma_f32_16x16x32_bf16 v[60:63], v[148:151], v[244:247], v[60:63]\n"
"s_waitcnt lgkmcnt(4)\n"
"v_mfma_f32_16x16x32_bf16 v[64:67], v[144:147], v[224:227], v[64:67]\n"
"ds_read_b128 v[244:247], v221 offset:37888\n"
"v_mfma_f32_16x16x32_bf16 v[68:71], v[148:151], v[224:227], v[68:71]\n"
"s_waitcnt lgkmcnt(4)\n"
"buffer_load_dwordx4 v2, s[48:51], s64 offen offset:2048 sc1 lds\n"
"v_mfma_f32_16x16x32_bf16 v[72:75], v[144:147], v[228:231], v[72:75]\n"
"ds_read_b128 v[224:227], v221 offset:38912\n"
"v_mfma_f32_16x16x32_bf16 v[76:79], v[148:151], v[228:231], v[76:79]\n"
"s_waitcnt lgkmcnt(4)\n"
"v_mfma_f32_16x16x32_bf16 v[80:83], v[144:147], v[232:235], v[80:83]\n"
"ds_read_b128 v[228:231], v221 offset:39936\n"
"v_mfma_f32_16x16x32_bf16 v[84:87], v[148:151], v[232:235], v[84:87]\n"
"s_waitcnt lgkmcnt(4)\n"
"buffer_load_dwordx4 v2, s[48:51], s64 offen offset:3072 sc1 lds\n"
"s_nop 0\n"
"v_pk_mul_f32 v[32:33], v[32:33], v[152:153]\n"
"s_add_u32 m0, s69, 4096\n"
"s_add_u32 s64, s68, 35000320\n"
"v_pk_mul_f32 v[34:35], v[34:35], v[154:155]\n"
"v_pk_mul_f32 v[36:37], v[36:37], v[156:157]\n"
"v_pk_mul_f32 v[38:39], v[38:39], v[158:159]\n"
"v_pk_mul_f32 v[180:181], v[32:33], v[32:33]\n"
"v_pk_mul_f32 v[182:183], v[34:35], v[34:35]\n"
"v_pk_mul_f32 v[184:185], v[36:37], v[36:37]\n"
"v_pk_mul_f32 v[186:187], v[38:39], v[38:39]\n"
"v_pk_fma_f32 v[180:181], v[180:181], v[162:163], v[162:163] op_sel:[0,0,1] op_sel_hi:[1,0,1]\n"
"v_pk_fma_f32 v[182:183], v[182:183], v[162:163], v[162:163] op_sel:[0,0,1] op_sel_hi:[1,0,1]\n"
"v_pk_fma_f32 v[184:185], v[184:185], v[162:163], v[162:163] op_sel:[0,0,1] op_sel_hi:[1,0,1]\n"
"v_pk_fma_f32 v[186:187], v[186:187], v[162:163], v[162:163] op_sel:[0,0,1] op_sel_hi:[1,0,1]\n"
"v_pk_mul_f32 v[180:181], v[180:181], v[32:33]\n"
"v_pk_mul_f32 v[182:183], v[182:183], v[34:35]\n"
"v_pk_mul_f32 v[184:185], v[184:185], v[36:37]\n"
"v_pk_mul_f32 v[186:187], v[186:187], v[38:39]\n"
"v_exp_f32 v180, v180\n"
"v_exp_f32 v182, v182\n"
"v_exp_f32 v184, v184\n"
"v_exp_f32 v186, v186\n"
"v_exp_f32 v181, v181\n"
"v_exp_f32 v183, v183\n"
"v_exp_f32 v185, v185\n"
"v_exp_f32 v187, v187\n"
"v_pk_add_f32 v[180:181], v[180:181], 1.0 op_sel_hi:[1,0]\n"
"v_pk_add_f32 v[182:183], v[182:183], 1.0 op_sel_hi:[1,0]\n"
"v_pk_add_f32 v[184:185], v[184:185], 1.0 op_sel_hi:[1,0]\n"
"v_pk_add_f32 v[186:187], v[186:187], 1.0 op_sel_hi:[1,0]\n"
"v_rcp_f32 v180, v180\n"
"v_rcp_f32 v182, v182\n"
"v_rcp_f32 v184, v184\n"
"v_rcp_f32 v186, v186\n"
"v_rcp_f32 v181, v181\n"
"v_rcp_f32 v183, v183\n"
"v_rcp_f32 v185, v185\n"
"v_rcp_f32 v187, v187\n"
"v_pk_mul_f32 v[32:33], v[32:33], v[180:181]\n"
"v_pk_mul_f32 v[34:35], v[34:35], v[182:183]\n"
"v_pk_mul_f32 v[36:37], v[36:37], v[184:185]\n"
"v_pk_mul_f32 v[38:39], v[38:39], v[186:187]\n"
"v_pk_mul_f32 v[164:165], v[32:33], 1.0 op_sel_hi:[1,0]\n"
"v_pk_mul_f32 v[172:173], v[32:33], v[32:33]\n"
"v_pk_mul_f32 v[166:167], v[34:35], 1.0 op_sel_hi:[1,0]\n"
"v_pk_mul_f32 v[174:175], v[34:35], v[34:35]\n"
"v_pk_mul_f32 v[168:169], v[36:37], 1.0 op_sel_hi:[1,0]\n"
"v_pk_mul_f32 v[176:177], v[36:37], v[36:37]\n"
"v_pk_mul_f32 v[170:171], v[38:39], 1.0 op_sel_hi:[1,0]\n"
"v_pk_mul_f32 v[178:179], v[38:39], v[38:39]\n"
"v_pk_mul_f32 v[40:41], v[40:41], v[152:153]\n"
"v_pk_mul_f32 v[42:43], v[42:43], v[154:155]\n"
"v_pk_mul_f32 v[44:45], v[44:45], v[156:157]\n"
"v_pk_mul_f32 v[46:47], v[46:47], v[158:159]\n"
"v_pk_mul_f32 v[180:181], v[40:41], v[40:41]\n"
"v_pk_mul_f32 v[182:183], v[42:43], v[42:43]\n"
"v_pk_mul_f32 v[184:185], v[44:45], v[44:45]\n"
"v_pk_mul_f32 v[186:187], v[46:47], v[46:47]\n"
"v_pk_fma_f32 v[180:181], v[180:181], v[162:163], v[162:163] op_sel:[0,0,1] op_sel_hi:[1,0,1]\n"
"v_pk_fma_f32 v[182:183], v[182:183], v[162:163], v[162:163] op_sel:[0,0,1] op_sel_hi:[1,0,1]\n"
"v_pk_fma_f32 v[184:185], v[184:185], v[162:163], v[162:163] op_sel:[0,0,1] op_sel_hi:[1,0,1]\n"
"v_pk_fma_f32 v[186:187], v[186:187], v[162:163], v[162:163] op_sel:[0,0,1] op_sel_hi:[1,0,1]\n"
"v_pk_mul_f32 v[180:181], v[180:181], v[40:41]\n"
"v_pk_mul_f32 v[182:183], v[182:183], v[42:43]\n"
"v_pk_mul_f32 v[184:185], v[184:185], v[44:45]\n"
"v_pk_mul_f32 v[186:187], v[186:187], v[46:47]\n"
"v_exp_f32 v180, v180\n"
"v_exp_f32 v182, v182\n"
"v_exp_f32 v184, v184\n"
"v_exp_f32 v186, v186\n"
"v_exp_f32 v181, v181\n"
"v_exp_f32 v183, v183\n"
"v_exp_f32 v185, v185\n"
"v_exp_f32 v187, v187\n"
"v_pk_add_f32 v[180:181], v[180:181], 1.0 op_sel_hi:[1,0]\n"
"v_pk_add_f32 v[182:183], v[182:183], 1.0 op_sel_hi:[1,0]\n"
"v_pk_add_f32 v[184:185], v[184:185], 1.0 op_sel_hi:[1,0]\n"
"v_pk_add_f32 v[186:187], v[186:187], 1.0 op_sel_hi:[1,0]\n"
"v_rcp_f32 v180, v180\n"
"v_rcp_f32 v182, v182\n"
"v_rcp_f32 v184, v184\n"
"v_rcp_f32 v186, v186\n"
"v_rcp_f32 v181, v181\n"
"v_rcp_f32 v183, v183\n"
"v_rcp_f32 v185, v185\n"
"v_rcp_f32 v187, v187\n"
"v_pk_mul_f32 v[40:41], v[40:41], v[180:181]\n"
"v_pk_mul_f32 v[42:43], v[42:43], v[182:183]\n"
"v_pk_mul_f32 v[44:45], v[44:45], v[184:185]\n"
"v_pk_mul_f32 v[46:47], v[46:47], v[186:187]\n"
"v_pk_add_f32 v[164:165], v[164:165], v[40:41]\n"
"v_pk_fma_f32 v[172:173], v[40:41], v[40:41], v[172:173]\n"
"v_pk_add_f32 v[166:167], v[166:167], v[42:43]\n"
"v_pk_fma_f32 v[174:175], v[42:43], v[42:43], v[174:175]\n"
"v_pk_add_f32 v[168:169], v[168:169], v[44:45]\n"
"v_pk_fma_f32 v[176:177], v[44:45], v[44:45], v[176:177]\n"
"v_pk_add_f32 v[170:171], v[170:171], v[46:47]\n"
"v_pk_fma_f32 v[178:179], v[46:47], v[46:47], v[178:179]\n"
"v_pk_mul_f32 v[48:49], v[48:49], v[152:153]\n"
"v_pk_mul_f32 v[50:51], v[50:51], v[154:155]\n"
"v_pk_mul_f32 v[52:53], v[52:53], v[156:157]\n"
"v_pk_mul_f32 v[54:55], v[54:55], v[158:159]\n"
"v_pk_mul_f32 v[180:181], v[48:49], v[48:49]\n"
"v_pk_mul_f32 v[182:183], v[50:51], v[50:51]\n"
"v_pk_mul_f32 v[184:185], v[52:53], v[52:53]\n"
"v_pk_mul_f32 v[186:187], v[54:55], v[54:55]\n"
"v_pk_fma_f32 v[180:181], v[180:181], v[162:163], v[162:163] op_sel:[0,0,1] op_sel_hi:[1,0,1]\n"
"v_pk_fma_f32 v[182:183], v[182:183], v[162:163], v[162:163] op_sel:[0,0,1] op_sel_hi:[1,0,1]\n"
"v_pk_fma_f32 v[184:185], v[184:185], v[162:163], v[162:163] op_sel:[0,0,1] op_sel_hi:[1,0,1]\n"
"v_pk_fma_f32 v[186:187], v[186:187], v[162:163], v[162:163] op_sel:[0,0,1] op_sel_hi:[1,0,1]\n"
"v_pk_mul_f32 v[180:181], v[180:181], v[48:49]\n"
"v_pk_mul_f32 v[182:183], v[182:183], v[50:51]\n"
"LPG12_%=:\n"
"s_cbranch_execz LPG16_%=\n"
"v_pk_mul_f32 v[184:185], v[184:185], v[52:53]\n"
"v_pk_mul_f32 v[186:187], v[186:187], v[54:55]\n"
"v_exp_f32 v180, v180\n"
"v_exp_f32 v182, v182\n"
"v_exp_f32 v184, v184\n"
"v_exp_f32 v186, v186\n"
"v_exp_f32 v181, v181\n"
"v_exp_f32 v183, v183\n"
"v_exp_f32 v185, v185\n"
"v_exp_f32_e64 v187, v187\n"
"v_pk_add_f32 v[180:181], v[180:181], 1.0 op_sel_hi:[1,0]\n"
"v_pk_add_f32 v[182:183], v[182:183], 1.0 op_sel_hi:[1,0]\n"
"v_pk_add_f32 v[184:185], v[184:185], 1.0 op_sel_hi:[1,0]\n"
"v_pk_add_f32 v[186:187], v[186:187], 1.0 op_sel_hi:[1,0]\n"
"v_rcp_f32 v180, v180\n"
"v_rcp_f32 v182, v182\n"
"v_rcp_f32 v184, v184\n"
"v_rcp_f32 v186, v186\n"
"v_rcp_f32 v181, v181\n"
"v_rcp_f32 v183, v183\n"
"v_rcp_f32 v185, v185\n"
"v_rcp_f32 v187, v187\n"
"v_pk_mul_f32 v[48:49], v[48:49], v[180:181]\n"
"v_pk_mul_f32 v[50:51], v[50:51], v[182:183]\n"
"v_pk_mul_f32 v[52:53], v[52:53], v[184:185]\n"
"v_pk_mul_f32 v[54:55], v[54:55], v[186:187]\n"
"v_pk_add_f32 v[164:165], v[164:165], v[48:49]\n"
"v_pk_fma_f32 v[172:173], v[48:49], v[48:49], v[172:173]\n"
"v_pk_add_f32 v[166:167], v[166:167], v[50:51]\n"
"v_pk_fma_f32 v[174:175], v[50:51], v[50:51], v[174:175]\n"
"v_pk_add_f32 v[168:169], v[168:169], v[52:53]\n"
"v_pk_fma_f32 v[176:177], v[52:53], v[52:53], v[176:177]\n"
"v_pk_add_f32 v[170:171], v[170:171], v[54:55]\n"
"v_pk_fma_f32 v[178:179], v[54:55], v[54:55], v[178:179]\n"
"v_pk_mul_f32 v[56:57], v[56:57], v[152:153]\n"
"v_pk_mul_f32 v[58:59], v[58:59], v[154:155]\n"
"v_pk_mul_f32 v[60:61], v[60:61], v[156:157]\n"
"v_pk_mul_f32 v[62:63], v[62:63], v[158:159]\n"
"v_pk_mul_f32 v[180:181], v[56:57], v[56:57]\n"
"v_pk_mul_f32 v[182:183], v[58:59], v[58:59]\n"
"v_pk_mul_f32 v[184:185], v[60:61], v[60:61]\n"
"v_pk_mul_f32 v[186:187], v[62:63], v[62:63]\n"
"v_pk_fma_f32 v[180:181], v[180:181], v[162:163], v[162:163] op_sel:[0,0,1] op_sel_hi:[1,0,1]\n"
"v_pk_fma_f32 v[182:183], v[182:183], v[162:163], v[162:163] op_sel:[0,0,1] op_sel_hi:[1,0,1]\n"
"v_pk_fma_f32 v[184:185], v[184:185], v[162:163], v[162:163] op_sel:[0,0,1] op_sel_hi:[1,0,1]\n"
"v_pk_fma_f32 v[186:187], v[186:187], v[162:163], v[162:163] op_sel:[0,0,1] op_sel_hi:[1,0,1]\n"
"v_pk_mul_f32 v[180:181], v[180:181], v[56:57]\n"
"v_pk_mul_f32 v[182:183], v[182:183], v[58:59]\n"
"v_pk_mul_f32 v[184:185], v[184:185], v[60:61]\n"
"v_pk_mul_f32 v[186:187], v[186:187], v[62:63]\n"
"v_exp_f32 v180, v180\n"
"v_exp_f32 v182, v182\n"
"v_exp_f32 v184, v184\n"
"v_exp_f32 v186, v186\n"
"v_exp_f32 v181, v181\n"
"v_exp_f32 v183, v183\n"
"v_exp_f32 v185, v185\n"
"v_exp_f32 v187, v187\n"
"v_pk_add_f32 v[180:181], v[180:181], 1.0 op_sel_hi:[1,0]\n"
"v_pk_add_f32 v[182:183], v[182:183], 1.0 op_sel_hi:[1,0]\n"
"v_pk_add_f32 v[184:185], v[184:185], 1.0 op_sel_hi:[1,0]\n"
"v_pk_add_f32 v[186:187], v[186:187], 1.0 op_sel_hi:[1,0]\n"
"v_rcp_f32 v180, v180\n"
"v_rcp_f32 v182, v182\n"
"v_rcp_f32 v184, v184\n"
"v_rcp_f32 v186, v186\n"
"v_rcp_f32 v181, v181\n"
"v_rcp_f32 v183, v183\n"
"v_rcp_f32 v185, v185\n"
"v_rcp_f32 v187, v187\n"
"v_pk_mul_f32 v[56:57], v[56:57], v[180:181]\n"
"v_pk_mul_f32 v[58:59], v[58:59], v[182:183]\n"
"v_pk_mul_f32 v[60:61], v[60:61], v[184:185]\n"
"v_pk_mul_f32 v[62:63], v[62:63], v[186:187]\n"
"v_pk_add_f32 v[164:165], v[164:165], v[56:57]\n"
"v_pk_fma_f32 v[172:173], v[56:57], v[56:57], v[172:173]\n"
"v_pk_add_f32 v[166:167], v[166:167], v[58:59]\n"
"v_pk_fma_f32 v[174:175], v[58:59], v[58:59], v[174:175]\n"
"v_pk_add_f32 v[168:169], v[168:169], v[60:61]\n"
"v_pk_fma_f32 v[176:177], v[60:61], v[60:61], v[176:177]\n"
"v_pk_add_f32 v[170:171], v[170:171], v[62:63]\n"
"v_pk_fma_f32 v[178:179], v[62:63], v[62:63], v[178:179]\n"
"v_pk_mul_f32 v[64:65], v[64:65], v[152:153]\n"
"v_pk_mul_f32 v[66:67], v[66:67], v[154:155]\n"
"v_pk_mul_f32 v[68:69], v[68:69], v[156:157]\n"
"buffer_load_dwordx4 v2, s[48:51], s64 offen sc1 lds\n"
"v_pk_mul_f32 v[70:71], v[70:71], v[158:159]\n"
"v_pk_mul_f32 v[180:181], v[64:65], v[64:65]\n"
"v_pk_mul_f32 v[182:183], v[66:67], v[66:67]\n"
"v_pk_mul_f32 v[184:185], v[68:69], v[68:69]\n"
"v_pk_mul_f32 v[186:187], v[70:71], v[70:71]\n"
"v_pk_fma_f32 v[180:181], v[180:181], v[162:163], v[162:163] op_sel:[0,0,1] op_sel_hi:[1,0,1]\n"
"v_pk_fma_f32 v[182:183], v[182:183], v[162:163], v[162:163] op_sel:[0,0,1] op_sel_hi:[1,0,1]\n"
"v_pk_fma_f32 v[184:185], v[184:185], v[162:163], v[162:163] op_sel:[0,0,1] op_sel_hi:[1,0,1]\n"
"v_pk_fma_f32 v[186:187], v[186:187], v[162:163], v[162:163] op_sel:[0,0,1] op_sel_hi:[1,0,1]\n"
"v_pk_mul_f32 v[180:181], v[180:181], v[64:65]\n"
"v_pk_mul_f32 v[182:183], v[182:183], v[66:67]\n"
"v_pk_mul_f32 v[184:185], v[184:185], v[68:69]\n"
"v_pk_mul_f32 v[186:187], v[186:187], v[70:71]\n"
"v_exp_f32 v180, v180\n"
"v_exp_f32 v182, v182\n"
"v_exp_f32 v184, v184\n"
"buffer_load_dwordx4 v2, s[48:51], s64 offen offset:1024 sc1 lds\n"
"v_exp_f32 v186, v186\n"
"v_exp_f32 v181, v181\n"
"v_exp_f32 v183, v183\n"
"v_exp_f32 v185, v185\n"
"v_exp_f32 v187, v187\n"
"v_pk_add_f32 v[180:181], v[180:181], 1.0 op_sel_hi:[1,0]\n"
"v_pk_add_f32 v[182:183], v[182:183], 1.0 op_sel_hi:[1,0]\n"
"v_pk_add_f32 v[184:185], v[184:185], 1.0 op_sel_hi:[1,0]\n"
"v_pk_add_f32 v[186:187], v[186:187], 1.0 op_sel_hi:[1,0]\n"
"v_rcp_f32 v180, v180\n"
"v_rcp_f32 v182, v182\n"
"buffer_load_dwordx4 v2, s[48:51], s64 offen offset:2048 sc1 lds\n"
"v_rcp_f32 v184, v184\n"
"s_add_u32 m0, s69, 28672\n"
"s_add_u32 s64, s68, 35454976\n"
"v_rcp_f32 v186, v186\n"
"v_rcp_f32 v181, v181\n"
"v_rcp_f32 v183, v183\n"
"v_rcp_f32 v185, v185\n"
"v_rcp_f32 v187, v187\n"
"v_pk_mul_f32 v[64:65], v[64:65], v[180:181]\n"
"v_pk_mul_f32 v[66:67], v[66:67], v[182:183]\n"
"v_pk_mul_f32 v[68:69], v[68:69], v[184:185]\n"
"v_pk_mul_f32 v[70:71], v[70:71], v[186:187]\n"
"buffer_load_dwordx4 v2, s[48:51], s64 offen sc1 lds\n"
"v_pk_add_f32 v[164:165], v[164:165], v[64:65]\n"
"v_pk_fma_f32 v[172:173], v[64:65], v[64:65], v[172:173]\n"
"v_pk_add_f32 v[166:167], v[166:167], v[66:67]\n"
"v_pk_fma_f32 v[174:175], v[66:67], v[66:67], v[174:175]\n"
"v_pk_add_f32 v[168:169], v[168:169], v[68:69]\n"
"v_pk_fma_f32 v[176:177], v[68:69], v[68:69], v[176:177]\n"
"v_pk_add_f32 v[170:171], v[170:171], v[70:71]\n"
"v_pk_fma_f32 v[178:179], v[70:71], v[70:71], v[178:179]\n"
"v_pk_mul_f32 v[72:73], v[72:73], v[152:153]\n"
"v_pk_mul_f32 v[74:75], v[74:75], v[154:155]\n"
"v_pk_mul_f32 v[76:77], v[76:77], v[156:157]\n"
"v_pk_mul_f32 v[78:79], v[78:79], v[158:159]\n"
"v_pk_mul_f32 v[180:181], v[72:73], v[72:73]\n"
"v_pk_mul_f32 v[182:183], v[74:75], v[74:75]\n"
"v_pk_mul_f32 v[184:185], v[76:77], v[76:77]\n"
"v_pk_mul_f32 v[186:187], v[78:79], v[78:79]\n"
"v_pk_fma_f32 v[180:181], v[180:181], v[162:163], v[162:163] op_sel:[0,0,1] op_sel_hi:[1,0,1]\n"
"v_pk_fma_f32 v[182:183], v[182:183], v[162:163], v[162:163] op_sel:[0,0,1] op_sel_hi:[1,0,1]\n"
"buffer_load_dwordx4 v2, s[48:51], s64 offen offset:1024 sc1 lds\n"
"v_pk_fma_f32 v[184:185], v[184:185], v[162:163], v[162:163] op_sel:[0,0,1] op_sel_hi:[1,0,1]\n"
"v_pk_fma_f32 v[186:187], v[186:187], v[162:163], v[162:163] op_sel:[0,0,1] op_sel_hi:[1,0,1]\n"
"v_pk_mul_f32 v[180:181], v[180:181], v[72:73]\n"
"v_pk_mul_f32 v[182:183], v[182:183], v[74:75]\n"
"v_pk_mul_f32 v[184:185], v[184:185], v[76:77]\n"
"v_pk_mul_f32 v[186:187], v[186:187], v[78:79]\n"
"v_exp_f32 v180, v180\n"
"v_exp_f32 v182, v182\n"
"v_exp_f32 v184, v184\n"
"v_exp_f32 v186, v186\n"
"v_exp_f32 v181, v181\n"
"v_exp_f32 v183, v183\n"
"buffer_load_dwordx4 v2, s[48:51], s64 offen offset:2048 sc1 lds\n"
"v_exp_f32 v185, v185\n"
"v_exp_f32 v187, v187\n"
"v_pk_add_f32 v[180:181], v[180:181], 1.0 op_sel_hi:[1,0]\n"
"v_pk_add_f32 v[182:183], v[182:183], 1.0 op_sel_hi:[1,0]\n"
"v_pk_add_f32 v[184:185], v[184:185], 1.0 op_sel_hi:[1,0]\n"
"v_pk_add_f32 v[186:187], v[186:187], 1.0 op_sel_hi:[1,0]\n"
"v_rcp_f32 v180, v180\n"
"v_rcp_f32 v182, v182\n"
"v_rcp_f32 v184, v184\n"
"v_rcp_f32 v186, v186\n"
"v_rcp_f32 v181, v181\n"
"buffer_load_dwordx4 v2, s[48:51], s64 offen offset:3072 sc1 lds\n"
"v_rcp_f32 v183, v183\n"
"s_add_u32 m0, s69, 32768\n"
"s_add_u32 s64, s68, 35459072\n"
"v_rcp_f32 v185, v185\n"
"v_rcp_f32 v187, v187\n"
"v_pk_mul_f32 v[72:73], v[72:73], v[180:181]\n"
"v_pk_mul_f32 v[74:75], v[74:75], v[182:183]\n"
"v_pk_mul_f32 v[76:77], v[76:77], v[184:185]\n"
"v_pk_mul_f32 v[78:79], v[78:79], v[186:187]\n"
"v_pk_add_f32 v[164:165], v[164:165], v[72:73]\n"
"v_pk_fma_f32 v[172:173], v[72:73], v[72:73], v[172:173]\n"
"v_pk_add_f32 v[166:167], v[166:167], v[74:75]\n"
"v_pk_fma_f32 v[174:175], v[74:75], v[74:75], v[174:175]\n"
"v_pk_add_f32 v[168:169], v[168:169], v[76:77]\n"
"v_pk_fma_f32 v[176:177], v[76:77], v[76:77], v[176:177]\n"
"v_pk_add_f32 v[170:171], v[170:171], v[78:79]\n"
"buffer_load_dwordx4 v2, s[48:51], s64 offen sc1 lds\n"
"v_pk_fma_f32 v[178:179], v[78:79], v[78:79], v[178:179]\n"
"v_pk_mul_f32 v[80:81], v[80:81], v[152:153]\n"
"v_pk_mul_f32 v[82:83], v[82:83], v[154:155]\n"
"v_pk_mul_f32 v[84:85], v[84:85], v[156:157]\n"
"v_pk_mul_f32 v[86:87], v[86:87], v[158:159]\n"
"v_pk_mul_f32 v[180:181], v[80:81], v[80:81]\n"
"v_pk_mul_f32 v[182:183], v[82:83], v[82:83]\n"
"v_pk_mul_f32 v[184:185], v[84:85], v[84:85]\n"
"v_pk_mul_f32 v[186:187], v[86:87], v[86:87]\n"
"v_pk_fma_f32 v[180:181], v[180:181], v[162:163], v[162:163] op_sel:[0,0,1] op_sel_hi:[1,0,1]\n"
"v_pk_fma_f32 v[182:183], v[182:183], v[162:163], v[162:163] op_sel:[0,0,1] op_sel_hi:[1,0,1]\n"
"v_pk_fma_f32 v[184:185], v[184:185], v[162:163], v[162:163] op_sel:[0,0,1] op_sel_hi:[1,0,1]\n"
"v_pk_fma_f32 v[186:187], v[186:187], v[162:163], v[162:163] op_sel:[0,0,1] op_sel_hi:[1,0,1]\n"
"v_pk_mul_f32 v[180:181], v[180:181], v[80:81]\n"
"v_pk_mul_f32 v[182:183], v[182:183], v[82:83]\n"
"v_pk_mul_f32 v[184:185], v[184:185], v[84:85]\n"
"v_pk_mul_f32 v[186:187], v[186:187], v[86:87]\n"
"v_exp_f32 v180, v180\n"
"buffer_load_dwordx4 v2, s[48:51], s64 offen offset:1024 sc1 lds\n"
"v_exp_f32 v182, v182\n"
"v_exp_f32 v184, v184\n"
"v_exp_f32 v186, v186\n"
"v_exp_f32 v181, v181\n"
"v_exp_f32 v183, v183\n"
"v_exp_f32 v185, v185\n"
"v_exp_f32 v187, v187\n"
"v_pk_add_f32 v[180:181], v[180:181], 1.0 op_sel_hi:[1,0]\n"
"v_pk_add_f32 v[182:183], v[182:183], 1.0 op_sel_hi:[1,0]\n"
"v_pk_add_f32 v[184:185], v[184:185], 1.0 op_sel_hi:[1,0]\n"
"buffer_load_dwordx4 v2, s[48:51], s64 offen offset:2048 sc1 lds\n"
"v_pk_add_f32 v[186:187], v[186:187], 1.0 op_sel_hi:[1,0]\n"
"v_rcp_f32 v180, v180\n"
"v_rcp_f32 v182, v182\n"
"v_rcp_f32 v184, v184\n"
"v_rcp_f32 v186, v186\n"
"v_rcp_f32 v181, v181\n"
"v_rcp_f32 v183, v183\n"
"v_rcp_f32 v185, v185\n"
"v_rcp_f32 v187, v187\n"
"v_pk_mul_f32 v[80:81], v[80:81], v[180:181]\n"
"v_pk_mul_f32 v[82:83], v[82:83], v[182:183]\n"
"v_pk_mul_f32 v[84:85], v[84:85], v[184:185]\n"
"v_pk_mul_f32 v[86:87], v[86:87], v[186:187]\n"
"v_pk_add_f32 v[164:165], v[164:165], v[80:81]\n"
"v_pk_fma_f32 v[172:173], v[80:81], v[80:81], v[172:173]\n"
"v_pk_add_f32 v[166:167], v[166:167], v[82:83]\n"
"v_pk_fma_f32 v[174:175], v[82:83], v[82:83], v[174:175]\n"
"v_pk_add_f32 v[168:169], v[168:169], v[84:85]\n"
"v_pk_fma_f32 v[176:177], v[84:85], v[84:85], v[176:177]\n"
"v_pk_add_f32 v[170:171], v[170:171], v[86:87]\n"
"v_pk_fma_f32 v[178:179], v[86:87], v[86:87], v[178:179]\n"
"v_add_f32_dpp v164, v164, v164 row_ror:8 row_mask:0xf bank_mask:0x3\n"
"v_add_f32_dpp v172, v172, v172 row_ror:8 row_mask:0xf bank_mask:0x3\n"
"v_add_f32_dpp v168, v168, v168 row_ror:8 row_mask:0xf bank_mask:0x3\n"
"v_add_f32_dpp v176, v176, v176 row_ror:8 row_mask:0xf bank_mask:0x3\n"
"v_add_f32_dpp v164, v166, v166 row_ror:8 row_mask:0xf bank_mask:0xc\n"
"v_add_f32_dpp v172, v174, v174 row_ror:8 row_mask:0xf bank_mask:0xc\n"
"v_add_f32_dpp v168, v170, v170 row_ror:8 row_mask:0xf bank_mask:0xc\n"
"v_add_f32_dpp v176, v178, v178 row_ror:8 row_mask:0xf bank_mask:0xc\n"
"v_add_f32_dpp v165, v165, v165 row_ror:8 row_mask:0xf bank_mask:0x3\n"
"v_add_f32_dpp v173, v173, v173 row_ror:8 row_mask:0xf bank_mask:0x3\n"
"v_add_f32_dpp v169, v169, v169 row_ror:8 row_mask:0xf bank_mask:0x3\n"
"v_add_f32_dpp v177, v177, v177 row_ror:8 row_mask:0xf bank_mask:0x3\n"
"v_add_f32_dpp v165, v167, v167 row_ror:8 row_mask:0xf bank_mask:0xc\n"
"v_add_f32_dpp v173, v175, v175 row_ror:8 row_mask:0xf bank_mask:0xc\n"
"v_add_f32_dpp v169, v171, v171 row_ror:8 row_mask:0xf bank_mask:0xc\n"
"v_add_f32_dpp v177, v179, v179 row_ror:8 row_mask:0xf bank_mask:0xc\n"
"v_add_f32_dpp v164, v164, v164 row_half_mirror row_mask:0xf bank_mask:0x5\n"
"v_add_f32_dpp v172, v172, v172 row_half_mirror row_mask:0xf bank_mask:0x5\n"
"v_add_f32_dpp v168, v168, v168 row_half_mirror row_mask:0xf bank_mask:0x5\n"
"v_add_f32_dpp v176, v176, v176 row_half_mirror row_mask:0xf bank_mask:0x5\n"
"v_add_f32_dpp v164, v165, v165 row_half_mirror row_mask:0xf bank_mask:0xa\n"
"v_add_f32_dpp v172, v173, v173 row_half_mirror row_mask:0xf bank_mask:0xa\n"
"v_add_f32_dpp v168, v169, v169 row_half_mirror row_mask:0xf bank_mask:0xa\n"
"v_add_f32_dpp v176, v177, v177 row_half_mirror row_mask:0xf bank_mask:0xa\n"
"v_add_f32_dpp v164, v164, v164 quad_perm:[1,0,3,2] row_mask:0xf bank_mask:0xf\n"
"v_add_f32_dpp v172, v172, v172 quad_perm:[1,0,3,2] row_mask:0xf bank_mask:0xf\n"
"v_add_f32_dpp v168, v168, v168 quad_perm:[1,0,3,2] row_mask:0xf bank_mask:0xf\n"
"v_add_f32_dpp v176, v176, v176 quad_perm:[1,0,3,2] row_mask:0xf bank_mask:0xf\n"
"v_add_f32_dpp v164, v164, v164 quad_perm:[2,3,0,1] row_mask:0xf bank_mask:0xf\n"
"v_add_f32_dpp v172, v172, v172 quad_perm:[2,3,0,1] row_mask:0xf bank_mask:0xf\n"
"v_add_f32_dpp v168, v168, v168 quad_perm:[2,3,0,1] row_mask:0xf bank_mask:0xf\n"
"v_add_f32_dpp v176, v176, v176 quad_perm:[2,3,0,1] row_mask:0xf bank_mask:0xf\n"
"v_cndmask_b32 v180, v164, v172, s[72:73]\n"
"v_cndmask_b32 v180, v180, v168, s[74:75]\n"
"v_cndmask_b32 v180, v180, v176, s[76:77]\n"
"v_min_u32 v180, -2, v180\n"
"buffer_store_dword v180, v248, s[48:51], s24 offen sc1\n"
"v_mfma_f32_16x16x32_bf16 v[88:91], v[236:239], a[0:3], 0\n"
"ds_read_b128 v[232:235], v221 offset:40960\n"
"v_mfma_f32_16x16x32_bf16 v[92:95], v[236:239], a[4:7], 0\n"
"s_waitcnt lgkmcnt(4)\n"
"v_mfma_f32_16x16x32_bf16 v[96:99], v[240:243], a[0:3], 0\n"
"ds_read_b128 v[236:239], v221 offset:41984\n"
"v_mfma_f32_16x16x32_bf16 v[100:103], v[240:243], a[4:7], 0\n"
"s_waitcnt lgkmcnt(4)\n"
"v_mfma_f32_16x16x32_bf16 v[104:107], v[244:247], a[0:3], 0\n"
"ds_read_b128 v[240:243], v221 offset:43008\n"
"v_mfma_f32_16x16x32_bf16 v[108:111], v[244:247], a[4:7], 0\n"
"s_waitcnt lgkmcnt(4)\n"
"v_mfma_f32_16x16x32_bf16 v[112:115], v[224:227], a[0:3], 0\n"
"ds_read_b128 v[244:247], v221 offset:50176\n"
"v_mfma_f32_16x16x32_bf16 v[116:119], v[224:227], a[4:7], 0\n"
"s_waitcnt lgkmcnt(4)\n"
"v_mfma_f32_16x16x32_bf16 v[120:123], v[228:231], a[0:3], 0\n"
"ds_read_b128 v[224:227], v221 offset:44032\n"
"v_mfma_f32_16x16x32_bf16 v[124:127], v[228:231], a[4:7], 0\n"
"s_waitcnt lgkmcnt(4)\n"
"v_mfma_f32_16x16x32_bf16 v[128:131], v[232:235], a[0:3], 0\n"
"ds_read_b128 v[228:231], v221 offset:51200\n"
"v_mfma_f32_16x16x32_bf16 v[132:135], v[232:235], a[4:7], 0\n"
"s_waitcnt lgkmcnt(4)\n"
"v_mfma_f32_16x16x32_bf16 v[136:139], v[236:239], a[0:3], 0\n"
"ds_read_b128 v[232:235], v221 offset:45056\n"
"v_mfma_f32_16x16x32_bf16 v[140:143], v[236:239], a[4:7], 0\n"
"s_waitcnt lgkmcnt(3)\n"
"v_mfma_f32_16x16x32_bf16 v[88:91], v[240:243], a[8:11], v[88:91]\n"
"ds_read_b128 v[236:239], v221 offset:52224\n"
"s_add_u32 s63, s85, 22020096\n"
"v_mfma_f32_16x16x32_bf16 v[88:91], v[244:247], a[16:19], v[88:91]\n"
"buffer_load_dwordx4 a[0:3], v2, s[48:51], s63 offen sc1\n"
"v_mfma_f32_16x16x32_bf16 v[92:95], v[240:243], a[12:15], v[92:95]\n"
"ds_read_b128 v[240:243], v221 offset:46080\n"
"v_mfma_f32_16x16x32_bf16 v[92:95], v[244:247], a[20:23], v[92:95]\n"
"s_waitcnt lgkmcnt(3)\n"
"v_mfma_f32_16x16x32_bf16 v[96:99], v[224:227], a[8:11], v[96:99]\n"
"ds_read_b128 v[244:247], v221 offset:53248\n"
"v_mfma_f32_16x16x32_bf16 v[96:99], v[228:231], a[16:19], v[96:99]\n"
"buffer_load_dwordx4 a[4:7], v2, s[48:51], s63 offen offset:1024 sc1\n"
"v_mfma_f32_16x16x32_bf16 v[100:103], v[224:227], a[12:15], v[100:103]\n"
"ds_read_b128 v[224:227], v221 offset:47104\n"
"v_mfma_f32_16x16x32_bf16 v[100:103], v[228:231], a[20:23], v[100:103]\n"
"s_waitcnt lgkmcnt(3)\n"
"v_mfma_f32_16x16x32_bf16 v[104:107], v[232:235], a[8:11], v[104:107]\n"
"ds_read_b128 v[228:231], v221 offset:54272\n"
"v_mfma_f32_16x16x32_bf16 v[104:107], v[236:239], a[16:19], v[104:107]\n"
"v_mfma_f32_16x16x32_bf16 v[108:111], v[232:235], a[12:15], v[108:111]\n"
"ds_read_b128 v[232:235], v221 offset:48128\n"
"v_mfma_f32_16x16x32_bf16 v[108:111], v[236:239], a[20:23], v[108:111]\n"
"s_waitcnt lgkmcnt(3)\n"
"v_mfma_f32_16x16x32_bf16 v[112:115], v[240:243], a[8:11], v[112:115]\n"
"ds_read_b128 v[236:239], v221 offset:55296\n"
"v_mfma_f32_16x16x32_bf16 v[112:115], v[244:247], a[16:19], v[112:115]\n"
"v_mfma_f32_16x16x32_bf16 v[116:119], v[240:243], a[12:15], v[116:119]\n"
"ds_read_b128 v[240:243], v221 offset:49152\n"
"v_mfma_f32_16x16x32_bf16 v[116:119], v[244:247], a[20:23], v[116:119]\n"
"s_waitcnt lgkmcnt(3)\n"
"v_mfma_f32_16x16x32_bf16 v[120:123], v[224:227], a[8:11], v[120:123]\n"
"ds_read_b128 v[244:247], v221 offset:56320\n"
"v_mfma_f32_16x16x32_bf16 v[120:123], v[228:231], a[16:19], v[120:123]\n"
"v_mfma_f32_16x16x32_bf16 v[124:127], v[224:227], a[12:15], v[124:127]\n"
"s_waitcnt vmcnt(18)\n"
"s_barrier\n"
"ds_read_b128 v[224:227], v222\n"
"v_mfma_f32_16x16x32_bf16 v[124:127], v[228:231], a[20:23], v[124:127]\n"
"s_waitcnt lgkmcnt(3)\n"
"v_mfma_f32_16x16x32_bf16 v[128:131], v[232:235], a[8:11], v[128:131]\n"
"ds_read_b128 v[228:231], v222 offset:7168\n"
"s_add_u32 m0, s69, 57344\n"
"v_mfma_f32_16x16x32_bf16 v[128:131], v[236:239], a[16:19], v[128:131]\n"
"s_add_u32 s64, s68, 35913728\n"
"v_mfma_f32_16x16x32_bf16 v[132:135], v[232:235], a[12:15], v[132:135]\n"
"ds_read_b128 v[232:235], v222 offset:1024\n"
"v_mfma_f32_16x16x32_bf16 v[132:135], v[236:239], a[20:23], v[132:135]\n"
"s_waitcnt lgkmcnt(3)\n"
"buffer_load_dwordx4 v2, s[48:51], s64 offen sc1 lds\n"
"v_mfma_f32_16x16x32_bf16 v[136:139], v[240:243], a[8:11], v[136:139]\n"
"ds_read_b128 v[236:239], v222 offset:8192\n"
"v_mfma_f32_16x16x32_bf16 v[136:139], v[244:247], a[16:19], v[136:139]\n"
"v_mfma_f32_16x16x32_bf16 v[140:143], v[240:243], a[12:15], v[140:143]\n"
"ds_read_b128 v[240:243], v222 offset:2048\n"
"v_mfma_f32_16x16x32_bf16 v[140:143], v[244:247], a[20:23], v[140:143]\n"
"s_waitcnt lgkmcnt(3)\n"
"buffer_load_dwordx4 v2, s[48:51], s64 offen offset:1024 sc1 lds\n"
"v_mfma_f32_16x16x32_bf16 v[88:91], v[224:227], a[24:27], v[88:91]\n"
"ds_read_b128 v[244:247], v222 offset:9216\n"
"s_add_u32 s63, s85, 22151168\n"
"v_mfma_f32_16x16x32_bf16 v[88:91], v[228:231], a[32:35], v[88:91]\n"
"v_mfma_f32_16x16x32_bf16 v[92:95], v[224:227], a[28:31], v[92:95]\n"
"ds_read_b128 v[224:227], v222 offset:3072\n"
"v_mfma_f32_16x16x32_bf16 v[92:95], v[228:231], a[36:39], v[92:95]\n"
"s_waitcnt lgkmcnt(3)\n"
"buffer_load_dwordx4 v2, s[48:51], s64 offen offset:2048 sc1 lds\n"
"v_mfma_f32_16x16x32_bf16 v[96:99], v[232:235], a[24:27], v[96:99]\n"
"ds_read_b128 v[228:231], v222 offset:10240\n"
"v_mfma_f32_16x16x32_bf16 v[96:99], v[236:239], a[32:35], v[96:99]\n"
"v_mfma_f32_16x16x32_bf16 v[100:103], v[232:235], a[28:31], v[100:103]\n"
"ds_read_b128 v[232:235], v222 offset:4096\n"
"v_mfma_f32_16x16x32_bf16 v[100:103], v[236:239], a[36:39], v[100:103]\n"
"s_waitcnt lgkmcnt(3)\n"
"v_mfma_f32_16x16x32_bf16 v[104:107], v[240:243], a[24:27], v[104:107]\n"
"ds_read_b128 v[236:239], v222 offset:11264\n"
"v_mfma_f32_16x16x32_bf16 v[104:107], v[244:247], a[32:35], v[104:107]\n"
"buffer_load_dwordx4 v2, s[48:51], s64 offen offset:3072 sc1 lds\n"
"v_mfma_f32_16x16x32_bf16 v[108:111], v[240:243], a[28:31], v[108:111]\n"
"ds_read_b128 v[240:243], v222 offset:5120\n"
"s_add_u32 m0, s69, 61440\n"
"v_mfma_f32_16x16x32_bf16 v[108:111], v[244:247], a[36:39], v[108:111]\n"
"s_waitcnt lgkmcnt(3)\n"
"s_add_u32 s64, s68, 35917824\n"
"v_mfma_f32_16x16x32_bf16 v[112:115], v[224:227], a[24:27], v[112:115]\n"
"ds_read_b128 v[244:247], v222 offset:12288\n"
"v_mfma_f32_16x16x32_bf16 v[112:115], v[228:231], a[32:35], v[112:115]\n"
"buffer_load_dwordx4 v2, s[48:51], s64 offen sc1 lds\n"
"v_mfma_f32_16x16x32_bf16 v[116:119], v[224:227], a[28:31], v[116:119]\n"
"ds_read_b128 v[224:227], v222 offset:6144\n"
"v_mfma_f32_16x16x32_bf16 v[116:119], v[228:231], a[36:39], v[116:119]\n"
"s_waitcnt lgkmcnt(3)\n"
"v_mfma_f32_16x16x32_bf16 v[120:123], v[232:235], a[24:27], v[120:123]\n"
"ds_read_b128 v[228:231], v222 offset:13312\n"
"v_mfma_f32_16x16x32_bf16 v[120:123], v[236:239], a[32:35], v[120:123]\n"
"buffer_load_dwordx4 v2, s[48:51], s64 offen offset:1024 sc1 lds\n"
"v_mfma_f32_16x16x32_bf16 v[124:127], v[232:235], a[28:31], v[124:127]\n"
"ds_read_b128 v[232:235], v222 offset:14336\n"
"v_mfma_f32_16x16x32_bf16 v[124:127], v[236:239], a[36:39], v[124:127]\n"
"s_waitcnt lgkmcnt(3)\n"
"v_mfma_f32_16x16x32_bf16 v[128:131], v[240:243], a[24:27], v[128:131]\n"
"ds_read_b128 v[236:239], v222 offset:21504\n"
"v_mfma_f32_16x16x32_bf16 v[128:131], v[244:247], a[32:35], v[128:131]\n"
"v_mfma_f32_16x16x32_bf16 v[132:135], v[240:243], a[28:31], v[132:135]\n"
"ds_read_b128 v[240:243], v222 offset:15360\n"
"v_mfma_f32_16x16x32_bf16 v[132:135], v[244:247], a[36:39], v[132:135]\n"
"s_waitcnt lgkmcnt(3)\n"
"buffer_load_dwordx4 v2, s[48:51], s64 offen offset:2048 sc1 lds\n"
"v_mfma_f32_16x16x32_bf16 v[136:139], v[224:227], a[24:27], v[136:139]\n"
"ds_read_b128 v[244:247], v222 offset:22528\n"
"v_mfma_f32_16x16x32_bf16 v[136:139], v[228:231], a[32:35], v[136:139]\n"
"v_mfma_f32_16x16x32_bf16 v[140:143], v[224:227], a[28:31], v[140:143]\n"
"ds_read_b128 v[224:227], v222 offset:16384\n"
"v_mfma_f32_16x16x32_bf16 v[140:143], v[228:231], a[36:39], v[140:143]\n"
"s_waitcnt lgkmcnt(3)\n"
"buffer_load_dwordx4 a[8:11], v2, s[48:51], s63 offen sc1\n"
"buffer_load_dwordx4 v[164:167], v220, s[48:51], s25 offen sc1\n"
"buffer_load_dwordx4 v[168:171], v220, s[48:51], s25 offen offset:1024 sc1\n"
"buffer_load_dwordx4 v[172:175], v220, s[48:51], s25 offen offset:2048 sc1\n"
"buffer_load_dwordx4 v[176:179], v220, s[48:51], s25 offen offset:3072 sc1\n"
"v_mfma_f32_16x16x32_bf16 v[88:91], v[232:235], a[40:43], v[88:91]\n"
"ds_read_b128 v[228:231], v222 offset:23552\n"
"v_mfma_f32_16x16x32_bf16 v[88:91], v[236:239], a[48:51], v[88:91]\n"
"v_mfma_f32_16x16x32_bf16 v[92:95], v[232:235], a[44:47], v[92:95]\n"
"ds_read_b128 v[232:235], v222 offset:17408\n"
"v_mfma_f32_16x16x32_bf16 v[92:95], v[236:239], a[52:55], v[92:95]\n"
"s_waitcnt lgkmcnt(3)\n"
"v_mfma_f32_16x16x32_bf16 v[96:99], v[240:243], a[40:43], v[96:99]\n"
"ds_read_b128 v[236:239], v222 offset:24576\n"
"v_mfma_f32_16x16x32_bf16 v[96:99], v[244:247], a[48:51], v[96:99]\n"
"v_mfma_f32_16x16x32_bf16 v[100:103], v[240:243], a[44:47], v[100:103]\n"
"ds_read_b128 v[240:243], v222 offset:18432\n"
"v_mfma_f32_16x16x32_bf16 v[100:103], v[244:247], a[52:55], v[100:103]\n"
"s_waitcnt lgkmcnt(3)\n"
"v_mfma_f32_16x16x32_bf16 v[104:107], v[224:227], a[40:43], v[104:107]\n"
"ds_read_b128 v[244:247], v222 offset:25600\n"
"v_mfma_f32_16x16x32_bf16 v[104:107], v[228:231], a[48:51], v[104:107]\n"
"v_mfma_f32_16x16x32_bf16 v[108:111], v[224:227], a[44:47], v[108:111]\n"
"ds_read_b128 v[224:227], v222 offset:19456\n"
"v_mfma_f32_16x16x32_bf16 v[108:111], v[228:231], a[52:55], v[108:111]\n"
"s_waitcnt lgkmcnt(3)\n"
"v_mfma_f32_16x16x32_bf16 v[112:115], v[232:235], a[40:43], v[112:115]\n"
"ds_read_b128 v[228:231], v222 offset:26624\n"
"v_mfma_f32_16x16x32_bf16 v[112:115], v[236:239], a[48:51], v[112:115]\n"
"v_mfma_f32_16x16x32_bf16 v[116:119], v[232:235], a[44:47], v[116:119]\n"
"ds_read_b128 v[232:235], v222 offset:20480\n"
"v_mfma_f32_16x16x32_bf16 v[116:119], v[236:239], a[52:55], v[116:119]\n"
"s_waitcnt lgkmcnt(3)\n"
"v_mfma_f32_16x16x32_bf16 v[120:123], v[240:243], a[40:43], v[120:123]\n"
"ds_read_b128 v[236:239], v222 offset:27648\n"
"v_mfma_f32_16x16x32_bf16 v[120:123], v[244:247], a[48:51], v[120:123]\n"
"v_mfma_f32_16x16x32_bf16 v[124:127], v[240:243], a[44:47], v[124:127]\n"
"s_waitcnt vmcnt(22)\n"
"s_barrier\n"
"ds_read_b128 v[240:243], v2\n"
"v_mfma_f32_16x16x32_bf16 v[124:127], v[244:247], a[52:55], v[124:127]\n"
"s_waitcnt lgkmcnt(3)\n"
"v_mfma_f32_16x16x32_bf16 v[128:131], v[224:227], a[40:43], v[128:131]\n"
"ds_read_b128 v[244:247], v2 offset:7168\n"
"s_add_u32 m0, s69, 86016\n"
"v_mfma_f32_16x16x32_bf16 v[128:131], v[228:231], a[48:51], v[128:131]\n"
"s_add_u32 s64, s68, 36372480\n"
"v_mfma_f32_16x16x32_bf16 v[132:135], v[224:227], a[44:47], v[132:135]\n"
"ds_read_b128 v[224:227], v2 offset:1024\n"
"v_mfma_f32_16x16x32_bf16 v[132:135], v[228:231], a[52:55], v[132:135]\n"
"s_waitcnt lgkmcnt(3)\n"
"buffer_load_dwordx4 v2, s[48:51], s64 offen sc1 lds\n"
"v_mfma_f32_16x16x32_bf16 v[136:139], v[232:235], a[40:43], v[136:139]\n"
"ds_read_b128 v[228:231], v2 offset:8192\n"
"v_mfma_f32_16x16x32_bf16 v[136:139], v[236:239], a[48:51], v[136:139]\n"
"v_mfma_f32_16x16x32_bf16 v[140:143], v[232:235], a[44:47], v[140:143]\n"
"ds_read_b128 v[232:235], v2 offset:2048\n"
"v_mfma_f32_16x16x32_bf16 v[140:143], v[236:239], a[52:55], v[140:143]\n"
"s_waitcnt lgkmcnt(3)\n"
"buffer_load_dwordx4 v2, s[48:51], s64 offen offset:1024 sc1 lds\n"
"v_mfma_f32_16x16x32_bf16 v[88:91], v[240:243], a[56:59], v[88:91]\n"
"ds_read_b128 v[236:239], v2 offset:9216\n"
"v_mfma_f32_16x16x32_bf16 v[88:91], v[244:247], a[64:67], v[88:91]\n"
"v_mfma_f32_16x16x32_bf16 v[92:95], v[240:243], a[60:63], v[92:95]\n"
"ds_read_b128 v[240:243], v2 offset:3072\n"
"v_mfma_f32_16x16x32_bf16 v[92:95], v[244:247], a[68:71], v[92:95]\n"
"s_waitcnt lgkmcnt(3)\n"
"buffer_load_dwordx4 v2, s[48:51], s64 offen offset:2048 sc1 lds\n"
"v_mfma_f32_16x16x32_bf16 v[96:99], v[224:227], a[56:59], v[96:99]\n"
"ds_read_b128 v[244:247], v2 offset:10240\n"
"v_mfma_f32_16x16x32_bf16 v[96:99], v[228:231], a[64:67], v[96:99]\n"
"v_mfma_f32_16x16x32_bf16 v[100:103], v[224:227], a[60:63], v[100:103]\n"
"ds_read_b128 v[224:227], v2 offset:4096\n"
"v_mfma_f32_16x16x32_bf16 v[100:103], v[228:231], a[68:71], v[100:103]\n"
"s_waitcnt lgkmcnt(3)\n"
"v_mfma_f32_16x16x32_bf16 v[104:107], v[232:235], a[56:59], v[104:107]\n"
"ds_read_b128 v[228:231], v2 offset:11264\n"
"v_mfma_f32_16x16x32_bf16 v[104:107], v[236:239], a[64:67], v[104:107]\n"
"buffer_load_dwordx4 v2, s[48:51], s64 offen offset:3072 sc1 lds\n"
"v_mfma_f32_16x16x32_bf16 v[108:111], v[232:235], a[60:63], v[108:111]\n"
"ds_read_b128 v[232:235], v2 offset:5120\n"
"s_add_u32 m0, s69, 90112\n"
"v_mfma_f32_16x16x32_bf16 v[108:111], v[236:239], a[68:71], v[108:111]\n"
"s_waitcnt lgkmcnt(3)\n"
"s_add_u32 s64, s68, 36376576\n"
"v_mfma_f32_16x16x32_bf16 v[112:115], v[240:243], a[56:59], v[112:115]\n"
"ds_read_b128 v[236:239], v2 offset:12288\n"
"v_mfma_f32_16x16x32_bf16 v[112:115], v[244:247], a[64:67], v[112:115]\n"
"buffer_load_dwordx4 v2, s[48:51], s64 offen sc1 lds\n"
"v_mfma_f32_16x16x32_bf16 v[116:119], v[240:243], a[60:63], v[116:119]\n"
"ds_read_b128 v[240:243], v2 offset:6144\n"
"v_mfma_f32_16x16x32_bf16 v[116:119], v[244:247], a[68:71], v[116:119]\n"
"s_waitcnt lgkmcnt(3)\n"
"v_mfma_f32_16x16x32_bf16 v[120:123], v[224:227], a[56:59], v[120:123]\n"
"ds_read_b128 v[244:247], v2 offset:13312\n"
"v_mfma_f32_16x16x32_bf16 v[120:123], v[228:231], a[64:67], v[120:123]\n"
"buffer_load_dwordx4 v2, s[48:51], s64 offen offset:1024 sc1 lds\n"
"v_mfma_f32_16x16x32_bf16 v[124:127], v[224:227], a[60:63], v[124:127]\n"
"ds_read_b128 v[224:227], v2 offset:14336\n"
"v_mfma_f32_16x16x32_bf16 v[124:127], v[228:231], a[68:71], v[124:127]\n"
"s_waitcnt lgkmcnt(3)\n"
"v_mfma_f32_16x16x32_bf16 v[128:131], v[232:235], a[56:59], v[128:131]\n"
"ds_read_b128 v[228:231], v2 offset:21504\n"
"v_mfma_f32_16x16x32_bf16 v[128:131], v[236:239], a[64:67], v[128:131]\n"
"v_mfma_f32_16x16x32_bf16 v[132:135], v[232:235], a[60:63], v[132:135]\n"
"ds_read_b128 v[232:235], v2 offset:15360\n"
"v_mfma_f32_16x16x32_bf16 v[132:135], v[236:239], a[68:71], v[132:135]\n"
"s_waitcnt lgkmcnt(3)\n"
"buffer_load_dwordx4 v2, s[48:51], s64 offen offset:2048 sc1 lds\n"
"v_mfma_f32_16x16x32_bf16 v[136:139], v[240:243], a[56:59], v[136:139]\n"
"ds_read_b128 v[236:239], v2 offset:22528\n"
"v_mfma_f32_16x16x32_bf16 v[136:139], v[244:247], a[64:67], v[136:139]\n"
"v_mfma_f32_16x16x32_bf16 v[140:143], v[240:243], a[60:63], v[140:143]\n"
"ds_read_b128 v[240:243], v2 offset:16384\n"
"v_mfma_f32_16x16x32_bf16 v[140:143], v[244:247], a[68:71], v[140:143]\n"
"s_waitcnt lgkmcnt(3)\n"
"buffer_load_dwordx4 a[12:15], v2, s[48:51], s63 offen offset:1024 sc1\n"
"s_waitcnt vmcnt(11)\n"
"v_max3_u32 v196, v164, v165, 0\n"
"s_add_u32 s63, s85, 22282240\n"
"v_max3_u32 v196, v166, v167, v196\n"
"s_waitcnt vmcnt(10)\n"
"v_max3_u32 v196, v168, v169, v196\n"
"v_max3_u32 v196, v170, v171, v196\n"
"s_waitcnt vmcnt(9)\n"
"v_max3_u32 v196, v172, v173, v196\n"
"v_max3_u32 v196, v174, v175, v196\n"
"s_waitcnt vmcnt(8)\n"
"v_max3_u32 v196, v176, v177, v196\n"
"v_max3_u32 v196, v178, v179, v196\n"
"v_cmp_eq_u32 vcc, -1, v196\n"
"s_nop 4\n"
"s_cbranch_vccz Ld18_%=\n"
"s_mov_b32 s65, 0\n"
"Lp17_%=:\n"
"buffer_load_dwordx4 v[164:167], v220, s[48:51], s25 offen sc1\n"
"buffer_load_dwordx4 v[168:171], v220, s[48:51], s25 offen offset:1024 sc1\n"
"buffer_load_dwordx4 v[172:175], v220, s[48:51], s25 offen offset:2048 sc1\n"
"buffer_load_dwordx4 v[176:179], v220, s[48:51], s25 offen offset:3072 sc1\n"
"s_add_u32 s65, s65, 1\n"
"s_cmp_gt_u32 s65, 131072\n"
"s_waitcnt vmcnt(0)\n"
"s_cbranch_scc1 Ld18_%=\n"
"s_nop 0\n"
"v_max3_u32 v196, v164, v165, 0\n"
"v_max3_u32 v196, v166, v167, v196\n"
"v_max3_u32 v196, v168, v169, v196\n"
"v_max3_u32 v196, v170, v171, v196\n"
"v_max3_u32 v196, v172, v173, v196\n"
"v_max3_u32 v196, v174, v175, v196\n"
"v_max3_u32 v196, v176, v177, v196\n"
"v_max3_u32 v196, v178, v179, v196\n"
"v_cmp_eq_u32 vcc, -1, v196\n"
"s_nop 4\n"
"s_cbranch_vccnz Lp17_%=\n"
"Ld18_%=:\n"
"s_nop 0\n"
"v_pk_add_f32 v[164:165], v[164:165], v[168:169]\n"
"v_pk_add_f32 v[172:173], v[172:173], v[176:177]\n"
"v_pk_add_f32 v[166:167], v[166:167], v[170:171]\n"
"v_pk_add_f32 v[174:175], v[174:175], v[178:179]\n"
"LPG13_%=:\n"
"s_cbranch_execz LPG17_%=\n"
"s_nop 0\n"
"v_pk_add_f32 v[164:165], v[164:165], v[172:173]\n"
"buffer_load_dwordx4 a[16:19], v2, s[48:51], s63 offen sc1\n"
"v_pk_add_f32 v[166:167], v[166:167], v[174:175]\n"
"v_add_f32_dpp v164, v164, v164 quad_perm:[1,0,3,2] row_mask:0xf bank_mask:0xf\n"
"v_add_f32_dpp v165, v165, v165 quad_perm:[1,0,3,2] row_mask:0xf bank_mask:0xf\n"
"v_add_f32_dpp v166, v166, v166 quad_perm:[1,0,3,2] row_mask:0xf bank_mask:0xf\n"
"v_add_f32_dpp v167, v167, v167 quad_perm:[1,0,3,2] row_mask:0xf bank_mask:0xf\n"
"v_add_f32_dpp v164, v164, v164 quad_perm:[2,3,0,1] row_mask:0xf bank_mask:0xf\n"
"v_add_f32_dpp v165, v165, v165 quad_perm:[2,3,0,1] row_mask:0xf bank_mask:0xf\n"
"v_add_f32_dpp v166, v166, v166 quad_perm:[2,3,0,1] row_mask:0xf bank_mask:0xf\n"
"v_add_f32_dpp v167, v167, v167 quad_perm:[2,3,0,1] row_mask:0xf bank_mask:0xf\n"
"v_mul_f32 v164, 0xba124925, v164\n"
"v_mul_f32 v166, 0xba124925, v166\n"
"v_mul_f32 v165, 0x3a124925, v165\n"
"v_mul_f32 v167, 0x3a124925, v167\n"
"v_fma_f32 v165, -v164, v164, v165\n"
"v_fma_f32 v167, -v166, v166, v167\n"
"v_max_f32 v165, 0, v165\n"
"v_max_f32 v167, 0, v167\n"
"v_add_f32 v165, 0x3727c5ac, v165\n"
"buffer_load_dwordx4 a[20:23], v2, s[48:51], s63 offen offset:1024 sc1\n"
"v_add_f32 v167, 0x3727c5ac, v167\n"
"s_add_u32 s63, s85, 22413312\n"
"v_rsq_f32 v165, v165\n"
"v_rsq_f32 v167, v167\n"
"v_mul_f32 v164, v164, v165\n"
"v_mul_f32 v166, v166, v167\n"
"v_mov_b32_dpp v196, v165 row_newbcast:0 row_mask:0xf bank_mask:0xf\n"
"v_mov_b32_dpp v204, v164 row_newbcast:0 row_mask:0xf bank_mask:0xf\n"
"v_mov_b32_dpp v197, v165 row_newbcast:4 row_mask:0xf bank_mask:0xf\n"
"v_mov_b32_dpp v205, v164 row_newbcast:4 row_mask:0xf bank_mask:0xf\n"
"v_mov_b32_dpp v198, v165 row_newbcast:8 row_mask:0xf bank_mask:0xf\n"
"v_mov_b32_dpp v206, v164 row_newbcast:8 row_mask:0xf bank_mask:0xf\n"
"v_mov_b32_dpp v199, v165 row_newbcast:12 row_mask:0xf bank_mask:0xf\n"
"v_mov_b32_dpp v207, v164 row_newbcast:12 row_mask:0xf bank_mask:0xf\n"
"v_mov_b32_dpp v200, v167 row_newbcast:0 row_mask:0xf bank_mask:0xf\n"
"v_mov_b32_dpp v208, v166 row_newbcast:0 row_mask:0xf bank_mask:0xf\n"
"buffer_load_dwordx4 a[24:27], v2, s[48:51], s63 offen sc1\n"
"v_mov_b32_dpp v201, v167 row_newbcast:4 row_mask:0xf bank_mask:0xf\n"
"v_mov_b32_dpp v209, v166 row_newbcast:4 row_mask:0xf bank_mask:0xf\n"
"v_mov_b32_dpp v202, v167 row_newbcast:8 row_mask:0xf bank_mask:0xf\n"
"v_mov_b32_dpp v210, v166 row_newbcast:8 row_mask:0xf bank_mask:0xf\n"
"v_mov_b32_dpp v203, v167 row_newbcast:12 row_mask:0xf bank_mask:0xf\n"
"v_mov_b32_dpp v211, v166 row_newbcast:12 row_mask:0xf bank_mask:0xf\n"
"v_pk_fma_f32 v[32:33], v[32:33], v[196:197], v[204:205]\n"
"v_pk_fma_f32 v[34:35], v[34:35], v[198:199], v[206:207]\n"
"v_pk_fma_f32 v[36:37], v[36:37], v[200:201], v[208:209]\n"
"v_pk_fma_f32 v[38:39], v[38:39], v[202:203], v[210:211]\n"
"v_cvt_pk_bf16_f32 v164, v32, v33\n"
"v_cvt_pk_bf16_f32 v165, v34, v35\n"
"v_cvt_pk_bf16_f32 v166, v36, v37\n"
"v_cvt_pk_bf16_f32 v167, v38, v39\n"
"v_mfma_f32_16x16x32_bf16 v[88:91], v[224:227], a[72:75], v[88:91]\n"
"ds_read_b128 v[244:247], v2 offset:23552\n"
"v_mfma_f32_16x16x32_bf16 v[88:91], v[228:231], a[80:83], v[88:91]\n"
"buffer_load_dwordx4 a[28:31], v2, s[48:51], s63 offen offset:1024 sc1\n"
"v_mfma_f32_16x16x32_bf16 v[92:95], v[224:227], a[76:79], v[92:95]\n"
"ds_read_b128 v[224:227], v2 offset:17408\n"
"s_add_u32 s59, s26, 37978112\n"
"v_mfma_f32_16x16x32_bf16 v[92:95], v[228:231], a[84:87], v[92:95]\n"
"s_waitcnt lgkmcnt(3)\n"
"v_fma_f32 v40, v40, v196, v204\n"
"v_fma_f32 v41, v41, v197, v205\n"
"v_mfma_f32_16x16x32_bf16 v[96:99], v[232:235], a[72:75], v[96:99]\n"
"ds_read_b128 v[228:231], v2 offset:24576\n"
"v_fma_f32 v42, v42, v198, v206\n"
"v_mfma_f32_16x16x32_bf16 v[96:99], v[236:239], a[80:83], v[96:99]\n"
"buffer_store_dwordx4 v[164:167], v2, s[48:51], s59 offen sc1\n"
"v_mfma_f32_16x16x32_bf16 v[100:103], v[232:235], a[76:79], v[100:103]\n"
"ds_read_b128 v[232:235], v2 offset:18432\n"
"v_fma_f32 v43, v43, v199, v207\n"
"v_mfma_f32_16x16x32_bf16 v[100:103], v[236:239], a[84:87], v[100:103]\n"
"s_waitcnt lgkmcnt(3)\n"
"v_fma_f32 v44, v44, v200, v208\n"
"v_fma_f32 v45, v45, v201, v209\n"
"v_mfma_f32_16x16x32_bf16 v[104:107], v[240:243], a[72:75], v[104:107]\n"
"ds_read_b128 v[236:239], v2 offset:25600\n"
"v_fma_f32 v46, v46, v202, v210\n"
"v_mfma_f32_16x16x32_bf16 v[104:107], v[244:247], a[80:83], v[104:107]\n"
"v_fma_f32 v47, v47, v203, v211\n"
"v_cvt_pk_bf16_f32 v168, v40, v41\n"
"v_mfma_f32_16x16x32_bf16 v[108:111], v[240:243], a[76:79], v[108:111]\n"
"ds_read_b128 v[240:243], v2 offset:19456\n"
"v_cvt_pk_bf16_f32 v169, v42, v43\n"
"v_mfma_f32_16x16x32_bf16 v[108:111], v[244:247], a[84:87], v[108:111]\n"
"s_waitcnt lgkmcnt(3)\n"
"v_cvt_pk_bf16_f32 v170, v44, v45\n"
"v_cvt_pk_bf16_f32 v171, v46, v47\n"
"v_mfma_f32_16x16x32_bf16 v[112:115], v[224:227], a[72:75], v[112:115]\n"
"ds_read_b128 v[244:247], v2 offset:26624\n"
"s_add_u32 s63, s85, 22544384\n"
"v_mfma_f32_16x16x32_bf16 v[112:115], v[228:231], a[80:83], v[112:115]\n"
"buffer_load_dwordx4 a[32:35], v2, s[48:51], s63 offen sc1\n"
"v_mfma_f32_16x16x32_bf16 v[116:119], v[224:227], a[76:79], v[116:119]\n"
"ds_read_b128 v[224:227], v2 offset:20480\n"
"v_mfma_f32_16x16x32_bf16 v[116:119], v[228:231], a[84:87], v[116:119]\n"
"s_waitcnt lgkmcnt(3)\n"
"v_mfma_f32_16x16x32_bf16 v[120:123], v[232:235], a[72:75], v[120:123]\n"
"ds_read_b128 v[228:231], v2 offset:27648\n"
"s_add_u32 s59, s26, 38248448\n"
"v_mfma_f32_16x16x32_bf16 v[120:123], v[236:239], a[80:83], v[120:123]\n"
"buffer_store_dwordx4 v[168:171], v2, s[48:51], s59 offen sc1\n"
"v_mfma_f32_16x16x32_bf16 v[124:127], v[232:235], a[76:79], v[124:127]\n"
"s_barrier\n"
"ds_read_b128 v[232:235], v2 offset:28672\n"
"v_mfma_f32_16x16x32_bf16 v[124:127], v[236:239], a[84:87], v[124:127]\n"
"s_waitcnt lgkmcnt(3)\n"
"v_fma_f32 v48, v48, v196, v204\n"
"v_fma_f32 v49, v49, v197, v205\n"
"v_mfma_f32_16x16x32_bf16 v[128:131], v[240:243], a[72:75], v[128:131]\n"
"ds_read_b128 v[236:239], v2 offset:35840\n"
"v_fma_f32 v50, v50, v198, v206\n"
"v_mfma_f32_16x16x32_bf16 v[128:131], v[244:247], a[80:83], v[128:131]\n"
"v_fma_f32 v51, v51, v199, v207\n"
"v_fma_f32 v52, v52, v200, v208\n"
"v_mfma_f32_16x16x32_bf16 v[132:135], v[240:243], a[76:79], v[132:135]\n"
"ds_read_b128 v[240:243], v2 offset:29696\n"
"v_fma_f32 v53, v53, v201, v209\n"
"v_mfma_f32_16x16x32_bf16 v[132:135], v[244:247], a[84:87], v[132:135]\n"
"s_waitcnt lgkmcnt(3)\n"
"v_fma_f32 v54, v54, v202, v210\n"
"v_fma_f32 v55, v55, v203, v211\n"
"v_mfma_f32_16x16x32_bf16 v[136:139], v[224:227], a[72:75], v[136:139]\n"
"ds_read_b128 v[244:247], v2 offset:36864\n"
"v_cvt_pk_bf16_f32 v172, v48, v49\n"
"v_mfma_f32_16x16x32_bf16 v[136:139], v[228:231], a[80:83], v[136:139]\n"
"v_cvt_pk_bf16_f32 v173, v50, v51\n"
"v_cvt_pk_bf16_f32 v174, v52, v53\n"
"v_mfma_f32_16x16x32_bf16 v[140:143], v[224:227], a[76:79], v[140:143]\n"
"ds_read_b128 v[224:227], v2 offset:30720\n"
"v_cvt_pk_bf16_f32 v175, v54, v55\n"
"v_mfma_f32_16x16x32_bf16 v[140:143], v[228:231], a[84:87], v[140:143]\n"
"s_waitcnt lgkmcnt(3)\n"
"s_add_u32 m0, s69, 114688\n"
"s_add_u32 s64, s68, 36831232\n"
"v_mfma_f32_16x16x32_bf16 v[88:91], v[232:235], a[88:91], v[88:91]\n"
"ds_read_b128 v[228:231], v2 offset:37888\n"
"v_mfma_f32_16x16x32_bf16 v[88:91], v[236:239], a[96:99], v[88:91]\n"
"buffer_load_dwordx4 v2, s[48:51], s64 offen sc1 lds\n"
"v_mfma_f32_16x16x32_bf16 v[92:95], v[232:235], a[92:95], v[92:95]\n"
"ds_read_b128 v[232:235], v2 offset:31744\n"
"s_add_u32 s59, s26, 38518784\n"
"v_mfma_f32_16x16x32_bf16 v[92:95], v[236:239], a[100:103], v[92:95]\n"
"s_waitcnt lgkmcnt(3)\n"
"v_fma_f32 v56, v56, v196, v204\n"
"v_fma_f32 v57, v57, v197, v205\n"
"v_mfma_f32_16x16x32_bf16 v[96:99], v[240:243], a[88:91], v[96:99]\n"
"ds_read_b128 v[236:239], v2 offset:38912\n"
"v_fma_f32 v58, v58, v198, v206\n"
"v_mfma_f32_16x16x32_bf16 v[96:99], v[244:247], a[96:99], v[96:99]\n"
"buffer_store_dwordx4 v[172:175], v2, s[48:51], s59 offen sc1\n"
"v_mfma_f32_16x16x32_bf16 v[100:103], v[240:243], a[92:95], v[100:103]\n"
"ds_read_b128 v[240:243], v2 offset:32768\n"
"v_fma_f32 v59, v59, v199, v207\n"
"v_mfma_f32_16x16x32_bf16 v[100:103], v[244:247], a[100:103], v[100:103]\n"
"s_waitcnt lgkmcnt(3)\n"
"v_fma_f32 v60, v60, v200, v208\n"
"v_fma_f32 v61, v61, v201, v209\n"
"v_mfma_f32_16x16x32_bf16 v[104:107], v[224:227], a[88:91], v[104:107]\n"
"ds_read_b128 v[244:247], v2 offset:39936\n"
"v_fma_f32 v62, v62, v202, v210\n"
"v_mfma_f32_16x16x32_bf16 v[104:107], v[228:231], a[96:99], v[104:107]\n"
"v_fma_f32 v63, v63, v203, v211\n"
"v_cvt_pk_bf16_f32 v176, v56, v57\n"
"v_mfma_f32_16x16x32_bf16 v[108:111], v[224:227], a[92:95], v[108:111]\n"
"ds_read_b128 v[224:227], v2 offset:33792\n"
"v_cvt_pk_bf16_f32 v177, v58, v59\n"
"v_mfma_f32_16x16x32_bf16 v[108:111], v[228:231], a[100:103], v[108:111]\n"
"s_waitcnt lgkmcnt(3)\n"
"v_cvt_pk_bf16_f32 v178, v60, v61\n"
"v_cvt_pk_bf16_f32 v179, v62, v63\n"
"v_mfma_f32_16x16x32_bf16 v[112:115], v[232:235], a[88:91], v[112:115]\n"
"ds_read_b128 v[228:231], v2 offset:40960\n"
"v_mfma_f32_16x16x32_bf16 v[112:115], v[236:239], a[96:99], v[112:115]\n"
"buffer_load_dwordx4 v2, s[48:51], s64 offen offset:1024 sc1 lds\n"
"v_mfma_f32_16x16x32_bf16 v[116:119], v[232:235], a[92:95], v[116:119]\n"
"ds_read_b128 v[232:235], v2 offset:34816\n"
"v_mfma_f32_16x16x32_bf16 v[116:119], v[236:239], a[100:103], v[116:119]\n"
"s_waitcnt lgkmcnt(3)\n"
"v_mfma_f32_16x16x32_bf16 v[120:123], v[240:243], a[88:91], v[120:123]\n"
"ds_read_b128 v[236:239], v2 offset:41984\n"
"s_add_u32 s59, s26, 38789120\n"
"v_mfma_f32_16x16x32_bf16 v[120:123], v[244:247], a[96:99], v[120:123]\n"
"buffer_store_dwordx4 v[176:179], v2, s[48:51], s59 offen sc1\n"
"v_mfma_f32_16x16x32_bf16 v[124:127], v[240:243], a[92:95], v[124:127]\n"
"ds_read_b128 v[240:243], v2 offset:43008\n"
"v_fma_f32 v64, v64, v196, v204\n"
"v_mfma_f32_16x16x32_bf16 v[124:127], v[244:247], a[100:103], v[124:127]\n"
"s_waitcnt lgkmcnt(3)\n"
"v_fma_f32 v65, v65, v197, v205\n"
"v_fma_f32 v66, v66, v198, v206\n"
"v_mfma_f32_16x16x32_bf16 v[128:131], v[224:227], a[88:91], v[128:131]\n"
"ds_read_b128 v[244:247], v2 offset:50176\n"
"v_fma_f32 v67, v67, v199, v207\n"
"v_mfma_f32_16x16x32_bf16 v[128:131], v[228:231], a[96:99], v[128:131]\n"
"v_fma_f32 v68, v68, v200, v208\n"
"v_fma_f32 v69, v69, v201, v209\n"
"v_mfma_f32_16x16x32_bf16 v[132:135], v[224:227], a[92:95], v[132:135]\n"
"ds_read_b128 v[224:227], v2 offset:44032\n"
"v_fma_f32 v70, v70, v202, v210\n"
"v_mfma_f32_16x16x32_bf16 v[132:135], v[228:231], a[100:103], v[132:135]\n"
"s_waitcnt lgkmcnt(3)\n"
"v_fma_f32 v71, v71, v203, v211\n"
"v_cvt_pk_bf16_f32 v180, v64, v65\n"
"v_mfma_f32_16x16x32_bf16 v[136:139], v[232:235], a[88:91], v[136:139]\n"
"ds_read_b128 v[228:231], v2 offset:51200\n"
"v_cvt_pk_bf16_f32 v181, v66, v67\n"
"v_mfma_f32_16x16x32_bf16 v[136:139], v[236:239], a[96:99], v[136:139]\n"
"v_cvt_pk_bf16_f32 v182, v68, v69\n"
"v_cvt_pk_bf16_f32 v183, v70, v71\n"
"v_mfma_f32_16x16x32_bf16 v[140:143], v[232:235], a[92:95], v[140:143]\n"
"ds_read_b128 v[232:235], v2 offset:45056\n"
"v_mfma_f32_16x16x32_bf16 v[140:143], v[236:239], a[100:103], v[140:143]\n"
"s_waitcnt lgkmcnt(3)\n"
"buffer_load_dwordx4 v2, s[48:51], s64 offen offset:2048 sc1 lds\n"
"v_mfma_f32_16x16x32_bf16 v[88:91], v[240:243], a[104:107], v[88:91]\n"
"ds_read_b128 v[236:239], v2 offset:52224\n"
"v_mfma_f32_16x16x32_bf16 v[88:91], v[244:247], a[112:115], v[88:91]\n"
"v_mfma_f32_16x16x32_bf16 v[92:95], v[240:243], a[108:111], v[92:95]\n"
"ds_read_b128 v[240:243], v2 offset:46080\n"
"s_add_u32 s59, s26, 39059456\n"
"v_mfma_f32_16x16x32_bf16 v[92:95], v[244:247], a[116:119], v[92:95]\n"
"s_waitcnt lgkmcnt(3)\n"
"buffer_store_dwordx4 v[180:183], v2, s[48:51], s59 offen sc1\n"
"v_mfma_f32_16x16x32_bf16 v[96:99], v[224:227], a[104:107], v[96:99]\n"
"ds_read_b128 v[244:247], v2 offset:53248\n"
"v_fma_f32 v72, v72, v196, v204\n"
"v_mfma_f32_16x16x32_bf16 v[96:99], v[228:231], a[112:115], v[96:99]\n"
"v_fma_f32 v73, v73, v197, v205\n"
"v_fma_f32 v74, v74, v198, v206\n"
"v_mfma_f32_16x16x32_bf16 v[100:103], v[224:227], a[108:111], v[100:103]\n"
"ds_read_b128 v[224:227], v2 offset:47104\n"
"v_fma_f32 v75, v75, v199, v207\n"
"v_mfma_f32_16x16x32_bf16 v[100:103], v[228:231], a[116:119], v[100:103]\n"
"s_waitcnt lgkmcnt(3)\n"
"v_fma_f32 v76, v76, v200, v208\n"
"v_fma_f32 v77, v77, v201, v209\n"
"v_mfma_f32_16x16x32_bf16 v[104:107], v[232:235], a[104:107], v[104:107]\n"
"ds_read_b128 v[228:231], v2 offset:54272\n"
"v_fma_f32 v78, v78, v202, v210\n"
"v_mfma_f32_16x16x32_bf16 v[104:107], v[236:239], a[112:115], v[104:107]\n"
"v_fma_f32 v79, v79, v203, v211\n"
"v_cvt_pk_bf16_f32 v184, v72, v73\n"
"v_mfma_f32_16x16x32_bf16 v[108:111], v[232:235], a[108:111], v[108:111]\n"
"ds_read_b128 v[232:235], v2 offset:48128\n"
"v_cvt_pk_bf16_f32 v185, v74, v75\n"
"v_mfma_f32_16x16x32_bf16 v[108:111], v[236:239], a[116:119], v[108:111]\n"
"s_waitcnt lgkmcnt(3)\n"
"v_cvt_pk_bf16_f32 v186, v76, v77\n"
"v_cvt_pk_bf16_f32 v187, v78, v79\n"
"v_mfma_f32_16x16x32_bf16 v[112:115], v[240:243], a[104:107], v[112:115]\n"
"ds_read_b128 v[236:239], v2 offset:55296\n"
"v_mfma_f32_16x16x32_bf16 v[112:115], v[244:247], a[112:115], v[112:115]\n"
"buffer_load_dwordx4 v2, s[48:51], s64 offen offset:3072 sc1 lds\n"
"v_mfma_f32_16x16x32_bf16 v[116:119], v[240:243], a[108:111], v[116:119]\n"
"ds_read_b128 v[240:243], v2 offset:49152\n"
"s_add_u32 m0, s69, 118784\n"
"v_mfma_f32_16x16x32_bf16 v[116:119], v[244:247], a[116:119], v[116:119]\n"
"s_waitcnt lgkmcnt(3)\n"
"s_add_u32 s64, s68, 36835328\n"
"v_mfma_f32_16x16x32_bf16 v[120:123], v[224:227], a[104:107], v[120:123]\n"
"ds_read_b128 v[244:247], v2 offset:56320\n"
"s_add_u32 s59, s26, 39329792\n"
"v_mfma_f32_16x16x32_bf16 v[120:123], v[228:231], a[112:115], v[120:123]\n"
"buffer_store_dwordx4 v[184:187], v2, s[48:51], s59 offen sc1\n"
"v_mfma_f32_16x16x32_bf16 v[124:127], v[224:227], a[108:111], v[124:127]\n"
"s_barrier\n"
"ds_read_b128 v[224:227], v221\n"
"v_mfma_f32_16x16x32_bf16 v[124:127], v[228:231], a[116:119], v[124:127]\n"
"s_waitcnt lgkmcnt(3)\n"
"v_fma_f32 v80, v80, v196, v204\n"
"v_fma_f32 v81, v81, v197, v205\n"
"v_mfma_f32_16x16x32_bf16 v[128:131], v[232:235], a[104:107], v[128:131]\n"
"ds_read_b128 v[228:231], v221 offset:7168\n"
"v_fma_f32 v82, v82, v198, v206\n"
"v_mfma_f32_16x16x32_bf16 v[128:131], v[236:239], a[112:115], v[128:131]\n"
"v_fma_f32 v83, v83, v199, v207\n"
"v_fma_f32 v84, v84, v200, v208\n"
"v_mfma_f32_16x16x32_bf16 v[132:135], v[232:235], a[108:111], v[132:135]\n"
"ds_read_b128 v[232:235], v221 offset:1024\n"
"v_fma_f32 v85, v85, v201, v209\n"
"v_mfma_f32_16x16x32_bf16 v[132:135], v[236:239], a[116:119], v[132:135]\n"
"s_waitcnt lgkmcnt(3)\n"
"v_fma_f32 v86, v86, v202, v210\n"
"v_fma_f32 v87, v87, v203, v211\n"
"v_mfma_f32_16x16x32_bf16 v[136:139], v[240:243], a[104:107], v[136:139]\n"
"ds_read_b128 v[236:239], v221 offset:8192\n"
"v_cvt_pk_bf16_f32 v188, v80, v81\n"
"v_mfma_f32_16x16x32_bf16 v[136:139], v[244:247], a[112:115], v[136:139]\n"
"v_cvt_pk_bf16_f32 v189, v82, v83\n"
"v_cvt_pk_bf16_f32 v190, v84, v85\n"
"v_mfma_f32_16x16x32_bf16 v[140:143], v[240:243], a[108:111], v[140:143]\n"
"ds_read_b128 v[240:243], v221 offset:2048\n"
"v_cvt_pk_bf16_f32 v191, v86, v87\n"
"v_mfma_f32_16x16x32_bf16 v[140:143], v[244:247], a[116:119], v[140:143]\n"
"s_waitcnt lgkmcnt(3)\n"
"buffer_load_dwordx4 v2, s[48:51], s64 offen sc1 lds\n"
"LZ3_%=:\n"
"v_add_u32 v11, LZ4_%=-LPC_%=, v0\n"
"v_min_u32 v11, LZ6_%=-LPC_%=-4, v11\n"
"global_load_dword v11, v11, s[70:71]\n"
"v_mfma_f32_16x16x32_bf16 v[88:91], v[224:227], a[120:123], v[88:91]\n"
"ds_read_b128 v[244:247], v221 offset:9216\n"
"v_mfma_f32_16x16x32_bf16 v[88:91], v[228:231], a[128:131], v[88:91]\n"
"v_mfma_f32_16x16x32_bf16 v[92:95], v[224:227], a[124:127], v[92:95]\n"
"ds_read_b128 v[224:227], v221 offset:3072\n"
"s_add_u32 s59, s26, 39600128\n"
"v_mfma_f32_16x16x32_bf16 v[92:95], v[228:231], a[132:135], v[92:95]\n"
"s_waitcnt lgkmcnt(3)\n"
"buffer_store_dwordx4 v[188:191], v2, s[48:51], s59 offen sc1\n"
"v_mfma_f32_16x16x32_bf16 v[96:99], v[232:235], a[120:123], v[96:99]\n"
"ds_read_b128 v[228:231], v221 offset:10240\n"
"v_mfma_f32_16x16x32_bf16 v[96:99], v[236:239], a[128:131], v[96:99]\n"
"v_mfma_f32_16x16x32_bf16 v[100:103], v[232:235], a[124:127], v[100:103]\n"
"ds_read_b128 v[232:235], v221 offset:4096\n"
"v_mfma_f32_16x16x32_bf16 v[100:103], v[236:239], a[132:135], v[100:103]\n"
"s_waitcnt lgkmcnt(3)\n"
"buffer_load_dwordx4 v2, s[48:51], s64 offen offset:1024 sc1 lds\n"
"v_mfma_f32_16x16x32_bf16 v[104:107], v[240:243], a[120:123], v[104:107]\n"
"ds_read_b128 v[236:239], v221 offset:11264\n"
"v_mfma_f32_16x16x32_bf16 v[104:107], v[244:247], a[128:131], v[104:107]\n"
"v_mfma_f32_16x16x32_bf16 v[108:111], v[240:243], a[124:127], v[108:111]\n"
"ds_read_b128 v[240:243], v221 offset:5120\n"
"v_mfma_f32_16x16x32_bf16 v[108:111], v[244:247], a[132:135], v[108:111]\n"
"s_waitcnt lgkmcnt(3)\n"
"v_mfma_f32_16x16x32_bf16 v[112:115], v[224:227], a[120:123], v[112:115]\n"
"ds_read_b128 v[244:247], v221 offset:12288\n"
"v_mfma_f32_16x16x32_bf16 v[112:115], v[228:231], a[128:131], v[112:115]\n"
"buffer_load_dwordx4 v2, s[48:51], s64 offen offset:2048 sc1 lds\n"
"v_mfma_f32_16x16x32_bf16 v[116:119], v[224:227], a[124:127], v[116:119]\n"
"ds_read_b128 v[224:227], v221 offset:6144\n"
"s_add_u32 m0, s69, 0\n"
"v_mfma_f32_16x16x32_bf16 v[116:119], v[228:231], a[132:135], v[116:119]\n"
"s_waitcnt lgkmcnt(3)\n"
"s_add_u32 s64, s68, 37289984\n"
"v_mfma_f32_16x16x32_bf16 v[120:123], v[232:235], a[120:123], v[120:123]\n"
"ds_read_b128 v[228:231], v221 offset:13312\n"
"v_mfma_f32_16x16x32_bf16 v[120:123], v[236:239], a[128:131], v[120:123]\n"
"buffer_load_dwordx4 v2, s[48:51], s64 offen sc1 lds\n"
"v_mfma_f32_16x16x32_bf16 v[124:127], v[232:235], a[124:127], v[124:127]\n"
"ds_read_b128 v[232:235], v221 offset:14336\n"
"v_mfma_f32_16x16x32_bf16 v[124:127], v[236:239], a[132:135], v[124:127]\n"
"s_waitcnt lgkmcnt(3)\n"
"v_mfma_f32_16x16x32_bf16 v[128:131], v[240:243], a[120:123], v[128:131]\n"
"ds_read_b128 v[236:239], v221 offset:21504\n"
"v_mfma_f32_16x16x32_bf16 v[128:131], v[244:247], a[128:131], v[128:131]\n"
"v_mfma_f32_16x16x32_bf16 v[132:135], v[240:243], a[124:127], v[132:135]\n"
"ds_read_b128 v[240:243], v221 offset:15360\n"
"v_mfma_f32_16x16x32_bf16 v[132:135], v[244:247], a[132:135], v[132:135]\n"
"s_waitcnt lgkmcnt(3)\n"
"buffer_load_dwordx4 v2, s[48:51], s64 offen offset:1024 sc1 lds\n"
"v_mfma_f32_16x16x32_bf16 v[136:139], v[224:227], a[120:123], v[136:139]\n"
"ds_read_b128 v[244:247], v221 offset:22528\n"
"v_mfma_f32_16x16x32_bf16 v[136:139], v[228:231], a[128:131], v[136:139]\n"
"v_mfma_f32_16x16x32_bf16 v[140:143], v[224:227], a[124:127], v[140:143]\n"
"ds_read_b128 v[224:227], v221 offset:16384\n"
"v_mfma_f32_16x16x32_bf16 v[140:143], v[228:231], a[132:135], v[140:143]\n"
"s_waitcnt lgkmcnt(3)\n"
"buffer_load_dwordx4 v2, s[48:51], s64 offen offset:2048 sc1 lds\n"
"s_mov_b64 exec, 1\n"
"s_lshl4_add_u32 s60, s8, s9\n"
"s_lshl2_add_u32 s60, s60, s7\n"
"s_lshl_b32 s60, s60, 5\n"
"s_add_u32 s60, s60, 57081856\n"
"s_or_b32 s57, s6, s7\n"
"s_cselect_b32 s57, 57671680, 0\n"
"v_not_b32 v252, v12\n"
"v_not_b32 v253, v13\n"
"buffer_store_dwordx2 v[12:13], off, s[48:51], s57 sc1\n"
"buffer_store_dwordx2 v[252:253], off, s[48:51], s60 sc1\n"
"buffer_store_dwordx2 v[252:253], off, s[48:51], s95 sc1\n"
"s_mov_b64 exec, -1\n"
"v_mfma_f32_16x16x32_bf16 v[88:91], v[232:235], a[136:139], v[88:91]\n"
"ds_read_b128 v[228:231], v221 offset:23552\n"
"v_mfma_f32_16x16x32_bf16 v[88:91], v[236:239], a[144:147], v[88:91]\n"
"v_mfma_f32_16x16x32_bf16 v[92:95], v[232:235], a[140:143], v[92:95]\n"
"ds_read_b128 v[232:235], v221 offset:17408\n"
"v_mfma_f32_16x16x32_bf16 v[92:95], v[236:239], a[148:151], v[92:95]\n"
"s_waitcnt lgkmcnt(3)\n"
"buffer_load_dwordx4 v2, s[48:51], s64 offen offset:3072 sc1 lds\n"
"v_mfma_f32_16x16x32_bf16 v[96:99], v[240:243], a[136:139], v[96:99]\n"
"ds_read_b128 v[236:239], v221 offset:24576\n"
"s_add_u32 m0, s69, 4096\n"
"v_mfma_f32_16x16x32_bf16 v[96:99], v[244:247], a[144:147], v[96:99]\n"
"s_add_u32 s64, s68, 37294080\n"
"v_mfma_f32_16x16x32_bf16 v[100:103], v[240:243], a[140:143], v[100:103]\n"
"ds_read_b128 v[240:243], v221 offset:18432\n"
"v_mfma_f32_16x16x32_bf16 v[100:103], v[244:247], a[148:151], v[100:103]\n"
"s_waitcnt lgkmcnt(3)\n"
"v_mfma_f32_16x16x32_bf16 v[104:107], v[224:227], a[136:139], v[104:107]\n"
"ds_read_b128 v[244:247], v221 offset:25600\n"
"v_mfma_f32_16x16x32_bf16 v[104:107], v[228:231], a[144:147], v[104:107]\n"
"buffer_load_dwordx4 v2, s[48:51], s64 offen sc1 lds\n"
"v_mfma_f32_16x16x32_bf16 v[108:111], v[224:227], a[140:143], v[108:111]\n"
"ds_read_b128 v[224:227], v221 offset:19456\n"
"v_mfma_f32_16x16x32_bf16 v[108:111], v[228:231], a[148:151], v[108:111]\n"
"s_waitcnt lgkmcnt(3)\n"
"v_mfma_f32_16x16x32_bf16 v[112:115], v[232:235], a[136:139], v[112:115]\n"
"ds_read_b128 v[228:231], v221 offset:26624\n"
"v_mfma_f32_16x16x32_bf16 v[112:115], v[236:239], a[144:147], v[112:115]\n"
"buffer_load_dwordx4 v2, s[48:51], s64 offen offset:1024 sc1 lds\n"
"v_mfma_f32_16x16x32_bf16 v[116:119], v[232:235], a[140:143], v[116:119]\n"
"ds_read_b128 v[232:235], v221 offset:20480\n"
"v_mfma_f32_16x16x32_bf16 v[116:119], v[236:239], a[148:151], v[116:119]\n"
"s_waitcnt lgkmcnt(3)\n"
"v_mfma_f32_16x16x32_bf16 v[120:123], v[240:243], a[136:139], v[120:123]\n"
"ds_read_b128 v[236:239], v221 offset:27648\n"
"v_mfma_f32_16x16x32_bf16 v[120:123], v[244:247], a[144:147], v[120:123]\n"
"buffer_load_dwordx4 v2, s[48:51], s64 offen offset:2048 sc1 lds\n"
"v_mfma_f32_16x16x32_bf16 v[124:127], v[240:243], a[140:143], v[124:127]\n"
"s_waitcnt vmcnt(31)\n"
"s_barrier\n"
"ds_read_b128 v[240:243], v221 offset:28672\n"
"v_mfma_f32_16x16x32_bf16 v[124:127], v[244:247], a[148:151], v[124:127]\n"
"s_waitcnt lgkmcnt(3)\n"
"v_mfma_f32_16x16x32_bf16 v[128:131], v[224:227], a[136:139], v[128:131]\n"
"ds_read_b128 v[244:247], v221 offset:35840\n"
"s_add_u32 m0, s83, 28672\n"
"v_mfma_f32_16x16x32_bf16 v[128:131], v[228:231], a[144:147], v[128:131]\n"
"s_add_u32 s64, s82, 37748736\n"
"v_mfma_f32_16x16x32_bf16 v[132:135], v[224:227], a[140:143], v[132:135]\n"
"ds_read_b128 v[224:227], v221 offset:29696\n"
"v_mfma_f32_16x16x32_bf16 v[132:135], v[228:231], a[148:151], v[132:135]\n"
"s_waitcnt lgkmcnt(3)\n"
"buffer_load_dwordx4 v2, s[48:51], s64 offen sc1 lds\n"
"v_mfma_f32_16x16x32_bf16 v[136:139], v[232:235], a[136:139], v[136:139]\n"
"ds_read_b128 v[228:231], v221 offset:36864\n"
"v_mfma_f32_16x16x32_bf16 v[136:139], v[236:239], a[144:147], v[136:139]\n"
"v_mfma_f32_16x16x32_bf16 v[140:143], v[232:235], a[140:143], v[140:143]\n"
"ds_read_b128 v[232:235], v221 offset:30720\n"
"v_mfma_f32_16x16x32_bf16 v[140:143], v[236:239], a[148:151], v[140:143]\n"
"s_waitcnt lgkmcnt(3)\n"
"buffer_load_dwordx4 v2, s[48:51], s64 offen offset:1024 sc1 lds\n"
"v_mfma_f32_16x16x32_bf16 v[88:91], v[240:243], a[152:155], v[88:91]\n"
"ds_read_b128 v[236:239], v221 offset:37888\n"
"v_mfma_f32_16x16x32_bf16 v[88:91], v[244:247], a[160:163], v[88:91]\n"
"v_mfma_f32_16x16x32_bf16 v[92:95], v[240:243], a[156:159], v[92:95]\n"
"ds_read_b128 v[240:243], v221 offset:31744\n"
"v_mfma_f32_16x16x32_bf16 v[92:95], v[244:247], a[164:167], v[92:95]\n"
"s_waitcnt lgkmcnt(3)\n"
"buffer_load_dwordx4 v2, s[48:51], s64 offen offset:2048 sc1 lds\n"
"v_mfma_f32_16x16x32_bf16 v[96:99], v[224:227], a[152:155], v[96:99]\n"
"ds_read_b128 v[244:247], v221 offset:38912\n"
"v_mfma_f32_16x16x32_bf16 v[96:99], v[228:231], a[160:163], v[96:99]\n"
"v_mfma_f32_16x16x32_bf16 v[100:103], v[224:227], a[156:159], v[100:103]\n"
"ds_read_b128 v[224:227], v221 offset:32768\n"
"v_mfma_f32_16x16x32_bf16 v[100:103], v[228:231], a[164:167], v[100:103]\n"
"s_waitcnt lgkmcnt(3)\n"
"v_mfma_f32_16x16x32_bf16 v[104:107], v[232:235], a[152:155], v[104:107]\n"
"ds_read_b128 v[228:231], v221 offset:39936\n"
"v_mfma_f32_16x16x32_bf16 v[104:107], v[236:239], a[160:163], v[104:107]\n"
"buffer_load_dwordx4 v2, s[48:51], s64 offen offset:3072 sc1 lds\n"
"v_mfma_f32_16x16x32_bf16 v[108:111], v[232:235], a[156:159], v[108:111]\n"
"ds_read_b128 v[232:235], v221 offset:33792\n"
"v_mfma_f32_16x16x32_bf16 v[108:111], v[236:239], a[164:167], v[108:111]\n"
"s_waitcnt lgkmcnt(3)\n"
"v_mfma_f32_16x16x32_bf16 v[112:115], v[240:243], a[152:155], v[112:115]\n"
"ds_read_b128 v[236:239], v221 offset:40960\n"
"v_mfma_f32_16x16x32_bf16 v[112:115], v[244:247], a[160:163], v[112:115]\n"
"buffer_load_dwordx4 a[36:39], v2, s[48:51], s63 offen offset:1024 sc1\n"
"s_waitcnt vmcnt(17)\n"
"s_add_u32 s62, s2, 1638400\n"
"s_mov_b64 exec, 2\n"
"buffer_atomic_add v1, off, s[48:51], s62 sc1\n"
"s_mov_b64 exec, -1\n"
"v_mfma_f32_16x16x32_bf16 v[116:119], v[240:243], a[156:159], v[116:119]\n"
"ds_read_b128 v[240:243], v221 offset:34816\n"
"s_add_u32 s63, s85, 22675456\n"
"v_mfma_f32_16x16x32_bf16 v[116:119], v[244:247], a[164:167], v[116:119]\n"
"s_waitcnt lgkmcnt(3)\n"
"v_mfma_f32_16x16x32_bf16 v[120:123], v[224:227], a[152:155], v[120:123]\n"
"ds_read_b128 v[244:247], v221 offset:41984\n"
"v_mov_b32 v164, v14\n"
"v_mfma_f32_16x16x32_bf16 v[120:123], v[228:231], a[160:163], v[120:123]\n"
"v_mov_b32 v165, v14\n"
"v_mov_b32 v166, v14\n"
"v_mfma_f32_16x16x32_bf16 v[124:127], v[224:227], a[156:159], v[124:127]\n"
"ds_read_b128 v[224:227], v221 offset:43008\n"
"v_mov_b32 v167, v14\n"
"v_mfma_f32_16x16x32_bf16 v[124:127], v[228:231], a[164:167], v[124:127]\n"
"s_waitcnt lgkmcnt(3)\n"
"buffer_load_dwordx4 a[40:43], v2, s[48:51], s63 offen sc1\n"
"v_mfma_f32_16x16x32_bf16 v[128:131], v[232:235], a[152:155], v[128:131]\n"
"ds_read_b128 v[228:231], v221 offset:50176\n"
"v_mov_b32 v168, v15\n"
"v_mfma_f32_16x16x32_bf16 v[128:131], v[236:239], a[160:163], v[128:131]\n"
"v_mov_b32 v169, v15\n"
"v_mov_b32 v170, v15\n"
"v_mfma_f32_16x16x32_bf16 v[132:135], v[232:235], a[156:159], v[132:135]\n"
"ds_read_b128 v[232:235], v221 offset:44032\n"
"v_mov_b32 v171, v15\n"
"v_mfma_f32_16x16x32_bf16 v[132:135], v[236:239], a[164:167], v[132:135]\n"
"s_waitcnt lgkmcnt(3)\n"
"buffer_load_dwordx4 a[44:47], v2, s[48:51], s63 offen offset:1024 sc1\n"
"v_mfma_f32_16x16x32_bf16 v[136:139], v[240:243], a[152:155], v[136:139]\n"
"ds_read_b128 v[236:239], v221 offset:51200\n"
"s_add_u32 s63, s85, 22806528\n"
"v_mfma_f32_16x16x32_bf16 v[136:139], v[244:247], a[160:163], v[136:139]\n"
"v_mov_b32 v172, v14\n"
"v_mov_b32 v173, v14\n"
"v_mfma_f32_16x16x32_bf16 v[140:143], v[240:243], a[156:159], v[140:143]\n"
"ds_read_b128 v[240:243], v221 offset:45056\n"
"v_mov_b32 v174, v14\n"
"v_mfma_f32_16x16x32_bf16 v[140:143], v[244:247], a[164:167], v[140:143]\n"
"s_waitcnt lgkmcnt(3)\n"
"buffer_load_dwordx4 a[48:51], v2, s[48:51], s63 offen sc1\n"
"v_mfma_f32_16x16x32_bf16 v[88:91], v[224:227], a[168:171], v[88:91]\n"
"ds_read_b128 v[244:247], v221 offset:52224\n"
"v_mov_b32 v175, v14\n"
"v_mfma_f32_16x16x32_bf16 v[88:91], v[228:231], a[176:179], v[88:91]\n"
"v_mov_b32 v176, v15\n"
"v_mov_b32 v177, v15\n"
"v_mfma_f32_16x16x32_bf16 v[92:95], v[224:227], a[172:175], v[92:95]\n"
"ds_read_b128 v[224:227], v221 offset:46080\n"
"v_mov_b32 v178, v15\n"
"v_mfma_f32_16x16x32_bf16 v[92:95], v[228:231], a[180:183], v[92:95]\n"
"s_waitcnt lgkmcnt(3)\n"
"v_mov_b32 v179, v15\n"
"v_mov_b32 v180, v14\n"
"v_mfma_f32_16x16x32_bf16 v[96:99], v[232:235], a[168:171], v[96:99]\n"
"ds_read_b128 v[228:231], v221 offset:53248\n"
"v_mov_b32 v181, v14\n"
"v_mfma_f32_16x16x32_bf16 v[96:99], v[236:239], a[176:179], v[96:99]\n"
"buffer_load_dwordx4 a[52:55], v2, s[48:51], s63 offen offset:1024 sc1\n"
"v_mfma_f32_16x16x32_bf16 v[100:103], v[232:235], a[172:175], v[100:103]\n"
"ds_read_b128 v[232:235], v221 offset:47104\n"
"s_add_u32 s63, s85, 22937600\n"
"v_mfma_f32_16x16x32_bf16 v[100:103], v[236:239], a[180:183], v[100:103]\n"
"s_waitcnt lgkmcnt(3)\n"
"v_mov_b32 v182, v14\n"
"v_mov_b32 v183, v14\n"
"v_mfma_f32_16x16x32_bf16 v[104:107], v[240:243], a[168:171], v[104:107]\n"
"ds_read_b128 v[236:239], v221 offset:54272\n"
"v_mov_b32 v184, v15\n"
"v_mfma_f32_16x16x32_bf16 v[104:107], v[244:247], a[176:179], v[104:107]\n"
"buffer_load_dwordx4 a[56:59], v2, s[48:51], s63 offen sc1\n"
"v_mfma_f32_16x16x32_bf16 v[108:111], v[240:243], a[172:175], v[108:111]\n"
"ds_read_b128 v[240:243], v221 offset:48128\n"
"v_mov_b32 v185, v15\n"
"v_mfma_f32_16x16x32_bf16 v[108:111], v[244:247], a[180:183], v[108:111]\n"
"s_waitcnt lgkmcnt(3)\n"
"v_mov_b32 v186, v15\n"
"v_mov_b32 v187, v15\n"
"v_mfma_f32_16x16x32_bf16 v[112:115], v[224:227], a[168:171], v[112:115]\n"
"ds_read_b128 v[244:247], v221 offset:55296\n"
"v_mov_b32 v188, v14\n"
"v_mfma_f32_16x16x32_bf16 v[112:115], v[228:231], a[176:179], v[112:115]\n"
"buffer_load_dwordx4 a[60:63], v2, s[48:51], s63 offen offset:1024 sc1\n"
"v_mfma_f32_16x16x32_bf16 v[116:119], v[224:227], a[172:175], v[116:119]\n"
"ds_read_b128 v[224:227], v221 offset:49152\n"
"s_add_u32 s63, s85, 23068672\n"
"v_mfma_f32_16x16x32_bf16 v[116:119], v[228:231], a[180:183], v[116:119]\n"
"s_waitcnt lgkmcnt(3)\n"
"v_mov_b32 v189, v14\n"
"v_mov_b32 v190, v14\n"
"v_mfma_f32_16x16x32_bf16 v[120:123], v[232:235], a[168:171], v[120:123]\n"
"ds_read_b128 v[228:231], v221 offset:56320\n"
"v_mov_b32 v191, v14\n"
"v_mfma_f32_16x16x32_bf16 v[120:123], v[236:239], a[176:179], v[120:123]\n"
"v_mov_b32 v192, v15\n"
"v_mov_b32 v193, v15\n"
"LPG14_%=:\n"
"s_cbranch_execz LPG18_%=\n"
"v_mfma_f32_16x16x32_bf16 v[124:127], v[232:235], a[172:175], v[124:127]\n"
"s_waitcnt vmcnt(22)\n"
"s_barrier\n"
"ds_read_b128 v[232:235], v222\n"
"v_mfma_f32_16x16x32_bf16 v[124:127], v[236:239], a[180:183], v[124:127]\n"
"s_waitcnt lgkmcnt(3)\n"
"buffer_load_dwordx4 a[64:67], v2, s[48:51], s63 offen sc1\n"
"v_mfma_f32_16x16x32_bf16 v[128:131], v[240:243], a[168:171], v[128:131]\n"
"ds_read_b128 v[236:239], v222 offset:7168\n"
"v_mov_b32 v194, v15\n"
"v_mfma_f32_16x16x32_bf16 v[128:131], v[244:247], a[176:179], v[128:131]\n"
"v_mov_b32 v195, v15\n"
"v_mov_b32 v196, v14\n"
"v_mfma_f32_16x16x32_bf16 v[132:135], v[240:243], a[172:175], v[132:135]\n"
"ds_read_b128 v[240:243], v222 offset:1024\n"
"v_mov_b32 v197, v14\n"
"v_mfma_f32_16x16x32_bf16 v[132:135], v[244:247], a[180:183], v[132:135]\n"
"s_waitcnt lgkmcnt(3)\n"
"buffer_load_dwordx4 a[68:71], v2, s[48:51], s63 offen offset:1024 sc1\n"
"v_mfma_f32_16x16x32_bf16 v[136:139], v[224:227], a[168:171], v[136:139]\n"
"ds_read_b128 v[244:247], v222 offset:8192\n"
"s_add_u32 s63, s85, 23199744\n"
"v_mfma_f32_16x16x32_bf16 v[136:139], v[228:231], a[176:179], v[136:139]\n"
"v_mov_b32 v198, v14\n"
"v_mov_b32 v199, v14\n"
"v_mfma_f32_16x16x32_bf16 v[140:143], v[224:227], a[172:175], v[140:143]\n"
"ds_read_b128 v[224:227], v222 offset:2048\n"
"v_mov_b32 v200, v15\n"
"v_mfma_f32_16x16x32_bf16 v[140:143], v[228:231], a[180:183], v[140:143]\n"
"s_waitcnt lgkmcnt(3)\n"
"v_mov_b32 v201, v15\n"
"v_mov_b32 v202, v15\n"
"v_mfma_f32_16x16x32_bf16 v[88:91], v[232:235], a[184:187], v[88:91]\n"
"ds_read_b128 v[228:231], v222 offset:9216\n"
"v_mov_b32 v203, v15\n"
"v_mfma_f32_16x16x32_bf16 v[88:91], v[236:239], a[192:195], v[88:91]\n"
"buffer_load_dwordx4 a[72:75], v2, s[48:51], s63 offen sc1\n"
"v_mfma_f32_16x16x32_bf16 v[92:95], v[232:235], a[188:191], v[92:95]\n"
"ds_read_b128 v[232:235], v222 offset:3072\n"
"v_mov_b32 v204, v14\n"
"v_mfma_f32_16x16x32_bf16 v[92:95], v[236:239], a[196:199], v[92:95]\n"
"s_waitcnt lgkmcnt(3)\n"
"v_mov_b32 v205, v14\n"
"v_mov_b32 v206, v14\n"
"v_mfma_f32_16x16x32_bf16 v[96:99], v[240:243], a[184:187], v[96:99]\n"
"ds_read_b128 v[236:239], v222 offset:10240\n"
"v_mov_b32 v207, v14\n"
"v_mfma_f32_16x16x32_bf16 v[96:99], v[244:247], a[192:195], v[96:99]\n"
"buffer_load_dwordx4 a[76:79], v2, s[48:51], s63 offen offset:1024 sc1\n"
"v_mfma_f32_16x16x32_bf16 v[100:103], v[240:243], a[188:191], v[100:103]\n"
"ds_read_b128 v[240:243], v222 offset:4096\n"
"s_add_u32 s63, s85, 23330816\n"
"v_mfma_f32_16x16x32_bf16 v[100:103], v[244:247], a[196:199], v[100:103]\n"
"s_waitcnt lgkmcnt(3)\n"
"v_mov_b32 v208, v15\n"
"v_mov_b32 v209, v15\n"
"v_mfma_f32_16x16x32_bf16 v[104:107], v[224:227], a[184:187], v[104:107]\n"
"ds_read_b128 v[244:247], v222 offset:11264\n"
"v_mov_b32 v210, v15\n"
"v_mfma_f32_16x16x32_bf16 v[104:107], v[228:231], a[192:195], v[104:107]\n"
"buffer_load_dwordx4 a[80:83], v2, s[48:51], s63 offen sc1\n"
"v_mfma_f32_16x16x32_bf16 v[108:111], v[224:227], a[188:191], v[108:111]\n"
"ds_read_b128 v[224:227], v222 offset:5120\n"
"v_mov_b32 v211, v15\n"
"v_mfma_f32_16x16x32_bf16 v[108:111], v[228:231], a[196:199], v[108:111]\n"
"s_waitcnt lgkmcnt(3)\n"
"v_mov_b32 v212, v14\n"
"v_mov_b32 v213, v14\n"
"v_mfma_f32_16x16x32_bf16 v[112:115], v[232:235], a[184:187], v[112:115]\n"
"ds_read_b128 v[228:231], v222 offset:12288\n"
"v_mov_b32 v214, v14\n"
"v_mfma_f32_16x16x32_bf16 v[112:115], v[236:239], a[192:195], v[112:115]\n"
"v_mov_b32 v215, v14\n"
"v_mov_b32 v216, v15\n"
"s_add_u32 s62, s35, 1638400\n"
"v_mov_b32 v252, 0\n"
"s_mov_b64 exec, 1\n"
"buffer_atomic_or v252, off, s[48:51], s62 sc0 sc1\n"
"s_mov_b64 exec, -1\n"
"v_mfma_f32_16x16x32_bf16 v[116:119], v[232:235], a[188:191], v[116:119]\n"
"ds_read_b128 v[232:235], v222 offset:6144\n"
"v_mov_b32 v217, v15\n"
"v_mfma_f32_16x16x32_bf16 v[116:119], v[236:239], a[196:199], v[116:119]\n"
"s_waitcnt lgkmcnt(3)\n"
"buffer_load_dwordx4 a[84:87], v2, s[48:51], s63 offen offset:1024 sc1\n"
"v_mfma_f32_16x16x32_bf16 v[120:123], v[240:243], a[184:187], v[120:123]\n"
"ds_read_b128 v[236:239], v222 offset:13312\n"
"s_add_u32 s63, s85, 23461888\n"
"v_mfma_f32_16x16x32_bf16 v[120:123], v[244:247], a[192:195], v[120:123]\n"
"v_mov_b32 v218, v15\n"
"v_mov_b32 v219, v15\n"
"v_mfma_f32_16x16x32_bf16 v[124:127], v[240:243], a[188:191], v[124:127]\n"
"ds_read_b128 v[240:243], v222 offset:14336\n"
"v_mov_b32 v154, 0x3f80\n"
"v_mfma_f32_16x16x32_bf16 v[124:127], v[244:247], a[196:199], v[124:127]\n"
"s_waitcnt lgkmcnt(3)\n"
"buffer_load_dwordx4 a[88:91], v2, s[48:51], s63 offen sc1\n"
"v_mfma_f32_16x16x32_bf16 v[128:131], v[224:227], a[184:187], v[128:131]\n"
"ds_read_b128 v[244:247], v222 offset:21504\n"
"v_and_b32 v155, 1, v1\n"
"v_mfma_f32_16x16x32_bf16 v[128:131], v[228:231], a[192:195], v[128:131]\n"
"v_lshlrev_b32 v155, 4, v155\n"
"v_lshlrev_b32 v154, v155, v154\n"
"v_mfma_f32_16x16x32_bf16 v[132:135], v[224:227], a[188:191], v[132:135]\n"
"ds_read_b128 v[224:227], v222 offset:15360\n"
"v_cndmask_b32 v154, 0, v154, s[78:79]\n"
"v_mfma_f32_16x16x32_bf16 v[132:135], v[228:231], a[196:199], v[132:135]\n"
"s_waitcnt lgkmcnt(3)\n"
"v_bfe_i32 v155, v1, 1, 1\n"
"v_and_b32 v153, v155, v154\n"
"v_mfma_f32_16x16x32_bf16 v[136:139], v[232:235], a[184:187], v[136:139]\n"
"ds_read_b128 v[228:231], v222 offset:22528\n"
"v_xor_b32 v152, v153, v154\n"
"v_mfma_f32_16x16x32_bf16 v[136:139], v[236:239], a[192:195], v[136:139]\n"
"buffer_load_dwordx4 a[92:95], v2, s[48:51], s63 offen offset:1024 sc1\n"
"v_mfma_f32_16x16x32_bf16 v[140:143], v[232:235], a[188:191], v[140:143]\n"
"ds_read_b128 v[232:235], v222 offset:16384\n"
"s_add_u32 s63, s85, 23592960\n"
"v_mfma_f32_16x16x32_bf16 v[140:143], v[236:239], a[196:199], v[140:143]\n"
"s_waitcnt lgkmcnt(3)\n"
"v_mov_b32 v154, 0\n"
"v_mov_b32 v155, 0\n"
"v_mfma_f32_16x16x32_bf16 v[88:91], v[240:243], a[200:203], v[88:91]\n"
"ds_read_b128 v[236:239], v222 offset:23552\n"
"v_mov_b32 v156, 0\n"
"v_mfma_f32_16x16x32_bf16 v[88:91], v[244:247], a[208:211], v[88:91]\n"
"buffer_load_dwordx4 a[96:99], v2, s[48:51], s63 offen sc1\n"
"v_mfma_f32_16x16x32_bf16 v[92:95], v[240:243], a[204:207], v[92:95]\n"
"ds_read_b128 v[240:243], v222 offset:17408\n"
"v_mov_b32 v157, 0\n"
"v_mfma_f32_16x16x32_bf16 v[92:95], v[244:247], a[212:215], v[92:95]\n"
"s_waitcnt lgkmcnt(3)\n"
"v_mov_b32 v158, v152\n"
"v_mov_b32 v159, v153\n"
"v_mfma_f32_16x16x32_bf16 v[96:99], v[224:227], a[200:203], v[96:99]\n"
"ds_read_b128 v[244:247], v222 offset:24576\n"
"v_mfma_f32_16x16x32_bf16 v[96:99], v[228:231], a[208:211], v[96:99]\n"
"buffer_load_dwordx4 a[100:103], v2, s[48:51], s63 offen offset:1024 sc1\n"
"v_mfma_f32_16x16x32_bf16 v[100:103], v[224:227], a[204:207], v[100:103]\n"
"ds_read_b128 v[224:227], v222 offset:18432\n"
"s_add_u32 s63, s85, 23724032\n"
"v_mfma_f32_16x16x32_bf16 v[100:103], v[228:231], a[212:215], v[100:103]\n"
"s_waitcnt lgkmcnt(3)\n"
"v_mfma_f32_16x16x32_bf16 v[104:107], v[232:235], a[200:203], v[104:107]\n"
"ds_read_b128 v[228:231], v222 offset:25600\n"
"v_mfma_f32_16x16x32_bf16 v[104:107], v[236:239], a[208:211], v[104:107]\n"
"v_mfma_f32_16x16x32_bf16 v[108:111], v[232:235], a[204:207], v[108:111]\n"
"ds_read_b128 v[232:235], v222 offset:19456\n"
"v_mfma_f32_16x16x32_bf16 v[108:111], v[236:239], a[212:215], v[108:111]\n"
"s_waitcnt lgkmcnt(3)\n"
"buffer_load_dwordx4 a[104:107], v2, s[48:51], s63 offen sc1\n"
"v_mfma_f32_16x16x32_bf16 v[112:115], v[240:243], a[200:203], v[112:115]\n"
"ds_read_b128 v[236:239], v222 offset:26624\n"
"v_mfma_f32_16x16x32_bf16 v[112:115], v[244:247], a[208:211], v[112:115]\n"
"v_mfma_f32_16x16x32_bf16 v[116:119], v[240:243], a[204:207], v[116:119]\n"
"ds_read_b128 v[240:243], v222 offset:20480\n"
"v_mfma_f32_16x16x32_bf16 v[116:119], v[244:247], a[212:215], v[116:119]\n"
"s_waitcnt lgkmcnt(3)\n"
"buffer_load_dwordx4 a[108:111], v2, s[48:51], s63 offen offset:1024 sc1\n"
"v_mfma_f32_16x16x32_bf16 v[120:123], v[224:227], a[200:203], v[120:123]\n"
"ds_read_b128 v[244:247], v222 offset:27648\n"
"s_add_u32 s63, s85, 23855104\n"
"v_mfma_f32_16x16x32_bf16 v[120:123], v[228:231], a[208:211], v[120:123]\n"
"v_mfma_f32_16x16x32_bf16 v[124:127], v[224:227], a[204:207], v[124:127]\n"
"s_waitcnt vmcnt(25)\n"
"s_barrier\n"
"ds_read_b128 v[224:227], v2\n"
"v_mfma_f32_16x16x32_bf16 v[124:127], v[228:231], a[212:215], v[124:127]\n"
"s_waitcnt lgkmcnt(3)\n"
"v_mfma_f32_16x16x32_bf16 v[128:131], v[232:235], a[200:203], v[128:131]\n"
"ds_read_b128 v[228:231], v2 offset:7168\n"
"v_mfma_f32_16x16x32_bf16 v[128:131], v[236:239], a[208:211], v[128:131]\n"
"buffer_load_dwordx4 a[112:115], v2, s[48:51], s63 offen sc1\n"
"v_mfma_f32_16x16x32_bf16 v[132:135], v[232:235], a[204:207], v[132:135]\n"
"ds_read_b128 v[232:235], v2 offset:1024\n"
"v_mfma_f32_16x16x32_bf16 v[132:135], v[236:239], a[212:215], v[132:135]\n"
"s_waitcnt lgkmcnt(3)\n"
"v_mfma_f32_16x16x32_bf16 v[136:139], v[240:243], a[200:203], v[136:139]\n"
"ds_read_b128 v[236:239], v2 offset:8192\n"
"v_mfma_f32_16x16x32_bf16 v[136:139], v[244:247], a[208:211], v[136:139]\n"
"buffer_load_dwordx4 a[116:119], v2, s[48:51], s63 offen offset:1024 sc1\n"
"v_mfma_f32_16x16x32_bf16 v[140:143], v[240:243], a[204:207], v[140:143]\n"
"ds_read_b128 v[240:243], v2 offset:2048\n"
"s_add_u32 s63, s85, 23986176\n"
"v_mfma_f32_16x16x32_bf16 v[140:143], v[244:247], a[212:215], v[140:143]\n"
"s_waitcnt lgkmcnt(3)\n"
"s_mov_b64 exec, 1\n"
"s_waitcnt vmcnt(9)\n"
"v_cmp_ne_u32 vcc, 16, v252\n"
"s_mov_b64 exec, -1\n"
"s_nop 3\n"
"s_cbranch_vccz Ld20_%=\n"
"s_mov_b32 s65, 0\n"
"Lp19_%=:\n"
"s_add_u32 s62, s35, 1638400\n"
"v_mov_b32 v252, 0\n"
"s_mov_b64 exec, 1\n"
"buffer_atomic_or v252, off, s[48:51], s62 sc0 sc1\n"
"s_mov_b64 exec, -1\n"
"s_add_u32 s65, s65, 1\n"
"s_cmp_gt_u32 s65, 131072\n"
"s_waitcnt vmcnt(0)\n"
"s_cbranch_scc1 Ld20_%=\n"
"s_mov_b64 exec, 1\n"
"v_cmp_ne_u32 vcc, 16, v252\n"
"s_mov_b64 exec, -1\n"
"s_nop 3\n"
"s_cbranch_vccnz Lp19_%=\n"
"Ld20_%=:\n"
"v_mfma_f32_16x16x32_bf16 v[88:91], v[224:227], a[216:219], v[88:91]\n"
"ds_read_b128 v[244:247], v2 offset:9216\n"
"v_mfma_f32_16x16x32_bf16 v[88:91], v[228:231], a[224:227], v[88:91]\n"
"buffer_load_dwordx4 a[120:123], v2, s[48:51], s63 offen sc1\n"
"v_mfma_f32_16x16x32_bf16 v[92:95], v[224:227], a[220:223], v[92:95]\n"
"ds_read_b128 v[224:227], v2 offset:3072\n"
"s_add_u32 m0, s84, 49152\n"
"v_mfma_f32_16x16x32_bf16 v[92:95], v[228:231], a[228:231], v[92:95]\n"
"s_waitcnt lgkmcnt(3)\n"
"s_add_u32 s64, s80, 37978112\n"
"v_mfma_f32_16x16x32_bf16 v[96:99], v[232:235], a[216:219], v[96:99]\n"
"ds_read_b128 v[228:231], v2 offset:10240\n"
"v_mfma_f32_16x16x32_bf16 v[96:99], v[236:239], a[224:227], v[96:99]\n"
"buffer_load_dwordx4 v2, s[48:51], s64 offen sc1 lds\n"
"v_mfma_f32_16x16x32_bf16 v[100:103], v[232:235], a[220:223], v[100:103]\n"
"ds_read_b128 v[232:235], v2 offset:4096\n"
"v_mfma_f32_16x16x32_bf16 v[100:103], v[236:239], a[228:231], v[100:103]\n"
"s_waitcnt lgkmcnt(3)\n"
"v_mfma_f32_16x16x32_bf16 v[104:107], v[240:243], a[216:219], v[104:107]\n"
"ds_read_b128 v[236:239], v2 offset:11264\n"
"v_mfma_f32_16x16x32_bf16 v[104:107], v[244:247], a[224:227], v[104:107]\n"
"buffer_load_dwordx4 v2, s[48:51], s64 offen offset:1024 sc1 lds\n"
"v_mfma_f32_16x16x32_bf16 v[108:111], v[240:243], a[220:223], v[108:111]\n"
"ds_read_b128 v[240:243], v2 offset:5120\n"
"s_add_u32 m0, s84, 51200\n"
"v_mfma_f32_16x16x32_bf16 v[108:111], v[244:247], a[228:231], v[108:111]\n"
"s_waitcnt lgkmcnt(3)\n"
"s_add_u32 s64, s80, 38248448\n"
"v_mfma_f32_16x16x32_bf16 v[112:115], v[224:227], a[216:219], v[112:115]\n"
"ds_read_b128 v[244:247], v2 offset:12288\n"
"v_mfma_f32_16x16x32_bf16 v[112:115], v[228:231], a[224:227], v[112:115]\n"
"buffer_load_dwordx4 v2, s[48:51], s64 offen sc1 lds\n"
"v_mfma_f32_16x16x32_bf16 v[116:119], v[224:227], a[220:223], v[116:119]\n"
"ds_read_b128 v[224:227], v2 offset:6144\n"
"v_mfma_f32_16x16x32_bf16 v[116:119], v[228:231], a[228:231], v[116:119]\n"
"s_waitcnt lgkmcnt(3)\n"
"buffer_load_dwordx4 v2, s[48:51], s64 offen offset:1024 sc1 lds\n"
"v_mfma_f32_16x16x32_bf16 v[120:123], v[232:235], a[216:219], v[120:123]\n"
"ds_read_b128 v[228:231], v2 offset:13312\n"
"s_add_u32 m0, s84, 53248\n"
"v_mfma_f32_16x16x32_bf16 v[120:123], v[236:239], a[224:227], v[120:123]\n"
"s_add_u32 s64, s80, 38518784\n"
"v_mfma_f32_16x16x32_bf16 v[124:127], v[232:235], a[220:223], v[124:127]\n"
"ds_read_b128 v[232:235], v2 offset:14336\n"
"v_mfma_f32_16x16x32_bf16 v[124:127], v[236:239], a[228:231], v[124:127]\n"
"s_waitcnt lgkmcnt(3)\n"
"buffer_load_dwordx4 v2, s[48:51], s64 offen sc1 lds\n"
"v_mfma_f32_16x16x32_bf16 v[128:131], v[240:243], a[216:219], v[128:131]\n"
"ds_read_b128 v[236:239], v2 offset:21504\n"
"v_mfma_f32_16x16x32_bf16 v[128:131], v[244:247], a[224:227], v[128:131]\n"
"buffer_load_dwordx4 v2, s[48:51], s64 offen offset:1024 sc1 lds\n"
"v_mfma_f32_16x16x32_bf16 v[132:135], v[240:243], a[220:223], v[132:135]\n"
"ds_read_b128 v[240:243], v2 offset:15360\n"
"s_add_u32 m0, s84, 55296\n"
"v_mfma_f32_16x16x32_bf16 v[132:135], v[244:247], a[228:231], v[132:135]\n"
"s_waitcnt lgkmcnt(3)\n"
"s_add_u32 s64, s80, 38789120\n"
"v_mfma_f32_16x16x32_bf16 v[136:139], v[224:227], a[216:219], v[136:139]\n"
"ds_read_b128 v[244:247], v2 offset:22528\n"
"v_mfma_f32_16x16x32_bf16 v[136:139], v[228:231], a[224:227], v[136:139]\n"
"buffer_load_dwordx4 v2, s[48:51], s64 offen sc1 lds\n"
"v_mfma_f32_16x16x32_bf16 v[140:143], v[224:227], a[220:223], v[140:143]\n"
"ds_read_b128 v[224:227], v2 offset:16384\n"
"v_mfma_f32_16x16x32_bf16 v[140:143], v[228:231], a[228:231], v[140:143]\n"
"s_waitcnt lgkmcnt(3)\n"
"buffer_load_dwordx4 v2, s[48:51], s64 offen offset:1024 sc1 lds\n"
"v_mfma_f32_16x16x32_bf16 v[88:91], v[232:235], a[232:235], v[88:91]\n"
"ds_read_b128 v[228:231], v2 offset:23552\n"
"s_add_u32 m0, s84, 81920\n"
"v_mfma_f32_16x16x32_bf16 v[88:91], v[236:239], a[240:243], v[88:91]\n"
"s_add_u32 s64, s80, 39870464\n"
"v_mfma_f32_16x16x32_bf16 v[92:95], v[232:235], a[236:239], v[92:95]\n"
"ds_read_b128 v[232:235], v2 offset:17408\n"
"v_mfma_f32_16x16x32_bf16 v[92:95], v[236:239], a[244:247], v[92:95]\n"
"s_waitcnt lgkmcnt(3)\n"
"v_mfma_f32_16x16x32_bf16 v[96:99], v[240:243], a[232:235], v[96:99]\n"
"ds_read_b128 v[236:239], v2 offset:24576\n"
"v_mfma_f32_16x16x32_bf16 v[96:99], v[244:247], a[240:243], v[96:99]\n"
"v_mfma_f32_16x16x32_bf16 v[100:103], v[240:243], a[236:239], v[100:103]\n"
"ds_read_b128 v[240:243], v2 offset:18432\n"
"v_mfma_f32_16x16x32_bf16 v[100:103], v[244:247], a[244:247], v[100:103]\n"
"s_waitcnt lgkmcnt(3)\n"
"v_mfma_f32_16x16x32_bf16 v[104:107], v[224:227], a[232:235], v[104:107]\n"
"ds_read_b128 v[244:247], v2 offset:25600\n"
"v_mfma_f32_16x16x32_bf16 v[104:107], v[228:231], a[240:243], v[104:107]\n"
"v_mfma_f32_16x16x32_bf16 v[108:111], v[224:227], a[236:239], v[108:111]\n"
"ds_read_b128 v[224:227], v2 offset:19456\n"
"v_mfma_f32_16x16x32_bf16 v[108:111], v[228:231], a[244:247], v[108:111]\n"
"s_waitcnt lgkmcnt(3)\n"
"v_mfma_f32_16x16x32_bf16 v[112:115], v[232:235], a[232:235], v[112:115]\n"
"ds_read_b128 v[228:231], v2 offset:26624\n"
"v_mfma_f32_16x16x32_bf16 v[112:115], v[236:239], a[240:243], v[112:115]\n"
"v_mfma_f32_16x16x32_bf16 v[116:119], v[232:235], a[236:239], v[116:119]\n"
"ds_read_b128 v[232:235], v2 offset:20480\n"
"v_mfma_f32_16x16x32_bf16 v[116:119], v[236:239], a[244:247], v[116:119]\n"
"s_waitcnt lgkmcnt(3)\n"
"buffer_load_dwordx4 v2, s[48:51], s64 offen sc1 lds\n"
"v_mfma_f32_16x16x32_bf16 v[120:123], v[240:243], a[232:235], v[120:123]\n"
"ds_read_b128 v[236:239], v2 offset:27648\n"
"v_mfma_f32_16x16x32_bf16 v[120:123], v[244:247], a[240:243], v[120:123]\n"
"v_mfma_f32_16x16x32_bf16 v[124:127], v[240:243], a[236:239], v[124:127]\n"
"s_barrier\n"
"ds_read_b128 v[240:243], v2 offset:28672\n"
"v_mfma_f32_16x16x32_bf16 v[124:127], v[244:247], a[244:247], v[124:127]\n"
"s_waitcnt lgkmcnt(3)\n"
"v_mfma_f32_16x16x32_bf16 v[128:131], v[224:227], a[232:235], v[128:131]\n"
"ds_read_b128 v[244:247], v2 offset:35840\n"
"v_mfma_f32_16x16x32_bf16 v[128:131], v[228:231], a[240:243], v[128:131]\n"
"buffer_load_dwordx4 v2, s[48:51], s64 offen offset:1024 sc1 lds\n"
"v_mfma_f32_16x16x32_bf16 v[132:135], v[224:227], a[236:239], v[132:135]\n"
"ds_read_b128 v[224:227], v2 offset:29696\n"
"s_add_u32 m0, s84, 83968\n"
"v_mfma_f32_16x16x32_bf16 v[132:135], v[228:231], a[244:247], v[132:135]\n"
"s_waitcnt lgkmcnt(3)\n"
"s_add_u32 s64, s80, 40140800\n"
"v_mfma_f32_16x16x32_bf16 v[136:139], v[232:235], a[232:235], v[136:139]\n"
"ds_read_b128 v[228:231], v2 offset:36864\n"
"v_mfma_f32_16x16x32_bf16 v[136:139], v[236:239], a[240:243], v[136:139]\n"
"buffer_load_dwordx4 v2, s[48:51], s64 offen sc1 lds\n"
"v_mfma_f32_16x16x32_bf16 v[140:143], v[232:235], a[236:239], v[140:143]\n"
"ds_read_b128 v[232:235], v2 offset:30720\n"
"v_mfma_f32_16x16x32_bf16 v[140:143], v[236:239], a[244:247], v[140:143]\n"
"s_waitcnt lgkmcnt(3)\n"
"LZ4_%=:\n"
"v_add_u32 v11, LZ5_%=-LPC_%=, v0\n"
"v_min_u32 v11, LZ7_%=-LPC_%=-4, v11\n"
"global_load_dword v11, v11, s[70:71]\n"
"v_mfma_f32_16x16x32_bf16 v[88:91], v[240:243], a[248:251], v[88:91]\n"
"ds_read_b128 v[236:239], v2 offset:37888\n"
"v_mfma_f32_16x16x32_bf16 v[88:91], v[244:247], v[144:147], v[88:91]\n"
"buffer_load_dwordx4 v2, s[48:51], s64 offen offset:1024 sc1 lds\n"
"v_mfma_f32_16x16x32_bf16 v[92:95], v[240:243], a[252:255], v[92:95]\n"
"ds_read_b128 v[240:243], v2 offset:31744\n"
"s_add_u32 m0, s84, 86016\n"
"v_mfma_f32_16x16x32_bf16 v[92:95], v[244:247], v[148:151], v[92:95]\n"
"s_waitcnt lgkmcnt(3)\n"
"s_add_u32 s64, s80, 40411136\n"
"v_mfma_f32_16x16x32_bf16 v[96:99], v[224:227], a[248:251], v[96:99]\n"
"ds_read_b128 v[244:247], v2 offset:38912\n"
"v_mfma_f32_16x16x32_bf16 v[96:99], v[228:231], v[144:147], v[96:99]\n"
"buffer_load_dwordx4 v2, s[48:51], s64 offen sc1 lds\n"
"v_mfma_f32_16x16x32_bf16 v[100:103], v[224:227], a[252:255], v[100:103]\n"
"ds_read_b128 v[224:227], v2 offset:32768\n"
"v_mfma_f32_16x16x32_bf16 v[100:103], v[228:231], v[148:151], v[100:103]\n"
"s_waitcnt lgkmcnt(3)\n"
"v_mfma_f32_16x16x32_bf16 v[104:107], v[232:235], a[248:251], v[104:107]\n"
"ds_read_b128 v[228:231], v2 offset:39936\n"
"v_mfma_f32_16x16x32_bf16 v[104:107], v[236:239], v[144:147], v[104:107]\n"
"buffer_load_dwordx4 v2, s[48:51], s64 offen offset:1024 sc1 lds\n"
"v_mfma_f32_16x16x32_bf16 v[108:111], v[232:235], a[252:255], v[108:111]\n"
"ds_read_b128 v[232:235], v2 offset:33792\n"
"s_add_u32 m0, s84, 88064\n"
"v_mfma_f32_16x16x32_bf16 v[108:111], v[236:239], v[148:151], v[108:111]\n"
"s_waitcnt lgkmcnt(3)\n"
"s_add_u32 s64, s80, 40681472\n"
"v_mfma_f32_16x16x32_bf16 v[112:115], v[240:243], a[248:251], v[112:115]\n"
"ds_read_b128 v[236:239], v2 offset:40960\n"
"v_mfma_f32_16x16x32_bf16 v[112:115], v[244:247], v[144:147], v[112:115]\n"
"buffer_load_dwordx4 v2, s[48:51], s64 offen sc1 lds\n"
"v_mfma_f32_16x16x32_bf16 v[116:119], v[240:243], a[252:255], v[116:119]\n"
"ds_read_b128 v[240:243], v2 offset:34816\n"
"v_mfma_f32_16x16x32_bf16 v[116:119], v[244:247], v[148:151], v[116:119]\n"
"s_waitcnt lgkmcnt(3)\n"
"v_mfma_f32_16x16x32_bf16 v[120:123], v[224:227], a[248:251], v[120:123]\n"
"ds_read_b128 v[244:247], v2 offset:41984\n"
"v_mfma_f32_16x16x32_bf16 v[120:123], v[228:231], v[144:147], v[120:123]\n"
"buffer_load_dwordx4 v2, s[48:51], s64 offen offset:1024 sc1 lds\n"
"v_mfma_f32_16x16x32_bf16 v[124:127], v[224:227], a[252:255], v[124:127]\n"
"s_waitcnt vmcnt(9)\n"
"s_barrier\n"
"ds_read_b128 v[224:227], v2 offset:49152\n"
"v_mfma_f32_16x16x32_bf16 v[124:127], v[228:231], v[148:151], v[124:127]\n"
"s_waitcnt lgkmcnt(3)\n"
"s_add_u32 m0, s84, 114688\n"
"s_add_u32 s64, s80, 41762816\n"
"v_mfma_f32_16x16x32_bf16 v[128:131], v[232:235], a[248:251], v[128:131]\n"
"ds_read_b128 v[228:231], v221\n"
"v_mfma_f32_16x16x32_bf16 v[128:131], v[236:239], v[144:147], v[128:131]\n"
"v_mfma_f32_16x16x32_bf16 v[132:135], v[232:235], a[252:255], v[132:135]\n"
"ds_read_b128 v[232:235], v2 offset:51200\n"
"v_mfma_f32_16x16x32_bf16 v[132:135], v[236:239], v[148:151], v[132:135]\n"
"s_waitcnt lgkmcnt(3)\n"
"v_mfma_f32_16x16x32_bf16 v[136:139], v[240:243], a[248:251], v[136:139]\n"
"ds_read_b128 v[236:239], v221 offset:2048\n"
"v_mfma_f32_16x16x32_bf16 v[136:139], v[244:247], v[144:147], v[136:139]\n"
"v_mfma_f32_16x16x32_bf16 v[140:143], v[240:243], a[252:255], v[140:143]\n"
"ds_read_b128 v[240:243], v2 offset:53248\n"
"v_mfma_f32_16x16x32_bf16 v[140:143], v[244:247], v[148:151], v[140:143]\n"
"s_waitcnt lgkmcnt(3)\n"
"buffer_load_dwordx4 v2, s[48:51], s64 offen sc1 lds\n"
"v_mfma_f32_16x16x32_bf16 v[32:35], v[224:227], a[0:3], 0\n"
"ds_read_b128 v[244:247], v221 offset:4096\n"
"v_mfma_f32_16x16x32_bf16 v[32:35], v[228:231], a[8:11], v[32:35]\n"
"v_mfma_f32_16x16x32_bf16 v[36:39], v[224:227], a[4:7], 0\n"
"ds_read_b128 v[224:227], v2 offset:55296\n"
"v_mfma_f32_16x16x32_bf16 v[36:39], v[228:231], a[12:15], v[36:39]\n"
"s_waitcnt lgkmcnt(3)\n"
"buffer_load_dwordx4 v2, s[48:51], s64 offen offset:1024 sc1 lds\n"
"v_mfma_f32_16x16x32_bf16 v[40:43], v[232:235], a[0:3], 0\n"
"ds_read_b128 v[228:231], v221 offset:6144\n"
"s_add_u32 m0, s84, 116736\n"
"v_mfma_f32_16x16x32_bf16 v[40:43], v[236:239], a[8:11], v[40:43]\n"
"s_add_u32 s64, s80, 42033152\n"
"v_mfma_f32_16x16x32_bf16 v[44:47], v[232:235], a[4:7], 0\n"
"ds_read_b128 v[232:235], v221 offset:8192\n"
"v_mfma_f32_16x16x32_bf16 v[44:47], v[236:239], a[12:15], v[44:47]\n"
"s_waitcnt lgkmcnt(3)\n"
"buffer_load_dwordx4 v2, s[48:51], s64 offen sc1 lds\n"
"v_mfma_f32_16x16x32_bf16 v[48:51], v[240:243], a[0:3], 0\n"
"ds_read_b128 v[236:239], v221 offset:16384\n"
"v_mfma_f32_16x16x32_bf16 v[48:51], v[244:247], a[8:11], v[48:51]\n"
"v_mfma_f32_16x16x32_bf16 v[52:55], v[240:243], a[4:7], 0\n"
"ds_read_b128 v[240:243], v221 offset:10240\n"
"v_mfma_f32_16x16x32_bf16 v[52:55], v[244:247], a[12:15], v[52:55]\n"
"s_waitcnt lgkmcnt(3)\n"
"v_mfma_f32_16x16x32_bf16 v[56:59], v[224:227], a[0:3], 0\n"
"ds_read_b128 v[244:247], v221 offset:18432\n"
"v_mfma_f32_16x16x32_bf16 v[56:59], v[228:231], a[8:11], v[56:59]\n"
"buffer_load_dwordx4 v2, s[48:51], s64 offen offset:1024 sc1 lds\n"
"v_mfma_f32_16x16x32_bf16 v[60:63], v[224:227], a[4:7], 0\n"
"ds_read_b128 v[224:227], v221 offset:12288\n"
"s_add_u32 m0, s84, 118784\n"
"v_mfma_f32_16x16x32_bf16 v[60:63], v[228:231], a[12:15], v[60:63]\n"
"s_waitcnt lgkmcnt(3)\n"
"s_add_u32 s64, s80, 42303488\n"
"v_mfma_f32_16x16x32_bf16 v[32:35], v[232:235], a[16:19], v[32:35]\n"
"ds_read_b128 v[228:231], v221 offset:20480\n"
"v_mul_f32 v88, v88, v160\n"
"v_mfma_f32_16x16x32_bf16 v[32:35], v[236:239], a[24:27], v[32:35]\n"
"buffer_load_dwordx4 a[124:127], v2, s[48:51], s63 offen offset:1024 sc1\n"
"v_mfma_f32_16x16x32_bf16 v[36:39], v[232:235], a[20:23], v[36:39]\n"
"ds_read_b128 v[232:235], v221 offset:14336\n"
"s_add_u32 s63, s85, 24117248\n"
"v_mfma_f32_16x16x32_bf16 v[36:39], v[236:239], a[28:31], v[36:39]\n"
"s_waitcnt lgkmcnt(3)\n"
"v_mul_f32 v89, v89, v160\n"
"v_mul_f32 v144, v88, v88\n"
"v_mfma_f32_16x16x32_bf16 v[40:43], v[240:243], a[16:19], v[40:43]\n"
"ds_read_b128 v[236:239], v221 offset:22528\n"
"v_mul_f32 v90, v90, v160\n"
"v_mfma_f32_16x16x32_bf16 v[40:43], v[244:247], a[24:27], v[40:43]\n"
"buffer_load_dwordx4 v2, s[48:51], s64 offen sc1 lds\n"
"v_mfma_f32_16x16x32_bf16 v[44:47], v[240:243], a[20:23], v[44:47]\n"
"ds_read_b128 v[240:243], v2 offset:50176\n"
"v_mul_f32 v91, v91, v160\n"
"v_mfma_f32_16x16x32_bf16 v[44:47], v[244:247], a[28:31], v[44:47]\n"
"s_waitcnt lgkmcnt(3)\n"
"v_mul_f32 v145, v89, v89\n"
"v_mul_f32 v146, v90, v90\n"
"v_mfma_f32_16x16x32_bf16 v[48:51], v[224:227], a[16:19], v[48:51]\n"
"ds_read_b128 v[244:247], v221 offset:1024\n"
"v_mul_f32 v147, v91, v91\n"
"v_mfma_f32_16x16x32_bf16 v[48:51], v[228:231], a[24:27], v[48:51]\n"
"v_fma_f32 v144, v144, v162, v163\n"
"v_fma_f32 v145, v145, v162, v163\n"
"v_mfma_f32_16x16x32_bf16 v[52:55], v[224:227], a[20:23], v[52:55]\n"
"ds_read_b128 v[224:227], v2 offset:52224\n"
"v_fma_f32 v146, v146, v162, v163\n"
"v_mfma_f32_16x16x32_bf16 v[52:55], v[228:231], a[28:31], v[52:55]\n"
"s_waitcnt lgkmcnt(3)\n"
"buffer_load_dwordx4 v2, s[48:51], s64 offen offset:1024 sc1 lds\n"
"v_mfma_f32_16x16x32_bf16 v[56:59], v[232:235], a[16:19], v[56:59]\n"
"ds_read_b128 v[228:231], v221 offset:3072\n"
"s_add_u32 m0, s84, 120832\n"
"v_mfma_f32_16x16x32_bf16 v[56:59], v[236:239], a[24:27], v[56:59]\n"
"s_add_u32 s64, s80, 42573824\n"
"v_mfma_f32_16x16x32_bf16 v[60:63], v[232:235], a[20:23], v[60:63]\n"
"ds_read_b128 v[232:235], v2 offset:54272\n"
"v_fma_f32 v147, v147, v162, v163\n"
"v_mfma_f32_16x16x32_bf16 v[60:63], v[236:239], a[28:31], v[60:63]\n"
"s_waitcnt lgkmcnt(3)\n"
"buffer_load_dwordx4 v2, s[48:51], s64 offen sc1 lds\n"
"v_mfma_f32_16x16x32_bf16 v[32:35], v[240:243], a[32:35], v[32:35]\n"
"ds_read_b128 v[236:239], v221 offset:5120\n"
"v_mul_f32 v144, v144, v88\n"
"v_mfma_f32_16x16x32_bf16 v[32:35], v[244:247], a[40:43], v[32:35]\n"
"v_exp_f32 v144, v144\n"
"v_mfma_f32_16x16x32_bf16 v[36:39], v[240:243], a[36:39], v[36:39]\n"
"ds_read_b128 v[240:243], v2 offset:56320\n"
"v_mul_f32 v145, v145, v89\n"
"v_mfma_f32_16x16x32_bf16 v[36:39], v[244:247], a[44:47], v[36:39]\n"
"s_waitcnt lgkmcnt(3)\n"
"v_exp_f32 v145, v145\n"
"v_mfma_f32_16x16x32_bf16 v[40:43], v[224:227], a[32:35], v[40:43]\n"
"ds_read_b128 v[244:247], v221 offset:7168\n"
"v_mul_f32 v146, v146, v90\n"
"v_mfma_f32_16x16x32_bf16 v[40:43], v[228:231], a[40:43], v[40:43]\n"
"buffer_load_dwordx4 a[128:131], v2, s[48:51], s63 offen sc1\n"
"v_mfma_f32_16x16x32_bf16 v[44:47], v[224:227], a[36:39], v[44:47]\n"
"ds_read_b128 v[224:227], v221 offset:9216\n"
"v_mul_f32 v147, v147, v91\n"
"v_mfma_f32_16x16x32_bf16 v[44:47], v[228:231], a[44:47], v[44:47]\n"
"s_waitcnt lgkmcnt(3)\n"
"v_exp_f32 v146, v146\n"
"v_mfma_f32_16x16x32_bf16 v[48:51], v[232:235], a[32:35], v[48:51]\n"
"ds_read_b128 v[228:231], v221 offset:17408\n"
"v_add_f32 v144, 1.0, v144\n"
"v_mfma_f32_16x16x32_bf16 v[48:51], v[236:239], a[40:43], v[48:51]\n"
"buffer_load_dwordx4 a[132:135], v2, s[48:51], s63 offen offset:1024 sc1\n"
"v_mfma_f32_16x16x32_bf16 v[52:55], v[232:235], a[36:39], v[52:55]\n"
"ds_read_b128 v[232:235], v221 offset:11264\n"
"s_add_u32 s63, s85, 24248320\n"
"v_mfma_f32_16x16x32_bf16 v[52:55], v[236:239], a[44:47], v[52:55]\n"
"s_waitcnt lgkmcnt(3)\n"
"v_exp_f32 v147, v147\n"
"v_mfma_f32_16x16x32_bf16 v[56:59], v[240:243], a[32:35], v[56:59]\n"
"ds_read_b128 v[236:239], v221 offset:19456\n"
"v_add_f32 v145, 1.0, v145\n"
"v_mfma_f32_16x16x32_bf16 v[56:59], v[244:247], a[40:43], v[56:59]\n"
"buffer_load_dwordx4 v2, s[48:51], s64 offen offset:1024 sc1 lds\n"
"v_mfma_f32_16x16x32_bf16 v[60:63], v[240:243], a[36:39], v[60:63]\n"
"ds_read_b128 v[240:243], v221 offset:13312\n"
"v_add_f32 v146, 1.0, v146\n"
"v_mfma_f32_16x16x32_bf16 v[60:63], v[244:247], a[44:47], v[60:63]\n"
"s_waitcnt lgkmcnt(3)\n"
"v_rcp_f32 v144, v144\n"
"v_mfma_f32_16x16x32_bf16 v[32:35], v[224:227], a[48:51], v[32:35]\n"
"ds_read_b128 v[244:247], v221 offset:21504\n"
"v_add_f32 v147, 1.0, v147\n"
"v_mfma_f32_16x16x32_bf16 v[32:35], v[228:231], a[56:59], v[32:35]\n"
"v_rcp_f32 v145, v145\n"
"v_mfma_f32_16x16x32_bf16 v[36:39], v[224:227], a[52:55], v[36:39]\n"
"ds_read_b128 v[224:227], v221 offset:15360\n"
"v_mul_f32 v88, v88, v144\n"
"v_mfma_f32_16x16x32_bf16 v[36:39], v[228:231], a[60:63], v[36:39]\n"
"s_waitcnt lgkmcnt(3)\n"
"buffer_load_dwordx4 a[136:139], v2, s[48:51], s63 offen sc1\n"
"v_mfma_f32_16x16x32_bf16 v[40:43], v[232:235], a[48:51], v[40:43]\n"
"ds_read_b128 v[228:231], v221 offset:23552\n"
"v_mul_f32 v89, v89, v145\n"
"v_mfma_f32_16x16x32_bf16 v[40:43], v[236:239], a[56:59], v[40:43]\n"
"v_rcp_f32 v146, v146\n"
"v_mfma_f32_16x16x32_bf16 v[44:47], v[232:235], a[52:55], v[44:47]\n"
"s_waitcnt vmcnt(12)\n"
"s_barrier\n"
"ds_read_b128 v[232:235], v221 offset:24576\n"
"v_mfma_f32_16x16x32_bf16 v[44:47], v[236:239], a[60:63], v[44:47]\n"
"s_waitcnt lgkmcnt(3)\n"
"buffer_load_dwordx4 a[140:143], v2, s[48:51], s63 offen offset:1024 sc1\n"
"v_mfma_f32_16x16x32_bf16 v[48:51], v[240:243], a[48:51], v[48:51]\n"
"ds_read_b128 v[236:239], v221 offset:32768\n"
"s_add_u32 s63, s85, 24379392\n"
"v_mfma_f32_16x16x32_bf16 v[48:51], v[244:247], a[56:59], v[48:51]\n"
"s_add_u32 m0, s84, 16384\n"
"s_add_u32 s64, s80, 43655168\n"
"v_mfma_f32_16x16x32_bf16 v[52:55], v[240:243], a[52:55], v[52:55]\n"
"ds_read_b128 v[240:243], v221 offset:26624\n"
"v_mul_f32 v90, v90, v146\n"
"v_mfma_f32_16x16x32_bf16 v[52:55], v[244:247], a[60:63], v[52:55]\n"
"s_waitcnt lgkmcnt(3)\n"
"buffer_load_dwordx4 a[144:147], v2, s[48:51], s63 offen sc1\n"
"v_mfma_f32_16x16x32_bf16 v[56:59], v[224:227], a[48:51], v[56:59]\n"
"ds_read_b128 v[244:247], v221 offset:34816\n"
"v_mul_f32 v92, v92, v161\n"
"v_mfma_f32_16x16x32_bf16 v[56:59], v[228:231], a[56:59], v[56:59]\n"
"v_rcp_f32 v147, v147\n"
"v_mfma_f32_16x16x32_bf16 v[60:63], v[224:227], a[52:55], v[60:63]\n"
"ds_read_b128 v[224:227], v221 offset:28672\n"
"v_mul_f32 v91, v91, v147\n"
"v_mfma_f32_16x16x32_bf16 v[60:63], v[228:231], a[60:63], v[60:63]\n"
"s_waitcnt lgkmcnt(3)\n"
"buffer_load_dwordx4 a[148:151], v2, s[48:51], s63 offen offset:1024 sc1\n"
"v_mfma_f32_16x16x32_bf16 v[32:35], v[232:235], a[64:67], v[32:35]\n"
"ds_read_b128 v[228:231], v221 offset:36864\n"
"s_add_u32 s63, s85, 24510464\n"
"v_mfma_f32_16x16x32_bf16 v[32:35], v[236:239], a[72:75], v[32:35]\n"
"v_mul_f32 v93, v93, v161\n"
"v_mul_f32 v94, v94, v161\n"
"v_mfma_f32_16x16x32_bf16 v[36:39], v[232:235], a[68:71], v[36:39]\n"
"ds_read_b128 v[232:235], v221 offset:30720\n"
"v_mul_f32 v95, v95, v161\n"
"v_mfma_f32_16x16x32_bf16 v[36:39], v[236:239], a[76:79], v[36:39]\n"
"s_waitcnt lgkmcnt(3)\n"
"v_mul_f32 v144, v92, v92\n"
"v_mul_f32 v145, v93, v93\n"
"v_mfma_f32_16x16x32_bf16 v[40:43], v[240:243], a[64:67], v[40:43]\n"
"ds_read_b128 v[236:239], v221 offset:38912\n"
"v_mul_f32 v146, v94, v94\n"
"v_mfma_f32_16x16x32_bf16 v[40:43], v[244:247], a[72:75], v[40:43]\n"
"buffer_load_dwordx4 a[152:155], v2, s[48:51], s63 offen sc1\n"
"v_mfma_f32_16x16x32_bf16 v[44:47], v[240:243], a[68:71], v[44:47]\n"
"ds_read_b128 v[240:243], v221 offset:40960\n"
"v_mul_f32 v147, v95, v95\n"
"v_mfma_f32_16x16x32_bf16 v[44:47], v[244:247], a[76:79], v[44:47]\n"
"s_waitcnt lgkmcnt(3)\n"
"LPG15_%=:\n"
"s_cbranch_execz LPG19_%=\n"
"v_fma_f32 v144, v144, v162, v163\n"
"v_fma_f32 v145, v145, v162, v163\n"
"v_mfma_f32_16x16x32_bf16 v[48:51], v[224:227], a[64:67], v[48:51]\n"
"ds_read_b128 v[244:247], v221 offset:49152\n"
"v_fma_f32 v146, v146, v162, v163\n"
"v_mfma_f32_16x16x32_bf16 v[48:51], v[228:231], a[72:75], v[48:51]\n"
"buffer_load_dwordx4 a[156:159], v2, s[48:51], s63 offen offset:1024 sc1\n"
"v_mfma_f32_16x16x32_bf16 v[52:55], v[224:227], a[68:71], v[52:55]\n"
"ds_read_b128 v[224:227], v221 offset:43008\n"
"s_add_u32 s63, s85, 24641536\n"
"v_mfma_f32_16x16x32_bf16 v[52:55], v[228:231], a[76:79], v[52:55]\n"
"s_waitcnt lgkmcnt(3)\n"
"v_fma_f32 v147, v147, v162, v163\n"
"v_mul_f32 v144, v144, v92\n"
"v_mfma_f32_16x16x32_bf16 v[56:59], v[232:235], a[64:67], v[56:59]\n"
"ds_read_b128 v[228:231], v221 offset:51200\n"
"v_mul_f32 v145, v145, v93\n"
"v_mfma_f32_16x16x32_bf16 v[56:59], v[236:239], a[72:75], v[56:59]\n"
"buffer_load_dwordx4 a[160:163], v2, s[48:51], s63 offen sc1\n"
"v_mfma_f32_16x16x32_bf16 v[60:63], v[232:235], a[68:71], v[60:63]\n"
"ds_read_b128 v[232:235], v221 offset:45056\n"
"v_mul_f32 v146, v146, v94\n"
"v_mfma_f32_16x16x32_bf16 v[60:63], v[236:239], a[76:79], v[60:63]\n"
"s_waitcnt lgkmcnt(3)\n"
"v_exp_f32 v144, v144\n"
"v_mfma_f32_16x16x32_bf16 v[32:35], v[240:243], a[80:83], v[32:35]\n"
"ds_read_b128 v[236:239], v221 offset:53248\n"
"v_mul_f32 v147, v147, v95\n"
"v_mfma_f32_16x16x32_bf16 v[32:35], v[244:247], a[88:91], v[32:35]\n"
"buffer_load_dwordx4 a[164:167], v2, s[48:51], s63 offen offset:1024 sc1\n"
"v_mfma_f32_16x16x32_bf16 v[36:39], v[240:243], a[84:87], v[36:39]\n"
"ds_read_b128 v[240:243], v221 offset:47104\n"
"s_add_u32 s63, s85, 24772608\n"
"v_mfma_f32_16x16x32_bf16 v[36:39], v[244:247], a[92:95], v[36:39]\n"
"s_waitcnt lgkmcnt(3)\n"
"v_exp_f32 v145, v145\n"
"v_mfma_f32_16x16x32_bf16 v[40:43], v[224:227], a[80:83], v[40:43]\n"
"ds_read_b128 v[244:247], v221 offset:55296\n"
"v_add_f32 v144, 1.0, v144\n"
"v_mfma_f32_16x16x32_bf16 v[40:43], v[228:231], a[88:91], v[40:43]\n"
"buffer_load_dwordx4 a[168:171], v2, s[48:51], s63 offen sc1\n"
"v_mfma_f32_16x16x32_bf16 v[44:47], v[224:227], a[84:87], v[44:47]\n"
"ds_read_b128 v[224:227], v221 offset:25600\n"
"v_add_f32 v145, 1.0, v145\n"
"v_mfma_f32_16x16x32_bf16 v[44:47], v[228:231], a[92:95], v[44:47]\n"
"s_waitcnt lgkmcnt(3)\n"
"v_exp_f32 v146, v146\n"
"v_mfma_f32_16x16x32_bf16 v[48:51], v[232:235], a[80:83], v[48:51]\n"
"ds_read_b128 v[228:231], v221 offset:33792\n"
"v_add_f32 v146, 1.0, v146\n"
"v_mfma_f32_16x16x32_bf16 v[48:51], v[236:239], a[88:91], v[48:51]\n"
"buffer_load_dwordx4 a[172:175], v2, s[48:51], s63 offen offset:1024 sc1\n"
"v_mfma_f32_16x16x32_bf16 v[52:55], v[232:235], a[84:87], v[52:55]\n"
"ds_read_b128 v[232:235], v221 offset:27648\n"
"s_add_u32 s63, s85, 24903680\n"
"v_mfma_f32_16x16x32_bf16 v[52:55], v[236:239], a[92:95], v[52:55]\n"
"s_waitcnt lgkmcnt(3)\n"
"v_exp_f32 v147, v147\n"
"v_mfma_f32_16x16x32_bf16 v[56:59], v[240:243], a[80:83], v[56:59]\n"
"ds_read_b128 v[236:239], v221 offset:35840\n"
"v_add_f32 v147, 1.0, v147\n"
"v_mfma_f32_16x16x32_bf16 v[56:59], v[244:247], a[88:91], v[56:59]\n"
"buffer_load_dwordx4 v2, s[48:51], s64 offen sc1 lds\n"
"v_mfma_f32_16x16x32_bf16 v[60:63], v[240:243], a[84:87], v[60:63]\n"
"ds_read_b128 v[240:243], v221 offset:29696\n"
"v_mfma_f32_16x16x32_bf16 v[60:63], v[244:247], a[92:95], v[60:63]\n"
"s_waitcnt lgkmcnt(3)\n"
"v_rcp_f32 v144, v144\n"
"v_mfma_f32_16x16x32_bf16 v[32:35], v[224:227], a[96:99], v[32:35]\n"
"ds_read_b128 v[244:247], v221 offset:37888\n"
"v_mul_f32 v92, v92, v144\n"
"v_mfma_f32_16x16x32_bf16 v[32:35], v[228:231], a[104:107], v[32:35]\n"
"buffer_load_dwordx4 v2, s[48:51], s64 offen offset:1024 sc1 lds\n"
"v_mfma_f32_16x16x32_bf16 v[36:39], v[224:227], a[100:103], v[36:39]\n"
"ds_read_b128 v[224:227], v221 offset:31744\n"
"s_add_u32 m0, s84, 18432\n"
"v_mfma_f32_16x16x32_bf16 v[36:39], v[228:231], a[108:111], v[36:39]\n"
"s_waitcnt lgkmcnt(3)\n"
"s_add_u32 s64, s80, 43925504\n"
"v_mfma_f32_16x16x32_bf16 v[40:43], v[232:235], a[96:99], v[40:43]\n"
"ds_read_b128 v[228:231], v221 offset:39936\n"
"v_mul_f32 v96, v96, v160\n"
"v_mfma_f32_16x16x32_bf16 v[40:43], v[236:239], a[104:107], v[40:43]\n"
"buffer_load_dwordx4 a[176:179], v2, s[48:51], s63 offen sc1\n"
"v_mfma_f32_16x16x32_bf16 v[44:47], v[232:235], a[100:103], v[44:47]\n"
"ds_read_b128 v[232:235], v221 offset:41984\n"
"v_mul_f32 v144, v96, v96\n"
"v_mfma_f32_16x16x32_bf16 v[44:47], v[236:239], a[108:111], v[44:47]\n"
"s_waitcnt lgkmcnt(3)\n"
"v_rcp_f32 v145, v145\n"
"v_mfma_f32_16x16x32_bf16 v[48:51], v[240:243], a[96:99], v[48:51]\n"
"ds_read_b128 v[236:239], v221 offset:50176\n"
"v_mul_f32 v93, v93, v145\n"
"v_mfma_f32_16x16x32_bf16 v[48:51], v[244:247], a[104:107], v[48:51]\n"
"buffer_load_dwordx4 a[180:183], v2, s[48:51], s63 offen offset:1024 sc1\n"
"v_mfma_f32_16x16x32_bf16 v[52:55], v[240:243], a[100:103], v[52:55]\n"
"ds_read_b128 v[240:243], v221 offset:44032\n"
"s_add_u32 s63, s85, 25034752\n"
"v_mfma_f32_16x16x32_bf16 v[52:55], v[244:247], a[108:111], v[52:55]\n"
"s_waitcnt lgkmcnt(3)\n"
"v_rcp_f32 v146, v146\n"
"v_mfma_f32_16x16x32_bf16 v[56:59], v[224:227], a[96:99], v[56:59]\n"
"ds_read_b128 v[244:247], v221 offset:52224\n"
"v_mul_f32 v94, v94, v146\n"
"v_mfma_f32_16x16x32_bf16 v[56:59], v[228:231], a[104:107], v[56:59]\n"
"buffer_load_dwordx4 v2, s[48:51], s64 offen sc1 lds\n"
"v_mfma_f32_16x16x32_bf16 v[60:63], v[224:227], a[100:103], v[60:63]\n"
"ds_read_b128 v[224:227], v221 offset:46080\n"
"v_mul_f32 v97, v97, v160\n"
"v_mfma_f32_16x16x32_bf16 v[60:63], v[228:231], a[108:111], v[60:63]\n"
"s_waitcnt vmcnt(21) lgkmcnt(3)\n"
"buffer_load_dwordx4 v2, s[48:51], s64 offen offset:1024 sc1 lds\n"
"v_mfma_f32_16x16x32_bf16 v[32:35], v[232:235], a[112:115], v[32:35]\n"
"ds_read_b128 v[228:231], v221 offset:54272\n"
"s_add_u32 m0, s84, 20480\n"
"v_mfma_f32_16x16x32_bf16 v[32:35], v[236:239], a[120:123], v[32:35]\n"
"s_add_u32 s64, s80, 44195840\n"
"v_mfma_f32_16x16x32_bf16 v[36:39], v[232:235], a[116:119], v[36:39]\n"
"ds_read_b128 v[232:235], v221 offset:48128\n"
"v_mul_f32 v98, v98, v160\n"
"v_mfma_f32_16x16x32_bf16 v[36:39], v[236:239], a[124:127], v[36:39]\n"
"s_waitcnt lgkmcnt(3)\n"
"buffer_load_dwordx4 a[184:187], v2, s[48:51], s63 offen sc1\n"
"v_mfma_f32_16x16x32_bf16 v[40:43], v[240:243], a[112:115], v[40:43]\n"
"ds_read_b128 v[236:239], v221 offset:56320\n"
"v_mul_f32 v145, v97, v97\n"
"v_mfma_f32_16x16x32_bf16 v[40:43], v[244:247], a[120:123], v[40:43]\n"
"buffer_load_dwordx4 a[188:191], v2, s[48:51], s63 offen offset:1024 sc1\n"
"v_mfma_f32_16x16x32_bf16 v[44:47], v[240:243], a[116:119], v[44:47]\n"
"s_waitcnt vmcnt(18)\n"
"s_barrier\n"
"ds_read_b128 v[240:243], v222\n"
"v_mfma_f32_16x16x32_bf16 v[44:47], v[244:247], a[124:127], v[44:47]\n"
"s_waitcnt lgkmcnt(3)\n"
"buffer_load_dwordx4 v2, s[48:51], s64 offen sc1 lds\n"
"v_mfma_f32_16x16x32_bf16 v[48:51], v[224:227], a[112:115], v[48:51]\n"
"ds_read_b128 v[244:247], v222 offset:8192\n"
"s_add_u32 s63, s85, 25165824\n"
"v_mfma_f32_16x16x32_bf16 v[48:51], v[228:231], a[120:123], v[48:51]\n"
"buffer_load_dwordx4 v2, s[48:51], s64 offen offset:1024 sc1 lds\n"
"v_mfma_f32_16x16x32_bf16 v[52:55], v[224:227], a[116:119], v[52:55]\n"
"ds_read_b128 v[224:227], v222 offset:2048\n"
"s_add_u32 m0, s84, 22528\n"
"v_mfma_f32_16x16x32_bf16 v[52:55], v[228:231], a[124:127], v[52:55]\n"
"s_waitcnt lgkmcnt(3)\n"
"s_add_u32 s64, s80, 44466176\n"
"v_mfma_f32_16x16x32_bf16 v[56:59], v[232:235], a[112:115], v[56:59]\n"
"ds_read_b128 v[228:231], v222 offset:10240\n"
"v_mul_f32 v146, v98, v98\n"
"v_mfma_f32_16x16x32_bf16 v[56:59], v[236:239], a[120:123], v[56:59]\n"
"buffer_load_dwordx4 v2, s[48:51], s64 offen sc1 lds\n"
"v_mfma_f32_16x16x32_bf16 v[60:63], v[232:235], a[116:119], v[60:63]\n"
"ds_read_b128 v[232:235], v222 offset:4096\n"
"v_fma_f32 v144, v144, v162, v163\n"
"v_mfma_f32_16x16x32_bf16 v[60:63], v[236:239], a[124:127], v[60:63]\n"
"s_waitcnt vmcnt(19) lgkmcnt(3)\n"
"buffer_load_dwordx4 a[192:195], v2, s[48:51], s63 offen sc1\n"
"v_mfma_f32_16x16x32_bf16 v[32:35], v[240:243], a[128:131], v[32:35]\n"
"ds_read_b128 v[236:239], v222 offset:12288\n"
"v_fma_f32 v145, v145, v162, v163\n"
"v_mfma_f32_16x16x32_bf16 v[32:35], v[244:247], a[136:139], v[32:35]\n"
"buffer_load_dwordx4 a[196:199], v2, s[48:51], s63 offen offset:1024 sc1\n"
"v_mfma_f32_16x16x32_bf16 v[36:39], v[240:243], a[132:135], v[36:39]\n"
"ds_read_b128 v[240:243], v222 offset:6144\n"
"s_add_u32 s63, s85, 25296896\n"
"v_mfma_f32_16x16x32_bf16 v[36:39], v[244:247], a[140:143], v[36:39]\n"
"s_waitcnt lgkmcnt(3)\n"
"buffer_load_dwordx4 v2, s[48:51], s64 offen offset:1024 sc1 lds\n"
"v_mfma_f32_16x16x32_bf16 v[40:43], v[224:227], a[128:131], v[40:43]\n"
"ds_read_b128 v[244:247], v222 offset:14336\n"
"s_add_u32 m0, s84, 49152\n"
"v_mfma_f32_16x16x32_bf16 v[40:43], v[228:231], a[136:139], v[40:43]\n"
"buffer_load_dwordx4 a[200:203], v2, s[48:51], s63 offen sc1\n"
"v_mfma_f32_16x16x32_bf16 v[44:47], v[224:227], a[132:135], v[44:47]\n"
"ds_read_b128 v[224:227], v222 offset:16384\n"
"s_add_u32 s64, s80, 39059456\n"
"v_mfma_f32_16x16x32_bf16 v[44:47], v[228:231], a[140:143], v[44:47]\n"
"s_waitcnt lgkmcnt(3)\n"
"buffer_load_dwordx4 a[204:207], v2, s[48:51], s63 offen offset:1024 sc1\n"
"v_mfma_f32_16x16x32_bf16 v[48:51], v[232:235], a[128:131], v[48:51]\n"
"ds_read_b128 v[228:231], v222 offset:24576\n"
"s_add_u32 s63, s85, 25427968\n"
"v_mfma_f32_16x16x32_bf16 v[48:51], v[236:239], a[136:139], v[48:51]\n"
"buffer_load_dwordx4 a[208:211], v2, s[48:51], s63 offen sc1\n"
"v_mfma_f32_16x16x32_bf16 v[52:55], v[232:235], a[132:135], v[52:55]\n"
"ds_read_b128 v[232:235], v222 offset:18432\n"
"v_fma_f32 v146, v146, v162, v163\n"
"v_mfma_f32_16x16x32_bf16 v[52:55], v[236:239], a[140:143], v[52:55]\n"
"s_waitcnt lgkmcnt(3)\n"
"buffer_load_dwordx4 a[212:215], v2, s[48:51], s63 offen offset:1024 sc1\n"
"v_mfma_f32_16x16x32_bf16 v[56:59], v[240:243], a[128:131], v[56:59]\n"
"ds_read_b128 v[236:239], v222 offset:26624\n"
"s_add_u32 s63, s85, 25559040\n"
"v_mfma_f32_16x16x32_bf16 v[56:59], v[244:247], a[136:139], v[56:59]\n"
"v_rcp_f32 v147, v147\n"
"v_mfma_f32_16x16x32_bf16 v[60:63], v[240:243], a[132:135], v[60:63]\n"
"ds_read_b128 v[240:243], v222 offset:20480\n"
"v_mul_f32 v95, v95, v147\n"
"v_mfma_f32_16x16x32_bf16 v[60:63], v[244:247], a[140:143], v[60:63]\n"
"s_waitcnt vmcnt(22) lgkmcnt(3)\n"
"buffer_load_dwordx4 a[216:219], v2, s[48:51], s63 offen sc1\n"
"v_mfma_f32_16x16x32_bf16 v[32:35], v[224:227], a[144:147], v[32:35]\n"
"ds_read_b128 v[244:247], v222 offset:28672\n"
"v_mul_f32 v99, v99, v160\n"
"v_mfma_f32_16x16x32_bf16 v[32:35], v[228:231], a[152:155], v[32:35]\n"
"buffer_load_dwordx4 a[220:223], v2, s[48:51], s63 offen offset:1024 sc1\n"
"v_mfma_f32_16x16x32_bf16 v[36:39], v[224:227], a[148:151], v[36:39]\n"
"ds_read_b128 v[224:227], v222 offset:22528\n"
"s_add_u32 s63, s85, 25690112\n"
"v_mfma_f32_16x16x32_bf16 v[36:39], v[228:231], a[156:159], v[36:39]\n"
"s_waitcnt lgkmcnt(3)\n"
"v_mul_f32 v147, v99, v99\n"
"v_fma_f32 v147, v147, v162, v163\n"
"v_mfma_f32_16x16x32_bf16 v[40:43], v[232:235], a[144:147], v[40:43]\n"
"ds_read_b128 v[228:231], v222 offset:30720\n"
"v_mul_f32 v144, v144, v96\n"
"v_mfma_f32_16x16x32_bf16 v[40:43], v[236:239], a[152:155], v[40:43]\n"
"buffer_load_dwordx4 a[224:227], v2, s[48:51], s63 offen sc1\n"
"v_mfma_f32_16x16x32_bf16 v[44:47], v[232:235], a[148:151], v[44:47]\n"
"ds_read_b128 v[232:235], v222 offset:1024\n"
"v_mul_f32 v145, v145, v97\n"
"v_mfma_f32_16x16x32_bf16 v[44:47], v[236:239], a[156:159], v[44:47]\n"
"s_waitcnt lgkmcnt(3)\n"
"v_exp_f32 v144, v144\n"
"v_mfma_f32_16x16x32_bf16 v[48:51], v[240:243], a[144:147], v[48:51]\n"
"ds_read_b128 v[236:239], v222 offset:9216\n"
"v_mul_f32 v146, v146, v98\n"
"v_mfma_f32_16x16x32_bf16 v[48:51], v[244:247], a[152:155], v[48:51]\n"
"buffer_load_dwordx4 a[228:231], v2, s[48:51], s63 offen offset:1024 sc1\n"
"v_mfma_f32_16x16x32_bf16 v[52:55], v[240:243], a[148:151], v[52:55]\n"
"ds_read_b128 v[240:243], v222 offset:3072\n"
"s_add_u32 s63, s85, 25821184\n"
"v_mfma_f32_16x16x32_bf16 v[52:55], v[244:247], a[156:159], v[52:55]\n"
"s_waitcnt lgkmcnt(3)\n"
"v_exp_f32 v145, v145\n"
"v_mfma_f32_16x16x32_bf16 v[56:59], v[224:227], a[144:147], v[56:59]\n"
"ds_read_b128 v[244:247], v222 offset:11264\n"
"v_mul_f32 v147, v147, v99\n"
"v_mfma_f32_16x16x32_bf16 v[56:59], v[228:231], a[152:155], v[56:59]\n"
"buffer_load_dwordx4 a[232:235], v2, s[48:51], s63 offen sc1\n"
"v_mfma_f32_16x16x32_bf16 v[60:63], v[224:227], a[148:151], v[60:63]\n"
"ds_read_b128 v[224:227], v222 offset:5120\n"
"v_add_f32 v144, 1.0, v144\n"
"v_mfma_f32_16x16x32_bf16 v[60:63], v[228:231], a[156:159], v[60:63]\n"
"s_waitcnt vmcnt(23) lgkmcnt(3)\n"
"v_exp_f32 v146, v146\n"
"v_mfma_f32_16x16x32_bf16 v[32:35], v[232:235], a[160:163], v[32:35]\n"
"ds_read_b128 v[228:231], v222 offset:13312\n"
"v_add_f32 v145, 1.0, v145\n"
"v_mfma_f32_16x16x32_bf16 v[32:35], v[236:239], a[168:171], v[32:35]\n"
"buffer_load_dwordx4 a[236:239], v2, s[48:51], s63 offen offset:1024 sc1\n"
"v_mfma_f32_16x16x32_bf16 v[36:39], v[232:235], a[164:167], v[36:39]\n"
"ds_read_b128 v[232:235], v222 offset:7168\n"
"s_add_u32 s63, s85, 25952256\n"
"v_mfma_f32_16x16x32_bf16 v[36:39], v[236:239], a[172:175], v[36:39]\n"
"s_waitcnt lgkmcnt(3)\n"
"v_exp_f32 v147, v147\n"
"v_mfma_f32_16x16x32_bf16 v[40:43], v[240:243], a[160:163], v[40:43]\n"
"ds_read_b128 v[236:239], v222 offset:15360\n"
"v_add_f32 v146, 1.0, v146\n"
"v_mfma_f32_16x16x32_bf16 v[40:43], v[244:247], a[168:171], v[40:43]\n"
"buffer_load_dwordx4 v2, s[48:51], s64 offen sc1 lds\n"
"v_mfma_f32_16x16x32_bf16 v[44:47], v[240:243], a[164:167], v[44:47]\n"
"ds_read_b128 v[240:243], v222 offset:17408\n"
"v_add_f32 v147, 1.0, v147\n"
"v_mfma_f32_16x16x32_bf16 v[44:47], v[244:247], a[172:175], v[44:47]\n"
"s_waitcnt lgkmcnt(3)\n"
"v_rcp_f32 v144, v144\n"
"v_mfma_f32_16x16x32_bf16 v[48:51], v[224:227], a[160:163], v[48:51]\n"
"ds_read_b128 v[244:247], v222 offset:25600\n"
"v_mul_f32 v96, v96, v144\n"
"v_mfma_f32_16x16x32_bf16 v[48:51], v[228:231], a[168:171], v[48:51]\n"
"buffer_load_dwordx4 v2, s[48:51], s64 offen offset:1024 sc1 lds\n"
"v_mfma_f32_16x16x32_bf16 v[52:55], v[224:227], a[164:167], v[52:55]\n"
"ds_read_b128 v[224:227], v222 offset:19456\n"
"s_add_u32 m0, s84, 51200\n"
"v_mfma_f32_16x16x32_bf16 v[52:55], v[228:231], a[172:175], v[52:55]\n"
"s_waitcnt lgkmcnt(3)\n"
"buffer_load_dwordx4 a[240:243], v2, s[48:51], s63 offen sc1\n"
"v_mfma_f32_16x16x32_bf16 v[56:59], v[232:235], a[160:163], v[56:59]\n"
"ds_read_b128 v[228:231], v222 offset:27648\n"
"s_add_u32 s64, s80, 40951808\n"
"v_mfma_f32_16x16x32_bf16 v[56:59], v[236:239], a[168:171], v[56:59]\n"
"buffer_load_dwordx4 a[244:247], v2, s[48:51], s63 offen offset:1024 sc1\n"
"v_mfma_f32_16x16x32_bf16 v[60:63], v[232:235], a[164:167], v[60:63]\n"
"ds_read_b128 v[232:235], v222 offset:21504\n"
"s_add_u32 s63, s85, 26083328\n"
"v_mfma_f32_16x16x32_bf16 v[60:63], v[236:239], a[172:175], v[60:63]\n"
"s_waitcnt vmcnt(20) lgkmcnt(3)\n"
"buffer_load_dwordx4 v2, s[48:51], s64 offen sc1 lds\n"
"v_mfma_f32_16x16x32_bf16 v[32:35], v[240:243], a[176:179], v[32:35]\n"
"ds_read_b128 v[236:239], v222 offset:29696\n"
"v_mul_f32 v100, v100, v161\n"
"v_mfma_f32_16x16x32_bf16 v[32:35], v[244:247], a[184:187], v[32:35]\n"
"buffer_load_dwordx4 v2, s[48:51], s64 offen offset:1024 sc1 lds\n"
"v_mfma_f32_16x16x32_bf16 v[36:39], v[240:243], a[180:183], v[36:39]\n"
"ds_read_b128 v[240:243], v222 offset:23552\n"
"s_add_u32 m0, s84, 53248\n"
"v_mfma_f32_16x16x32_bf16 v[36:39], v[244:247], a[188:191], v[36:39]\n"
"s_waitcnt lgkmcnt(3)\n"
"s_add_u32 s64, s80, 39329792\n"
"v_mfma_f32_16x16x32_bf16 v[40:43], v[224:227], a[176:179], v[40:43]\n"
"ds_read_b128 v[244:247], v222 offset:31744\n"
"v_mul_f32 v144, v100, v100\n"
"v_mfma_f32_16x16x32_bf16 v[40:43], v[228:231], a[184:187], v[40:43]\n"
"buffer_load_dwordx4 a[248:251], v2, s[48:51], s63 offen sc1\n"
"v_mfma_f32_16x16x32_bf16 v[44:47], v[224:227], a[180:183], v[44:47]\n"
"s_waitcnt vmcnt(17)\n"
"s_barrier\n"
"ds_read_b128 v[224:227], v2 offset:16384\n"
"v_mfma_f32_16x16x32_bf16 v[44:47], v[228:231], a[188:191], v[44:47]\n"
"s_waitcnt lgkmcnt(3)\n"
"buffer_load_dwordx4 a[252:255], v2, s[48:51], s63 offen offset:1024 sc1\n"
"v_mfma_f32_16x16x32_bf16 v[48:51], v[232:235], a[176:179], v[48:51]\n"
"ds_read_b128 v[228:231], v2 offset:24576\n"
"v_fma_f32 v144, v144, v162, v163\n"
"v_mfma_f32_16x16x32_bf16 v[48:51], v[236:239], a[184:187], v[48:51]\n"
"buffer_load_dwordx4 v2, s[48:51], s64 offen sc1 lds\n"
"v_mfma_f32_16x16x32_bf16 v[52:55], v[232:235], a[180:183], v[52:55]\n"
"ds_read_b128 v[232:235], v2 offset:18432\n"
"v_mul_f32 v144, v144, v100\n"
"v_mfma_f32_16x16x32_bf16 v[52:55], v[236:239], a[188:191], v[52:55]\n"
"s_waitcnt lgkmcnt(3)\n"
"buffer_load_dwordx4 v2, s[48:51], s64 offen offset:1024 sc1 lds\n"
"v_mfma_f32_16x16x32_bf16 v[56:59], v[240:243], a[176:179], v[56:59]\n"
"ds_read_b128 v[236:239], v2 offset:26624\n"
"s_add_u32 m0, s84, 55296\n"
"v_mfma_f32_16x16x32_bf16 v[56:59], v[244:247], a[184:187], v[56:59]\n"
"s_add_u32 s64, s80, 41222144\n"
"v_mfma_f32_16x16x32_bf16 v[60:63], v[240:243], a[180:183], v[60:63]\n"
"ds_read_b128 v[240:243], v2 offset:20480\n"
"v_mfma_f32_16x16x32_bf16 v[60:63], v[244:247], a[188:191], v[60:63]\n"
"s_waitcnt vmcnt(18) lgkmcnt(3)\n"
"buffer_load_dwordx4 v2, s[48:51], s64 offen sc1 lds\n"
"v_mfma_f32_16x16x32_bf16 v[32:35], v[224:227], a[192:195], v[32:35]\n"
"ds_read_b128 v[244:247], v2 offset:28672\n"
"v_mfma_f32_16x16x32_bf16 v[32:35], v[228:231], a[200:203], v[32:35]\n"
"buffer_load_dwordx4 v2, s[48:51], s64 offen offset:1024 sc1 lds\n"
"v_mfma_f32_16x16x32_bf16 v[36:39], v[224:227], a[196:199], v[36:39]\n"
"ds_read_b128 v[224:227], v2 offset:22528\n"
"s_add_u32 m0, s55, 147456\n"
"v_mfma_f32_16x16x32_bf16 v[36:39], v[228:231], a[204:207], v[36:39]\n"
"s_waitcnt lgkmcnt(3)\n"
"s_add_u32 s64, s89, 0\n"
"LZ5_%=:\n"
"v_add_u32 v11, LZ6_%=-LPC_%=, v0\n"
"buffer_load_dwordx4 v9, s[40:43], s64 offen lds\n"
"v_min_u32 v11, LZ8_%=-LPC_%=-4, v11\n"
"s_add_u32 m0, s55, 151552\n"
"s_add_u32 s64, s89, 262144\n"
"global_load_dword v11, v11, s[70:71]\n"
"v_mfma_f32_16x16x32_bf16 v[40:43], v[232:235], a[192:195], v[40:43]\n"
"ds_read_b128 v[228:231], v2 offset:30720\n"
"v_mfma_f32_16x16x32_bf16 v[40:43], v[236:239], a[200:203], v[40:43]\n"
"buffer_load_dwordx4 v9, s[40:43], s64 offen lds\n"
"v_mfma_f32_16x16x32_bf16 v[44:47], v[232:235], a[196:199], v[44:47]\n"
"ds_read_b128 v[232:235], v2 offset:32768\n"
"s_add_u32 m0, s55, 155648\n"
"v_mfma_f32_16x16x32_bf16 v[44:47], v[236:239], a[204:207], v[44:47]\n"
"s_waitcnt lgkmcnt(3)\n"
"s_add_u32 s64, s89, 458752\n"
"v_mfma_f32_16x16x32_bf16 v[48:51], v[240:243], a[192:195], v[48:51]\n"
"ds_read_b128 v[236:239], v2 offset:40960\n"
"v_mfma_f32_16x16x32_bf16 v[48:51], v[244:247], a[200:203], v[48:51]\n"
"buffer_load_dwordx4 v9, s[40:43], s64 offen lds\n"
"v_mfma_f32_16x16x32_bf16 v[52:55], v[240:243], a[196:199], v[52:55]\n"
"ds_read_b128 v[240:243], v2 offset:34816\n"
"s_add_u32 m0, s55, 159744\n"
"v_mfma_f32_16x16x32_bf16 v[52:55], v[244:247], a[204:207], v[52:55]\n"
"s_waitcnt lgkmcnt(3)\n"
"s_add_u32 s64, s89, 720896\n"
"v_mfma_f32_16x16x32_bf16 v[56:59], v[224:227], a[192:195], v[56:59]\n"
"ds_read_b128 v[244:247], v2 offset:43008\n"
"v_mfma_f32_16x16x32_bf16 v[56:59], v[228:231], a[200:203], v[56:59]\n"
"buffer_load_dwordx4 v9, s[40:43], s64 offen lds\n"
"v_mfma_f32_16x16x32_bf16 v[60:63], v[224:227], a[196:199], v[60:63]\n"
"ds_read_b128 v[224:227], v2 offset:36864\n"
"s_add_u32 m0, s84, 81920\n"
"v_mfma_f32_16x16x32_bf16 v[60:63], v[228:231], a[204:207], v[60:63]\n"
"s_waitcnt vmcnt(21) lgkmcnt(3)\n"
"s_add_u32 s64, s80, 42844160\n"
"v_mfma_f32_16x16x32_bf16 v[32:35], v[232:235], a[208:211], v[32:35]\n"
"ds_read_b128 v[228:231], v2 offset:45056\n"
"v_mfma_f32_16x16x32_bf16 v[32:35], v[236:239], a[216:219], v[32:35]\n"
"v_rcp_f32 v145, v145\n"
"v_mfma_f32_16x16x32_bf16 v[36:39], v[232:235], a[212:215], v[36:39]\n"
"ds_read_b128 v[232:235], v2 offset:38912\n"
"v_mul_f32 v97, v97, v145\n"
"v_mfma_f32_16x16x32_bf16 v[36:39], v[236:239], a[220:223], v[36:39]\n"
"s_waitcnt lgkmcnt(3)\n"
"v_rcp_f32 v146, v146\n"
"v_mfma_f32_16x16x32_bf16 v[40:43], v[240:243], a[208:211], v[40:43]\n"
"ds_read_b128 v[236:239], v2 offset:47104\n"
"v_mul_f32 v98, v98, v146\n"
"v_mfma_f32_16x16x32_bf16 v[40:43], v[244:247], a[216:219], v[40:43]\n"
"v_rcp_f32 v147, v147\n"
"v_mfma_f32_16x16x32_bf16 v[44:47], v[240:243], a[212:215], v[44:47]\n"
"ds_read_b128 v[240:243], v2 offset:17408\n"
"v_mul_f32 v99, v99, v147\n"
"v_mfma_f32_16x16x32_bf16 v[44:47], v[244:247], a[220:223], v[44:47]\n"
"s_waitcnt lgkmcnt(3)\n"
"v_exp_f32 v144, v144\n"
"v_mfma_f32_16x16x32_bf16 v[48:51], v[224:227], a[208:211], v[48:51]\n"
"ds_read_b128 v[244:247], v2 offset:25600\n"
"v_mul_f32 v101, v101, v161\n"
"v_mfma_f32_16x16x32_bf16 v[48:51], v[228:231], a[216:219], v[48:51]\n"
"v_mul_f32 v102, v102, v161\n"
"v_mul_f32 v103, v103, v161\n"
"v_mfma_f32_16x16x32_bf16 v[52:55], v[224:227], a[212:215], v[52:55]\n"
"ds_read_b128 v[224:227], v2 offset:19456\n"
"v_mul_f32 v145, v101, v101\n"
"v_mfma_f32_16x16x32_bf16 v[52:55], v[228:231], a[220:223], v[52:55]\n"
"s_waitcnt lgkmcnt(3)\n"
"buffer_load_dwordx4 v2, s[48:51], s64 offen sc1 lds\n"
"v_mfma_f32_16x16x32_bf16 v[56:59], v[232:235], a[208:211], v[56:59]\n"
"ds_read_b128 v[228:231], v2 offset:27648\n"
"v_mul_f32 v146, v102, v102\n"
"v_mfma_f32_16x16x32_bf16 v[56:59], v[236:239], a[216:219], v[56:59]\n"
"v_mul_f32 v147, v103, v103\n"
"v_fma_f32 v145, v145, v162, v163\n"
"v_mfma_f32_16x16x32_bf16 v[60:63], v[232:235], a[212:215], v[60:63]\n"
"ds_read_b128 v[232:235], v2 offset:21504\n"
"v_fma_f32 v146, v146, v162, v163\n"
"v_mfma_f32_16x16x32_bf16 v[60:63], v[236:239], a[220:223], v[60:63]\n"
"s_waitcnt vmcnt(18) lgkmcnt(3)\n"
"buffer_load_dwordx4 v2, s[48:51], s64 offen offset:1024 sc1 lds\n"
"v_mfma_f32_16x16x32_bf16 v[32:35], v[240:243], a[224:227], v[32:35]\n"
"ds_read_b128 v[236:239], v2 offset:29696\n"
"s_add_u32 m0, s84, 83968\n"
"v_mfma_f32_16x16x32_bf16 v[32:35], v[244:247], a[232:235], v[32:35]\n"
"s_add_u32 s64, s80, 44736512\n"
"v_mfma_f32_16x16x32_bf16 v[36:39], v[240:243], a[228:231], v[36:39]\n"
"ds_read_b128 v[240:243], v2 offset:23552\n"
"v_fma_f32 v147, v147, v162, v163\n"
"v_mfma_f32_16x16x32_bf16 v[36:39], v[244:247], a[236:239], v[36:39]\n"
"s_waitcnt lgkmcnt(3)\n"
"buffer_load_dwordx4 v2, s[48:51], s64 offen sc1 lds\n"
"v_mfma_f32_16x16x32_bf16 v[40:43], v[224:227], a[224:227], v[40:43]\n"
"ds_read_b128 v[244:247], v2 offset:31744\n"
"v_mul_f32 v145, v145, v101\n"
"v_mfma_f32_16x16x32_bf16 v[40:43], v[228:231], a[232:235], v[40:43]\n"
"v_exp_f32 v145, v145\n"
"v_mfma_f32_16x16x32_bf16 v[44:47], v[224:227], a[228:231], v[44:47]\n"
"ds_read_b128 v[224:227], v2 offset:33792\n"
"v_mul_f32 v146, v146, v102\n"
"v_mfma_f32_16x16x32_bf16 v[44:47], v[228:231], a[236:239], v[44:47]\n"
"s_waitcnt lgkmcnt(3)\n"
"buffer_load_dwordx4 v2, s[48:51], s64 offen offset:1024 sc1 lds\n"
"v_mfma_f32_16x16x32_bf16 v[48:51], v[232:235], a[224:227], v[48:51]\n"
"ds_read_b128 v[228:231], v2 offset:41984\n"
"s_add_u32 m0, s84, 86016\n"
"v_mfma_f32_16x16x32_bf16 v[48:51], v[236:239], a[232:235], v[48:51]\n"
"s_add_u32 s64, s80, 43114496\n"
"v_mfma_f32_16x16x32_bf16 v[52:55], v[232:235], a[228:231], v[52:55]\n"
"ds_read_b128 v[232:235], v2 offset:35840\n"
"v_mul_f32 v147, v147, v103\n"
"v_mfma_f32_16x16x32_bf16 v[52:55], v[236:239], a[236:239], v[52:55]\n"
"s_waitcnt lgkmcnt(3)\n"
"buffer_load_dwordx4 v2, s[48:51], s64 offen sc1 lds\n"
"v_mfma_f32_16x16x32_bf16 v[56:59], v[240:243], a[224:227], v[56:59]\n"
"ds_read_b128 v[236:239], v2 offset:44032\n"
"v_add_f32 v144, 1.0, v144\n"
"v_mfma_f32_16x16x32_bf16 v[56:59], v[244:247], a[232:235], v[56:59]\n"
"v_exp_f32 v146, v146\n"
"v_mfma_f32_16x16x32_bf16 v[60:63], v[240:243], a[228:231], v[60:63]\n"
"ds_read_b128 v[240:243], v2 offset:37888\n"
"v_add_f32 v145, 1.0, v145\n"
"v_mfma_f32_16x16x32_bf16 v[60:63], v[244:247], a[236:239], v[60:63]\n"
"s_waitcnt vmcnt(14) lgkmcnt(3)\n"
"buffer_load_dwordx4 v2, s[48:51], s64 offen offset:1024 sc1 lds\n"
"v_mfma_f32_16x16x32_bf16 v[32:35], v[224:227], a[240:243], v[32:35]\n"
"ds_read_b128 v[244:247], v2 offset:46080\n"
"s_add_u32 m0, s84, 88064\n"
"v_mfma_f32_16x16x32_bf16 v[32:35], v[228:231], a[248:251], v[32:35]\n"
"s_add_u32 s64, s80, 45006848\n"
"v_mfma_f32_16x16x32_bf16 v[36:39], v[224:227], a[244:247], v[36:39]\n"
"ds_read_b128 v[224:227], v2 offset:39936\n"
"v_add_f32 v146, 1.0, v146\n"
"v_mfma_f32_16x16x32_bf16 v[36:39], v[228:231], a[252:255], v[36:39]\n"
"s_waitcnt lgkmcnt(3)\n"
"buffer_load_dwordx4 v2, s[48:51], s64 offen sc1 lds\n"
"v_mfma_f32_16x16x32_bf16 v[40:43], v[232:235], a[240:243], v[40:43]\n"
"ds_read_b128 v[228:231], v2 offset:48128\n"
"v_mfma_f32_16x16x32_bf16 v[40:43], v[236:239], a[248:251], v[40:43]\n"
"v_exp_f32 v147, v147\n"
"v_mfma_f32_16x16x32_bf16 v[44:47], v[232:235], a[244:247], v[44:47]\n"
"s_waitcnt vmcnt(12)\n"
"s_barrier\n"
"ds_read_b128 v[232:235], v2 offset:49152\n"
"v_mfma_f32_16x16x32_bf16 v[44:47], v[236:239], a[252:255], v[44:47]\n"
"s_waitcnt lgkmcnt(3)\n"
"buffer_load_dwordx4 v2, s[48:51], s64 offen offset:1024 sc1 lds\n"
"v_mfma_f32_16x16x32_bf16 v[48:51], v[240:243], a[240:243], v[48:51]\n"
"ds_read_b128 v[236:239], v221\n"
"s_add_u32 m0, s84, 114688\n"
"v_mfma_f32_16x16x32_bf16 v[48:51], v[244:247], a[248:251], v[48:51]\n"
"s_add_u32 s64, s80, 39600128\n"
"v_mfma_f32_16x16x32_bf16 v[52:55], v[240:243], a[244:247], v[52:55]\n"
"ds_read_b128 v[240:243], v2 offset:53248\n"
"v_add_f32 v147, 1.0, v147\n"
"v_mfma_f32_16x16x32_bf16 v[52:55], v[244:247], a[252:255], v[52:55]\n"
"s_waitcnt lgkmcnt(3)\n"
"v_rcp_f32 v144, v144\n"
"v_mfma_f32_16x16x32_bf16 v[56:59], v[224:227], a[240:243], v[56:59]\n"
"ds_read_b128 v[244:247], v221 offset:4096\n"
"v_mul_f32 v100, v100, v144\n"
"v_mfma_f32_16x16x32_bf16 v[56:59], v[228:231], a[248:251], v[56:59]\n"
"v_rcp_f32 v145, v145\n"
"v_mfma_f32_16x16x32_bf16 v[60:63], v[224:227], a[244:247], v[60:63]\n"
"ds_read_b128 v[224:227], v221 offset:8192\n"
"v_mul_f32 v101, v101, v145\n"
"v_mfma_f32_16x16x32_bf16 v[60:63], v[228:231], a[252:255], v[60:63]\n"
"s_waitcnt lgkmcnt(3)\n"
"v_rcp_f32 v146, v146\n"
"v_mfma_f32_16x16x32_bf16 v[64:67], v[232:235], a[0:3], 0\n"
"ds_read_b128 v[228:231], v221 offset:16384\n"
"v_mul_f32 v102, v102, v146\n"
"v_mfma_f32_16x16x32_bf16 v[64:67], v[236:239], a[8:11], v[64:67]\n"
"v_rcp_f32 v147, v147\n"
"v_mfma_f32_16x16x32_bf16 v[68:71], v[232:235], a[4:7], 0\n"
"ds_read_b128 v[232:235], v221 offset:12288\n"
"v_mul_f32 v103, v103, v147\n"
"v_mfma_f32_16x16x32_bf16 v[68:71], v[236:239], a[12:15], v[68:71]\n"
"s_waitcnt lgkmcnt(3)\n"
"v_mul_f32 v104, v104, v160\n"
"v_mul_f32 v105, v105, v160\n"
"v_mfma_f32_16x16x32_bf16 v[72:75], v[240:243], a[0:3], 0\n"
"ds_read_b128 v[236:239], v221 offset:20480\n"
"v_cvt_pk_bf16_f32 v32, v32, v33\n"
"v_mfma_f32_16x16x32_bf16 v[72:75], v[244:247], a[8:11], v[72:75]\n"
"v_cvt_pk_bf16_f32 v33, v34, v35\n"
"v_cvt_pk_bf16_f32 v34, v36, v37\n"
"v_mfma_f32_16x16x32_bf16 v[76:79], v[240:243], a[4:7], 0\n"
"ds_read_b128 v[240:243], v2 offset:50176\n"
"v_cvt_pk_bf16_f32 v35, v38, v39\n"
"v_mfma_f32_16x16x32_bf16 v[76:79], v[244:247], a[12:15], v[76:79]\n"
"s_waitcnt lgkmcnt(3)\n"
"v_mul_f32 v106, v106, v160\n"
"v_mul_f32 v107, v107, v160\n"
"v_mfma_f32_16x16x32_bf16 v[64:67], v[224:227], a[16:19], v[64:67]\n"
"ds_read_b128 v[244:247], v221 offset:1024\n"
"v_mul_f32 v144, v104, v104\n"
"v_mfma_f32_16x16x32_bf16 v[64:67], v[228:231], a[24:27], v[64:67]\n"
"v_mul_f32 v145, v105, v105\n"
"v_mul_f32 v146, v106, v106\n"
"v_mfma_f32_16x16x32_bf16 v[68:71], v[224:227], a[20:23], v[68:71]\n"
"ds_read_b128 v[224:227], v2 offset:54272\n"
"s_add_u32 s61, s30, 45547520\n"
"v_mfma_f32_16x16x32_bf16 v[68:71], v[228:231], a[28:31], v[68:71]\n"
"s_waitcnt lgkmcnt(3)\n"
"buffer_store_dwordx4 v[32:35], v2, s[48:51], s61 offen sc1\n"
"v_mfma_f32_16x16x32_bf16 v[72:75], v[232:235], a[16:19], v[72:75]\n"
"ds_read_b128 v[228:231], v221 offset:5120\n"
"v_mul_f32 v147, v107, v107\n"
"v_mfma_f32_16x16x32_bf16 v[72:75], v[236:239], a[24:27], v[72:75]\n"
"v_fma_f32 v144, v144, v162, v163\n"
"v_fma_f32 v145, v145, v162, v163\n"
"v_mfma_f32_16x16x32_bf16 v[76:79], v[232:235], a[20:23], v[76:79]\n"
"ds_read_b128 v[232:235], v221 offset:9216\n"
"v_fma_f32 v146, v146, v162, v163\n"
"v_mfma_f32_16x16x32_bf16 v[76:79], v[236:239], a[28:31], v[76:79]\n"
"s_waitcnt lgkmcnt(3)\n"
"v_fma_f32 v147, v147, v162, v163\n"
"v_mul_f32 v144, v144, v104\n"
"v_mfma_f32_16x16x32_bf16 v[64:67], v[240:243], a[32:35], v[64:67]\n"
"ds_read_b128 v[236:239], v221 offset:17408\n"
"v_mul_f32 v145, v145, v105\n"
"v_mfma_f32_16x16x32_bf16 v[64:67], v[244:247], a[40:43], v[64:67]\n"
"v_exp_f32 v144, v144\n"
"v_mfma_f32_16x16x32_bf16 v[68:71], v[240:243], a[36:39], v[68:71]\n"
"ds_read_b128 v[240:243], v221 offset:13312\n"
"v_mul_f32 v146, v146, v106\n"
"v_mfma_f32_16x16x32_bf16 v[68:71], v[244:247], a[44:47], v[68:71]\n"
"s_waitcnt lgkmcnt(3)\n"
"v_exp_f32 v145, v145\n"
"v_mfma_f32_16x16x32_bf16 v[72:75], v[224:227], a[32:35], v[72:75]\n"
"ds_read_b128 v[244:247], v221 offset:21504\n"
"v_mul_f32 v147, v147, v107\n"
"v_mfma_f32_16x16x32_bf16 v[72:75], v[228:231], a[40:43], v[72:75]\n"
"v_exp_f32 v146, v146\n"
"v_mfma_f32_16x16x32_bf16 v[76:79], v[224:227], a[36:39], v[76:79]\n"
"ds_read_b128 v[224:227], v2 offset:51200\n"
"v_add_f32 v144, 1.0, v144\n"
"v_mfma_f32_16x16x32_bf16 v[76:79], v[228:231], a[44:47], v[76:79]\n"
"s_waitcnt lgkmcnt(3)\n"
"v_exp_f32 v147, v147\n"
"v_mfma_f32_16x16x32_bf16 v[64:67], v[232:235], a[48:51], v[64:67]\n"
"ds_read_b128 v[228:231], v221 offset:2048\n"
"v_add_f32 v145, 1.0, v145\n"
"LPG16_%=:\n"
"s_cbranch_execz LPG20_%=\n"
"v_mfma_f32_16x16x32_bf16 v[64:67], v[236:239], a[56:59], v[64:67]\n"
"v_rcp_f32 v144, v144\n"
"v_mfma_f32_16x16x32_bf16 v[68:71], v[232:235], a[52:55], v[68:71]\n"
"ds_read_b128 v[232:235], v2 offset:55296\n"
"v_add_f32 v146, 1.0, v146\n"
"v_mfma_f32_16x16x32_bf16 v[68:71], v[236:239], a[60:63], v[68:71]\n"
"s_waitcnt lgkmcnt(3)\n"
"buffer_load_dwordx4 v2, s[48:51], s64 offen sc1 lds\n"
"v_mfma_f32_16x16x32_bf16 v[72:75], v[240:243], a[48:51], v[72:75]\n"
"ds_read_b128 v[236:239], v221 offset:6144\n"
"v_add_f32 v147, 1.0, v147\n"
"v_mfma_f32_16x16x32_bf16 v[72:75], v[244:247], a[56:59], v[72:75]\n"
"v_rcp_f32 v145, v145\n"
"v_mfma_f32_16x16x32_bf16 v[76:79], v[240:243], a[52:55], v[76:79]\n"
"ds_read_b128 v[240:243], v221 offset:10240\n"
"v_mul_f32 v104, v104, v144\n"
"v_mfma_f32_16x16x32_bf16 v[76:79], v[244:247], a[60:63], v[76:79]\n"
"s_waitcnt lgkmcnt(3)\n"
"buffer_load_dwordx4 v2, s[48:51], s64 offen offset:1024 sc1 lds\n"
"v_mfma_f32_16x16x32_bf16 v[64:67], v[224:227], a[64:67], v[64:67]\n"
"ds_read_b128 v[244:247], v221 offset:18432\n"
"s_add_u32 m0, s84, 116736\n"
"v_mfma_f32_16x16x32_bf16 v[64:67], v[228:231], a[72:75], v[64:67]\n"
"s_add_u32 s64, s80, 41492480\n"
"v_mfma_f32_16x16x32_bf16 v[68:71], v[224:227], a[68:71], v[68:71]\n"
"ds_read_b128 v[224:227], v221 offset:14336\n"
"v_cvt_pk_bf16_f32 v40, v40, v41\n"
"v_mfma_f32_16x16x32_bf16 v[68:71], v[228:231], a[76:79], v[68:71]\n"
"s_waitcnt lgkmcnt(3)\n"
"buffer_load_dwordx4 v2, s[48:51], s64 offen sc1 lds\n"
"v_cvt_pk_bf16_f32 v41, v42, v43\n"
"v_cvt_pk_bf16_f32 v42, v44, v45\n"
"v_cvt_pk_bf16_f32 v43, v46, v47\n"
"v_mfma_f32_16x16x32_bf16 v[72:75], v[232:235], a[64:67], v[72:75]\n"
"ds_read_b128 v[228:231], v221 offset:22528\n"
"v_mul_f32 v105, v105, v145\n"
"v_mfma_f32_16x16x32_bf16 v[72:75], v[236:239], a[72:75], v[72:75]\n"
"v_rcp_f32 v146, v146\n"
"v_mfma_f32_16x16x32_bf16 v[76:79], v[232:235], a[68:71], v[76:79]\n"
"ds_read_b128 v[232:235], v2 offset:52224\n"
"s_add_u32 s61, s30, 45548544\n"
"v_mfma_f32_16x16x32_bf16 v[76:79], v[236:239], a[76:79], v[76:79]\n"
"s_waitcnt lgkmcnt(3)\n"
"buffer_load_dwordx4 v2, s[48:51], s64 offen offset:1024 sc1 lds\n"
"v_mfma_f32_16x16x32_bf16 v[64:67], v[240:243], a[80:83], v[64:67]\n"
"ds_read_b128 v[236:239], v221 offset:3072\n"
"s_add_u32 m0, s84, 118784\n"
"v_mfma_f32_16x16x32_bf16 v[64:67], v[244:247], a[88:91], v[64:67]\n"
"s_add_u32 s64, s80, 43384832\n"
"v_mfma_f32_16x16x32_bf16 v[68:71], v[240:243], a[84:87], v[68:71]\n"
"ds_read_b128 v[240:243], v2 offset:56320\n"
"v_mul_f32 v106, v106, v146\n"
"v_mfma_f32_16x16x32_bf16 v[68:71], v[244:247], a[92:95], v[68:71]\n"
"s_waitcnt lgkmcnt(3)\n"
"buffer_load_dwordx4 v2, s[48:51], s64 offen sc1 lds\n"
"v_mfma_f32_16x16x32_bf16 v[72:75], v[224:227], a[80:83], v[72:75]\n"
"ds_read_b128 v[244:247], v221 offset:7168\n"
"v_mul_f32 v108, v108, v161\n"
"v_mfma_f32_16x16x32_bf16 v[72:75], v[228:231], a[88:91], v[72:75]\n"
"v_rcp_f32 v147, v147\n"
"v_mfma_f32_16x16x32_bf16 v[76:79], v[224:227], a[84:87], v[76:79]\n"
"ds_read_b128 v[224:227], v221 offset:11264\n"
"v_mul_f32 v107, v107, v147\n"
"v_mfma_f32_16x16x32_bf16 v[76:79], v[228:231], a[92:95], v[76:79]\n"
"s_waitcnt lgkmcnt(3)\n"
"buffer_store_dwordx4 v[40:43], v2, s[48:51], s61 offen sc1\n"
"v_mfma_f32_16x16x32_bf16 v[64:67], v[232:235], a[96:99], v[64:67]\n"
"ds_read_b128 v[228:231], v221 offset:19456\n"
"v_mul_f32 v109, v109, v161\n"
"v_mfma_f32_16x16x32_bf16 v[64:67], v[236:239], a[104:107], v[64:67]\n"
"v_mul_f32 v110, v110, v161\n"
"v_mul_f32 v111, v111, v161\n"
"v_mfma_f32_16x16x32_bf16 v[68:71], v[232:235], a[100:103], v[68:71]\n"
"ds_read_b128 v[232:235], v221 offset:15360\n"
"v_mul_f32 v144, v108, v108\n"
"v_mfma_f32_16x16x32_bf16 v[68:71], v[236:239], a[108:111], v[68:71]\n"
"s_waitcnt lgkmcnt(3)\n"
"buffer_load_dwordx4 v2, s[48:51], s64 offen offset:1024 sc1 lds\n"
"v_mfma_f32_16x16x32_bf16 v[72:75], v[240:243], a[96:99], v[72:75]\n"
"ds_read_b128 v[236:239], v221 offset:23552\n"
"s_add_u32 m0, s84, 120832\n"
"v_mfma_f32_16x16x32_bf16 v[72:75], v[244:247], a[104:107], v[72:75]\n"
"s_add_u32 s64, s80, 45277184\n"
"v_mfma_f32_16x16x32_bf16 v[76:79], v[240:243], a[100:103], v[76:79]\n"
"s_waitcnt vmcnt(8)\n"
"s_barrier\n"
"ds_read_b128 v[240:243], v221 offset:24576\n"
"v_mfma_f32_16x16x32_bf16 v[76:79], v[244:247], a[108:111], v[76:79]\n"
"s_waitcnt lgkmcnt(3)\n"
"buffer_load_dwordx4 v2, s[48:51], s64 offen sc1 lds\n"
"v_mfma_f32_16x16x32_bf16 v[64:67], v[224:227], a[112:115], v[64:67]\n"
"ds_read_b128 v[244:247], v221 offset:32768\n"
"v_mul_f32 v145, v109, v109\n"
"v_mfma_f32_16x16x32_bf16 v[64:67], v[228:231], a[120:123], v[64:67]\n"
"v_mul_f32 v146, v110, v110\n"
"v_mul_f32 v147, v111, v111\n"
"v_mfma_f32_16x16x32_bf16 v[68:71], v[224:227], a[116:119], v[68:71]\n"
"ds_read_b128 v[224:227], v221 offset:28672\n"
"v_fma_f32 v144, v144, v162, v163\n"
"v_mfma_f32_16x16x32_bf16 v[68:71], v[228:231], a[124:127], v[68:71]\n"
"s_waitcnt lgkmcnt(3)\n"
"buffer_load_dwordx4 v2, s[48:51], s64 offen offset:1024 sc1 lds\n"
"v_mfma_f32_16x16x32_bf16 v[72:75], v[232:235], a[112:115], v[72:75]\n"
"ds_read_b128 v[228:231], v221 offset:36864\n"
"s_add_u32 m0, s55, 0\n"
"v_mfma_f32_16x16x32_bf16 v[72:75], v[236:239], a[120:123], v[72:75]\n"
"s_add_u32 s64, s89, 917504\n"
"v_mfma_f32_16x16x32_bf16 v[76:79], v[232:235], a[116:119], v[76:79]\n"
"ds_read_b128 v[232:235], v221 offset:40960\n"
"v_cvt_pk_bf16_f32 v48, v48, v49\n"
"v_mfma_f32_16x16x32_bf16 v[76:79], v[236:239], a[124:127], v[76:79]\n"
"s_waitcnt lgkmcnt(3)\n"
"buffer_load_dwordx4 v9, s[40:43], s64 offen lds\n"
"v_cvt_pk_bf16_f32 v49, v50, v51\n"
"v_cvt_pk_bf16_f32 v50, v52, v53\n"
"v_cvt_pk_bf16_f32 v51, v54, v55\n"
"v_mfma_f32_16x16x32_bf16 v[64:67], v[240:243], a[128:131], v[64:67]\n"
"ds_read_b128 v[236:239], v221 offset:49152\n"
"s_add_u32 m0, s55, 4096\n"
"v_mfma_f32_16x16x32_bf16 v[64:67], v[244:247], a[136:139], v[64:67]\n"
"s_add_u32 s64, s89, 1179648\n"
"v_mfma_f32_16x16x32_bf16 v[68:71], v[240:243], a[132:135], v[68:71]\n"
"ds_read_b128 v[240:243], v221 offset:45056\n"
"s_add_u32 s61, s30, 45549568\n"
"v_mfma_f32_16x16x32_bf16 v[68:71], v[244:247], a[140:143], v[68:71]\n"
"s_waitcnt lgkmcnt(3)\n"
"buffer_load_dwordx4 v9, s[40:43], s64 offen lds\n"
"v_mfma_f32_16x16x32_bf16 v[72:75], v[224:227], a[128:131], v[72:75]\n"
"ds_read_b128 v[244:247], v221 offset:53248\n"
"s_add_u32 m0, s55, 8192\n"
"v_mfma_f32_16x16x32_bf16 v[72:75], v[228:231], a[136:139], v[72:75]\n"
"s_add_u32 s64, s89, 1376256\n"
"v_mfma_f32_16x16x32_bf16 v[76:79], v[224:227], a[132:135], v[76:79]\n"
"ds_read_b128 v[224:227], v221 offset:25600\n"
"v_fma_f32 v145, v145, v162, v163\n"
"v_mfma_f32_16x16x32_bf16 v[76:79], v[228:231], a[140:143], v[76:79]\n"
"s_waitcnt lgkmcnt(3)\n"
"buffer_load_dwordx4 v9, s[40:43], s64 offen lds\n"
"v_mfma_f32_16x16x32_bf16 v[64:67], v[232:235], a[144:147], v[64:67]\n"
"ds_read_b128 v[228:231], v221 offset:33792\n"
"s_add_u32 m0, s55, 12288\n"
"v_mfma_f32_16x16x32_bf16 v[64:67], v[236:239], a[152:155], v[64:67]\n"
"s_add_u32 s64, s89, 1638400\n"
"v_mfma_f32_16x16x32_bf16 v[68:71], v[232:235], a[148:151], v[68:71]\n"
"ds_read_b128 v[232:235], v221 offset:29696\n"
"v_fma_f32 v146, v146, v162, v163\n"
"v_mfma_f32_16x16x32_bf16 v[68:71], v[236:239], a[156:159], v[68:71]\n"
"s_waitcnt lgkmcnt(3)\n"
"buffer_load_dwordx4 v9, s[40:43], s64 offen lds\n"
"v_mfma_f32_16x16x32_bf16 v[72:75], v[240:243], a[144:147], v[72:75]\n"
"ds_read_b128 v[236:239], v221 offset:37888\n"
"s_add_u32 m0, s84, 16384\n"
"v_mfma_f32_16x16x32_bf16 v[72:75], v[244:247], a[152:155], v[72:75]\n"
"s_add_u32 s64, s81, 37978112\n"
"v_mfma_f32_16x16x32_bf16 v[76:79], v[240:243], a[148:151], v[76:79]\n"
"ds_read_b128 v[240:243], v221 offset:41984\n"
"v_fma_f32 v147, v147, v162, v163\n"
"v_mfma_f32_16x16x32_bf16 v[76:79], v[244:247], a[156:159], v[76:79]\n"
"s_waitcnt lgkmcnt(3)\n"
"buffer_store_dwordx4 v[48:51], v2, s[48:51], s61 offen sc1\n"
"v_mfma_f32_16x16x32_bf16 v[64:67], v[224:227], a[160:163], v[64:67]\n"
"ds_read_b128 v[244:247], v221 offset:50176\n"
"v_mul_f32 v144, v144, v108\n"
"v_mfma_f32_16x16x32_bf16 v[64:67], v[228:231], a[168:171], v[64:67]\n"
"v_exp_f32 v144, v144\n"
"v_mfma_f32_16x16x32_bf16 v[68:71], v[224:227], a[164:167], v[68:71]\n"
"ds_read_b128 v[224:227], v221 offset:46080\n"
"v_mul_f32 v145, v145, v109\n"
"v_mfma_f32_16x16x32_bf16 v[68:71], v[228:231], a[172:175], v[68:71]\n"
"s_waitcnt lgkmcnt(3)\n"
"v_exp_f32 v145, v145\n"
"v_mfma_f32_16x16x32_bf16 v[72:75], v[232:235], a[160:163], v[72:75]\n"
"ds_read_b128 v[228:231], v221 offset:54272\n"
"v_mul_f32 v146, v146, v110\n"
"v_mfma_f32_16x16x32_bf16 v[72:75], v[236:239], a[168:171], v[72:75]\n"
"v_exp_f32 v146, v146\n"
"v_mfma_f32_16x16x32_bf16 v[76:79], v[232:235], a[164:167], v[76:79]\n"
"ds_read_b128 v[232:235], v221 offset:26624\n"
"v_mul_f32 v147, v147, v111\n"
"v_mfma_f32_16x16x32_bf16 v[76:79], v[236:239], a[172:175], v[76:79]\n"
"s_waitcnt lgkmcnt(3)\n"
"v_exp_f32 v147, v147\n"
"v_mfma_f32_16x16x32_bf16 v[64:67], v[240:243], a[176:179], v[64:67]\n"
"ds_read_b128 v[236:239], v221 offset:34816\n"
"v_cvt_pk_bf16_f32 v56, v56, v57\n"
"v_mfma_f32_16x16x32_bf16 v[64:67], v[244:247], a[184:187], v[64:67]\n"
"v_cvt_pk_bf16_f32 v57, v58, v59\n"
"v_cvt_pk_bf16_f32 v58, v60, v61\n"
"v_mfma_f32_16x16x32_bf16 v[68:71], v[240:243], a[180:183], v[68:71]\n"
"ds_read_b128 v[240:243], v221 offset:30720\n"
"v_cvt_pk_bf16_f32 v59, v62, v63\n"
"v_mfma_f32_16x16x32_bf16 v[68:71], v[244:247], a[188:191], v[68:71]\n"
"s_waitcnt lgkmcnt(3)\n"
"buffer_load_dwordx4 v2, s[48:51], s64 offen sc1 lds\n"
"v_mfma_f32_16x16x32_bf16 v[72:75], v[224:227], a[176:179], v[72:75]\n"
"ds_read_b128 v[244:247], v221 offset:38912\n"
"v_add_f32 v144, 1.0, v144\n"
"v_mfma_f32_16x16x32_bf16 v[72:75], v[228:231], a[184:187], v[72:75]\n"
"v_rcp_f32 v144, v144\n"
"v_mfma_f32_16x16x32_bf16 v[76:79], v[224:227], a[180:183], v[76:79]\n"
"ds_read_b128 v[224:227], v221 offset:43008\n"
"s_add_u32 s61, s30, 45550592\n"
"v_mfma_f32_16x16x32_bf16 v[76:79], v[228:231], a[188:191], v[76:79]\n"
"s_waitcnt lgkmcnt(3)\n"
"buffer_load_dwordx4 v2, s[48:51], s64 offen offset:1024 sc1 lds\n"
"v_mfma_f32_16x16x32_bf16 v[64:67], v[232:235], a[192:195], v[64:67]\n"
"ds_read_b128 v[228:231], v221 offset:51200\n"
"s_add_u32 m0, s84, 18432\n"
"v_mfma_f32_16x16x32_bf16 v[64:67], v[236:239], a[200:203], v[64:67]\n"
"s_add_u32 s64, s81, 39870464\n"
"v_mfma_f32_16x16x32_bf16 v[68:71], v[232:235], a[196:199], v[68:71]\n"
"ds_read_b128 v[232:235], v221 offset:47104\n"
"v_add_f32 v145, 1.0, v145\n"
"v_mfma_f32_16x16x32_bf16 v[68:71], v[236:239], a[204:207], v[68:71]\n"
"s_waitcnt lgkmcnt(3)\n"
"buffer_store_dwordx4 v[56:59], v2, s[48:51], s61 offen sc1\n"
"v_mfma_f32_16x16x32_bf16 v[72:75], v[240:243], a[192:195], v[72:75]\n"
"ds_read_b128 v[236:239], v221 offset:55296\n"
"v_add_f32 v146, 1.0, v146\n"
"v_mfma_f32_16x16x32_bf16 v[72:75], v[244:247], a[200:203], v[72:75]\n"
"v_rcp_f32 v145, v145\n"
"v_mfma_f32_16x16x32_bf16 v[76:79], v[240:243], a[196:199], v[76:79]\n"
"ds_read_b128 v[240:243], v221 offset:27648\n"
"v_add_f32 v147, 1.0, v147\n"
"v_mfma_f32_16x16x32_bf16 v[76:79], v[244:247], a[204:207], v[76:79]\n"
"s_waitcnt lgkmcnt(3)\n"
"buffer_load_dwordx4 v2, s[48:51], s64 offen sc1 lds\n"
"v_mfma_f32_16x16x32_bf16 v[64:67], v[224:227], a[208:211], v[64:67]\n"
"ds_read_b128 v[244:247], v221 offset:35840\n"
"v_mul_f32 v108, v108, v144\n"
"v_mfma_f32_16x16x32_bf16 v[64:67], v[228:231], a[216:219], v[64:67]\n"
"v_rcp_f32 v146, v146\n"
"v_mfma_f32_16x16x32_bf16 v[68:71], v[224:227], a[212:215], v[68:71]\n"
"ds_read_b128 v[224:227], v221 offset:31744\n"
"v_mul_f32 v109, v109, v145\n"
"v_mfma_f32_16x16x32_bf16 v[68:71], v[228:231], a[220:223], v[68:71]\n"
"s_waitcnt lgkmcnt(3)\n"
"buffer_load_dwordx4 v2, s[48:51], s64 offen offset:1024 sc1 lds\n"
"v_mfma_f32_16x16x32_bf16 v[72:75], v[232:235], a[208:211], v[72:75]\n"
"ds_read_b128 v[228:231], v221 offset:39936\n"
"s_add_u32 m0, s84, 20480\n"
"v_mfma_f32_16x16x32_bf16 v[72:75], v[236:239], a[216:219], v[72:75]\n"
"s_add_u32 s64, s81, 38248448\n"
"v_mfma_f32_16x16x32_bf16 v[76:79], v[232:235], a[212:215], v[76:79]\n"
"ds_read_b128 v[232:235], v221 offset:44032\n"
"v_mul_f32 v110, v110, v146\n"
"v_mfma_f32_16x16x32_bf16 v[76:79], v[236:239], a[220:223], v[76:79]\n"
"s_waitcnt lgkmcnt(3)\n"
"buffer_load_dwordx4 v2, s[48:51], s64 offen sc1 lds\n"
"v_mfma_f32_16x16x32_bf16 v[64:67], v[240:243], a[224:227], v[64:67]\n"
"ds_read_b128 v[236:239], v221 offset:52224\n"
"v_mul_f32 v112, v112, v160\n"
"v_mfma_f32_16x16x32_bf16 v[64:67], v[244:247], a[232:235], v[64:67]\n"
"v_rcp_f32 v147, v147\n"
"v_mfma_f32_16x16x32_bf16 v[68:71], v[240:243], a[228:231], v[68:71]\n"
"ds_read_b128 v[240:243], v221 offset:48128\n"
"v_mul_f32 v111, v111, v147\n"
"v_mfma_f32_16x16x32_bf16 v[68:71], v[244:247], a[236:239], v[68:71]\n"
"s_waitcnt lgkmcnt(3)\n"
"buffer_load_dwordx4 v2, s[48:51], s64 offen offset:1024 sc1 lds\n"
"v_mfma_f32_16x16x32_bf16 v[72:75], v[224:227], a[224:227], v[72:75]\n"
"ds_read_b128 v[244:247], v221 offset:56320\n"
"s_add_u32 m0, s84, 22528\n"
"v_mfma_f32_16x16x32_bf16 v[72:75], v[228:231], a[232:235], v[72:75]\n"
"s_add_u32 s64, s81, 40140800\n"
"v_mfma_f32_16x16x32_bf16 v[76:79], v[224:227], a[228:231], v[76:79]\n"
"s_waitcnt vmcnt(12)\n"
"s_barrier\n"
"ds_read_b128 v[224:227], v222\n"
"v_mfma_f32_16x16x32_bf16 v[76:79], v[228:231], a[236:239], v[76:79]\n"
"s_waitcnt lgkmcnt(3)\n"
"buffer_load_dwordx4 v2, s[48:51], s64 offen sc1 lds\n"
"v_mfma_f32_16x16x32_bf16 v[64:67], v[232:235], a[240:243], v[64:67]\n"
"ds_read_b128 v[228:231], v222 offset:8192\n"
"v_mul_f32 v113, v113, v160\n"
"v_mfma_f32_16x16x32_bf16 v[64:67], v[236:239], a[248:251], v[64:67]\n"
"v_mul_f32 v114, v114, v160\n"
"v_mul_f32 v115, v115, v160\n"
"v_mfma_f32_16x16x32_bf16 v[68:71], v[232:235], a[244:247], v[68:71]\n"
"ds_read_b128 v[232:235], v222 offset:16384\n"
"v_mul_f32 v144, v112, v112\n"
"v_mfma_f32_16x16x32_bf16 v[68:71], v[236:239], a[252:255], v[68:71]\n"
"s_waitcnt lgkmcnt(3)\n"
"buffer_load_dwordx4 v2, s[48:51], s64 offen offset:1024 sc1 lds\n"
"v_mfma_f32_16x16x32_bf16 v[72:75], v[240:243], a[240:243], v[72:75]\n"
"ds_read_b128 v[236:239], v222 offset:24576\n"
"s_add_u32 m0, s84, 49152\n"
"v_mfma_f32_16x16x32_bf16 v[72:75], v[244:247], a[248:251], v[72:75]\n"
"s_add_u32 s64, s81, 41762816\n"
"v_mfma_f32_16x16x32_bf16 v[76:79], v[240:243], a[244:247], v[76:79]\n"
"ds_read_b128 v[240:243], v222 offset:1024\n"
"v_mul_f32 v145, v113, v113\n"
"v_mfma_f32_16x16x32_bf16 v[76:79], v[244:247], a[252:255], v[76:79]\n"
"s_waitcnt lgkmcnt(3)\n"
"v_mul_f32 v146, v114, v114\n"
"v_mul_f32 v147, v115, v115\n"
"v_mfma_f32_16x16x32_bf16 v[80:83], v[224:227], a[0:3], 0\n"
"ds_read_b128 v[244:247], v222 offset:9216\n"
"v_fma_f32 v144, v144, v162, v163\n"
"v_mfma_f32_16x16x32_bf16 v[80:83], v[228:231], a[8:11], v[80:83]\n"
"v_fma_f32 v145, v145, v162, v163\n"
"v_fma_f32 v146, v146, v162, v163\n"
"v_mfma_f32_16x16x32_bf16 v[84:87], v[224:227], a[4:7], 0\n"
"ds_read_b128 v[224:227], v222 offset:17408\n"
"v_fma_f32 v147, v147, v162, v163\n"
"v_mfma_f32_16x16x32_bf16 v[84:87], v[228:231], a[12:15], v[84:87]\n"
"s_waitcnt lgkmcnt(3)\n"
"v_mul_f32 v144, v144, v112\n"
"v_mul_f32 v145, v145, v113\n"
"v_mfma_f32_16x16x32_bf16 v[80:83], v[232:235], a[16:19], v[80:83]\n"
"ds_read_b128 v[228:231], v222 offset:25600\n"
"v_cvt_pk_bf16_f32 v64, v64, v65\n"
"v_mfma_f32_16x16x32_bf16 v[80:83], v[236:239], a[24:27], v[80:83]\n"
"v_cvt_pk_bf16_f32 v65, v66, v67\n"
"v_cvt_pk_bf16_f32 v66, v68, v69\n"
"v_mfma_f32_16x16x32_bf16 v[84:87], v[232:235], a[20:23], v[84:87]\n"
"ds_read_b128 v[232:235], v222 offset:2048\n"
"v_cvt_pk_bf16_f32 v67, v70, v71\n"
"v_mfma_f32_16x16x32_bf16 v[84:87], v[236:239], a[28:31], v[84:87]\n"
"s_waitcnt lgkmcnt(3)\n"
"v_exp_f32 v144, v144\n"
"v_mfma_f32_16x16x32_bf16 v[80:83], v[240:243], a[32:35], v[80:83]\n"
"ds_read_b128 v[236:239], v222 offset:10240\n"
"v_mul_f32 v146, v146, v114\n"
"v_mfma_f32_16x16x32_bf16 v[80:83], v[244:247], a[40:43], v[80:83]\n"
"v_exp_f32 v145, v145\n"
"v_mfma_f32_16x16x32_bf16 v[84:87], v[240:243], a[36:39], v[84:87]\n"
"ds_read_b128 v[240:243], v222 offset:18432\n"
"s_add_u32 s61, s30, 45551616\n"
"v_mfma_f32_16x16x32_bf16 v[84:87], v[244:247], a[44:47], v[84:87]\n"
"s_waitcnt lgkmcnt(3)\n"
"buffer_store_dwordx4 v[64:67], v2, s[48:51], s61 offen sc1\n"
"v_mfma_f32_16x16x32_bf16 v[80:83], v[224:227], a[48:51], v[80:83]\n"
"ds_read_b128 v[244:247], v222 offset:26624\n"
"v_mul_f32 v147, v147, v115\n"
"v_mfma_f32_16x16x32_bf16 v[80:83], v[228:231], a[56:59], v[80:83]\n"
"v_exp_f32 v146, v146\n"
"v_mfma_f32_16x16x32_bf16 v[84:87], v[224:227], a[52:55], v[84:87]\n"
"ds_read_b128 v[224:227], v222 offset:3072\n"
"v_add_f32 v144, 1.0, v144\n"
"v_mfma_f32_16x16x32_bf16 v[84:87], v[228:231], a[60:63], v[84:87]\n"
"s_waitcnt lgkmcnt(3)\n"
"v_exp_f32 v147, v147\n"
"v_mfma_f32_16x16x32_bf16 v[80:83], v[232:235], a[64:67], v[80:83]\n"
"ds_read_b128 v[228:231], v222 offset:11264\n"
"v_add_f32 v145, 1.0, v145\n"
"v_mfma_f32_16x16x32_bf16 v[80:83], v[236:239], a[72:75], v[80:83]\n"
"v_rcp_f32 v144, v144\n"
"v_mfma_f32_16x16x32_bf16 v[84:87], v[232:235], a[68:71], v[84:87]\n"
"ds_read_b128 v[232:235], v222 offset:19456\n"
"v_add_f32 v146, 1.0, v146\n"
"v_mfma_f32_16x16x32_bf16 v[84:87], v[236:239], a[76:79], v[84:87]\n"
"s_waitcnt lgkmcnt(3)\n"
"v_rcp_f32 v145, v145\n"
"v_mfma_f32_16x16x32_bf16 v[80:83], v[240:243], a[80:83], v[80:83]\n"
"ds_read_b128 v[236:239], v222 offset:27648\n"
"v_add_f32 v147, 1.0, v147\n"
"v_mfma_f32_16x16x32_bf16 v[80:83], v[244:247], a[88:91], v[80:83]\n"
"v_rcp_f32 v146, v146\n"
"v_mfma_f32_16x16x32_bf16 v[84:87], v[240:243], a[84:87], v[84:87]\n"
"ds_read_b128 v[240:243], v222 offset:4096\n"
"v_mul_f32 v112, v112, v144\n"
"v_mfma_f32_16x16x32_bf16 v[84:87], v[244:247], a[92:95], v[84:87]\n"
"s_waitcnt lgkmcnt(3)\n"
"v_rcp_f32 v147, v147\n"
"v_mfma_f32_16x16x32_bf16 v[80:83], v[224:227], a[96:99], v[80:83]\n"
"ds_read_b128 v[244:247], v222 offset:12288\n"
"v_mul_f32 v113, v113, v145\n"
"v_mfma_f32_16x16x32_bf16 v[80:83], v[228:231], a[104:107], v[80:83]\n"
"v_mul_f32 v114, v114, v146\n"
"v_mul_f32 v115, v115, v147\n"
"v_mfma_f32_16x16x32_bf16 v[84:87], v[224:227], a[100:103], v[84:87]\n"
"ds_read_b128 v[224:227], v222 offset:20480\n"
"v_mul_f32 v116, v116, v161\n"
"v_mfma_f32_16x16x32_bf16 v[84:87], v[228:231], a[108:111], v[84:87]\n"
"s_waitcnt lgkmcnt(3)\n"
"buffer_load_dwordx4 v2, s[48:51], s64 offen sc1 lds\n"
"v_mfma_f32_16x16x32_bf16 v[80:83], v[232:235], a[112:115], v[80:83]\n"
"ds_read_b128 v[228:231], v222 offset:28672\n"
"v_mul_f32 v117, v117, v161\n"
"v_mfma_f32_16x16x32_bf16 v[80:83], v[236:239], a[120:123], v[80:83]\n"
"v_mul_f32 v118, v118, v161\n"
"v_mul_f32 v119, v119, v161\n"
"v_mfma_f32_16x16x32_bf16 v[84:87], v[232:235], a[116:119], v[84:87]\n"
"ds_read_b128 v[232:235], v222 offset:5120\n"
"v_mul_f32 v144, v116, v116\n"
"v_mfma_f32_16x16x32_bf16 v[84:87], v[236:239], a[124:127], v[84:87]\n"
"s_waitcnt lgkmcnt(3)\n"
"buffer_load_dwordx4 v2, s[48:51], s64 offen offset:1024 sc1 lds\n"
"v_mfma_f32_16x16x32_bf16 v[80:83], v[240:243], a[128:131], v[80:83]\n"
"ds_read_b128 v[236:239], v222 offset:13312\n"
"s_add_u32 m0, s84, 51200\n"
"v_mfma_f32_16x16x32_bf16 v[80:83], v[244:247], a[136:139], v[80:83]\n"
"s_add_u32 s64, s81, 43655168\n"
"v_mfma_f32_16x16x32_bf16 v[84:87], v[240:243], a[132:135], v[84:87]\n"
"ds_read_b128 v[240:243], v222 offset:21504\n"
"v_cvt_pk_bf16_f32 v72, v72, v73\n"
"v_mfma_f32_16x16x32_bf16 v[84:87], v[244:247], a[140:143], v[84:87]\n"
"s_waitcnt lgkmcnt(3)\n"
"buffer_load_dwordx4 v2, s[48:51], s64 offen sc1 lds\n"
"LZ6_%=:\n"
"v_add_u32 v11, LZ7_%=-LPC_%=, v0\n"
"v_min_u32 v11, LZ9_%=-LPC_%=-4, v11\n"
"global_load_dword v11, v11, s[70:71]\n"
"v_cvt_pk_bf16_f32 v73, v74, v75\n"
"v_cvt_pk_bf16_f32 v74, v76, v77\n"
"v_cvt_pk_bf16_f32 v75, v78, v79\n"
"v_mfma_f32_16x16x32_bf16 v[80:83], v[224:227], a[144:147], v[80:83]\n"
"ds_read_b128 v[244:247], v222 offset:29696\n"
"v_mul_f32 v145, v117, v117\n"
"v_mfma_f32_16x16x32_bf16 v[80:83], v[228:231], a[152:155], v[80:83]\n"
"v_mul_f32 v146, v118, v118\n"
"v_mul_f32 v147, v119, v119\n"
"v_mfma_f32_16x16x32_bf16 v[84:87], v[224:227], a[148:151], v[84:87]\n"
"ds_read_b128 v[224:227], v222 offset:6144\n"
"s_add_u32 s61, s30, 45552640\n"
"v_mfma_f32_16x16x32_bf16 v[84:87], v[228:231], a[156:159], v[84:87]\n"
"s_waitcnt lgkmcnt(3)\n"
"buffer_load_dwordx4 v2, s[48:51], s64 offen offset:1024 sc1 lds\n"
"v_mfma_f32_16x16x32_bf16 v[80:83], v[232:235], a[160:163], v[80:83]\n"
"ds_read_b128 v[228:231], v222 offset:14336\n"
"s_add_u32 m0, s84, 53248\n"
"v_mfma_f32_16x16x32_bf16 v[80:83], v[236:239], a[168:171], v[80:83]\n"
"s_add_u32 s64, s81, 42033152\n"
"v_mfma_f32_16x16x32_bf16 v[84:87], v[232:235], a[164:167], v[84:87]\n"
"ds_read_b128 v[232:235], v222 offset:22528\n"
"v_fma_f32 v144, v144, v162, v163\n"
"v_mfma_f32_16x16x32_bf16 v[84:87], v[236:239], a[172:175], v[84:87]\n"
"s_waitcnt lgkmcnt(3)\n"
"buffer_load_dwordx4 v2, s[48:51], s64 offen sc1 lds\n"
"v_mfma_f32_16x16x32_bf16 v[80:83], v[240:243], a[176:179], v[80:83]\n"
"ds_read_b128 v[236:239], v222 offset:30720\n"
"v_fma_f32 v145, v145, v162, v163\n"
"v_mfma_f32_16x16x32_bf16 v[80:83], v[244:247], a[184:187], v[80:83]\n"
"v_fma_f32 v146, v146, v162, v163\n"
"v_fma_f32 v147, v147, v162, v163\n"
"v_mfma_f32_16x16x32_bf16 v[84:87], v[240:243], a[180:183], v[84:87]\n"
"ds_read_b128 v[240:243], v222 offset:7168\n"
"v_mul_f32 v144, v144, v116\n"
"v_mfma_f32_16x16x32_bf16 v[84:87], v[244:247], a[188:191], v[84:87]\n"
"s_waitcnt lgkmcnt(3)\n"
"buffer_store_dwordx4 v[72:75], v2, s[48:51], s61 offen sc1\n"
"v_mfma_f32_16x16x32_bf16 v[80:83], v[224:227], a[192:195], v[80:83]\n"
"ds_read_b128 v[244:247], v222 offset:15360\n"
"v_mul_f32 v145, v145, v117\n"
"v_mfma_f32_16x16x32_bf16 v[80:83], v[228:231], a[200:203], v[80:83]\n"
"v_exp_f32 v144, v144\n"
"v_mfma_f32_16x16x32_bf16 v[84:87], v[224:227], a[196:199], v[84:87]\n"
"ds_read_b128 v[224:227], v222 offset:23552\n"
"v_mul_f32 v146, v146, v118\n"
"v_mfma_f32_16x16x32_bf16 v[84:87], v[228:231], a[204:207], v[84:87]\n"
"s_waitcnt lgkmcnt(3)\n"
"buffer_load_dwordx4 v2, s[48:51], s64 offen offset:1024 sc1 lds\n"
"v_mfma_f32_16x16x32_bf16 v[80:83], v[232:235], a[208:211], v[80:83]\n"
"ds_read_b128 v[228:231], v222 offset:31744\n"
"s_add_u32 m0, s84, 55296\n"
"v_mfma_f32_16x16x32_bf16 v[80:83], v[236:239], a[216:219], v[80:83]\n"
"s_add_u32 s64, s81, 43925504\n"
"v_mfma_f32_16x16x32_bf16 v[84:87], v[232:235], a[212:215], v[84:87]\n"
"s_waitcnt vmcnt(9)\n"
"s_barrier\n"
"ds_read_b128 v[232:235], v2 offset:16384\n"
"v_mfma_f32_16x16x32_bf16 v[84:87], v[236:239], a[220:223], v[84:87]\n"
"s_waitcnt lgkmcnt(3)\n"
"buffer_load_dwordx4 v2, s[48:51], s64 offen sc1 lds\n"
"v_mfma_f32_16x16x32_bf16 v[80:83], v[240:243], a[224:227], v[80:83]\n"
"ds_read_b128 v[236:239], v2 offset:24576\n"
"v_mul_f32 v147, v147, v119\n"
"v_mfma_f32_16x16x32_bf16 v[80:83], v[244:247], a[232:235], v[80:83]\n"
"v_exp_f32 v145, v145\n"
"v_mfma_f32_16x16x32_bf16 v[84:87], v[240:243], a[228:231], v[84:87]\n"
"ds_read_b128 v[240:243], v2 offset:20480\n"
"v_add_f32 v144, 1.0, v144\n"
"v_mfma_f32_16x16x32_bf16 v[84:87], v[244:247], a[236:239], v[84:87]\n"
"s_waitcnt lgkmcnt(3)\n"
"buffer_load_dwordx4 v2, s[48:51], s64 offen offset:1024 sc1 lds\n"
"v_mfma_f32_16x16x32_bf16 v[80:83], v[224:227], a[240:243], v[80:83]\n"
"ds_read_b128 v[244:247], v2 offset:28672\n"
"s_add_u32 m0, s84, 81920\n"
"v_mfma_f32_16x16x32_bf16 v[80:83], v[228:231], a[248:251], v[80:83]\n"
"s_add_u32 s64, s81, 38518784\n"
"v_mfma_f32_16x16x32_bf16 v[84:87], v[224:227], a[244:247], v[84:87]\n"
"ds_read_b128 v[224:227], v2 offset:32768\n"
"v_add_f32 v145, 1.0, v145\n"
"v_mfma_f32_16x16x32_bf16 v[84:87], v[228:231], a[252:255], v[84:87]\n"
"s_waitcnt lgkmcnt(3)\n"
"v_exp_f32 v146, v146\n"
"v_mfma_f32_16x16x32_bf16 v[164:167], v[232:235], a[0:3], v[164:167]\n"
"ds_read_b128 v[228:231], v2 offset:40960\n"
"v_add_f32 v146, 1.0, v146\n"
"v_mfma_f32_16x16x32_bf16 v[164:167], v[236:239], a[8:11], v[164:167]\n"
"v_exp_f32 v147, v147\n"
"v_mfma_f32_16x16x32_bf16 v[168:171], v[232:235], a[4:7], v[168:171]\n"
"ds_read_b128 v[232:235], v2 offset:36864\n"
"v_add_f32 v147, 1.0, v147\n"
"v_mfma_f32_16x16x32_bf16 v[168:171], v[236:239], a[12:15], v[168:171]\n"
"s_waitcnt lgkmcnt(3)\n"
"v_rcp_f32 v144, v144\n"
"v_mfma_f32_16x16x32_bf16 v[172:175], v[240:243], a[0:3], v[172:175]\n"
"ds_read_b128 v[236:239], v2 offset:45056\n"
"v_cvt_pk_bf16_f32 v80, v80, v81\n"
"v_mfma_f32_16x16x32_bf16 v[172:175], v[244:247], a[8:11], v[172:175]\n"
"v_cvt_pk_bf16_f32 v81, v82, v83\n"
"v_cvt_pk_bf16_f32 v82, v84, v85\n"
"v_mfma_f32_16x16x32_bf16 v[176:179], v[240:243], a[4:7], v[176:179]\n"
"ds_read_b128 v[240:243], v2 offset:17408\n"
"v_cvt_pk_bf16_f32 v83, v86, v87\n"
"v_mfma_f32_16x16x32_bf16 v[176:179], v[244:247], a[12:15], v[176:179]\n"
"s_waitcnt lgkmcnt(3)\n"
"v_rcp_f32 v145, v145\n"
"v_mfma_f32_16x16x32_bf16 v[164:167], v[224:227], a[16:19], v[164:167]\n"
"ds_read_b128 v[244:247], v2 offset:25600\n"
"v_mul_f32 v116, v116, v144\n"
"v_mfma_f32_16x16x32_bf16 v[164:167], v[228:231], a[24:27], v[164:167]\n"
"v_rcp_f32 v146, v146\n"
"v_mfma_f32_16x16x32_bf16 v[168:171], v[224:227], a[20:23], v[168:171]\n"
"ds_read_b128 v[224:227], v2 offset:21504\n"
"s_add_u32 s61, s30, 45553664\n"
"v_mfma_f32_16x16x32_bf16 v[168:171], v[228:231], a[28:31], v[168:171]\n"
"s_waitcnt lgkmcnt(3)\n"
"buffer_store_dwordx4 v[80:83], v2, s[48:51], s61 offen sc1\n"
"v_mfma_f32_16x16x32_bf16 v[172:175], v[232:235], a[16:19], v[172:175]\n"
"ds_read_b128 v[228:231], v2 offset:29696\n"
"v_mul_f32 v117, v117, v145\n"
"v_mfma_f32_16x16x32_bf16 v[172:175], v[236:239], a[24:27], v[172:175]\n"
"v_rcp_f32 v147, v147\n"
"v_mfma_f32_16x16x32_bf16 v[176:179], v[232:235], a[20:23], v[176:179]\n"
"ds_read_b128 v[232:235], v2 offset:33792\n"
"v_mul_f32 v118, v118, v146\n"
"v_mfma_f32_16x16x32_bf16 v[176:179], v[236:239], a[28:31], v[176:179]\n"
"s_waitcnt lgkmcnt(3)\n"
"v_mul_f32 v119, v119, v147\n"
"v_mul_f32 v120, v120, v160\n"
"v_mfma_f32_16x16x32_bf16 v[164:167], v[240:243], a[32:35], v[164:167]\n"
"ds_read_b128 v[236:239], v2 offset:41984\n"
"v_mul_f32 v121, v121, v160\n"
"v_mfma_f32_16x16x32_bf16 v[164:167], v[244:247], a[40:43], v[164:167]\n"
"v_mul_f32 v122, v122, v160\n"
"v_mul_f32 v123, v123, v160\n"
"v_mfma_f32_16x16x32_bf16 v[168:171], v[240:243], a[36:39], v[168:171]\n"
"ds_read_b128 v[240:243], v2 offset:37888\n"
"v_mul_f32 v144, v120, v120\n"
"v_mfma_f32_16x16x32_bf16 v[168:171], v[244:247], a[44:47], v[168:171]\n"
"s_waitcnt lgkmcnt(3)\n"
"v_mul_f32 v145, v121, v121\n"
"v_mul_f32 v146, v122, v122\n"
"v_mfma_f32_16x16x32_bf16 v[172:175], v[224:227], a[32:35], v[172:175]\n"
"ds_read_b128 v[244:247], v2 offset:46080\n"
"v_mul_f32 v147, v123, v123\n"
"v_mfma_f32_16x16x32_bf16 v[172:175], v[228:231], a[40:43], v[172:175]\n"
"v_fma_f32 v144, v144, v162, v163\n"
"v_fma_f32 v145, v145, v162, v163\n"
"v_mfma_f32_16x16x32_bf16 v[176:179], v[224:227], a[36:39], v[176:179]\n"
"ds_read_b128 v[224:227], v2 offset:18432\n"
"v_fma_f32 v146, v146, v162, v163\n"
"v_mfma_f32_16x16x32_bf16 v[176:179], v[228:231], a[44:47], v[176:179]\n"
"s_waitcnt lgkmcnt(3)\n"
"v_fma_f32 v147, v147, v162, v163\n"
"v_mul_f32 v144, v144, v120\n"
"s_add_u32 s62, s16, 1572928\n"
"s_mov_b64 exec, 2\n"
"buffer_atomic_add v1, off, s[48:51], s62 sc1\n"
"s_mov_b64 exec, -1\n"
"v_mfma_f32_16x16x32_bf16 v[164:167], v[232:235], a[48:51], v[164:167]\n"
"ds_read_b128 v[228:231], v2 offset:26624\n"
"v_mul_f32 v145, v145, v121\n"
"v_mfma_f32_16x16x32_bf16 v[164:167], v[236:239], a[56:59], v[164:167]\n"
"v_exp_f32 v144, v144\n"
"v_mfma_f32_16x16x32_bf16 v[168:171], v[232:235], a[52:55], v[168:171]\n"
"ds_read_b128 v[232:235], v2 offset:22528\n"
"v_mul_f32 v146, v146, v122\n"
"v_mfma_f32_16x16x32_bf16 v[168:171], v[236:239], a[60:63], v[168:171]\n"
"s_waitcnt lgkmcnt(3)\n"
"buffer_load_dwordx4 v2, s[48:51], s64 offen sc1 lds\n"
"v_mfma_f32_16x16x32_bf16 v[172:175], v[240:243], a[48:51], v[172:175]\n"
"ds_read_b128 v[236:239], v2 offset:30720\n"
"v_mul_f32 v147, v147, v123\n"
"v_mfma_f32_16x16x32_bf16 v[172:175], v[244:247], a[56:59], v[172:175]\n"
"v_exp_f32 v145, v145\n"
"v_mfma_f32_16x16x32_bf16 v[176:179], v[240:243], a[52:55], v[176:179]\n"
"ds_read_b128 v[240:243], v2 offset:34816\n"
"v_add_f32 v144, 1.0, v144\n"
"v_mfma_f32_16x16x32_bf16 v[176:179], v[244:247], a[60:63], v[176:179]\n"
"s_waitcnt lgkmcnt(3)\n"
"buffer_load_dwordx4 v2, s[48:51], s64 offen offset:1024 sc1 lds\n"
"v_mfma_f32_16x16x32_bf16 v[164:167], v[224:227], a[64:67], v[164:167]\n"
"ds_read_b128 v[244:247], v2 offset:43008\n"
"s_add_u32 m0, s84, 83968\n"
"v_mfma_f32_16x16x32_bf16 v[164:167], v[228:231], a[72:75], v[164:167]\n"
"s_add_u32 s64, s81, 40411136\n"
"v_mfma_f32_16x16x32_bf16 v[168:171], v[224:227], a[68:71], v[168:171]\n"
"ds_read_b128 v[224:227], v2 offset:38912\n"
"v_add_f32 v145, 1.0, v145\n"
"v_mfma_f32_16x16x32_bf16 v[168:171], v[228:231], a[76:79], v[168:171]\n"
"s_waitcnt lgkmcnt(3)\n"
"buffer_load_dwordx4 v2, s[48:51], s64 offen sc1 lds\n"
"v_mfma_f32_16x16x32_bf16 v[172:175], v[232:235], a[64:67], v[172:175]\n"
"ds_read_b128 v[228:231], v2 offset:47104\n"
"LPG17_%=:\n"
"s_cbranch_execz LPG21_%=\n"
"v_mfma_f32_16x16x32_bf16 v[172:175], v[236:239], a[72:75], v[172:175]\n"
"v_exp_f32 v146, v146\n"
"v_mfma_f32_16x16x32_bf16 v[176:179], v[232:235], a[68:71], v[176:179]\n"
"ds_read_b128 v[232:235], v2 offset:19456\n"
"v_add_f32 v146, 1.0, v146\n"
"v_mfma_f32_16x16x32_bf16 v[176:179], v[236:239], a[76:79], v[176:179]\n"
"s_waitcnt lgkmcnt(3)\n"
"buffer_load_dwordx4 v2, s[48:51], s64 offen offset:1024 sc1 lds\n"
"v_mfma_f32_16x16x32_bf16 v[164:167], v[240:243], a[80:83], v[164:167]\n"
"ds_read_b128 v[236:239], v2 offset:27648\n"
"s_add_u32 m0, s84, 86016\n"
"v_mfma_f32_16x16x32_bf16 v[164:167], v[244:247], a[88:91], v[164:167]\n"
"s_add_u32 s64, s81, 38789120\n"
"v_mfma_f32_16x16x32_bf16 v[168:171], v[240:243], a[84:87], v[168:171]\n"
"ds_read_b128 v[240:243], v2 offset:23552\n"
"v_mfma_f32_16x16x32_bf16 v[168:171], v[244:247], a[92:95], v[168:171]\n"
"s_waitcnt lgkmcnt(3)\n"
"buffer_load_dwordx4 v2, s[48:51], s64 offen sc1 lds\n"
"v_mfma_f32_16x16x32_bf16 v[172:175], v[224:227], a[80:83], v[172:175]\n"
"ds_read_b128 v[244:247], v2 offset:31744\n"
"v_mfma_f32_16x16x32_bf16 v[172:175], v[228:231], a[88:91], v[172:175]\n"
"v_exp_f32 v147, v147\n"
"v_mfma_f32_16x16x32_bf16 v[176:179], v[224:227], a[84:87], v[176:179]\n"
"ds_read_b128 v[224:227], v2 offset:35840\n"
"v_add_f32 v147, 1.0, v147\n"
"v_mfma_f32_16x16x32_bf16 v[176:179], v[228:231], a[92:95], v[176:179]\n"
"s_waitcnt lgkmcnt(3)\n"
"buffer_load_dwordx4 v2, s[48:51], s64 offen offset:1024 sc1 lds\n"
"v_mfma_f32_16x16x32_bf16 v[164:167], v[232:235], a[96:99], v[164:167]\n"
"ds_read_b128 v[228:231], v2 offset:44032\n"
"s_add_u32 m0, s84, 88064\n"
"v_mfma_f32_16x16x32_bf16 v[164:167], v[236:239], a[104:107], v[164:167]\n"
"s_add_u32 s64, s81, 40681472\n"
"v_mfma_f32_16x16x32_bf16 v[168:171], v[232:235], a[100:103], v[168:171]\n"
"ds_read_b128 v[232:235], v2 offset:39936\n"
"v_mfma_f32_16x16x32_bf16 v[168:171], v[236:239], a[108:111], v[168:171]\n"
"s_waitcnt lgkmcnt(3)\n"
"buffer_load_dwordx4 v2, s[48:51], s64 offen sc1 lds\n"
"v_mfma_f32_16x16x32_bf16 v[172:175], v[240:243], a[96:99], v[172:175]\n"
"ds_read_b128 v[236:239], v2 offset:48128\n"
"v_mfma_f32_16x16x32_bf16 v[172:175], v[244:247], a[104:107], v[172:175]\n"
"v_rcp_f32 v144, v144\n"
"v_mfma_f32_16x16x32_bf16 v[176:179], v[240:243], a[100:103], v[176:179]\n"
"s_waitcnt vmcnt(9)\n"
"s_barrier\n"
"ds_read_b128 v[240:243], v2 offset:49152\n"
"v_mfma_f32_16x16x32_bf16 v[176:179], v[244:247], a[108:111], v[176:179]\n"
"s_waitcnt lgkmcnt(3)\n"
"buffer_load_dwordx4 v2, s[48:51], s64 offen offset:1024 sc1 lds\n"
"v_mfma_f32_16x16x32_bf16 v[164:167], v[224:227], a[112:115], v[164:167]\n"
"ds_read_b128 v[244:247], v221\n"
"s_add_u32 m0, s84, 114688\n"
"v_mfma_f32_16x16x32_bf16 v[164:167], v[228:231], a[120:123], v[164:167]\n"
"s_add_u32 s64, s81, 42303488\n"
"v_mfma_f32_16x16x32_bf16 v[168:171], v[224:227], a[116:119], v[168:171]\n"
"ds_read_b128 v[224:227], v2 offset:53248\n"
"v_mul_f32 v120, v120, v144\n"
"v_mfma_f32_16x16x32_bf16 v[168:171], v[228:231], a[124:127], v[168:171]\n"
"s_waitcnt lgkmcnt(3)\n"
"v_rcp_f32 v145, v145\n"
"v_mfma_f32_16x16x32_bf16 v[172:175], v[232:235], a[112:115], v[172:175]\n"
"ds_read_b128 v[228:231], v221 offset:4096\n"
"v_mul_f32 v121, v121, v145\n"
"v_mfma_f32_16x16x32_bf16 v[172:175], v[236:239], a[120:123], v[172:175]\n"
"v_rcp_f32 v146, v146\n"
"v_mfma_f32_16x16x32_bf16 v[176:179], v[232:235], a[116:119], v[176:179]\n"
"ds_read_b128 v[232:235], v221 offset:8192\n"
"v_mul_f32 v122, v122, v146\n"
"v_mfma_f32_16x16x32_bf16 v[176:179], v[236:239], a[124:127], v[176:179]\n"
"s_waitcnt lgkmcnt(3)\n"
"v_rcp_f32 v147, v147\n"
"v_mfma_f32_16x16x32_bf16 v[164:167], v[240:243], a[128:131], v[164:167]\n"
"ds_read_b128 v[236:239], v221 offset:16384\n"
"v_mul_f32 v123, v123, v147\n"
"v_mfma_f32_16x16x32_bf16 v[164:167], v[244:247], a[136:139], v[164:167]\n"
"v_mul_f32 v124, v124, v161\n"
"v_mul_f32 v125, v125, v161\n"
"v_mfma_f32_16x16x32_bf16 v[168:171], v[240:243], a[132:135], v[168:171]\n"
"ds_read_b128 v[240:243], v221 offset:12288\n"
"v_mul_f32 v126, v126, v161\n"
"v_mfma_f32_16x16x32_bf16 v[168:171], v[244:247], a[140:143], v[168:171]\n"
"s_waitcnt lgkmcnt(3)\n"
"v_mul_f32 v127, v127, v161\n"
"v_mul_f32 v144, v124, v124\n"
"v_mfma_f32_16x16x32_bf16 v[172:175], v[224:227], a[128:131], v[172:175]\n"
"ds_read_b128 v[244:247], v221 offset:20480\n"
"v_mul_f32 v145, v125, v125\n"
"v_mfma_f32_16x16x32_bf16 v[172:175], v[228:231], a[136:139], v[172:175]\n"
"v_mul_f32 v146, v126, v126\n"
"v_mul_f32 v147, v127, v127\n"
"v_mfma_f32_16x16x32_bf16 v[176:179], v[224:227], a[132:135], v[176:179]\n"
"ds_read_b128 v[224:227], v2 offset:50176\n"
"v_fma_f32 v144, v144, v162, v163\n"
"v_mfma_f32_16x16x32_bf16 v[176:179], v[228:231], a[140:143], v[176:179]\n"
"s_waitcnt lgkmcnt(3)\n"
"v_fma_f32 v145, v145, v162, v163\n"
"v_fma_f32 v146, v146, v162, v163\n"
"v_mfma_f32_16x16x32_bf16 v[164:167], v[232:235], a[144:147], v[164:167]\n"
"ds_read_b128 v[228:231], v221 offset:1024\n"
"v_fma_f32 v147, v147, v162, v163\n"
"v_mfma_f32_16x16x32_bf16 v[164:167], v[236:239], a[152:155], v[164:167]\n"
"v_mul_f32 v144, v144, v124\n"
"v_mul_f32 v145, v145, v125\n"
"v_mfma_f32_16x16x32_bf16 v[168:171], v[232:235], a[148:151], v[168:171]\n"
"ds_read_b128 v[232:235], v2 offset:54272\n"
"v_mul_f32 v146, v146, v126\n"
"v_mfma_f32_16x16x32_bf16 v[168:171], v[236:239], a[156:159], v[168:171]\n"
"s_waitcnt lgkmcnt(3)\n"
"v_exp_f32 v144, v144\n"
"v_mfma_f32_16x16x32_bf16 v[172:175], v[240:243], a[144:147], v[172:175]\n"
"ds_read_b128 v[236:239], v221 offset:5120\n"
"v_mul_f32 v147, v147, v127\n"
"v_mfma_f32_16x16x32_bf16 v[172:175], v[244:247], a[152:155], v[172:175]\n"
"v_exp_f32 v145, v145\n"
"v_mfma_f32_16x16x32_bf16 v[176:179], v[240:243], a[148:151], v[176:179]\n"
"ds_read_b128 v[240:243], v221 offset:9216\n"
"v_add_f32 v144, 1.0, v144\n"
"v_mfma_f32_16x16x32_bf16 v[176:179], v[244:247], a[156:159], v[176:179]\n"
"s_waitcnt lgkmcnt(3)\n"
"buffer_load_dwordx4 v2, s[48:51], s64 offen sc1 lds\n"
"v_mfma_f32_16x16x32_bf16 v[164:167], v[224:227], a[160:163], v[164:167]\n"
"ds_read_b128 v[244:247], v221 offset:17408\n"
"v_add_f32 v145, 1.0, v145\n"
"v_mfma_f32_16x16x32_bf16 v[164:167], v[228:231], a[168:171], v[164:167]\n"
"v_exp_f32 v146, v146\n"
"s_add_u32 s62, s17, 1572928\n"
"v_mov_b32 v252, 0\n"
"s_mov_b64 exec, 1\n"
"buffer_atomic_or v252, off, s[48:51], s62 sc0 sc1\n"
"s_mov_b64 exec, -1\n"
"v_mfma_f32_16x16x32_bf16 v[168:171], v[224:227], a[164:167], v[168:171]\n"
"ds_read_b128 v[224:227], v221 offset:13312\n"
"v_add_f32 v146, 1.0, v146\n"
"v_mfma_f32_16x16x32_bf16 v[168:171], v[228:231], a[172:175], v[168:171]\n"
"s_waitcnt lgkmcnt(3)\n"
"v_exp_f32 v147, v147\n"
"v_mfma_f32_16x16x32_bf16 v[172:175], v[232:235], a[160:163], v[172:175]\n"
"ds_read_b128 v[228:231], v221 offset:21504\n"
"v_add_f32 v147, 1.0, v147\n"
"v_mfma_f32_16x16x32_bf16 v[172:175], v[236:239], a[168:171], v[172:175]\n"
"buffer_load_dwordx4 v2, s[48:51], s64 offen offset:1024 sc1 lds\n"
"v_mfma_f32_16x16x32_bf16 v[176:179], v[232:235], a[164:167], v[176:179]\n"
"ds_read_b128 v[232:235], v2 offset:51200\n"
"s_add_u32 m0, s84, 116736\n"
"v_mfma_f32_16x16x32_bf16 v[176:179], v[236:239], a[172:175], v[176:179]\n"
"s_waitcnt lgkmcnt(3)\n"
"s_add_u32 s64, s81, 44195840\n"
"v_mfma_f32_16x16x32_bf16 v[164:167], v[240:243], a[176:179], v[164:167]\n"
"ds_read_b128 v[236:239], v221 offset:2048\n"
"v_mfma_f32_16x16x32_bf16 v[164:167], v[244:247], a[184:187], v[164:167]\n"
"buffer_load_dwordx4 v2, s[48:51], s64 offen sc1 lds\n"
"v_mfma_f32_16x16x32_bf16 v[168:171], v[240:243], a[180:183], v[168:171]\n"
"ds_read_b128 v[240:243], v2 offset:55296\n"
"v_mfma_f32_16x16x32_bf16 v[168:171], v[244:247], a[188:191], v[168:171]\n"
"s_waitcnt lgkmcnt(3)\n"
"v_rcp_f32 v144, v144\n"
"v_mfma_f32_16x16x32_bf16 v[172:175], v[224:227], a[176:179], v[172:175]\n"
"ds_read_b128 v[244:247], v221 offset:6144\n"
"v_mul_f32 v124, v124, v144\n"
"v_mfma_f32_16x16x32_bf16 v[172:175], v[228:231], a[184:187], v[172:175]\n"
"v_rcp_f32 v145, v145\n"
"v_mfma_f32_16x16x32_bf16 v[176:179], v[224:227], a[180:183], v[176:179]\n"
"ds_read_b128 v[224:227], v221 offset:10240\n"
"v_mul_f32 v125, v125, v145\n"
"v_mfma_f32_16x16x32_bf16 v[176:179], v[228:231], a[188:191], v[176:179]\n"
"s_waitcnt lgkmcnt(3)\n"
"buffer_load_dwordx4 v2, s[48:51], s64 offen offset:1024 sc1 lds\n"
"s_waitcnt vmcnt(14)\n"
"s_add_u32 s62, s16, 1572992\n"
"s_mov_b64 exec, 2\n"
"buffer_atomic_add v1, off, s[48:51], s62 sc1\n"
"s_mov_b64 exec, -1\n"
"v_mfma_f32_16x16x32_bf16 v[164:167], v[232:235], a[192:195], v[164:167]\n"
"ds_read_b128 v[228:231], v221 offset:18432\n"
"s_add_u32 m0, s84, 118784\n"
"v_mfma_f32_16x16x32_bf16 v[164:167], v[236:239], a[200:203], v[164:167]\n"
"s_add_u32 s64, s81, 42573824\n"
"v_mfma_f32_16x16x32_bf16 v[168:171], v[232:235], a[196:199], v[168:171]\n"
"ds_read_b128 v[232:235], v221 offset:14336\n"
"v_mul_f32 v128, v128, v160\n"
"v_mfma_f32_16x16x32_bf16 v[168:171], v[236:239], a[204:207], v[168:171]\n"
"s_waitcnt lgkmcnt(3)\n"
"buffer_load_dwordx4 v2, s[48:51], s64 offen sc1 lds\n"
"v_mfma_f32_16x16x32_bf16 v[172:175], v[240:243], a[192:195], v[172:175]\n"
"ds_read_b128 v[236:239], v221 offset:22528\n"
"v_mul_f32 v129, v129, v160\n"
"v_mfma_f32_16x16x32_bf16 v[172:175], v[244:247], a[200:203], v[172:175]\n"
"v_rcp_f32 v146, v146\n"
"v_mfma_f32_16x16x32_bf16 v[176:179], v[240:243], a[196:199], v[176:179]\n"
"ds_read_b128 v[240:243], v2 offset:52224\n"
"v_mul_f32 v126, v126, v146\n"
"v_mfma_f32_16x16x32_bf16 v[176:179], v[244:247], a[204:207], v[176:179]\n"
"s_waitcnt lgkmcnt(3)\n"
"v_rcp_f32 v147, v147\n"
"v_mfma_f32_16x16x32_bf16 v[164:167], v[224:227], a[208:211], v[164:167]\n"
"ds_read_b128 v[244:247], v221 offset:3072\n"
"v_mul_f32 v127, v127, v147\n"
"v_mfma_f32_16x16x32_bf16 v[164:167], v[228:231], a[216:219], v[164:167]\n"
"buffer_load_dwordx4 v2, s[48:51], s64 offen offset:1024 sc1 lds\n"
"v_mfma_f32_16x16x32_bf16 v[168:171], v[224:227], a[212:215], v[168:171]\n"
"ds_read_b128 v[224:227], v2 offset:56320\n"
"s_add_u32 m0, s84, 120832\n"
"v_mfma_f32_16x16x32_bf16 v[168:171], v[228:231], a[220:223], v[168:171]\n"
"s_waitcnt lgkmcnt(3)\n"
"s_add_u32 s64, s81, 44466176\n"
"v_mfma_f32_16x16x32_bf16 v[172:175], v[232:235], a[208:211], v[172:175]\n"
"ds_read_b128 v[228:231], v221 offset:7168\n"
"v_mul_f32 v130, v130, v160\n"
"v_mfma_f32_16x16x32_bf16 v[172:175], v[236:239], a[216:219], v[172:175]\n"
"buffer_load_dwordx4 v2, s[48:51], s64 offen sc1 lds\n"
"v_mfma_f32_16x16x32_bf16 v[176:179], v[232:235], a[212:215], v[176:179]\n"
"ds_read_b128 v[232:235], v221 offset:11264\n"
"v_mul_f32 v131, v131, v160\n"
"v_mfma_f32_16x16x32_bf16 v[176:179], v[236:239], a[220:223], v[176:179]\n"
"s_waitcnt lgkmcnt(3)\n"
"v_mul_f32 v144, v128, v128\n"
"v_mul_f32 v145, v129, v129\n"
"v_mfma_f32_16x16x32_bf16 v[164:167], v[240:243], a[224:227], v[164:167]\n"
"ds_read_b128 v[236:239], v221 offset:19456\n"
"v_mul_f32 v146, v130, v130\n"
"v_mfma_f32_16x16x32_bf16 v[164:167], v[244:247], a[232:235], v[164:167]\n"
"buffer_load_dwordx4 v2, s[48:51], s64 offen offset:1024 sc1 lds\n"
"v_mfma_f32_16x16x32_bf16 v[168:171], v[240:243], a[228:231], v[168:171]\n"
"ds_read_b128 v[240:243], v221 offset:15360\n"
"v_mul_f32 v147, v131, v131\n"
"v_mfma_f32_16x16x32_bf16 v[168:171], v[244:247], a[236:239], v[168:171]\n"
"s_waitcnt lgkmcnt(3)\n"
"v_fma_f32 v144, v144, v162, v163\n"
"v_fma_f32 v145, v145, v162, v163\n"
"v_mfma_f32_16x16x32_bf16 v[172:175], v[224:227], a[224:227], v[172:175]\n"
"ds_read_b128 v[244:247], v221 offset:23552\n"
"v_fma_f32 v146, v146, v162, v163\n"
"v_mfma_f32_16x16x32_bf16 v[172:175], v[228:231], a[232:235], v[172:175]\n"
"v_fma_f32 v147, v147, v162, v163\n"
"v_mul_f32 v144, v144, v128\n"
"v_mfma_f32_16x16x32_bf16 v[176:179], v[224:227], a[228:231], v[176:179]\n"
"s_waitcnt vmcnt(10)\n"
"s_barrier\n"
"ds_read_b128 v[224:227], v221 offset:24576\n"
"v_mfma_f32_16x16x32_bf16 v[176:179], v[228:231], a[236:239], v[176:179]\n"
"s_waitcnt lgkmcnt(3)\n"
"v_exp_f32 v144, v144\n"
"v_mfma_f32_16x16x32_bf16 v[164:167], v[232:235], a[240:243], v[164:167]\n"
"ds_read_b128 v[228:231], v221 offset:32768\n"
"s_add_u32 m0, s84, 16384\n"
"v_mfma_f32_16x16x32_bf16 v[164:167], v[236:239], a[248:251], v[164:167]\n"
"s_add_u32 s64, s81, 39059456\n"
"v_mfma_f32_16x16x32_bf16 v[168:171], v[232:235], a[244:247], v[168:171]\n"
"ds_read_b128 v[232:235], v221 offset:28672\n"
"v_mul_f32 v145, v145, v129\n"
"v_mfma_f32_16x16x32_bf16 v[168:171], v[236:239], a[252:255], v[168:171]\n"
"s_waitcnt lgkmcnt(3)\n"
"buffer_load_dwordx4 v2, s[48:51], s64 offen sc1 lds\n"
"v_mfma_f32_16x16x32_bf16 v[172:175], v[240:243], a[240:243], v[172:175]\n"
"ds_read_b128 v[236:239], v221 offset:36864\n"
"v_mul_f32 v146, v146, v130\n"
"v_mfma_f32_16x16x32_bf16 v[172:175], v[244:247], a[248:251], v[172:175]\n"
"v_exp_f32 v145, v145\n"
"v_mfma_f32_16x16x32_bf16 v[176:179], v[240:243], a[244:247], v[176:179]\n"
"ds_read_b128 v[240:243], v221 offset:40960\n"
"v_mul_f32 v147, v147, v131\n"
"v_mfma_f32_16x16x32_bf16 v[176:179], v[244:247], a[252:255], v[176:179]\n"
"s_waitcnt lgkmcnt(3)\n"
"buffer_load_dwordx4 v2, s[48:51], s64 offen offset:1024 sc1 lds\n"
"buffer_load_dword v251, v251, s[40:43], s38 offen\n"
"s_mov_b64 exec, 1\n"
"s_waitcnt vmcnt(11)\n"
"v_cmp_ne_u32 vcc, 1, v252\n"
"s_mov_b64 exec, -1\n"
"s_nop 3\n"
"s_cbranch_vccz Ld22_%=\n"
"s_mov_b32 s65, 0\n"
"Lp21_%=:\n"
"s_add_u32 s62, s17, 1572928\n"
"v_mov_b32 v252, 0\n"
"s_mov_b64 exec, 1\n"
"buffer_atomic_or v252, off, s[48:51], s62 sc0 sc1\n"
"s_mov_b64 exec, -1\n"
"s_add_u32 s65, s65, 1\n"
"s_cmp_gt_u32 s65, 131072\n"
"s_waitcnt vmcnt(0)\n"
"s_cbranch_scc1 Ld22_%=\n"
"s_mov_b64 exec, 1\n"
"v_cmp_ne_u32 vcc, 1, v252\n"
"s_mov_b64 exec, -1\n"
"s_nop 3\n"
"s_cbranch_vccnz Lp21_%=\n"
"Ld22_%=:\n"
"v_mfma_f32_16x16x32_bf16 v[180:183], v[224:227], a[0:3], v[180:183]\n"
"ds_read_b128 v[244:247], v221 offset:49152\n"
"s_add_u32 m0, s84, 18432\n"
"v_mfma_f32_16x16x32_bf16 v[180:183], v[228:231], a[8:11], v[180:183]\n"
"s_add_u32 s64, s81, 40951808\n"
"v_mfma_f32_16x16x32_bf16 v[184:187], v[224:227], a[4:7], v[184:187]\n"
"ds_read_b128 v[224:227], v221 offset:45056\n"
"s_add_u32 s61, s31, 45547520\n"
"v_mfma_f32_16x16x32_bf16 v[184:187], v[228:231], a[12:15], v[184:187]\n"
"s_waitcnt lgkmcnt(3)\n"
"buffer_load_dwordx4 v[36:39], v2, s[48:51], s61 offen sc1\n"
"v_mfma_f32_16x16x32_bf16 v[188:191], v[232:235], a[0:3], v[188:191]\n"
"ds_read_b128 v[228:231], v221 offset:53248\n"
"v_add_f32 v144, 1.0, v144\n"
"v_mfma_f32_16x16x32_bf16 v[188:191], v[236:239], a[8:11], v[188:191]\n"
"v_exp_f32 v146, v146\n"
"v_mfma_f32_16x16x32_bf16 v[192:195], v[232:235], a[4:7], v[192:195]\n"
"ds_read_b128 v[232:235], v221 offset:25600\n"
"v_add_f32 v145, 1.0, v145\n"
"v_mfma_f32_16x16x32_bf16 v[192:195], v[236:239], a[12:15], v[192:195]\n"
"s_waitcnt lgkmcnt(3)\n"
"buffer_load_dwordx4 v[44:47], v2, s[48:51], s61 offen offset:1024 sc1\n"
"v_mfma_f32_16x16x32_bf16 v[180:183], v[240:243], a[16:19], v[180:183]\n"
"ds_read_b128 v[236:239], v221 offset:33792\n"
"v_add_f32 v146, 1.0, v146\n"
"v_mfma_f32_16x16x32_bf16 v[180:183], v[244:247], a[24:27], v[180:183]\n"
"v_exp_f32 v147, v147\n"
"v_mfma_f32_16x16x32_bf16 v[184:187], v[240:243], a[20:23], v[184:187]\n"
"ds_read_b128 v[240:243], v221 offset:29696\n"
"v_add_f32 v147, 1.0, v147\n"
"v_mfma_f32_16x16x32_bf16 v[184:187], v[244:247], a[28:31], v[184:187]\n"
"s_waitcnt lgkmcnt(3)\n"
"buffer_load_dwordx4 v[52:55], v2, s[48:51], s61 offen offset:2048 sc1\n"
"v_mfma_f32_16x16x32_bf16 v[188:191], v[224:227], a[16:19], v[188:191]\n"
"ds_read_b128 v[244:247], v221 offset:37888\n"
"v_mfma_f32_16x16x32_bf16 v[188:191], v[228:231], a[24:27], v[188:191]\n"
"v_rcp_f32 v144, v144\n"
"v_mfma_f32_16x16x32_bf16 v[192:195], v[224:227], a[20:23], v[192:195]\n"
"ds_read_b128 v[224:227], v221 offset:41984\n"
"v_mul_f32 v128, v128, v144\n"
"v_mfma_f32_16x16x32_bf16 v[192:195], v[228:231], a[28:31], v[192:195]\n"
"s_waitcnt lgkmcnt(3)\n"
"buffer_load_dwordx4 v[60:63], v2, s[48:51], s61 offen offset:3072 sc1\n"
"v_mfma_f32_16x16x32_bf16 v[180:183], v[232:235], a[32:35], v[180:183]\n"
"ds_read_b128 v[228:231], v221 offset:50176\n"
"v_mul_f32 v132, v132, v161\n"
"v_mfma_f32_16x16x32_bf16 v[180:183], v[236:239], a[40:43], v[180:183]\n"
"v_rcp_f32 v145, v145\n"
"v_mfma_f32_16x16x32_bf16 v[184:187], v[232:235], a[36:39], v[184:187]\n"
"ds_read_b128 v[232:235], v221 offset:46080\n"
"v_mul_f32 v129, v129, v145\n"
"v_mfma_f32_16x16x32_bf16 v[184:187], v[236:239], a[44:47], v[184:187]\n"
"s_waitcnt lgkmcnt(3)\n"
"v_rcp_f32 v146, v146\n"
"v_mfma_f32_16x16x32_bf16 v[188:191], v[240:243], a[32:35], v[188:191]\n"
"ds_read_b128 v[236:239], v221 offset:54272\n"
"v_mul_f32 v130, v130, v146\n"
"v_mfma_f32_16x16x32_bf16 v[188:191], v[244:247], a[40:43], v[188:191]\n"
"v_rcp_f32 v147, v147\n"
"v_mfma_f32_16x16x32_bf16 v[192:195], v[240:243], a[36:39], v[192:195]\n"
"ds_read_b128 v[240:243], v221 offset:26624\n"
"v_mul_f32 v131, v131, v147\n"
"v_mfma_f32_16x16x32_bf16 v[192:195], v[244:247], a[44:47], v[192:195]\n"
"s_waitcnt lgkmcnt(3)\n"
"buffer_load_dwordx4 v2, s[48:51], s64 offen sc1 lds\n"
"v_mfma_f32_16x16x32_bf16 v[180:183], v[224:227], a[48:51], v[180:183]\n"
"ds_read_b128 v[244:247], v221 offset:34816\n"
"v_mul_f32 v133, v133, v161\n"
"v_mfma_f32_16x16x32_bf16 v[180:183], v[228:231], a[56:59], v[180:183]\n"
"v_mul_f32 v134, v134, v161\n"
"v_mul_f32 v135, v135, v161\n"
"v_mfma_f32_16x16x32_bf16 v[184:187], v[224:227], a[52:55], v[184:187]\n"
"ds_read_b128 v[224:227], v221 offset:30720\n"
"v_mul_f32 v144, v132, v132\n"
"v_mfma_f32_16x16x32_bf16 v[184:187], v[228:231], a[60:63], v[184:187]\n"
"s_waitcnt lgkmcnt(3)\n"
"buffer_load_dwordx4 v2, s[48:51], s64 offen offset:1024 sc1 lds\n"
"v_mfma_f32_16x16x32_bf16 v[188:191], v[232:235], a[48:51], v[188:191]\n"
"ds_read_b128 v[228:231], v221 offset:38912\n"
"s_add_u32 m0, s84, 20480\n"
"v_mfma_f32_16x16x32_bf16 v[188:191], v[236:239], a[56:59], v[188:191]\n"
"s_add_u32 s64, s81, 39329792\n"
"v_mfma_f32_16x16x32_bf16 v[192:195], v[232:235], a[52:55], v[192:195]\n"
"ds_read_b128 v[232:235], v221 offset:43008\n"
"v_mul_f32 v145, v133, v133\n"
"v_mfma_f32_16x16x32_bf16 v[192:195], v[236:239], a[60:63], v[192:195]\n"
"s_waitcnt lgkmcnt(3)\n"
"v_mul_f32 v146, v134, v134\n"
"v_mul_f32 v147, v135, v135\n"
"v_mfma_f32_16x16x32_bf16 v[180:183], v[240:243], a[64:67], v[180:183]\n"
"ds_read_b128 v[236:239], v221 offset:51200\n"
"v_fma_f32 v144, v144, v162, v163\n"
"v_mfma_f32_16x16x32_bf16 v[180:183], v[244:247], a[72:75], v[180:183]\n"
"buffer_load_dwordx4 v2, s[48:51], s64 offen sc1 lds\n"
"v_mfma_f32_16x16x32_bf16 v[184:187], v[240:243], a[68:71], v[184:187]\n"
"ds_read_b128 v[240:243], v221 offset:47104\n"
"v_fma_f32 v145, v145, v162, v163\n"
"v_mfma_f32_16x16x32_bf16 v[184:187], v[244:247], a[76:79], v[184:187]\n"
"s_waitcnt lgkmcnt(3)\n"
"v_fma_f32 v146, v146, v162, v163\n"
"v_fma_f32 v147, v147, v162, v163\n"
"v_mfma_f32_16x16x32_bf16 v[188:191], v[224:227], a[64:67], v[188:191]\n"
"ds_read_b128 v[244:247], v221 offset:55296\n"
"v_mul_f32 v144, v144, v132\n"
"v_mfma_f32_16x16x32_bf16 v[188:191], v[228:231], a[72:75], v[188:191]\n"
"buffer_load_dwordx4 v2, s[48:51], s64 offen offset:1024 sc1 lds\n"
"v_mfma_f32_16x16x32_bf16 v[192:195], v[224:227], a[68:71], v[192:195]\n"
"ds_read_b128 v[224:227], v221 offset:27648\n"
"s_add_u32 m0, s84, 22528\n"
"v_mfma_f32_16x16x32_bf16 v[192:195], v[228:231], a[76:79], v[192:195]\n"
"s_waitcnt lgkmcnt(3)\n"
"s_add_u32 s64, s81, 41222144\n"
"v_mfma_f32_16x16x32_bf16 v[180:183], v[232:235], a[80:83], v[180:183]\n"
"ds_read_b128 v[228:231], v221 offset:35840\n"
"v_mul_f32 v145, v145, v133\n"
"v_mfma_f32_16x16x32_bf16 v[180:183], v[236:239], a[88:91], v[180:183]\n"
"buffer_load_dwordx4 v2, s[48:51], s64 offen sc1 lds\n"
"v_mfma_f32_16x16x32_bf16 v[184:187], v[232:235], a[84:87], v[184:187]\n"
"ds_read_b128 v[232:235], v221 offset:31744\n"
"v_mul_f32 v146, v146, v134\n"
"v_mfma_f32_16x16x32_bf16 v[184:187], v[236:239], a[92:95], v[184:187]\n"
"s_waitcnt lgkmcnt(3)\n"
"v_exp_f32 v144, v144\n"
"v_mfma_f32_16x16x32_bf16 v[188:191], v[240:243], a[80:83], v[188:191]\n"
"ds_read_b128 v[236:239], v221 offset:39936\n"
"v_mul_f32 v147, v147, v135\n"
"v_mfma_f32_16x16x32_bf16 v[188:191], v[244:247], a[88:91], v[188:191]\n"
"v_exp_f32 v145, v145\n"
"v_mfma_f32_16x16x32_bf16 v[192:195], v[240:243], a[84:87], v[192:195]\n"
"ds_read_b128 v[240:243], v221 offset:44032\n"
"v_add_f32 v144, 1.0, v144\n"
"v_mfma_f32_16x16x32_bf16 v[192:195], v[244:247], a[92:95], v[192:195]\n"
"s_waitcnt lgkmcnt(3)\n"
"buffer_load_dwordx4 v2, s[48:51], s64 offen offset:1024 sc1 lds\n"
"v_mfma_f32_16x16x32_bf16 v[180:183], v[224:227], a[96:99], v[180:183]\n"
"ds_read_b128 v[244:247], v221 offset:52224\n"
"v_add_f32 v145, 1.0, v145\n"
"v_mfma_f32_16x16x32_bf16 v[180:183], v[228:231], a[104:107], v[180:183]\n"
"v_exp_f32 v146, v146\n"
"v_mfma_f32_16x16x32_bf16 v[184:187], v[224:227], a[100:103], v[184:187]\n"
"ds_read_b128 v[224:227], v221 offset:48128\n"
"v_add_f32 v146, 1.0, v146\n"
"v_mfma_f32_16x16x32_bf16 v[184:187], v[228:231], a[108:111], v[184:187]\n"
"s_waitcnt lgkmcnt(3)\n"
"v_exp_f32 v147, v147\n"
"v_mfma_f32_16x16x32_bf16 v[188:191], v[232:235], a[96:99], v[188:191]\n"
"ds_read_b128 v[228:231], v221 offset:56320\n"
"v_add_f32 v147, 1.0, v147\n"
"v_mfma_f32_16x16x32_bf16 v[188:191], v[236:239], a[104:107], v[188:191]\n"
"v_rcp_f32 v144, v144\n"
"v_mfma_f32_16x16x32_bf16 v[192:195], v[232:235], a[100:103], v[192:195]\n"
"s_waitcnt vmcnt(13)\n"
"s_barrier\n"
"ds_read_b128 v[232:235], v222\n"
"v_mfma_f32_16x16x32_bf16 v[192:195], v[236:239], a[108:111], v[192:195]\n"
"s_waitcnt lgkmcnt(3)\n"
"v_rcp_f32 v145, v145\n"
"v_mfma_f32_16x16x32_bf16 v[180:183], v[240:243], a[112:115], v[180:183]\n"
"ds_read_b128 v[236:239], v222 offset:8192\n"
"s_add_u32 m0, s84, 49152\n"
"v_mfma_f32_16x16x32_bf16 v[180:183], v[244:247], a[120:123], v[180:183]\n"
"s_add_u32 s64, s81, 42844160\n"
"v_mfma_f32_16x16x32_bf16 v[184:187], v[240:243], a[116:119], v[184:187]\n"
"ds_read_b128 v[240:243], v222 offset:4096\n"
"v_mul_f32 v132, v132, v144\n"
"v_mfma_f32_16x16x32_bf16 v[184:187], v[244:247], a[124:127], v[184:187]\n"
"s_waitcnt lgkmcnt(3)\n"
"buffer_load_dwordx4 v2, s[48:51], s64 offen sc1 lds\n"
"v_mfma_f32_16x16x32_bf16 v[188:191], v[224:227], a[112:115], v[188:191]\n"
"ds_read_b128 v[244:247], v222 offset:12288\n"
"v_mul_f32 v133, v133, v145\n"
"v_mfma_f32_16x16x32_bf16 v[188:191], v[228:231], a[120:123], v[188:191]\n"
"v_rcp_f32 v146, v146\n"
"v_mfma_f32_16x16x32_bf16 v[192:195], v[224:227], a[116:119], v[192:195]\n"
"ds_read_b128 v[224:227], v222 offset:16384\n"
"v_mul_f32 v134, v134, v146\n"
"v_mfma_f32_16x16x32_bf16 v[192:195], v[228:231], a[124:127], v[192:195]\n"
"s_waitcnt lgkmcnt(3)\n"
"buffer_load_dwordx4 v2, s[48:51], s64 offen offset:1024 sc1 lds\n"
"s_waitcnt vmcnt(10)\n"
"v_mfma_f32_16x16x32_bf16 v[164:167], v[152:155], v[36:39], v[164:167]\n"
"s_add_u32 m0, s84, 51200\n"
"s_add_u32 s64, s81, 44736512\n"
"v_mfma_f32_16x16x32_bf16 v[168:171], v[156:159], v[36:39], v[168:171]\n"
"v_mfma_f32_16x16x32_bf16 v[172:175], v[152:155], v[44:47], v[172:175]\n"
"v_mfma_f32_16x16x32_bf16 v[176:179], v[156:159], v[44:47], v[176:179]\n"
"buffer_load_dwordx4 v2, s[48:51], s64 offen sc1 lds\n"
"v_mfma_f32_16x16x32_bf16 v[180:183], v[232:235], a[128:131], v[180:183]\n"
"ds_read_b128 v[228:231], v222 offset:24576\n"
"v_mul_f32 v136, v136, v160\n"
"v_mfma_f32_16x16x32_bf16 v[180:183], v[236:239], a[136:139], v[180:183]\n"
"v_rcp_f32 v147, v147\n"
"v_mfma_f32_16x16x32_bf16 v[184:187], v[232:235], a[132:135], v[184:187]\n"
"ds_read_b128 v[232:235], v222 offset:20480\n"
"v_mul_f32 v135, v135, v147\n"
"v_mfma_f32_16x16x32_bf16 v[184:187], v[236:239], a[140:143], v[184:187]\n"
"s_waitcnt lgkmcnt(3)\n"
"buffer_load_dwordx4 v2, s[48:51], s64 offen offset:1024 sc1 lds\n"
"LZ7_%=:\n"
"v_add_u32 v11, LZ8_%=-LPC_%=, v0\n"
"s_add_u32 m0, s84, 53248\n"
"s_add_u32 s64, s81, 43114496\n"
"v_min_u32 v11, LEND_%=-LPC_%=-4, v11\n"
"global_load_dword v11, v11, s[70:71]\n"
"v_mfma_f32_16x16x32_bf16 v[188:191], v[240:243], a[128:131], v[188:191]\n"
"ds_read_b128 v[236:239], v222 offset:28672\n"
"v_mul_f32 v137, v137, v160\n"
"v_mfma_f32_16x16x32_bf16 v[188:191], v[244:247], a[136:139], v[188:191]\n"
"v_mul_f32 v138, v138, v160\n"
"v_mul_f32 v139, v139, v160\n"
"v_mfma_f32_16x16x32_bf16 v[192:195], v[240:243], a[132:135], v[192:195]\n"
"ds_read_b128 v[240:243], v222 offset:1024\n"
"v_fma_f32 v164, v164, v88, 0\n"
"v_mfma_f32_16x16x32_bf16 v[192:195], v[244:247], a[140:143], v[192:195]\n"
"s_waitcnt lgkmcnt(3)\n"
"v_fma_f32 v165, v165, v89, 0\n"
"v_fma_f32 v166, v166, v90, 0\n"
"v_mfma_f32_16x16x32_bf16 v[180:183], v[224:227], a[144:147], v[180:183]\n"
"ds_read_b128 v[244:247], v222 offset:9216\n"
"v_fma_f32 v167, v167, v91, 0\n"
"v_mfma_f32_16x16x32_bf16 v[180:183], v[228:231], a[152:155], v[180:183]\n"
"v_cvt_pk_bf16_f32 v32, v164, v165\n"
"v_cvt_pk_bf16_f32 v33, v166, v167\n"
"v_mfma_f32_16x16x32_bf16 v[184:187], v[224:227], a[148:151], v[184:187]\n"
"ds_read_b128 v[224:227], v222 offset:5120\n"
"v_fma_f32 v172, v172, v96, 0\n"
"v_mfma_f32_16x16x32_bf16 v[184:187], v[228:231], a[156:159], v[184:187]\n"
"s_waitcnt lgkmcnt(3)\n"
"v_fma_f32 v173, v173, v97, 0\n"
"v_fma_f32 v174, v174, v98, 0\n"
"v_mfma_f32_16x16x32_bf16 v[188:191], v[232:235], a[144:147], v[188:191]\n"
"ds_read_b128 v[228:231], v222 offset:13312\n"
"v_fma_f32 v175, v175, v99, 0\n"
"v_mfma_f32_16x16x32_bf16 v[188:191], v[236:239], a[152:155], v[188:191]\n"
"v_cvt_pk_bf16_f32 v34, v172, v173\n"
"v_cvt_pk_bf16_f32 v35, v174, v175\n"
"v_mfma_f32_16x16x32_bf16 v[192:195], v[232:235], a[148:151], v[192:195]\n"
"ds_read_b128 v[232:235], v222 offset:17408\n"
"v_fma_f32 v168, v168, v92, 0\n"
"v_mfma_f32_16x16x32_bf16 v[192:195], v[236:239], a[156:159], v[192:195]\n"
"s_waitcnt lgkmcnt(3)\n"
"v_fma_f32 v169, v169, v93, 0\n"
"v_fma_f32 v170, v170, v94, 0\n"
"v_mfma_f32_16x16x32_bf16 v[180:183], v[240:243], a[160:163], v[180:183]\n"
"ds_read_b128 v[236:239], v222 offset:25600\n"
"v_fma_f32 v171, v171, v95, 0\n"
"v_mfma_f32_16x16x32_bf16 v[180:183], v[244:247], a[168:171], v[180:183]\n"
"v_cvt_pk_bf16_f32 v40, v168, v169\n"
"v_cvt_pk_bf16_f32 v41, v170, v171\n"
"v_mfma_f32_16x16x32_bf16 v[184:187], v[240:243], a[164:167], v[184:187]\n"
"ds_read_b128 v[240:243], v222 offset:21504\n"
"v_fma_f32 v176, v176, v100, 0\n"
"v_mfma_f32_16x16x32_bf16 v[184:187], v[244:247], a[172:175], v[184:187]\n"
"s_waitcnt lgkmcnt(3)\n"
"v_fma_f32 v177, v177, v101, 0\n"
"v_fma_f32 v178, v178, v102, 0\n"
"v_mfma_f32_16x16x32_bf16 v[188:191], v[224:227], a[160:163], v[188:191]\n"
"ds_read_b128 v[244:247], v222 offset:29696\n"
"s_add_u32 s61, s91, 14680064\n"
"v_mfma_f32_16x16x32_bf16 v[188:191], v[228:231], a[168:171], v[188:191]\n"
"buffer_store_dwordx4 v[32:35], v2, s[48:51], s61 offen sc1\n"
"v_mfma_f32_16x16x32_bf16 v[192:195], v[224:227], a[164:167], v[192:195]\n"
"ds_read_b128 v[224:227], v222 offset:2048\n"
"v_fma_f32 v179, v179, v103, 0\n"
"v_mfma_f32_16x16x32_bf16 v[192:195], v[228:231], a[172:175], v[192:195]\n"
"s_waitcnt lgkmcnt(3)\n"
"v_cvt_pk_bf16_f32 v42, v176, v177\n"
"v_cvt_pk_bf16_f32 v43, v178, v179\n"
"v_mfma_f32_16x16x32_bf16 v[180:183], v[232:235], a[176:179], v[180:183]\n"
"ds_read_b128 v[228:231], v222 offset:10240\n"
"v_mul_f32 v144, v136, v136\n"
"v_mfma_f32_16x16x32_bf16 v[180:183], v[236:239], a[184:187], v[180:183]\n"
"buffer_load_dwordx4 v2, s[48:51], s64 offen sc1 lds\n"
"v_mfma_f32_16x16x32_bf16 v[184:187], v[232:235], a[180:183], v[184:187]\n"
"ds_read_b128 v[232:235], v222 offset:6144\n"
"v_mul_f32 v145, v137, v137\n"
"v_mfma_f32_16x16x32_bf16 v[184:187], v[236:239], a[188:191], v[184:187]\n"
"s_waitcnt lgkmcnt(3)\n"
"v_mul_f32 v146, v138, v138\n"
"v_mul_f32 v147, v139, v139\n"
"v_mfma_f32_16x16x32_bf16 v[188:191], v[240:243], a[176:179], v[188:191]\n"
"ds_read_b128 v[236:239], v222 offset:14336\n"
"v_fma_f32 v144, v144, v162, v163\n"
"v_mfma_f32_16x16x32_bf16 v[188:191], v[244:247], a[184:187], v[188:191]\n"
"buffer_load_dwordx4 v2, s[48:51], s64 offen offset:1024 sc1 lds\n"
"v_mfma_f32_16x16x32_bf16 v[192:195], v[240:243], a[180:183], v[192:195]\n"
"ds_read_b128 v[240:243], v222 offset:18432\n"
"s_add_u32 m0, s84, 55296\n"
"v_mfma_f32_16x16x32_bf16 v[192:195], v[244:247], a[188:191], v[192:195]\n"
"s_waitcnt lgkmcnt(3)\n"
"s_add_u32 s64, s81, 45006848\n"
"v_mfma_f32_16x16x32_bf16 v[180:183], v[224:227], a[192:195], v[180:183]\n"
"ds_read_b128 v[244:247], v222 offset:26624\n"
"v_fma_f32 v145, v145, v162, v163\n"
"v_mfma_f32_16x16x32_bf16 v[180:183], v[228:231], a[200:203], v[180:183]\n"
"v_fma_f32 v146, v146, v162, v163\n"
"v_fma_f32 v147, v147, v162, v163\n"
"v_mfma_f32_16x16x32_bf16 v[184:187], v[224:227], a[196:199], v[184:187]\n"
"ds_read_b128 v[224:227], v222 offset:22528\n"
"v_mul_f32 v144, v144, v136\n"
"v_mfma_f32_16x16x32_bf16 v[184:187], v[228:231], a[204:207], v[184:187]\n"
"s_waitcnt lgkmcnt(3)\n"
"buffer_load_dwordx4 v2, s[48:51], s64 offen sc1 lds\n"
"v_mfma_f32_16x16x32_bf16 v[188:191], v[232:235], a[192:195], v[188:191]\n"
"ds_read_b128 v[228:231], v222 offset:30720\n"
"v_mul_f32 v145, v145, v137\n"
"v_mfma_f32_16x16x32_bf16 v[188:191], v[236:239], a[200:203], v[188:191]\n"
"v_exp_f32 v144, v144\n"
"v_mfma_f32_16x16x32_bf16 v[192:195], v[232:235], a[196:199], v[192:195]\n"
"ds_read_b128 v[232:235], v222 offset:3072\n"
"s_add_u32 s61, s91, 14681088\n"
"v_mfma_f32_16x16x32_bf16 v[192:195], v[236:239], a[204:207], v[192:195]\n"
"s_waitcnt lgkmcnt(3)\n"
"buffer_store_dwordx4 v[40:43], v2, s[48:51], s61 offen sc1\n"
"v_mfma_f32_16x16x32_bf16 v[180:183], v[240:243], a[208:211], v[180:183]\n"
"ds_read_b128 v[236:239], v222 offset:11264\n"
"v_mul_f32 v146, v146, v138\n"
"v_mfma_f32_16x16x32_bf16 v[180:183], v[244:247], a[216:219], v[180:183]\n"
"v_exp_f32 v145, v145\n"
"v_mfma_f32_16x16x32_bf16 v[184:187], v[240:243], a[212:215], v[184:187]\n"
"ds_read_b128 v[240:243], v222 offset:7168\n"
"LPG18_%=:\n"
"s_cbranch_execz LWRc_%=\n"
"v_mul_f32 v147, v147, v139\n"
"v_mfma_f32_16x16x32_bf16 v[184:187], v[244:247], a[220:223], v[184:187]\n"
"s_waitcnt lgkmcnt(3)\n"
"buffer_load_dwordx4 v2, s[48:51], s64 offen offset:1024 sc1 lds\n"
"v_mfma_f32_16x16x32_bf16 v[188:191], v[224:227], a[208:211], v[188:191]\n"
"ds_read_b128 v[244:247], v222 offset:15360\n"
"v_add_f32 v144, 1.0, v144\n"
"v_mfma_f32_16x16x32_bf16 v[188:191], v[228:231], a[216:219], v[188:191]\n"
"v_exp_f32 v146, v146\n"
"v_mfma_f32_16x16x32_bf16 v[192:195], v[224:227], a[212:215], v[192:195]\n"
"ds_read_b128 v[224:227], v222 offset:19456\n"
"v_add_f32 v145, 1.0, v145\n"
"v_mfma_f32_16x16x32_bf16 v[192:195], v[228:231], a[220:223], v[192:195]\n"
"s_waitcnt lgkmcnt(3)\n"
"v_exp_f32 v147, v147\n"
"v_mfma_f32_16x16x32_bf16 v[180:183], v[232:235], a[224:227], v[180:183]\n"
"ds_read_b128 v[228:231], v222 offset:27648\n"
"v_add_f32 v146, 1.0, v146\n"
"v_mfma_f32_16x16x32_bf16 v[180:183], v[236:239], a[232:235], v[180:183]\n"
"v_rcp_f32 v144, v144\n"
"v_mfma_f32_16x16x32_bf16 v[184:187], v[232:235], a[228:231], v[184:187]\n"
"ds_read_b128 v[232:235], v222 offset:23552\n"
"v_add_f32 v147, 1.0, v147\n"
"v_mfma_f32_16x16x32_bf16 v[184:187], v[236:239], a[236:239], v[184:187]\n"
"s_waitcnt lgkmcnt(3)\n"
"v_rcp_f32 v145, v145\n"
"v_mfma_f32_16x16x32_bf16 v[188:191], v[240:243], a[224:227], v[188:191]\n"
"ds_read_b128 v[236:239], v222 offset:31744\n"
"v_mul_f32 v136, v136, v144\n"
"v_mfma_f32_16x16x32_bf16 v[188:191], v[244:247], a[232:235], v[188:191]\n"
"v_rcp_f32 v146, v146\n"
"v_mfma_f32_16x16x32_bf16 v[192:195], v[240:243], a[228:231], v[192:195]\n"
"s_waitcnt vmcnt(11)\n"
"s_barrier\n"
"ds_read_b128 v[240:243], v2 offset:16384\n"
"v_mfma_f32_16x16x32_bf16 v[192:195], v[244:247], a[236:239], v[192:195]\n"
"s_waitcnt lgkmcnt(3)\n"
"v_rcp_f32 v147, v147\n"
"s_add_u32 s62, s17, 1572992\n"
"v_mov_b32 v252, 0\n"
"s_mov_b64 exec, 1\n"
"buffer_atomic_or v252, off, s[48:51], s62 sc0 sc1\n"
"s_mov_b64 exec, -1\n"
"v_mfma_f32_16x16x32_bf16 v[180:183], v[224:227], a[240:243], v[180:183]\n"
"ds_read_b128 v[244:247], v2 offset:24576\n"
"s_add_u32 m0, s84, 81920\n"
"v_mfma_f32_16x16x32_bf16 v[180:183], v[228:231], a[248:251], v[180:183]\n"
"s_add_u32 s64, s81, 39600128\n"
"v_mfma_f32_16x16x32_bf16 v[184:187], v[224:227], a[244:247], v[184:187]\n"
"ds_read_b128 v[224:227], v2 offset:20480\n"
"v_mul_f32 v137, v137, v145\n"
"v_mfma_f32_16x16x32_bf16 v[184:187], v[228:231], a[252:255], v[184:187]\n"
"s_waitcnt lgkmcnt(3)\n"
"buffer_load_dwordx4 v2, s[48:51], s64 offen sc1 lds\n"
"v_mfma_f32_16x16x32_bf16 v[188:191], v[232:235], a[240:243], v[188:191]\n"
"ds_read_b128 v[228:231], v2 offset:28672\n"
"v_mul_f32 v138, v138, v146\n"
"v_mfma_f32_16x16x32_bf16 v[188:191], v[236:239], a[248:251], v[188:191]\n"
"v_mul_f32 v139, v139, v147\n"
"v_mul_f32 v140, v140, v161\n"
"v_mfma_f32_16x16x32_bf16 v[192:195], v[232:235], a[244:247], v[192:195]\n"
"ds_read_b128 v[232:235], v2 offset:32768\n"
"v_mul_f32 v141, v141, v161\n"
"v_mfma_f32_16x16x32_bf16 v[192:195], v[236:239], a[252:255], v[192:195]\n"
"s_waitcnt lgkmcnt(3)\n"
"buffer_load_dwordx4 v2, s[48:51], s64 offen offset:1024 sc1 lds\n"
"s_barrier\n"
"v_mfma_f32_16x16x32_bf16 v[180:183], v[152:155], v[52:55], v[180:183]\n"
"s_add_u32 m0, s84, 83968\n"
"s_add_u32 s64, s81, 41492480\n"
"v_mfma_f32_16x16x32_bf16 v[184:187], v[156:159], v[52:55], v[184:187]\n"
"v_mfma_f32_16x16x32_bf16 v[188:191], v[152:155], v[60:63], v[188:191]\n"
"v_mfma_f32_16x16x32_bf16 v[192:195], v[156:159], v[60:63], v[192:195]\n"
"buffer_load_dwordx4 v2, s[48:51], s64 offen sc1 lds\n"
"v_mfma_f32_16x16x32_bf16 v[196:199], v[240:243], a[0:3], v[196:199]\n"
"ds_read_b128 v[236:239], v2 offset:40960\n"
"v_mul_f32 v142, v142, v161\n"
"v_mfma_f32_16x16x32_bf16 v[196:199], v[244:247], a[8:11], v[196:199]\n"
"v_mul_f32 v143, v143, v161\n"
"v_mul_f32 v144, v140, v140\n"
"v_mfma_f32_16x16x32_bf16 v[200:203], v[240:243], a[4:7], v[200:203]\n"
"ds_read_b128 v[240:243], v2 offset:36864\n"
"v_mul_f32 v145, v141, v141\n"
"v_mfma_f32_16x16x32_bf16 v[200:203], v[244:247], a[12:15], v[200:203]\n"
"s_waitcnt lgkmcnt(3)\n"
"buffer_load_dwordx4 v2, s[48:51], s64 offen offset:1024 sc1 lds\n"
"v_mfma_f32_16x16x32_bf16 v[204:207], v[224:227], a[0:3], v[204:207]\n"
"ds_read_b128 v[244:247], v2 offset:45056\n"
"s_add_u32 m0, s84, 86016\n"
"v_mfma_f32_16x16x32_bf16 v[204:207], v[228:231], a[8:11], v[204:207]\n"
"s_add_u32 s64, s81, 43384832\n"
"v_mfma_f32_16x16x32_bf16 v[208:211], v[224:227], a[4:7], v[208:211]\n"
"ds_read_b128 v[224:227], v2 offset:17408\n"
"v_fma_f32 v180, v180, v104, 0\n"
"v_mfma_f32_16x16x32_bf16 v[208:211], v[228:231], a[12:15], v[208:211]\n"
"s_waitcnt lgkmcnt(3)\n"
"v_fma_f32 v181, v181, v105, 0\n"
"v_fma_f32 v182, v182, v106, 0\n"
"v_mfma_f32_16x16x32_bf16 v[196:199], v[232:235], a[16:19], v[196:199]\n"
"ds_read_b128 v[228:231], v2 offset:25600\n"
"v_fma_f32 v183, v183, v107, 0\n"
"v_mfma_f32_16x16x32_bf16 v[196:199], v[236:239], a[24:27], v[196:199]\n"
"v_cvt_pk_bf16_f32 v48, v180, v181\n"
"v_cvt_pk_bf16_f32 v49, v182, v183\n"
"v_mfma_f32_16x16x32_bf16 v[200:203], v[232:235], a[20:23], v[200:203]\n"
"ds_read_b128 v[232:235], v2 offset:21504\n"
"v_fma_f32 v188, v188, v112, 0\n"
"v_mfma_f32_16x16x32_bf16 v[200:203], v[236:239], a[28:31], v[200:203]\n"
"s_waitcnt lgkmcnt(3)\n"
"v_fma_f32 v189, v189, v113, 0\n"
"v_fma_f32 v190, v190, v114, 0\n"
"v_mfma_f32_16x16x32_bf16 v[204:207], v[240:243], a[16:19], v[204:207]\n"
"ds_read_b128 v[236:239], v2 offset:29696\n"
"v_fma_f32 v191, v191, v115, 0\n"
"v_mfma_f32_16x16x32_bf16 v[204:207], v[244:247], a[24:27], v[204:207]\n"
"v_cvt_pk_bf16_f32 v50, v188, v189\n"
"v_cvt_pk_bf16_f32 v51, v190, v191\n"
"v_mfma_f32_16x16x32_bf16 v[208:211], v[240:243], a[20:23], v[208:211]\n"
"ds_read_b128 v[240:243], v2 offset:33792\n"
"v_fma_f32 v184, v184, v108, 0\n"
"v_mfma_f32_16x16x32_bf16 v[208:211], v[244:247], a[28:31], v[208:211]\n"
"s_waitcnt lgkmcnt(3)\n"
"v_fma_f32 v185, v185, v109, 0\n"
"v_fma_f32 v186, v186, v110, 0\n"
"v_mfma_f32_16x16x32_bf16 v[196:199], v[224:227], a[32:35], v[196:199]\n"
"ds_read_b128 v[244:247], v2 offset:41984\n"
"v_fma_f32 v187, v187, v111, 0\n"
"v_mfma_f32_16x16x32_bf16 v[196:199], v[228:231], a[40:43], v[196:199]\n"
"v_cvt_pk_bf16_f32 v56, v184, v185\n"
"v_cvt_pk_bf16_f32 v57, v186, v187\n"
"v_mfma_f32_16x16x32_bf16 v[200:203], v[224:227], a[36:39], v[200:203]\n"
"ds_read_b128 v[224:227], v2 offset:37888\n"
"v_fma_f32 v192, v192, v116, 0\n"
"v_mfma_f32_16x16x32_bf16 v[200:203], v[228:231], a[44:47], v[200:203]\n"
"s_waitcnt lgkmcnt(3)\n"
"v_fma_f32 v193, v193, v117, 0\n"
"v_fma_f32 v194, v194, v118, 0\n"
"v_mfma_f32_16x16x32_bf16 v[204:207], v[232:235], a[32:35], v[204:207]\n"
"ds_read_b128 v[228:231], v2 offset:46080\n"
"s_add_u32 s61, s91, 14811136\n"
"v_mfma_f32_16x16x32_bf16 v[204:207], v[236:239], a[40:43], v[204:207]\n"
"buffer_store_dwordx4 v[48:51], v2, s[48:51], s61 offen sc1\n"
"v_mfma_f32_16x16x32_bf16 v[208:211], v[232:235], a[36:39], v[208:211]\n"
"ds_read_b128 v[232:235], v2 offset:18432\n"
"v_fma_f32 v195, v195, v119, 0\n"
"v_mfma_f32_16x16x32_bf16 v[208:211], v[236:239], a[44:47], v[208:211]\n"
"s_waitcnt lgkmcnt(3)\n"
"v_cvt_pk_bf16_f32 v58, v192, v193\n"
"v_cvt_pk_bf16_f32 v59, v194, v195\n"
"v_mfma_f32_16x16x32_bf16 v[196:199], v[240:243], a[48:51], v[196:199]\n"
"ds_read_b128 v[236:239], v2 offset:26624\n"
"v_mul_f32 v146, v142, v142\n"
"v_mfma_f32_16x16x32_bf16 v[196:199], v[244:247], a[56:59], v[196:199]\n"
"buffer_load_dwordx4 v2, s[48:51], s64 offen sc1 lds\n"
"v_mfma_f32_16x16x32_bf16 v[200:203], v[240:243], a[52:55], v[200:203]\n"
"ds_read_b128 v[240:243], v2 offset:22528\n"
"v_mul_f32 v147, v143, v143\n"
"v_mfma_f32_16x16x32_bf16 v[200:203], v[244:247], a[60:63], v[200:203]\n"
"s_waitcnt lgkmcnt(3)\n"
"v_fma_f32 v144, v144, v162, v163\n"
"v_fma_f32 v145, v145, v162, v163\n"
"v_mfma_f32_16x16x32_bf16 v[204:207], v[224:227], a[48:51], v[204:207]\n"
"ds_read_b128 v[244:247], v2 offset:30720\n"
"v_fma_f32 v146, v146, v162, v163\n"
"v_mfma_f32_16x16x32_bf16 v[204:207], v[228:231], a[56:59], v[204:207]\n"
"buffer_load_dwordx4 v2, s[48:51], s64 offen offset:1024 sc1 lds\n"
"v_mfma_f32_16x16x32_bf16 v[208:211], v[224:227], a[52:55], v[208:211]\n"
"ds_read_b128 v[224:227], v2 offset:34816\n"
"s_add_u32 m0, s84, 88064\n"
"v_mfma_f32_16x16x32_bf16 v[208:211], v[228:231], a[60:63], v[208:211]\n"
"s_waitcnt lgkmcnt(3)\n"
"s_add_u32 s64, s81, 45277184\n"
"v_mfma_f32_16x16x32_bf16 v[196:199], v[232:235], a[64:67], v[196:199]\n"
"ds_read_b128 v[228:231], v2 offset:43008\n"
"v_fma_f32 v147, v147, v162, v163\n"
"v_mfma_f32_16x16x32_bf16 v[196:199], v[236:239], a[72:75], v[196:199]\n"
"v_mul_f32 v144, v144, v140\n"
"v_mul_f32 v145, v145, v141\n"
"v_mfma_f32_16x16x32_bf16 v[200:203], v[232:235], a[68:71], v[200:203]\n"
"ds_read_b128 v[232:235], v2 offset:38912\n"
"v_mul_f32 v146, v146, v142\n"
"v_mfma_f32_16x16x32_bf16 v[200:203], v[236:239], a[76:79], v[200:203]\n"
"s_waitcnt lgkmcnt(3)\n"
"buffer_load_dwordx4 v2, s[48:51], s64 offen sc1 lds\n"
"v_mfma_f32_16x16x32_bf16 v[204:207], v[240:243], a[64:67], v[204:207]\n"
"ds_read_b128 v[236:239], v2 offset:47104\n"
"v_mul_f32 v147, v147, v143\n"
"v_mfma_f32_16x16x32_bf16 v[204:207], v[244:247], a[72:75], v[204:207]\n"
"v_exp_f32 v144, v144\n"
"v_mfma_f32_16x16x32_bf16 v[208:211], v[240:243], a[68:71], v[208:211]\n"
"ds_read_b128 v[240:243], v2 offset:19456\n"
"s_add_u32 s61, s91, 14812160\n"
"v_mfma_f32_16x16x32_bf16 v[208:211], v[244:247], a[76:79], v[208:211]\n"
"s_waitcnt lgkmcnt(3)\n"
"buffer_store_dwordx4 v[56:59], v2, s[48:51], s61 offen sc1\n"
"v_mfma_f32_16x16x32_bf16 v[196:199], v[224:227], a[80:83], v[196:199]\n"
"ds_read_b128 v[244:247], v2 offset:27648\n"
"v_add_f32 v144, 1.0, v144\n"
"v_mfma_f32_16x16x32_bf16 v[196:199], v[228:231], a[88:91], v[196:199]\n"
"v_exp_f32 v145, v145\n"
"v_mfma_f32_16x16x32_bf16 v[200:203], v[224:227], a[84:87], v[200:203]\n"
"ds_read_b128 v[224:227], v2 offset:23552\n"
"v_add_f32 v145, 1.0, v145\n"
"v_mfma_f32_16x16x32_bf16 v[200:203], v[228:231], a[92:95], v[200:203]\n"
"s_waitcnt lgkmcnt(3)\n"
"buffer_load_dwordx4 v2, s[48:51], s64 offen offset:1024 sc1 lds\n"
"v_mfma_f32_16x16x32_bf16 v[204:207], v[232:235], a[80:83], v[204:207]\n"
"ds_read_b128 v[228:231], v2 offset:31744\n"
"s_add_u32 m0, s55, 114688\n"
"v_mfma_f32_16x16x32_bf16 v[204:207], v[236:239], a[88:91], v[204:207]\n"
"s_add_u32 s64, s89, 65536\n"
"v_mfma_f32_16x16x32_bf16 v[208:211], v[232:235], a[84:87], v[208:211]\n"
"ds_read_b128 v[232:235], v2 offset:35840\n"
"v_mfma_f32_16x16x32_bf16 v[208:211], v[236:239], a[92:95], v[208:211]\n"
"s_waitcnt lgkmcnt(3)\n"
"v_exp_f32 v146, v146\n"
"v_mfma_f32_16x16x32_bf16 v[196:199], v[240:243], a[96:99], v[196:199]\n"
"ds_read_b128 v[236:239], v2 offset:44032\n"
"v_add_f32 v146, 1.0, v146\n"
"v_mfma_f32_16x16x32_bf16 v[196:199], v[244:247], a[104:107], v[196:199]\n"
"buffer_load_dwordx4 v9, s[40:43], s64 offen lds\n"
"s_mov_b64 exec, 1\n"
"s_waitcnt vmcnt(11)\n"
"v_cmp_ne_u32 vcc, 1, v252\n"
"s_mov_b64 exec, -1\n"
"s_nop 3\n"
"s_cbranch_vccz Ld24_%=\n"
"s_mov_b32 s65, 0\n"
"Lp23_%=:\n"
"s_add_u32 s62, s17, 1572992\n"
"v_mov_b32 v252, 0\n"
"s_mov_b64 exec, 1\n"
"buffer_atomic_or v252, off, s[48:51], s62 sc0 sc1\n"
"s_mov_b64 exec, -1\n"
"s_add_u32 s65, s65, 1\n"
"s_cmp_gt_u32 s65, 131072\n"
"s_waitcnt vmcnt(0)\n"
"s_cbranch_scc1 Ld24_%=\n"
"s_mov_b64 exec, 1\n"
"v_cmp_ne_u32 vcc, 1, v252\n"
"s_mov_b64 exec, -1\n"
"s_nop 3\n"
"s_cbranch_vccnz Lp23_%=\n"
"Ld24_%=:\n"
"v_mfma_f32_16x16x32_bf16 v[200:203], v[240:243], a[100:103], v[200:203]\n"
"ds_read_b128 v[240:243], v2 offset:39936\n"
"s_add_u32 m0, s55, 118784\n"
"v_mfma_f32_16x16x32_bf16 v[200:203], v[244:247], a[108:111], v[200:203]\n"
"s_waitcnt lgkmcnt(3)\n"
"s_add_u32 s64, s89, 327680\n"
"v_mfma_f32_16x16x32_bf16 v[204:207], v[224:227], a[96:99], v[204:207]\n"
"ds_read_b128 v[244:247], v2 offset:48128\n"
"s_add_u32 s61, s31, 45551616\n"
"v_mfma_f32_16x16x32_bf16 v[204:207], v[228:231], a[104:107], v[204:207]\n"
"buffer_load_dwordx4 v[68:71], v2, s[48:51], s61 offen sc1\n"
"v_mfma_f32_16x16x32_bf16 v[208:211], v[224:227], a[100:103], v[208:211]\n"
"s_barrier\n"
"ds_read_b128 v[224:227], v2 offset:49152\n"
"v_mfma_f32_16x16x32_bf16 v[208:211], v[228:231], a[108:111], v[208:211]\n"
"s_waitcnt lgkmcnt(3)\n"
"v_exp_f32 v147, v147\n"
"v_mfma_f32_16x16x32_bf16 v[196:199], v[232:235], a[112:115], v[196:199]\n"
"ds_read_b128 v[228:231], v221\n"
"v_add_f32 v147, 1.0, v147\n"
"v_mfma_f32_16x16x32_bf16 v[196:199], v[236:239], a[120:123], v[196:199]\n"
"buffer_load_dwordx4 v[76:79], v2, s[48:51], s61 offen offset:1024 sc1\n"
"v_mfma_f32_16x16x32_bf16 v[200:203], v[232:235], a[116:119], v[200:203]\n"
"ds_read_b128 v[232:235], v2 offset:53248\n"
"v_mfma_f32_16x16x32_bf16 v[200:203], v[236:239], a[124:127], v[200:203]\n"
"s_waitcnt lgkmcnt(3)\n"
"v_rcp_f32 v144, v144\n"
"v_mfma_f32_16x16x32_bf16 v[204:207], v[240:243], a[112:115], v[204:207]\n"
"ds_read_b128 v[236:239], v221 offset:4096\n"
"v_mul_f32 v140, v140, v144\n"
"v_mfma_f32_16x16x32_bf16 v[204:207], v[244:247], a[120:123], v[204:207]\n"
"buffer_load_dwordx4 v[84:87], v2, s[48:51], s61 offen offset:2048 sc1\n"
"v_mfma_f32_16x16x32_bf16 v[208:211], v[240:243], a[116:119], v[208:211]\n"
"ds_read_b128 v[240:243], v221 offset:8192\n"
"v_mfma_f32_16x16x32_bf16 v[208:211], v[244:247], a[124:127], v[208:211]\n"
"s_waitcnt lgkmcnt(3)\n"
"v_rcp_f32 v145, v145\n"
"s_barrier\n"
"v_mfma_f32_16x16x32_bf16 v[196:199], v[224:227], a[128:131], v[196:199]\n"
"ds_read_b128 v[244:247], v221 offset:16384\n"
"v_lshlrev_b32 v88, 16, v16\n"
"v_mfma_f32_16x16x32_bf16 v[196:199], v[228:231], a[136:139], v[196:199]\n"
"v_and_b32 v89, 0xffff0000, v16\n"
"v_lshlrev_b32 v90, 16, v17\n"
"v_mfma_f32_16x16x32_bf16 v[200:203], v[224:227], a[132:135], v[200:203]\n"
"ds_read_b128 v[224:227], v221 offset:12288\n"
"v_and_b32 v91, 0xffff0000, v17\n"
"v_mfma_f32_16x16x32_bf16 v[200:203], v[228:231], a[140:143], v[200:203]\n"
"s_waitcnt lgkmcnt(3)\n"
"buffer_load_dwordx4 v9, s[40:43], s64 offen lds\n"
"v_mfma_f32_16x16x32_bf16 v[204:207], v[232:235], a[128:131], v[204:207]\n"
"ds_read_b128 v[228:231], v221 offset:20480\n"
"s_add_u32 m0, s55, 122880\n"
"v_mfma_f32_16x16x32_bf16 v[204:207], v[236:239], a[136:139], v[204:207]\n"
"s_add_u32 s64, s89, 524288\n"
"v_mfma_f32_16x16x32_bf16 v[208:211], v[232:235], a[132:135], v[208:211]\n"
"ds_read_b128 v[232:235], v2 offset:50176\n"
"v_lshlrev_b32 v92, 16, v24\n"
"v_mfma_f32_16x16x32_bf16 v[208:211], v[236:239], a[140:143], v[208:211]\n"
"s_waitcnt lgkmcnt(3)\n"
"buffer_load_dwordx4 v9, s[40:43], s64 offen lds\n"
"v_mfma_f32_16x16x32_bf16 v[196:199], v[240:243], a[144:147], v[196:199]\n"
"ds_read_b128 v[236:239], v221 offset:1024\n"
"s_add_u32 m0, s55, 126976\n"
"v_mfma_f32_16x16x32_bf16 v[196:199], v[244:247], a[152:155], v[196:199]\n"
"s_add_u32 s64, s89, 786432\n"
"v_mfma_f32_16x16x32_bf16 v[200:203], v[240:243], a[148:151], v[200:203]\n"
"ds_read_b128 v[240:243], v2 offset:54272\n"
"v_and_b32 v93, 0xffff0000, v24\n"
"v_mfma_f32_16x16x32_bf16 v[200:203], v[244:247], a[156:159], v[200:203]\n"
"s_waitcnt lgkmcnt(3)\n"
"v_lshlrev_b32 v94, 16, v25\n"
"v_and_b32 v95, 0xffff0000, v25\n"
"v_mfma_f32_16x16x32_bf16 v[204:207], v[224:227], a[144:147], v[204:207]\n"
"ds_read_b128 v[244:247], v221 offset:5120\n"
"v_lshlrev_b32 v96, 16, v18\n"
"v_mfma_f32_16x16x32_bf16 v[204:207], v[228:231], a[152:155], v[204:207]\n"
"buffer_load_dwordx4 v9, s[40:43], s64 offen lds\n"
"v_mfma_f32_16x16x32_bf16 v[208:211], v[224:227], a[148:151], v[208:211]\n"
"ds_read_b128 v[224:227], v221 offset:9216\n"
"s_add_u32 m0, s55, 131072\n"
"v_mfma_f32_16x16x32_bf16 v[208:211], v[228:231], a[156:159], v[208:211]\n"
"s_waitcnt lgkmcnt(3)\n"
"s_add_u32 s64, s89, 983040\n"
"v_mfma_f32_16x16x32_bf16 v[196:199], v[232:235], a[160:163], v[196:199]\n"
"ds_read_b128 v[228:231], v221 offset:17408\n"
"v_and_b32 v97, 0xffff0000, v18\n"
"v_mfma_f32_16x16x32_bf16 v[196:199], v[236:239], a[168:171], v[196:199]\n"
"buffer_load_dwordx4 v9, s[40:43], s64 offen lds\n"
"s_waitcnt vmcnt(9)\n"
"s_add_u32 s62, s36, 1654784\n"
"s_mov_b64 exec, 2\n"
"buffer_atomic_add v1, off, s[48:51], s62 sc1\n"
"s_mov_b64 exec, -1\n"
"v_mfma_f32_16x16x32_bf16 v[200:203], v[232:235], a[164:167], v[200:203]\n"
"ds_read_b128 v[232:235], v221 offset:13312\n"
"s_add_u32 m0, s55, 135168\n"
"v_mfma_f32_16x16x32_bf16 v[200:203], v[236:239], a[172:175], v[200:203]\n"
"s_waitcnt lgkmcnt(3)\n"
"s_add_u32 s64, s89, 1245184\n"
"v_mfma_f32_16x16x32_bf16 v[204:207], v[240:243], a[160:163], v[204:207]\n"
"ds_read_b128 v[236:239], v221 offset:21504\n"
"v_lshlrev_b32 v98, 16, v19\n"
"v_mfma_f32_16x16x32_bf16 v[204:207], v[244:247], a[168:171], v[204:207]\n"
"buffer_load_dwordx4 v9, s[40:43], s64 offen lds\n"
"v_mfma_f32_16x16x32_bf16 v[208:211], v[240:243], a[164:167], v[208:211]\n"
"ds_read_b128 v[240:243], v2 offset:51200\n"
"s_add_u32 m0, s55, 139264\n"
"v_mfma_f32_16x16x32_bf16 v[208:211], v[244:247], a[172:175], v[208:211]\n"
"s_waitcnt lgkmcnt(3)\n"
"s_add_u32 s64, s89, 1441792\n"
"v_mfma_f32_16x16x32_bf16 v[196:199], v[224:227], a[176:179], v[196:199]\n"
"ds_read_b128 v[244:247], v221 offset:2048\n"
"v_and_b32 v99, 0xffff0000, v19\n"
"v_mfma_f32_16x16x32_bf16 v[196:199], v[228:231], a[184:187], v[196:199]\n"
"v_lshlrev_b32 v100, 16, v26\n"
"v_and_b32 v101, 0xffff0000, v26\n"
"v_mfma_f32_16x16x32_bf16 v[200:203], v[224:227], a[180:183], v[200:203]\n"
"ds_read_b128 v[224:227], v2 offset:55296\n"
"v_lshlrev_b32 v102, 16, v27\n"
"v_mfma_f32_16x16x32_bf16 v[200:203], v[228:231], a[188:191], v[200:203]\n"
"s_waitcnt lgkmcnt(3)\n"
"buffer_load_dwordx4 v9, s[40:43], s64 offen lds\n"
"v_mfma_f32_16x16x32_bf16 v[204:207], v[232:235], a[176:179], v[204:207]\n"
"ds_read_b128 v[228:231], v221 offset:6144\n"
"s_add_u32 m0, s55, 143360\n"
"v_mfma_f32_16x16x32_bf16 v[204:207], v[236:239], a[184:187], v[204:207]\n"
"s_add_u32 s64, s89, 1703936\n"
"v_mfma_f32_16x16x32_bf16 v[208:211], v[232:235], a[180:183], v[208:211]\n"
"ds_read_b128 v[232:235], v221 offset:10240\n"
"v_and_b32 v103, 0xffff0000, v27\n"
"v_mfma_f32_16x16x32_bf16 v[208:211], v[236:239], a[188:191], v[208:211]\n"
"s_waitcnt lgkmcnt(3)\n"
"buffer_load_dwordx4 v9, s[40:43], s64 offen lds\n"
"v_mfma_f32_16x16x32_bf16 v[196:199], v[240:243], a[192:195], v[196:199]\n"
"ds_read_b128 v[236:239], v221 offset:18432\n"
"s_add_u32 m0, s55, 16384\n"
"v_mfma_f32_16x16x32_bf16 v[196:199], v[244:247], a[200:203], v[196:199]\n"
"s_add_u32 s64, s89, 131072\n"
"v_mfma_f32_16x16x32_bf16 v[200:203], v[240:243], a[196:199], v[200:203]\n"
"ds_read_b128 v[240:243], v221 offset:14336\n"
"v_lshlrev_b32 v104, 16, v20\n"
"v_mfma_f32_16x16x32_bf16 v[200:203], v[244:247], a[204:207], v[200:203]\n"
"s_waitcnt lgkmcnt(3)\n"
"v_and_b32 v105, 0xffff0000, v20\n"
"v_lshlrev_b32 v106, 16, v21\n"
"v_mfma_f32_16x16x32_bf16 v[204:207], v[224:227], a[192:195], v[204:207]\n"
"ds_read_b128 v[244:247], v221 offset:22528\n"
"v_and_b32 v107, 0xffff0000, v21\n"
"v_mfma_f32_16x16x32_bf16 v[204:207], v[228:231], a[200:203], v[204:207]\n"
"buffer_load_dwordx4 v9, s[40:43], s64 offen lds\n"
"v_mfma_f32_16x16x32_bf16 v[208:211], v[224:227], a[196:199], v[208:211]\n"
"ds_read_b128 v[224:227], v2 offset:52224\n"
"s_add_u32 m0, s55, 20480\n"
"v_mfma_f32_16x16x32_bf16 v[208:211], v[228:231], a[204:207], v[208:211]\n"
"s_waitcnt lgkmcnt(3)\n"
"s_add_u32 s64, s89, 393216\n"
"v_mfma_f32_16x16x32_bf16 v[196:199], v[232:235], a[208:211], v[196:199]\n"
"ds_read_b128 v[228:231], v221 offset:3072\n"
"v_lshlrev_b32 v108, 16, v28\n"
"v_mfma_f32_16x16x32_bf16 v[196:199], v[236:239], a[216:219], v[196:199]\n"
"buffer_load_dwordx4 v9, s[40:43], s64 offen lds\n"
"v_mfma_f32_16x16x32_bf16 v[200:203], v[232:235], a[212:215], v[200:203]\n"
"ds_read_b128 v[232:235], v2 offset:56320\n"
"s_add_u32 m0, s55, 24576\n"
"v_mfma_f32_16x16x32_bf16 v[200:203], v[236:239], a[220:223], v[200:203]\n"
"s_waitcnt lgkmcnt(3)\n"
"s_add_u32 s64, s89, 589824\n"
"v_mfma_f32_16x16x32_bf16 v[204:207], v[240:243], a[208:211], v[204:207]\n"
"ds_read_b128 v[236:239], v221 offset:7168\n"
"v_and_b32 v109, 0xffff0000, v28\n"
"v_mfma_f32_16x16x32_bf16 v[204:207], v[244:247], a[216:219], v[204:207]\n"
"buffer_load_dwordx4 v9, s[40:43], s64 offen lds\n"
"v_mfma_f32_16x16x32_bf16 v[208:211], v[240:243], a[212:215], v[208:211]\n"
"ds_read_b128 v[240:243], v221 offset:11264\n"
"s_add_u32 m0, s55, 28672\n"
"v_mfma_f32_16x16x32_bf16 v[208:211], v[244:247], a[220:223], v[208:211]\n"
"s_waitcnt lgkmcnt(3)\n"
"s_add_u32 s64, s89, 851968\n"
"v_mfma_f32_16x16x32_bf16 v[196:199], v[224:227], a[224:227], v[196:199]\n"
"ds_read_b128 v[244:247], v221 offset:19456\n"
"v_lshlrev_b32 v110, 16, v29\n"
"v_mfma_f32_16x16x32_bf16 v[196:199], v[228:231], a[232:235], v[196:199]\n"
"v_and_b32 v111, 0xffff0000, v29\n"
"v_lshlrev_b32 v112, 16, v22\n"
"v_mfma_f32_16x16x32_bf16 v[200:203], v[224:227], a[228:231], v[200:203]\n"
"ds_read_b128 v[224:227], v221 offset:15360\n"
"v_and_b32 v113, 0xffff0000, v22\n"
"v_mfma_f32_16x16x32_bf16 v[200:203], v[228:231], a[236:239], v[200:203]\n"
"s_waitcnt lgkmcnt(3)\n"
"buffer_load_dwordx4 v9, s[40:43], s64 offen lds\n"
"v_mfma_f32_16x16x32_bf16 v[204:207], v[232:235], a[224:227], v[204:207]\n"
"ds_read_b128 v[228:231], v221 offset:23552\n"
"s_add_u32 m0, s55, 32768\n"
"v_mfma_f32_16x16x32_bf16 v[204:207], v[236:239], a[232:235], v[204:207]\n"
"s_add_u32 s64, s89, 1048576\n"
"v_mfma_f32_16x16x32_bf16 v[208:211], v[232:235], a[228:231], v[208:211]\n"
"s_waitcnt vmcnt(16)\n"
"s_barrier\n"
"ds_read_b128 v[232:235], v221 offset:24576\n"
"v_mfma_f32_16x16x32_bf16 v[208:211], v[236:239], a[236:239], v[208:211]\n"
"s_waitcnt lgkmcnt(3)\n"
"buffer_load_dwordx4 v9, s[40:43], s64 offen lds\n"
"v_mfma_f32_16x16x32_bf16 v[196:199], v[240:243], a[240:243], v[196:199]\n"
"ds_read_b128 v[236:239], v221 offset:32768\n"
"s_add_u32 m0, s55, 36864\n"
"v_mfma_f32_16x16x32_bf16 v[196:199], v[244:247], a[248:251], v[196:199]\n"
"s_add_u32 s64, s89, 1310720\n"
"v_mfma_f32_16x16x32_bf16 v[200:203], v[240:243], a[244:247], v[200:203]\n"
"ds_read_b128 v[240:243], v221 offset:40960\n"
"v_lshlrev_b32 v114, 16, v23\n"
"v_mfma_f32_16x16x32_bf16 v[200:203], v[244:247], a[252:255], v[200:203]\n"
"s_waitcnt lgkmcnt(3)\n"
"buffer_load_dwordx4 v9, s[40:43], s64 offen lds\n"
"v_mfma_f32_16x16x32_bf16 v[204:207], v[224:227], a[240:243], v[204:207]\n"
"ds_read_b128 v[244:247], v221 offset:49152\n"
"s_add_u32 m0, s55, 40960\n"
"v_mfma_f32_16x16x32_bf16 v[204:207], v[228:231], a[248:251], v[204:207]\n"
"s_add_u32 s64, s89, 1507328\n"
"v_mfma_f32_16x16x32_bf16 v[208:211], v[224:227], a[244:247], v[208:211]\n"
"ds_read_b128 v[224:227], v221 offset:25600\n"
"v_and_b32 v115, 0xffff0000, v23\n"
"v_mfma_f32_16x16x32_bf16 v[208:211], v[228:231], a[252:255], v[208:211]\n"
"s_waitcnt lgkmcnt(3)\n"
"v_lshlrev_b32 v116, 16, v30\n"
"v_and_b32 v117, 0xffff0000, v30\n"
"s_barrier\n"
"s_waitcnt vmcnt(15)\n"
"v_mfma_f32_16x16x32_bf16 v[196:199], v[152:155], v[68:71], v[196:199]\n"
"buffer_load_dwordx4 v9, s[40:43], s64 offen lds\n"
"v_mfma_f32_16x16x32_bf16 v[200:203], v[156:159], v[68:71], v[200:203]\n"
"s_add_u32 m0, s55, 45056\n"
"s_add_u32 s64, s89, 1769472\n"
"v_mfma_f32_16x16x32_bf16 v[204:207], v[152:155], v[76:79], v[204:207]\n"
"v_mfma_f32_16x16x32_bf16 v[208:211], v[156:159], v[76:79], v[208:211]\n"
"v_mfma_f32_16x16x32_bf16 v[212:215], v[232:235], a[0:3], v[212:215]\n"
"ds_read_b128 v[228:231], v221 offset:33792\n"
"v_lshlrev_b32 v118, 16, v31\n"
"v_mfma_f32_16x16x32_bf16 v[212:215], v[236:239], a[8:11], v[212:215]\n"
"buffer_load_dwordx4 v9, s[40:43], s64 offen lds\n"
"v_mfma_f32_16x16x32_bf16 v[216:219], v[232:235], a[4:7], v[216:219]\n"
"ds_read_b128 v[232:235], v221 offset:41984\n"
"s_add_u32 m0, s55, 49152\n"
"v_mfma_f32_16x16x32_bf16 v[216:219], v[236:239], a[12:15], v[216:219]\n"
"s_waitcnt lgkmcnt(3)\n"
"s_add_u32 s64, s90, 0\n"
"v_mfma_f32_16x16x32_bf16 v[212:215], v[240:243], a[16:19], v[212:215]\n"
"ds_read_b128 v[236:239], v221 offset:50176\n"
"v_and_b32 v119, 0xffff0000, v31\n"
"v_mfma_f32_16x16x32_bf16 v[212:215], v[244:247], a[24:27], v[212:215]\n"
"buffer_load_dwordx4 v9, s[40:43], s64 offen lds\n"
"v_mfma_f32_16x16x32_bf16 v[216:219], v[240:243], a[20:23], v[216:219]\n"
"ds_read_b128 v[240:243], v221 offset:26624\n"
"v_fma_f32 v196, v196, v120, 0\n"
"v_mfma_f32_16x16x32_bf16 v[216:219], v[244:247], a[28:31], v[216:219]\n"
"s_waitcnt lgkmcnt(3)\n"
"v_fma_f32 v197, v197, v121, 0\n"
"v_fma_f32 v198, v198, v122, 0\n"
"v_mfma_f32_16x16x32_bf16 v[212:215], v[224:227], a[32:35], v[212:215]\n"
"ds_read_b128 v[244:247], v221 offset:34816\n"
"v_fma_f32 v199, v199, v123, 0\n"
"v_mfma_f32_16x16x32_bf16 v[212:215], v[228:231], a[40:43], v[212:215]\n"
"v_cvt_pk_bf16_f32 v64, v196, v197\n"
"v_cvt_pk_bf16_f32 v65, v198, v199\n"
"v_mfma_f32_16x16x32_bf16 v[216:219], v[224:227], a[36:39], v[216:219]\n"
"ds_read_b128 v[224:227], v221 offset:43008\n"
"v_fma_f32 v204, v204, v128, 0\n"
"v_mfma_f32_16x16x32_bf16 v[216:219], v[228:231], a[44:47], v[216:219]\n"
"s_waitcnt lgkmcnt(3)\n"
"v_fma_f32 v205, v205, v129, 0\n"
"v_fma_f32 v206, v206, v130, 0\n"
"s_add_u32 s62, s37, 1654784\n"
"v_mov_b32 v252, 0\n"
"s_mov_b64 exec, 1\n"
"buffer_atomic_or v252, off, s[48:51], s62 sc0 sc1\n"
"s_mov_b64 exec, -1\n"
"v_mfma_f32_16x16x32_bf16 v[212:215], v[232:235], a[48:51], v[212:215]\n"
"ds_read_b128 v[228:231], v221 offset:51200\n"
"v_fma_f32 v207, v207, v131, 0\n"
"v_mfma_f32_16x16x32_bf16 v[212:215], v[236:239], a[56:59], v[212:215]\n"
"v_cvt_pk_bf16_f32 v66, v204, v205\n"
"v_cvt_pk_bf16_f32 v67, v206, v207\n"
"v_mfma_f32_16x16x32_bf16 v[216:219], v[232:235], a[52:55], v[216:219]\n"
"ds_read_b128 v[232:235], v221 offset:27648\n"
"v_fma_f32 v200, v200, v124, 0\n"
"v_mfma_f32_16x16x32_bf16 v[216:219], v[236:239], a[60:63], v[216:219]\n"
"s_waitcnt lgkmcnt(3)\n"
"v_fma_f32 v201, v201, v125, 0\n"
"v_fma_f32 v202, v202, v126, 0\n"
"v_mfma_f32_16x16x32_bf16 v[212:215], v[240:243], a[64:67], v[212:215]\n"
"ds_read_b128 v[236:239], v221 offset:35840\n"
"v_fma_f32 v203, v203, v127, 0\n"
"v_mfma_f32_16x16x32_bf16 v[212:215], v[244:247], a[72:75], v[212:215]\n"
"v_cvt_pk_bf16_f32 v72, v200, v201\n"
"v_cvt_pk_bf16_f32 v73, v202, v203\n"
"v_mfma_f32_16x16x32_bf16 v[216:219], v[240:243], a[68:71], v[216:219]\n"
"ds_read_b128 v[240:243], v221 offset:44032\n"
"v_fma_f32 v208, v208, v132, 0\n"
"v_mfma_f32_16x16x32_bf16 v[216:219], v[244:247], a[76:79], v[216:219]\n"
"s_waitcnt lgkmcnt(3)\n"
"v_fma_f32 v209, v209, v133, 0\n"
"v_fma_f32 v210, v210, v134, 0\n"
"v_mfma_f32_16x16x32_bf16 v[212:215], v[224:227], a[80:83], v[212:215]\n"
"ds_read_b128 v[244:247], v221 offset:52224\n"
"s_add_u32 s61, s91, 14942208\n"
"v_mfma_f32_16x16x32_bf16 v[212:215], v[228:231], a[88:91], v[212:215]\n"
"buffer_store_dwordx4 v[64:67], v2, s[48:51], s61 offen sc1\n"
"v_mfma_f32_16x16x32_bf16 v[216:219], v[224:227], a[84:87], v[216:219]\n"
"ds_read_b128 v[224:227], v221 offset:28672\n"
"v_fma_f32 v211, v211, v135, 0\n"
"v_mfma_f32_16x16x32_bf16 v[216:219], v[228:231], a[92:95], v[216:219]\n"
"s_waitcnt lgkmcnt(3)\n"
"v_cvt_pk_bf16_f32 v74, v208, v209\n"
"v_cvt_pk_bf16_f32 v75, v210, v211\n"
"v_mfma_f32_16x16x32_bf16 v[212:215], v[232:235], a[96:99], v[212:215]\n"
"ds_read_b128 v[228:231], v221 offset:36864\n"
"s_add_u32 m0, s55, 53248\n"
"v_mfma_f32_16x16x32_bf16 v[212:215], v[236:239], a[104:107], v[212:215]\n"
"s_add_u32 s64, s90, 262144\n"
"v_mfma_f32_16x16x32_bf16 v[216:219], v[232:235], a[100:103], v[216:219]\n"
"ds_read_b128 v[232:235], v221 offset:45056\n"
"v_mul_f32 v141, v141, v145\n"
"v_mfma_f32_16x16x32_bf16 v[216:219], v[236:239], a[108:111], v[216:219]\n"
"s_waitcnt lgkmcnt(3)\n"
"buffer_load_dwordx4 v9, s[40:43], s64 offen lds\n"
"v_mfma_f32_16x16x32_bf16 v[212:215], v[240:243], a[112:115], v[212:215]\n"
"ds_read_b128 v[236:239], v221 offset:53248\n"
"s_add_u32 m0, s55, 57344\n"
"v_mfma_f32_16x16x32_bf16 v[212:215], v[244:247], a[120:123], v[212:215]\n"
"s_add_u32 s64, s90, 458752\n"
"v_mfma_f32_16x16x32_bf16 v[216:219], v[240:243], a[116:119], v[216:219]\n"
"ds_read_b128 v[240:243], v221 offset:29696\n"
"v_mfma_f32_16x16x32_bf16 v[216:219], v[244:247], a[124:127], v[216:219]\n"
"s_waitcnt lgkmcnt(3)\n"
"buffer_load_dwordx4 v9, s[40:43], s64 offen lds\n"
"v_mfma_f32_16x16x32_bf16 v[212:215], v[224:227], a[128:131], v[212:215]\n"
"ds_read_b128 v[244:247], v221 offset:37888\n"
"s_add_u32 m0, s55, 61440\n"
"LPG19_%=:\n"
"s_cbranch_execz LWRc_%=\n"
"v_mfma_f32_16x16x32_bf16 v[212:215], v[228:231], a[136:139], v[212:215]\n"
"s_add_u32 s64, s90, 720896\n"
"v_mfma_f32_16x16x32_bf16 v[216:219], v[224:227], a[132:135], v[216:219]\n"
"ds_read_b128 v[224:227], v221 offset:46080\n"
"v_mfma_f32_16x16x32_bf16 v[216:219], v[228:231], a[140:143], v[216:219]\n"
"s_waitcnt lgkmcnt(3)\n"
"buffer_load_dwordx4 v9, s[40:43], s64 offen lds\n"
"LZ8_%=:\n"
"v_add_u32 v11, LZ9_%=-LPC_%=, v0\n"
"s_add_u32 m0, s55, 65536\n"
"s_add_u32 s64, s90, 917504\n"
"v_min_u32 v11, LEND_%=-LPC_%=-4, v11\n"
"global_load_dword v11, v11, s[70:71]\n"
"v_mfma_f32_16x16x32_bf16 v[212:215], v[232:235], a[144:147], v[212:215]\n"
"ds_read_b128 v[228:231], v221 offset:54272\n"
"v_mfma_f32_16x16x32_bf16 v[212:215], v[236:239], a[152:155], v[212:215]\n"
"v_rcp_f32 v146, v146\n"
"v_mfma_f32_16x16x32_bf16 v[216:219], v[232:235], a[148:151], v[216:219]\n"
"ds_read_b128 v[232:235], v221 offset:30720\n"
"s_add_u32 s61, s91, 14943232\n"
"v_mfma_f32_16x16x32_bf16 v[216:219], v[236:239], a[156:159], v[216:219]\n"
"s_waitcnt lgkmcnt(3)\n"
"buffer_store_dwordx4 v[72:75], v2, s[48:51], s61 offen sc1\n"
"v_mfma_f32_16x16x32_bf16 v[212:215], v[240:243], a[160:163], v[212:215]\n"
"ds_read_b128 v[236:239], v221 offset:38912\n"
"v_mul_f32 v142, v142, v146\n"
"v_mfma_f32_16x16x32_bf16 v[212:215], v[244:247], a[168:171], v[212:215]\n"
"v_rcp_f32 v147, v147\n"
"v_mfma_f32_16x16x32_bf16 v[216:219], v[240:243], a[164:167], v[216:219]\n"
"ds_read_b128 v[240:243], v221 offset:47104\n"
"v_mul_f32 v143, v143, v147\n"
"v_mfma_f32_16x16x32_bf16 v[216:219], v[244:247], a[172:175], v[216:219]\n"
"s_waitcnt lgkmcnt(3)\n"
"v_mfma_f32_16x16x32_bf16 v[212:215], v[224:227], a[176:179], v[212:215]\n"
"ds_read_b128 v[244:247], v221 offset:55296\n"
"v_mfma_f32_16x16x32_bf16 v[212:215], v[228:231], a[184:187], v[212:215]\n"
"buffer_load_dwordx4 v9, s[40:43], s64 offen lds\n"
"v_mfma_f32_16x16x32_bf16 v[216:219], v[224:227], a[180:183], v[216:219]\n"
"ds_read_b128 v[224:227], v221 offset:31744\n"
"s_add_u32 m0, s55, 69632\n"
"v_mfma_f32_16x16x32_bf16 v[216:219], v[228:231], a[188:191], v[216:219]\n"
"s_waitcnt lgkmcnt(3)\n"
"s_add_u32 s64, s90, 1179648\n"
"s_mov_b64 exec, 1\n"
"s_waitcnt vmcnt(7)\n"
"v_cmp_ne_u32 vcc, 8, v252\n"
"s_mov_b64 exec, -1\n"
"s_nop 3\n"
"s_cbranch_vccz Ld26_%=\n"
"s_mov_b32 s65, 0\n"
"Lp25_%=:\n"
"s_add_u32 s62, s37, 1654784\n"
"v_mov_b32 v252, 0\n"
"s_mov_b64 exec, 1\n"
"buffer_atomic_or v252, off, s[48:51], s62 sc0 sc1\n"
"s_mov_b64 exec, -1\n"
"s_add_u32 s65, s65, 1\n"
"s_cmp_gt_u32 s65, 131072\n"
"s_waitcnt vmcnt(0)\n"
"s_cbranch_scc1 Ld26_%=\n"
"s_mov_b64 exec, 1\n"
"v_cmp_ne_u32 vcc, 8, v252\n"
"s_mov_b64 exec, -1\n"
"s_nop 3\n"
"s_cbranch_vccnz Lp25_%=\n"
"Ld26_%=:\n"
"v_mfma_f32_16x16x32_bf16 v[212:215], v[232:235], a[192:195], v[212:215]\n"
"ds_read_b128 v[228:231], v221 offset:39936\n"
"v_mfma_f32_16x16x32_bf16 v[212:215], v[236:239], a[200:203], v[212:215]\n"
"buffer_load_dwordx4 v9, s[40:43], s64 offen lds\n"
"v_mfma_f32_16x16x32_bf16 v[216:219], v[232:235], a[196:199], v[216:219]\n"
"ds_read_b128 v[232:235], v221 offset:48128\n"
"s_add_u32 s63, s29, 14680064\n"
"v_mfma_f32_16x16x32_bf16 v[216:219], v[236:239], a[204:207], v[216:219]\n"
"s_waitcnt lgkmcnt(3)\n"
"buffer_load_dwordx4 a[0:3], v2, s[48:51], s63 offen sc1\n"
"v_mfma_f32_16x16x32_bf16 v[212:215], v[240:243], a[208:211], v[212:215]\n"
"ds_read_b128 v[236:239], v221 offset:56320\n"
"s_add_u32 m0, s55, 73728\n"
"v_mfma_f32_16x16x32_bf16 v[212:215], v[244:247], a[216:219], v[212:215]\n"
"buffer_load_dwordx4 a[4:7], v2, s[48:51], s63 offen offset:1024 sc1\n"
"v_mfma_f32_16x16x32_bf16 v[216:219], v[240:243], a[212:215], v[216:219]\n"
"s_barrier\n"
"ds_read_b64_tr_b16 v[240:241], v5 offset:45056\n"
"v_mfma_f32_16x16x32_bf16 v[216:219], v[244:247], a[220:223], v[216:219]\n"
"ds_read_b64_tr_b16 v[242:243], v5 offset:45568\n"
"s_waitcnt lgkmcnt(4)\n"
"v_mfma_f32_16x16x32_bf16 v[212:215], v[224:227], a[224:227], v[212:215]\n"
"ds_read_b64_tr_b16 v[244:245], v6 offset:45056\n"
"s_add_u32 s63, s29, 15204352\n"
"s_add_u32 s64, s90, 1376256\n"
"v_mfma_f32_16x16x32_bf16 v[212:215], v[228:231], a[232:235], v[212:215]\n"
"ds_read_b64_tr_b16 v[246:247], v6 offset:45568\n"
"v_mfma_f32_16x16x32_bf16 v[216:219], v[224:227], a[228:231], v[216:219]\n"
"ds_read_b64_tr_b16 v[224:225], v7 offset:45056\n"
"buffer_load_dwordx4 a[8:11], v2, s[48:51], s63 offen sc1\n"
"v_mfma_f32_16x16x32_bf16 v[216:219], v[228:231], a[236:239], v[216:219]\n"
"ds_read_b64_tr_b16 v[226:227], v7 offset:45568\n"
"s_waitcnt lgkmcnt(6)\n"
"v_mfma_f32_16x16x32_bf16 v[212:215], v[232:235], a[240:243], v[212:215]\n"
"ds_read_b64_tr_b16 v[228:229], v8 offset:45056\n"
"buffer_load_dwordx4 a[12:15], v2, s[48:51], s63 offen offset:1024 sc1\n"
"v_mfma_f32_16x16x32_bf16 v[212:215], v[236:239], a[248:251], v[212:215]\n"
"ds_read_b64_tr_b16 v[230:231], v8 offset:45568\n"
"s_add_u32 s63, s29, 15597568\n"
"v_mfma_f32_16x16x32_bf16 v[216:219], v[232:235], a[244:247], v[216:219]\n"
"ds_read_b64_tr_b16 v[232:233], v5 offset:49152\n"
"buffer_load_dwordx4 a[16:19], v2, s[48:51], s63 offen sc1\n"
"v_mfma_f32_16x16x32_bf16 v[216:219], v[236:239], a[252:255], v[216:219]\n"
"ds_read_b64_tr_b16 v[234:235], v5 offset:49664\n"
"s_waitcnt vmcnt(3) lgkmcnt(6)\n"
"s_barrier\n"
"LP6_%=:\n"
"v_mfma_f32_16x16x32_bf16 v[212:215], v[152:155], v[84:87], v[212:215]\n"
"buffer_load_dwordx4 a[20:23], v2, s[48:51], s63 offen offset:1024 sc1\n"
"v_mfma_f32_16x16x32_bf16 v[216:219], v[156:159], v[84:87], v[216:219]\n"
"s_add_u32 s63, s29, 16121856\n"
"v_mfma_f32_16x16x32_bf16 v[164:167], v[240:243], a[0:3], 0\n"
"ds_read_b64_tr_b16 v[236:237], v6 offset:49152\n"
"buffer_load_dwordx4 a[24:27], v2, s[48:51], s63 offen sc1\n"
"v_mfma_f32_16x16x32_bf16 v[168:171], v[240:243], a[4:7], 0\n"
"ds_read_b64_tr_b16 v[238:239], v6 offset:49664\n"
"v_mfma_f32_16x16x32_bf16 v[172:175], v[244:247], a[0:3], 0\n"
"ds_read_b64_tr_b16 v[240:241], v7 offset:49152\n"
"buffer_load_dwordx4 a[28:31], v2, s[48:51], s63 offen offset:1024 sc1\n"
"v_mfma_f32_16x16x32_bf16 v[176:179], v[244:247], a[4:7], 0\n"
"ds_read_b64_tr_b16 v[242:243], v7 offset:49664\n"
"s_waitcnt lgkmcnt(6)\n"
"v_fma_f32 v212, v212, v136, 0\n"
"s_add_u32 s63, s29, 16515072\n"
"v_fma_f32 v213, v213, v137, 0\n"
"buffer_load_dwordx4 a[32:35], v2, s[48:51], s63 offen sc1\n"
"v_fma_f32 v214, v214, v138, 0\n"
"buffer_load_dwordx4 a[36:39], v2, s[48:51], s63 offen offset:1024 sc1\n"
"v_fma_f32 v215, v215, v139, 0\n"
"s_add_u32 s63, s29, 17039360\n"
"v_cvt_pk_bf16_f32 v80, v212, v213\n"
"buffer_load_dwordx4 a[40:43], v2, s[48:51], s63 offen sc1\n"
"v_cvt_pk_bf16_f32 v81, v214, v215\n"
"buffer_load_dwordx4 a[44:47], v2, s[48:51], s63 offen offset:1024 sc1\n"
"v_fma_f32 v216, v216, v140, 0\n"
"s_add_u32 s63, s29, 17432576\n"
"v_fma_f32 v217, v217, v141, 0\n"
"v_fma_f32 v218, v218, v142, 0\n"
"v_fma_f32 v219, v219, v143, 0\n"
"v_cvt_pk_bf16_f32 v82, v216, v217\n"
"v_cvt_pk_bf16_f32 v83, v218, v219\n"
"s_nop 0\n"
"v_permlane16_swap_b32 v80, v82\n"
"v_permlane16_swap_b32 v81, v83\n"
"buffer_store_dwordx4 v[80:83], v249, s[48:51], s92 offen sc1\n"
"v_mfma_f32_16x16x32_bf16 v[180:183], v[224:227], a[0:3], 0\n"
"ds_read_b64_tr_b16 v[244:245], v8 offset:49152\n"
"v_mfma_f32_16x16x32_bf16 v[184:187], v[224:227], a[4:7], 0\n"
"ds_read_b64_tr_b16 v[246:247], v8 offset:49664\n"
"v_mfma_f32_16x16x32_bf16 v[188:191], v[228:231], a[0:3], 0\n"
"ds_read_b64_tr_b16 v[224:225], v5 offset:53248\n"
"buffer_load_dwordx4 a[48:51], v2, s[48:51], s63 offen sc1\n"
"v_mfma_f32_16x16x32_bf16 v[192:195], v[228:231], a[4:7], 0\n"
"ds_read_b64_tr_b16 v[226:227], v5 offset:53760\n"
"s_waitcnt vmcnt(10) lgkmcnt(6)\n"
"v_mfma_f32_16x16x32_bf16 v[164:167], v[232:235], a[8:11], v[164:167]\n"
"ds_read_b64_tr_b16 v[228:229], v6 offset:53248\n"
"v_mfma_f32_16x16x32_bf16 v[168:171], v[232:235], a[12:15], v[168:171]\n"
"ds_read_b64_tr_b16 v[230:231], v6 offset:53760\n"
"v_mfma_f32_16x16x32_bf16 v[172:175], v[236:239], a[8:11], v[172:175]\n"
"ds_read_b64_tr_b16 v[232:233], v7 offset:53248\n"
"buffer_load_dwordx4 a[52:55], v2, s[48:51], s63 offen offset:1024 sc1\n"
"v_mfma_f32_16x16x32_bf16 v[176:179], v[236:239], a[12:15], v[176:179]\n"
"ds_read_b64_tr_b16 v[234:235], v7 offset:53760\n"
"s_waitcnt lgkmcnt(6)\n"
"v_mfma_f32_16x16x32_bf16 v[180:183], v[240:243], a[8:11], v[180:183]\n"
"ds_read_b64_tr_b16 v[236:237], v8 offset:53248\n"
"s_add_u32 s63, s29, 17956864\n"
"v_mfma_f32_16x16x32_bf16 v[184:187], v[240:243], a[12:15], v[184:187]\n"
"ds_read_b64_tr_b16 v[238:239], v8 offset:53760\n"
"v_mfma_f32_16x16x32_bf16 v[188:191], v[244:247], a[8:11], v[188:191]\n"
"ds_read_b64_tr_b16 v[240:241], v5 offset:57344\n"
"buffer_load_dwordx4 a[56:59], v2, s[48:51], s63 offen sc1\n"
"v_mfma_f32_16x16x32_bf16 v[192:195], v[244:247], a[12:15], v[192:195]\n"
"ds_read_b64_tr_b16 v[242:243], v5 offset:57856\n"
"s_waitcnt vmcnt(10) lgkmcnt(6)\n"
"s_add_u32 s62, s36, 1687552\n"
"s_mov_b64 exec, 2\n"
"buffer_atomic_add v1, off, s[48:51], s62 sc1\n"
"s_mov_b64 exec, -1\n"
"v_mfma_f32_16x16x32_bf16 v[164:167], v[224:227], a[16:19], v[164:167]\n"
"ds_read_b64_tr_b16 v[244:245], v6 offset:57344\n"
"v_mfma_f32_16x16x32_bf16 v[168:171], v[224:227], a[20:23], v[168:171]\n"
"ds_read_b64_tr_b16 v[246:247], v6 offset:57856\n"
"v_mfma_f32_16x16x32_bf16 v[172:175], v[228:231], a[16:19], v[172:175]\n"
"ds_read_b64_tr_b16 v[224:225], v7 offset:57344\n"
"buffer_load_dwordx4 a[60:63], v2, s[48:51], s63 offen offset:1024 sc1\n"
"v_mfma_f32_16x16x32_bf16 v[176:179], v[228:231], a[20:23], v[176:179]\n"
"ds_read_b64_tr_b16 v[226:227], v7 offset:57856\n"
"s_waitcnt lgkmcnt(6)\n"
"v_mfma_f32_16x16x32_bf16 v[180:183], v[232:235], a[16:19], v[180:183]\n"
"ds_read_b64_tr_b16 v[228:229], v8 offset:57344\n"
"s_add_u32 s63, s29, 14811136\n"
"v_mfma_f32_16x16x32_bf16 v[184:187], v[232:235], a[20:23], v[184:187]\n"
"ds_read_b64_tr_b16 v[230:231], v8 offset:57856\n"
"v_mfma_f32_16x16x32_bf16 v[188:191], v[236:239], a[16:19], v[188:191]\n"
"v_add_u32 v5, 4294864896, v5\n"
"v_add_u32 v6, 4294864896, v6\n"
"v_add_u32 v7, 4294864896, v7\n"
"v_add_u32 v8, 4294864896, v8\n"
"ds_read_b64_tr_b16 v[232:233], v5\n"
"v_mfma_f32_16x16x32_bf16 v[192:195], v[236:239], a[20:23], v[192:195]\n"
"ds_read_b64_tr_b16 v[234:235], v5 offset:512\n"
"s_waitcnt vmcnt(10) lgkmcnt(6)\n"
"v_mfma_f32_16x16x32_bf16 v[164:167], v[240:243], a[24:27], v[164:167]\n"
"ds_read_b64_tr_b16 v[236:237], v6\n"
"buffer_load_dwordx4 a[64:67], v2, s[48:51], s63 offen sc1\n"
"v_mfma_f32_16x16x32_bf16 v[168:171], v[240:243], a[28:31], v[168:171]\n"
"ds_read_b64_tr_b16 v[238:239], v6 offset:512\n"
"v_mfma_f32_16x16x32_bf16 v[172:175], v[244:247], a[24:27], v[172:175]\n"
"ds_read_b64_tr_b16 v[240:241], v7\n"
"v_mfma_f32_16x16x32_bf16 v[176:179], v[244:247], a[28:31], v[176:179]\n"
"ds_read_b64_tr_b16 v[242:243], v7 offset:512\n"
"s_waitcnt lgkmcnt(6)\n"
"v_mfma_f32_16x16x32_bf16 v[180:183], v[224:227], a[24:27], v[180:183]\n"
"ds_read_b64_tr_b16 v[244:245], v8\n"
"buffer_load_dwordx4 a[68:71], v2, s[48:51], s63 offen offset:1024 sc1\n"
"v_mfma_f32_16x16x32_bf16 v[184:187], v[224:227], a[28:31], v[184:187]\n"
"ds_read_b64_tr_b16 v[246:247], v8 offset:512\n"
"s_add_u32 s63, s29, 15335424\n"
"v_mfma_f32_16x16x32_bf16 v[188:191], v[228:231], a[24:27], v[188:191]\n"
"ds_read_b64_tr_b16 v[224:225], v5 offset:4096\n"
"v_mfma_f32_16x16x32_bf16 v[192:195], v[228:231], a[28:31], v[192:195]\n"
"ds_read_b64_tr_b16 v[226:227], v5 offset:4608\n"
"s_waitcnt vmcnt(10) lgkmcnt(6)\n"
"v_mfma_f32_16x16x32_bf16 v[164:167], v[232:235], a[32:35], v[164:167]\n"
"ds_read_b64_tr_b16 v[228:229], v6 offset:4096\n"
"buffer_load_dwordx4 a[72:75], v2, s[48:51], s63 offen sc1\n"
"v_mfma_f32_16x16x32_bf16 v[168:171], v[232:235], a[36:39], v[168:171]\n"
"ds_read_b64_tr_b16 v[230:231], v6 offset:4608\n"
"v_mfma_f32_16x16x32_bf16 v[172:175], v[236:239], a[32:35], v[172:175]\n"
"ds_read_b64_tr_b16 v[232:233], v7 offset:4096\n"
"v_mfma_f32_16x16x32_bf16 v[176:179], v[236:239], a[36:39], v[176:179]\n"
"ds_read_b64_tr_b16 v[234:235], v7 offset:4608\n"
"s_waitcnt lgkmcnt(6)\n"
"v_mfma_f32_16x16x32_bf16 v[180:183], v[240:243], a[32:35], v[180:183]\n"
"ds_read_b64_tr_b16 v[236:237], v8 offset:4096\n"
"buffer_load_dwordx4 a[76:79], v2, s[48:51], s63 offen offset:1024 sc1\n"
"v_mfma_f32_16x16x32_bf16 v[184:187], v[240:243], a[36:39], v[184:187]\n"
"ds_read_b64_tr_b16 v[238:239], v8 offset:4608\n"
"s_add_u32 s63, s29, 15728640\n"
"v_mfma_f32_16x16x32_bf16 v[188:191], v[244:247], a[32:35], v[188:191]\n"
"ds_read_b64_tr_b16 v[240:241], v5 offset:8192\n"
"v_mfma_f32_16x16x32_bf16 v[192:195], v[244:247], a[36:39], v[192:195]\n"
"ds_read_b64_tr_b16 v[242:243], v5 offset:8704\n"
"s_waitcnt vmcnt(10) lgkmcnt(6)\n"
"v_mfma_f32_16x16x32_bf16 v[164:167], v[224:227], a[40:43], v[164:167]\n"
"ds_read_b64_tr_b16 v[244:245], v6 offset:8192\n"
"buffer_load_dwordx4 a[80:83], v2, s[48:51], s63 offen sc1\n"
"v_mfma_f32_16x16x32_bf16 v[168:171], v[224:227], a[44:47], v[168:171]\n"
"ds_read_b64_tr_b16 v[246:247], v6 offset:8704\n"
"v_mfma_f32_16x16x32_bf16 v[172:175], v[228:231], a[40:43], v[172:175]\n"
"ds_read_b64_tr_b16 v[224:225], v7 offset:8192\n"
"v_mfma_f32_16x16x32_bf16 v[176:179], v[228:231], a[44:47], v[176:179]\n"
"ds_read_b64_tr_b16 v[226:227], v7 offset:8704\n"
"s_waitcnt lgkmcnt(6)\n"
"v_mfma_f32_16x16x32_bf16 v[180:183], v[232:235], a[40:43], v[180:183]\n"
"ds_read_b64_tr_b16 v[228:229], v8 offset:8192\n"
"buffer_load_dwordx4 a[84:87], v2, s[48:51], s63 offen offset:1024 sc1\n"
"v_mfma_f32_16x16x32_bf16 v[184:187], v[232:235], a[44:47], v[184:187]\n"
"ds_read_b64_tr_b16 v[230:231], v8 offset:8704\n"
"s_add_u32 s63, s29, 16252928\n"
"v_mfma_f32_16x16x32_bf16 v[188:191], v[236:239], a[40:43], v[188:191]\n"
"ds_read_b64_tr_b16 v[232:233], v5 offset:12288\n"
"v_mfma_f32_16x16x32_bf16 v[192:195], v[236:239], a[44:47], v[192:195]\n"
"ds_read_b64_tr_b16 v[234:235], v5 offset:12800\n"
"s_waitcnt vmcnt(9) lgkmcnt(6)\n"
"v_mfma_f32_16x16x32_bf16 v[164:167], v[240:243], a[48:51], v[164:167]\n"
"ds_read_b64_tr_b16 v[236:237], v6 offset:12288\n"
"buffer_load_dwordx4 a[88:91], v2, s[48:51], s63 offen sc1\n"
"v_mfma_f32_16x16x32_bf16 v[168:171], v[240:243], a[52:55], v[168:171]\n"
"ds_read_b64_tr_b16 v[238:239], v6 offset:12800\n"
"v_mfma_f32_16x16x32_bf16 v[172:175], v[244:247], a[48:51], v[172:175]\n"
"ds_read_b64_tr_b16 v[240:241], v7 offset:12288\n"
"v_mfma_f32_16x16x32_bf16 v[176:179], v[244:247], a[52:55], v[176:179]\n"
"ds_read_b64_tr_b16 v[242:243], v7 offset:12800\n"
"s_waitcnt lgkmcnt(6)\n"
"v_mfma_f32_16x16x32_bf16 v[180:183], v[224:227], a[48:51], v[180:183]\n"
"ds_read_b64_tr_b16 v[244:245], v8 offset:12288\n"
"buffer_load_dwordx4 a[92:95], v2, s[48:51], s63 offen offset:1024 sc1\n"
"v_mfma_f32_16x16x32_bf16 v[184:187], v[224:227], a[52:55], v[184:187]\n"
"ds_read_b64_tr_b16 v[246:247], v8 offset:12800\n"
"s_add_u32 s63, s29, 16646144\n"
"v_mfma_f32_16x16x32_bf16 v[188:191], v[228:231], a[48:51], v[188:191]\n"
"v_add_u32 v5, 114688, v5\n"
"v_add_u32 v6, 114688, v6\n"
"v_add_u32 v7, 114688, v7\n"
"v_add_u32 v8, 114688, v8\n"
"ds_read_b64_tr_b16 v[224:225], v5\n"
"v_mfma_f32_16x16x32_bf16 v[192:195], v[228:231], a[52:55], v[192:195]\n"
"ds_read_b64_tr_b16 v[226:227], v5 offset:512\n"
"s_waitcnt vmcnt(8) lgkmcnt(6)\n"
"v_mfma_f32_16x16x32_bf16 v[164:167], v[232:235], a[56:59], v[164:167]\n"
"ds_read_b64_tr_b16 v[228:229], v6\n"
"buffer_load_dwordx4 a[96:99], v2, s[48:51], s63 offen sc1\n"
"v_mfma_f32_16x16x32_bf16 v[168:171], v[232:235], a[60:63], v[168:171]\n"
"ds_read_b64_tr_b16 v[230:231], v6 offset:512\n"
"v_mfma_f32_16x16x32_bf16 v[172:175], v[236:239], a[56:59], v[172:175]\n"
"ds_read_b64_tr_b16 v[232:233], v7\n"
"v_mfma_f32_16x16x32_bf16 v[176:179], v[236:239], a[60:63], v[176:179]\n"
"ds_read_b64_tr_b16 v[234:235], v7 offset:512\n"
"s_waitcnt lgkmcnt(6)\n"
"v_mfma_f32_16x16x32_bf16 v[180:183], v[240:243], a[56:59], v[180:183]\n"
"ds_read_b64_tr_b16 v[236:237], v8\n"
"buffer_load_dwordx4 a[100:103], v2, s[48:51], s63 offen offset:1024 sc1\n"
"v_mfma_f32_16x16x32_bf16 v[184:187], v[240:243], a[60:63], v[184:187]\n"
"ds_read_b64_tr_b16 v[238:239], v8 offset:512\n"
"s_add_u32 s63, s29, 17170432\n"
"v_mfma_f32_16x16x32_bf16 v[188:191], v[244:247], a[56:59], v[188:191]\n"
"ds_read_b64_tr_b16 v[240:241], v5 offset:4096\n"
"v_mfma_f32_16x16x32_bf16 v[192:195], v[244:247], a[60:63], v[192:195]\n"
"ds_read_b64_tr_b16 v[242:243], v5 offset:4608\n"
"s_waitcnt vmcnt(8) lgkmcnt(6)\n"
"v_mfma_f32_16x16x32_bf16 v[164:167], v[224:227], a[64:67], v[164:167]\n"
"ds_read_b64_tr_b16 v[244:245], v6 offset:4096\n"
"buffer_load_dwordx4 a[104:107], v2, s[48:51], s63 offen sc1\n"
"v_mfma_f32_16x16x32_bf16 v[168:171], v[224:227], a[68:71], v[168:171]\n"
"ds_read_b64_tr_b16 v[246:247], v6 offset:4608\n"
"v_mfma_f32_16x16x32_bf16 v[172:175], v[228:231], a[64:67], v[172:175]\n"
"ds_read_b64_tr_b16 v[224:225], v7 offset:4096\n"
"v_mfma_f32_16x16x32_bf16 v[176:179], v[228:231], a[68:71], v[176:179]\n"
"ds_read_b64_tr_b16 v[226:227], v7 offset:4608\n"
"s_waitcnt lgkmcnt(6)\n"
"s_add_u32 s62, s37, 1687552\n"
"v_mov_b32 v252, 0\n"
"s_mov_b64 exec, 1\n"
"buffer_atomic_or v252, off, s[48:51], s62 sc0 sc1\n"
"s_mov_b64 exec, -1\n"
"v_mfma_f32_16x16x32_bf16 v[180:183], v[232:235], a[64:67], v[180:183]\n"
"ds_read_b64_tr_b16 v[228:229], v8 offset:4096\n"
"buffer_load_dwordx4 a[108:111], v2, s[48:51], s63 offen offset:1024 sc1\n"
"v_mfma_f32_16x16x32_bf16 v[184:187], v[232:235], a[68:71], v[184:187]\n"
"ds_read_b64_tr_b16 v[230:231], v8 offset:4608\n"
"s_add_u32 s63, s29, 17563648\n"
"v_mfma_f32_16x16x32_bf16 v[188:191], v[236:239], a[64:67], v[188:191]\n"
"ds_read_b64_tr_b16 v[232:233], v5 offset:8192\n"
"v_mfma_f32_16x16x32_bf16 v[192:195], v[236:239], a[68:71], v[192:195]\n"
"ds_read_b64_tr_b16 v[234:235], v5 offset:8704\n"
"s_waitcnt vmcnt(9) lgkmcnt(6)\n"
"v_mfma_f32_16x16x32_bf16 v[164:167], v[240:243], a[72:75], v[164:167]\n"
"ds_read_b64_tr_b16 v[236:237], v6 offset:8192\n"
"buffer_load_dwordx4 a[112:115], v2, s[48:51], s63 offen sc1\n"
"v_mfma_f32_16x16x32_bf16 v[168:171], v[240:243], a[76:79], v[168:171]\n"
"ds_read_b64_tr_b16 v[238:239], v6 offset:8704\n"
"v_mfma_f32_16x16x32_bf16 v[172:175], v[244:247], a[72:75], v[172:175]\n"
"ds_read_b64_tr_b16 v[240:241], v7 offset:8192\n"
"v_mfma_f32_16x16x32_bf16 v[176:179], v[244:247], a[76:79], v[176:179]\n"
"ds_read_b64_tr_b16 v[242:243], v7 offset:8704\n"
"s_waitcnt lgkmcnt(6)\n"
"v_mfma_f32_16x16x32_bf16 v[180:183], v[224:227], a[72:75], v[180:183]\n"
"ds_read_b64_tr_b16 v[244:245], v8 offset:8192\n"
"buffer_load_dwordx4 a[116:119], v2, s[48:51], s63 offen offset:1024 sc1\n"
"v_mfma_f32_16x16x32_bf16 v[184:187], v[224:227], a[76:79], v[184:187]\n"
"ds_read_b64_tr_b16 v[246:247], v8 offset:8704\n"
"s_add_u32 s63, s29, 18087936\n"
"v_mfma_f32_16x16x32_bf16 v[188:191], v[228:231], a[72:75], v[188:191]\n"
"ds_read_b64_tr_b16 v[224:225], v5 offset:12288\n"
"v_mfma_f32_16x16x32_bf16 v[192:195], v[228:231], a[76:79], v[192:195]\n"
"ds_read_b64_tr_b16 v[226:227], v5 offset:12800\n"
"s_waitcnt vmcnt(9) lgkmcnt(6)\n"
"s_add_u32 s62, s1, 1622016\n"
"s_mov_b64 exec, 2\n"
"buffer_atomic_add v1, off, s[48:51], s62 sc1\n"
"s_mov_b64 exec, -1\n"
"v_mfma_f32_16x16x32_bf16 v[164:167], v[232:235], a[80:83], v[164:167]\n"
"ds_read_b64_tr_b16 v[228:229], v6 offset:12288\n"
"buffer_load_dwordx4 a[120:123], v2, s[48:51], s63 offen sc1\n"
"v_mfma_f32_16x16x32_bf16 v[168:171], v[232:235], a[84:87], v[168:171]\n"
"ds_read_b64_tr_b16 v[230:231], v6 offset:12800\n"
"v_mfma_f32_16x16x32_bf16 v[172:175], v[236:239], a[80:83], v[172:175]\n"
"ds_read_b64_tr_b16 v[232:233], v7 offset:12288\n"
"v_mfma_f32_16x16x32_bf16 v[176:179], v[236:239], a[84:87], v[176:179]\n"
"ds_read_b64_tr_b16 v[234:235], v7 offset:12800\n"
"s_waitcnt lgkmcnt(6)\n"
"v_mfma_f32_16x16x32_bf16 v[180:183], v[240:243], a[80:83], v[180:183]\n"
"ds_read_b64_tr_b16 v[236:237], v8 offset:12288\n"
"buffer_load_dwordx4 a[124:127], v2, s[48:51], s63 offen offset:1024 sc1\n"
"v_mfma_f32_16x16x32_bf16 v[184:187], v[240:243], a[84:87], v[184:187]\n"
"ds_read_b64_tr_b16 v[238:239], v8 offset:12800\n"
"v_mfma_f32_16x16x32_bf16 v[188:191], v[244:247], a[80:83], v[188:191]\n"
"ds_read_b64_tr_b16 v[240:241], v5 offset:16384\n"
"v_mfma_f32_16x16x32_bf16 v[192:195], v[244:247], a[84:87], v[192:195]\n"
"ds_read_b64_tr_b16 v[242:243], v5 offset:16896\n"
"s_waitcnt vmcnt(10) lgkmcnt(6)\n"
"v_mfma_f32_16x16x32_bf16 v[164:167], v[224:227], a[88:91], v[164:167]\n"
"ds_read_b64_tr_b16 v[244:245], v6 offset:16384\n"
"v_mfma_f32_16x16x32_bf16 v[168:171], v[224:227], a[92:95], v[168:171]\n"
"ds_read_b64_tr_b16 v[246:247], v6 offset:16896\n"
"v_mfma_f32_16x16x32_bf16 v[172:175], v[228:231], a[88:91], v[172:175]\n"
"ds_read_b64_tr_b16 v[224:225], v7 offset:16384\n"
"v_mfma_f32_16x16x32_bf16 v[176:179], v[228:231], a[92:95], v[176:179]\n"
"ds_read_b64_tr_b16 v[226:227], v7 offset:16896\n"
"s_waitcnt lgkmcnt(6)\n"
"v_mfma_f32_16x16x32_bf16 v[180:183], v[232:235], a[88:91], v[180:183]\n"
"ds_read_b64_tr_b16 v[228:229], v8 offset:16384\n"
"v_mfma_f32_16x16x32_bf16 v[184:187], v[232:235], a[92:95], v[184:187]\n"
"ds_read_b64_tr_b16 v[230:231], v8 offset:16896\n"
"v_mfma_f32_16x16x32_bf16 v[188:191], v[236:239], a[88:91], v[188:191]\n"
"ds_read_b64_tr_b16 v[232:233], v5 offset:20480\n"
"v_mfma_f32_16x16x32_bf16 v[192:195], v[236:239], a[92:95], v[192:195]\n"
"ds_read_b64_tr_b16 v[234:235], v5 offset:20992\n"
"s_waitcnt vmcnt(8) lgkmcnt(6)\n"
"s_mov_b64 exec, 1\n"
"s_waitcnt vmcnt(6)\n"
"v_cmp_ne_u32 vcc, 8, v252\n"
"s_mov_b64 exec, -1\n"
"s_nop 3\n"
"s_cbranch_vccz Ld28_%=\n"
"s_mov_b32 s65, 0\n"
"Lp27_%=:\n"
"s_add_u32 s62, s37, 1687552\n"
"v_mov_b32 v252, 0\n"
"s_mov_b64 exec, 1\n"
"buffer_atomic_or v252, off, s[48:51], s62 sc0 sc1\n"
"s_mov_b64 exec, -1\n"
"s_add_u32 s65, s65, 1\n"
"s_cmp_gt_u32 s65, 131072\n"
"s_waitcnt vmcnt(0)\n"
"s_cbranch_scc1 Ld28_%=\n"
"s_mov_b64 exec, 1\n"
"v_cmp_ne_u32 vcc, 8, v252\n"
"s_mov_b64 exec, -1\n"
"s_nop 3\n"
"s_cbranch_vccnz Lp27_%=\n"
"Ld28_%=:\n"
"v_mfma_f32_16x16x32_bf16 v[164:167], v[240:243], a[96:99], v[164:167]\n"
"ds_read_b64_tr_b16 v[236:237], v6 offset:20480\n"
"v_mfma_f32_16x16x32_bf16 v[168:171], v[240:243], a[100:103], v[168:171]\n"
"ds_read_b64_tr_b16 v[238:239], v6 offset:20992\n"
"v_mfma_f32_16x16x32_bf16 v[172:175], v[244:247], a[96:99], v[172:175]\n"
"ds_read_b64_tr_b16 v[240:241], v7 offset:20480\n"
"s_add_u32 s63, s29, 14942208\n"
"v_mfma_f32_16x16x32_bf16 v[176:179], v[244:247], a[100:103], v[176:179]\n"
"ds_read_b64_tr_b16 v[242:243], v7 offset:20992\n"
"s_waitcnt lgkmcnt(6)\n"
"v_mfma_f32_16x16x32_bf16 v[180:183], v[224:227], a[96:99], v[180:183]\n"
"ds_read_b64_tr_b16 v[244:245], v8 offset:20480\n"
"buffer_load_dwordx4 a[128:131], v2, s[48:51], s63 offen sc1\n"
"v_mfma_f32_16x16x32_bf16 v[184:187], v[224:227], a[100:103], v[184:187]\n"
"ds_read_b64_tr_b16 v[246:247], v8 offset:20992\n"
"v_mfma_f32_16x16x32_bf16 v[188:191], v[228:231], a[96:99], v[188:191]\n"
"ds_read_b64_tr_b16 v[224:225], v5 offset:24576\n"
"buffer_load_dwordx4 a[132:135], v2, s[48:51], s63 offen offset:1024 sc1\n"
"v_mfma_f32_16x16x32_bf16 v[192:195], v[228:231], a[100:103], v[192:195]\n"
"ds_read_b64_tr_b16 v[226:227], v5 offset:25088\n"
"s_waitcnt vmcnt(7) lgkmcnt(6)\n"
"v_mfma_f32_16x16x32_bf16 v[164:167], v[232:235], a[104:107], v[164:167]\n"
"ds_read_b64_tr_b16 v[228:229], v6 offset:24576\n"
"s_add_u32 s63, s29, 15466496\n"
"v_mfma_f32_16x16x32_bf16 v[168:171], v[232:235], a[108:111], v[168:171]\n"
"ds_read_b64_tr_b16 v[230:231], v6 offset:25088\n"
"v_mfma_f32_16x16x32_bf16 v[172:175], v[236:239], a[104:107], v[172:175]\n"
"ds_read_b64_tr_b16 v[232:233], v7 offset:24576\n"
"buffer_load_dwordx4 a[136:139], v2, s[48:51], s63 offen sc1\n"
"v_mfma_f32_16x16x32_bf16 v[176:179], v[236:239], a[108:111], v[176:179]\n"
"ds_read_b64_tr_b16 v[234:235], v7 offset:25088\n"
"s_waitcnt lgkmcnt(6)\n"
"v_mfma_f32_16x16x32_bf16 v[180:183], v[240:243], a[104:107], v[180:183]\n"
"ds_read_b64_tr_b16 v[236:237], v8 offset:24576\n"
"buffer_load_dwordx4 a[140:143], v2, s[48:51], s63 offen offset:1024 sc1\n"
"v_mfma_f32_16x16x32_bf16 v[184:187], v[240:243], a[108:111], v[184:187]\n"
"ds_read_b64_tr_b16 v[238:239], v8 offset:25088\n"
"s_add_u32 s63, s29, 15859712\n"
"v_mfma_f32_16x16x32_bf16 v[188:191], v[244:247], a[104:107], v[188:191]\n"
"ds_read_b64_tr_b16 v[240:241], v5 offset:28672\n"
"buffer_load_dwordx4 a[144:147], v2, s[48:51], s63 offen sc1\n"
"v_mfma_f32_16x16x32_bf16 v[192:195], v[244:247], a[108:111], v[192:195]\n"
"ds_read_b64_tr_b16 v[242:243], v5 offset:29184\n"
"s_waitcnt vmcnt(8) lgkmcnt(6)\n"
"v_mfma_f32_16x16x32_bf16 v[164:167], v[224:227], a[112:115], v[164:167]\n"
"ds_read_b64_tr_b16 v[244:245], v6 offset:28672\n"
"buffer_load_dwordx4 a[148:151], v2, s[48:51], s63 offen offset:1024 sc1\n"
"v_mfma_f32_16x16x32_bf16 v[168:171], v[224:227], a[116:119], v[168:171]\n"
"ds_read_b64_tr_b16 v[246:247], v6 offset:29184\n"
"s_add_u32 s63, s29, 16384000\n"
"v_mfma_f32_16x16x32_bf16 v[172:175], v[228:231], a[112:115], v[172:175]\n"
"ds_read_b64_tr_b16 v[224:225], v7 offset:28672\n"
"v_mfma_f32_16x16x32_bf16 v[176:179], v[228:231], a[116:119], v[176:179]\n"
"ds_read_b64_tr_b16 v[226:227], v7 offset:29184\n"
"s_waitcnt lgkmcnt(6)\n"
"v_mfma_f32_16x16x32_bf16 v[180:183], v[232:235], a[112:115], v[180:183]\n"
"ds_read_b64_tr_b16 v[228:229], v8 offset:28672\n"
"buffer_load_dwordx4 a[152:155], v2, s[48:51], s63 offen sc1\n"
"v_mfma_f32_16x16x32_bf16 v[184:187], v[232:235], a[116:119], v[184:187]\n"
"ds_read_b64_tr_b16 v[230:231], v8 offset:29184\n"
"v_mfma_f32_16x16x32_bf16 v[188:191], v[236:239], a[112:115], v[188:191]\n"
"s_barrier\n"
"s_nop 0\n"
"v_add_u32 v5, 4294868992, v5\n"
"v_add_u32 v6, 4294868992, v6\n"
"v_add_u32 v7, 4294868992, v7\n"
"v_add_u32 v8, 4294868992, v8\n"
"ds_read_b64_tr_b16 v[232:233], v5\n"
"v_mfma_f32_16x16x32_bf16 v[192:195], v[236:239], a[116:119], v[192:195]\n"
"ds_read_b64_tr_b16 v[234:235], v5 offset:512\n"
"s_waitcnt vmcnt(7) lgkmcnt(6)\n"
"v_mfma_f32_16x16x32_bf16 v[164:167], v[240:243], a[120:123], v[164:167]\n"
"ds_read_b64_tr_b16 v[236:237], v6\n"
"buffer_load_dwordx4 a[156:159], v2, s[48:51], s63 offen offset:1024 sc1\n"
"v_mfma_f32_16x16x32_bf16 v[168:171], v[240:243], a[124:127], v[168:171]\n"
"ds_read_b64_tr_b16 v[238:239], v6 offset:512\n"
"s_add_u32 s63, s29, 16777216\n"
"v_mfma_f32_16x16x32_bf16 v[172:175], v[244:247], a[120:123], v[172:175]\n"
"ds_read_b64_tr_b16 v[240:241], v7\n"
"v_mfma_f32_16x16x32_bf16 v[176:179], v[244:247], a[124:127], v[176:179]\n"
"ds_read_b64_tr_b16 v[242:243], v7 offset:512\n"
"s_waitcnt lgkmcnt(6)\n"
"v_mfma_f32_16x16x32_bf16 v[180:183], v[224:227], a[120:123], v[180:183]\n"
"ds_read_b64_tr_b16 v[244:245], v8\n"
"buffer_load_dwordx4 a[160:163], v2, s[48:51], s63 offen sc1\n"
"v_mfma_f32_16x16x32_bf16 v[184:187], v[224:227], a[124:127], v[184:187]\n"
"ds_read_b64_tr_b16 v[246:247], v8 offset:512\n"
"v_mfma_f32_16x16x32_bf16 v[188:191], v[228:231], a[120:123], v[188:191]\n"
"ds_read_b64_tr_b16 v[224:225], v5 offset:4096\n"
"buffer_load_dwordx4 a[164:167], v2, s[48:51], s63 offen offset:1024 sc1\n"
"v_mfma_f32_16x16x32_bf16 v[192:195], v[228:231], a[124:127], v[192:195]\n"
"ds_read_b64_tr_b16 v[226:227], v5 offset:4608\n"
"s_waitcnt vmcnt(8) lgkmcnt(6)\n"
"v_mfma_f32_16x16x32_bf16 v[164:167], v[232:235], a[128:131], v[164:167]\n"
"ds_read_b64_tr_b16 v[228:229], v6 offset:4096\n"
"s_add_u32 s63, s29, 17301504\n"
"v_mfma_f32_16x16x32_bf16 v[168:171], v[232:235], a[132:135], v[168:171]\n"
"ds_read_b64_tr_b16 v[230:231], v6 offset:4608\n"
"v_mfma_f32_16x16x32_bf16 v[172:175], v[236:239], a[128:131], v[172:175]\n"
"ds_read_b64_tr_b16 v[232:233], v7 offset:4096\n"
"buffer_load_dwordx4 a[168:171], v2, s[48:51], s63 offen sc1\n"
"v_mfma_f32_16x16x32_bf16 v[176:179], v[236:239], a[132:135], v[176:179]\n"
"ds_read_b64_tr_b16 v[234:235], v7 offset:4608\n"
"s_waitcnt lgkmcnt(6)\n"
"v_mfma_f32_16x16x32_bf16 v[180:183], v[240:243], a[128:131], v[180:183]\n"
"ds_read_b64_tr_b16 v[236:237], v8 offset:4096\n"
"buffer_load_dwordx4 a[172:175], v2, s[48:51], s63 offen offset:1024 sc1\n"
"v_mfma_f32_16x16x32_bf16 v[184:187], v[240:243], a[132:135], v[184:187]\n"
"ds_read_b64_tr_b16 v[238:239], v8 offset:4608\n"
"s_add_u32 s63, s29, 17694720\n"
"v_mfma_f32_16x16x32_bf16 v[188:191], v[244:247], a[128:131], v[188:191]\n"
"ds_read_b64_tr_b16 v[240:241], v5 offset:8192\n"
"buffer_load_dwordx4 v9, s[40:43], s64 offen lds\n"
"v_mfma_f32_16x16x32_bf16 v[192:195], v[244:247], a[132:135], v[192:195]\n"
"ds_read_b64_tr_b16 v[242:243], v5 offset:8704\n"
"s_waitcnt vmcnt(9) lgkmcnt(6)\n"
"v_mfma_f32_16x16x32_bf16 v[164:167], v[224:227], a[136:139], v[164:167]\n"
"ds_read_b64_tr_b16 v[244:245], v6 offset:8192\n"
"s_add_u32 m0, s55, 77824\n"
"s_add_u32 s64, s90, 1638400\n"
"v_mfma_f32_16x16x32_bf16 v[168:171], v[224:227], a[140:143], v[168:171]\n"
"ds_read_b64_tr_b16 v[246:247], v6 offset:8704\n"
"v_mfma_f32_16x16x32_bf16 v[172:175], v[228:231], a[136:139], v[172:175]\n"
"ds_read_b64_tr_b16 v[224:225], v7 offset:8192\n"
"buffer_load_dwordx4 v9, s[40:43], s64 offen lds\n"
"v_mfma_f32_16x16x32_bf16 v[176:179], v[228:231], a[140:143], v[176:179]\n"
"ds_read_b64_tr_b16 v[226:227], v7 offset:8704\n"
"s_waitcnt lgkmcnt(6)\n"
"v_mfma_f32_16x16x32_bf16 v[180:183], v[232:235], a[136:139], v[180:183]\n"
"ds_read_b64_tr_b16 v[228:229], v8 offset:8192\n"
"buffer_load_dwordx4 a[176:179], v2, s[48:51], s63 offen sc1\n"
"v_mfma_f32_16x16x32_bf16 v[184:187], v[232:235], a[140:143], v[184:187]\n"
"ds_read_b64_tr_b16 v[230:231], v8 offset:8704\n"
"s_add_u32 m0, s55, 81920\n"
"v_mfma_f32_16x16x32_bf16 v[188:191], v[236:239], a[136:139], v[188:191]\n"
"LPG20_%=:\n"
"s_cbranch_execz LWRc_%=\n"
"ds_read_b64_tr_b16 v[232:233], v5 offset:12288\n"
"buffer_load_dwordx4 a[180:183], v2, s[48:51], s63 offen offset:1024 sc1\n"
"v_mfma_f32_16x16x32_bf16 v[192:195], v[236:239], a[140:143], v[192:195]\n"
"ds_read_b64_tr_b16 v[234:235], v5 offset:12800\n"
"s_waitcnt vmcnt(10) lgkmcnt(6)\n"
"v_mfma_f32_16x16x32_bf16 v[164:167], v[240:243], a[144:147], v[164:167]\n"
"ds_read_b64_tr_b16 v[236:237], v6 offset:12288\n"
"s_add_u32 s63, s29, 18219008\n"
"s_add_u32 s64, s90, 65536\n"
"v_mfma_f32_16x16x32_bf16 v[168:171], v[240:243], a[148:151], v[168:171]\n"
"ds_read_b64_tr_b16 v[238:239], v6 offset:12800\n"
"v_mfma_f32_16x16x32_bf16 v[172:175], v[244:247], a[144:147], v[172:175]\n"
"ds_read_b64_tr_b16 v[240:241], v7 offset:12288\n"
"buffer_load_dwordx4 a[184:187], v2, s[48:51], s63 offen sc1\n"
"v_mfma_f32_16x16x32_bf16 v[176:179], v[244:247], a[148:151], v[176:179]\n"
"ds_read_b64_tr_b16 v[242:243], v7 offset:12800\n"
"s_waitcnt lgkmcnt(6)\n"
"v_mfma_f32_16x16x32_bf16 v[180:183], v[224:227], a[144:147], v[180:183]\n"
"ds_read_b64_tr_b16 v[244:245], v8 offset:12288\n"
"buffer_load_dwordx4 a[188:191], v2, s[48:51], s63 offen offset:1024 sc1\n"
"v_mfma_f32_16x16x32_bf16 v[184:187], v[224:227], a[148:151], v[184:187]\n"
"ds_read_b64_tr_b16 v[246:247], v8 offset:12800\n"
"v_mfma_f32_16x16x32_bf16 v[188:191], v[228:231], a[144:147], v[188:191]\n"
"ds_read_b64_tr_b16 v[224:225], v5 offset:16384\n"
"v_mfma_f32_16x16x32_bf16 v[192:195], v[228:231], a[148:151], v[192:195]\n"
"ds_read_b64_tr_b16 v[226:227], v5 offset:16896\n"
"s_waitcnt vmcnt(10) lgkmcnt(6)\n"
"v_mfma_f32_16x16x32_bf16 v[164:167], v[232:235], a[152:155], v[164:167]\n"
"ds_read_b64_tr_b16 v[228:229], v6 offset:16384\n"
"buffer_load_dwordx4 v9, s[40:43], s64 offen lds\n"
"v_mfma_f32_16x16x32_bf16 v[168:171], v[232:235], a[156:159], v[168:171]\n"
"ds_read_b64_tr_b16 v[230:231], v6 offset:16896\n"
"s_add_u32 m0, s55, 86016\n"
"v_mfma_f32_16x16x32_bf16 v[172:175], v[236:239], a[152:155], v[172:175]\n"
"ds_read_b64_tr_b16 v[232:233], v7 offset:16384\n"
"s_add_u32 s64, s90, 327680\n"
"v_mfma_f32_16x16x32_bf16 v[176:179], v[236:239], a[156:159], v[176:179]\n"
"ds_read_b64_tr_b16 v[234:235], v7 offset:16896\n"
"s_waitcnt lgkmcnt(6)\n"
"v_mfma_f32_16x16x32_bf16 v[180:183], v[240:243], a[152:155], v[180:183]\n"
"ds_read_b64_tr_b16 v[236:237], v8 offset:16384\n"
"buffer_load_dwordx4 v9, s[40:43], s64 offen lds\n"
"v_mfma_f32_16x16x32_bf16 v[184:187], v[240:243], a[156:159], v[184:187]\n"
"ds_read_b64_tr_b16 v[238:239], v8 offset:16896\n"
"s_add_u32 m0, s55, 90112\n"
"v_mfma_f32_16x16x32_bf16 v[188:191], v[244:247], a[152:155], v[188:191]\n"
"ds_read_b64_tr_b16 v[240:241], v5 offset:20480\n"
"s_add_u32 s64, s90, 524288\n"
"v_mfma_f32_16x16x32_bf16 v[192:195], v[244:247], a[156:159], v[192:195]\n"
"ds_read_b64_tr_b16 v[242:243], v5 offset:20992\n"
"s_waitcnt vmcnt(10) lgkmcnt(6)\n"
"v_mfma_f32_16x16x32_bf16 v[164:167], v[224:227], a[160:163], v[164:167]\n"
"ds_read_b64_tr_b16 v[244:245], v6 offset:20480\n"
"v_mfma_f32_16x16x32_bf16 v[168:171], v[224:227], a[164:167], v[168:171]\n"
"ds_read_b64_tr_b16 v[246:247], v6 offset:20992\n"
"v_mfma_f32_16x16x32_bf16 v[172:175], v[228:231], a[160:163], v[172:175]\n"
"ds_read_b64_tr_b16 v[224:225], v7 offset:20480\n"
"buffer_load_dwordx4 v9, s[40:43], s64 offen lds\n"
"v_mfma_f32_16x16x32_bf16 v[176:179], v[228:231], a[164:167], v[176:179]\n"
"ds_read_b64_tr_b16 v[226:227], v7 offset:20992\n"
"s_waitcnt lgkmcnt(6)\n"
"v_mfma_f32_16x16x32_bf16 v[180:183], v[232:235], a[160:163], v[180:183]\n"
"ds_read_b64_tr_b16 v[228:229], v8 offset:20480\n"
"s_add_u32 m0, s55, 94208\n"
"s_add_u32 s64, s90, 786432\n"
"v_mfma_f32_16x16x32_bf16 v[184:187], v[232:235], a[164:167], v[184:187]\n"
"ds_read_b64_tr_b16 v[230:231], v8 offset:20992\n"
"v_mfma_f32_16x16x32_bf16 v[188:191], v[236:239], a[160:163], v[188:191]\n"
"ds_read_b64_tr_b16 v[232:233], v5 offset:24576\n"
"v_mfma_f32_16x16x32_bf16 v[192:195], v[236:239], a[164:167], v[192:195]\n"
"ds_read_b64_tr_b16 v[234:235], v5 offset:25088\n"
"s_waitcnt vmcnt(9) lgkmcnt(6)\n"
"v_mfma_f32_16x16x32_bf16 v[164:167], v[240:243], a[168:171], v[164:167]\n"
"ds_read_b64_tr_b16 v[236:237], v6 offset:24576\n"
"buffer_load_dwordx4 v9, s[40:43], s64 offen lds\n"
"v_mfma_f32_16x16x32_bf16 v[168:171], v[240:243], a[172:175], v[168:171]\n"
"ds_read_b64_tr_b16 v[238:239], v6 offset:25088\n"
"s_add_u32 m0, s55, 98304\n"
"v_mfma_f32_16x16x32_bf16 v[172:175], v[244:247], a[168:171], v[172:175]\n"
"ds_read_b64_tr_b16 v[240:241], v7 offset:24576\n"
"s_add_u32 s64, s90, 983040\n"
"v_mfma_f32_16x16x32_bf16 v[176:179], v[244:247], a[172:175], v[176:179]\n"
"ds_read_b64_tr_b16 v[242:243], v7 offset:25088\n"
"s_waitcnt lgkmcnt(6)\n"
"v_mfma_f32_16x16x32_bf16 v[180:183], v[224:227], a[168:171], v[180:183]\n"
"ds_read_b64_tr_b16 v[244:245], v8 offset:24576\n"
"v_mfma_f32_16x16x32_bf16 v[184:187], v[224:227], a[172:175], v[184:187]\n"
"ds_read_b64_tr_b16 v[246:247], v8 offset:25088\n"
"v_mfma_f32_16x16x32_bf16 v[188:191], v[228:231], a[168:171], v[188:191]\n"
"ds_read_b64_tr_b16 v[224:225], v5 offset:28672\n"
"buffer_load_dwordx4 v9, s[40:43], s64 offen lds\n"
"v_mfma_f32_16x16x32_bf16 v[192:195], v[228:231], a[172:175], v[192:195]\n"
"ds_read_b64_tr_b16 v[226:227], v5 offset:29184\n"
"s_waitcnt vmcnt(7) lgkmcnt(6)\n"
"v_mfma_f32_16x16x32_bf16 v[164:167], v[232:235], a[176:179], v[164:167]\n"
"ds_read_b64_tr_b16 v[228:229], v6 offset:28672\n"
"s_add_u32 m0, s55, 102400\n"
"s_add_u32 s64, s90, 1245184\n"
"v_mfma_f32_16x16x32_bf16 v[168:171], v[232:235], a[180:183], v[168:171]\n"
"ds_read_b64_tr_b16 v[230:231], v6 offset:29184\n"
"v_mfma_f32_16x16x32_bf16 v[172:175], v[236:239], a[176:179], v[172:175]\n"
"ds_read_b64_tr_b16 v[232:233], v7 offset:28672\n"
"v_mfma_f32_16x16x32_bf16 v[176:179], v[236:239], a[180:183], v[176:179]\n"
"ds_read_b64_tr_b16 v[234:235], v7 offset:29184\n"
"s_waitcnt lgkmcnt(6)\n"
"v_mfma_f32_16x16x32_bf16 v[180:183], v[240:243], a[176:179], v[180:183]\n"
"ds_read_b64_tr_b16 v[236:237], v8 offset:28672\n"
"buffer_load_dwordx4 v9, s[40:43], s64 offen lds\n"
"v_mfma_f32_16x16x32_bf16 v[184:187], v[240:243], a[180:183], v[184:187]\n"
"ds_read_b64_tr_b16 v[238:239], v8 offset:29184\n"
"s_add_u32 m0, s55, 106496\n"
"v_mfma_f32_16x16x32_bf16 v[188:191], v[244:247], a[176:179], v[188:191]\n"
"s_barrier\n"
"ds_read_b64_tr_b16 v[240:241], v5 offset:32768\n"
"v_mfma_f32_16x16x32_bf16 v[192:195], v[244:247], a[180:183], v[192:195]\n"
"ds_read_b64_tr_b16 v[242:243], v5 offset:33280\n"
"s_waitcnt vmcnt(6) lgkmcnt(6)\n"
"v_mfma_f32_16x16x32_bf16 v[164:167], v[224:227], a[184:187], v[164:167]\n"
"ds_read_b64_tr_b16 v[244:245], v6 offset:32768\n"
"s_add_u32 s64, s90, 1441792\n"
"v_mfma_f32_16x16x32_bf16 v[168:171], v[224:227], a[188:191], v[168:171]\n"
"ds_read_b64_tr_b16 v[246:247], v6 offset:33280\n"
"v_mfma_f32_16x16x32_bf16 v[172:175], v[228:231], a[184:187], v[172:175]\n"
"ds_read_b64_tr_b16 v[224:225], v7 offset:32768\n"
"buffer_load_dwordx4 v9, s[40:43], s64 offen lds\n"
"v_mfma_f32_16x16x32_bf16 v[176:179], v[228:231], a[188:191], v[176:179]\n"
"ds_read_b64_tr_b16 v[226:227], v7 offset:33280\n"
"s_waitcnt lgkmcnt(6)\n"
"v_mfma_f32_16x16x32_bf16 v[180:183], v[232:235], a[184:187], v[180:183]\n"
"ds_read_b64_tr_b16 v[228:229], v8 offset:32768\n"
"s_add_u32 m0, s55, 110592\n"
"s_add_u32 s64, s90, 1703936\n"
"v_mfma_f32_16x16x32_bf16 v[184:187], v[232:235], a[188:191], v[184:187]\n"
"ds_read_b64_tr_b16 v[230:231], v8 offset:33280\n"
"v_mfma_f32_16x16x32_bf16 v[188:191], v[236:239], a[184:187], v[188:191]\n"
"ds_read_b64_tr_b16 v[232:233], v5 offset:36864\n"
"v_mfma_f32_16x16x32_bf16 v[192:195], v[236:239], a[188:191], v[192:195]\n"
"ds_read_b64_tr_b16 v[234:235], v5 offset:37376\n"
"s_waitcnt lgkmcnt(6)\n"
"v_mfma_f32_16x16x32_bf16 v[88:91], v[240:243], a[0:3], v[88:91]\n"
"ds_read_b64_tr_b16 v[236:237], v6 offset:36864\n"
"buffer_load_dwordx4 v9, s[40:43], s64 offen lds\n"
"v_mfma_f32_16x16x32_bf16 v[92:95], v[240:243], a[4:7], v[92:95]\n"
"ds_read_b64_tr_b16 v[238:239], v6 offset:37376\n"
"s_add_u32 m0, s55, 147456\n"
"v_mfma_f32_16x16x32_bf16 v[96:99], v[244:247], a[0:3], v[96:99]\n"
"ds_read_b64_tr_b16 v[240:241], v7 offset:36864\n"
"v_cvt_pk_bf16_f32 v164, v164, v165\n"
"v_mfma_f32_16x16x32_bf16 v[100:103], v[244:247], a[4:7], v[100:103]\n"
"ds_read_b64_tr_b16 v[242:243], v7 offset:37376\n"
"s_waitcnt lgkmcnt(6)\n"
"LZ9_%=:\n"
"v_add_u32 v11, LP6_%=-LPC_%=, v0\n"
"v_min_u32 v11, LEND_%=-LPC_%=-4, v11\n"
"global_load_dword v11, v11, s[70:71]\n"
"v_mfma_f32_16x16x32_bf16 v[104:107], v[224:227], a[0:3], v[104:107]\n"
"ds_read_b64_tr_b16 v[244:245], v8 offset:36864\n"
"v_cvt_pk_bf16_f32 v165, v166, v167\n"
"v_mfma_f32_16x16x32_bf16 v[108:111], v[224:227], a[4:7], v[108:111]\n"
"ds_read_b64_tr_b16 v[246:247], v8 offset:37376\n"
"v_cvt_pk_bf16_f32 v166, v168, v169\n"
"v_cvt_pk_bf16_f32 v167, v170, v171\n"
"v_mfma_f32_16x16x32_bf16 v[112:115], v[228:231], a[0:3], v[112:115]\n"
"ds_read_b64_tr_b16 v[224:225], v5 offset:40960\n"
"s_add_u32 s64, s90, 131072\n"
"v_mfma_f32_16x16x32_bf16 v[116:119], v[228:231], a[4:7], v[116:119]\n"
"ds_read_b64_tr_b16 v[226:227], v5 offset:41472\n"
"s_waitcnt lgkmcnt(6)\n"
"v_mfma_f32_16x16x32_bf16 v[88:91], v[232:235], a[8:11], v[88:91]\n"
"ds_read_b64_tr_b16 v[228:229], v6 offset:40960\n"
"s_add_u32 s61, s32, 52887552\n"
"v_mfma_f32_16x16x32_bf16 v[92:95], v[232:235], a[12:15], v[92:95]\n"
"ds_read_b64_tr_b16 v[230:231], v6 offset:41472\n"
"v_mfma_f32_16x16x32_bf16 v[96:99], v[236:239], a[8:11], v[96:99]\n"
"ds_read_b64_tr_b16 v[232:233], v7 offset:40960\n"
"buffer_store_dwordx4 v[164:167], v2, s[48:51], s61 offen sc1\n"
"v_mfma_f32_16x16x32_bf16 v[100:103], v[236:239], a[12:15], v[100:103]\n"
"ds_read_b64_tr_b16 v[234:235], v7 offset:41472\n"
"s_waitcnt lgkmcnt(6)\n"
"v_mfma_f32_16x16x32_bf16 v[104:107], v[240:243], a[8:11], v[104:107]\n"
"ds_read_b64_tr_b16 v[236:237], v8 offset:40960\n"
"v_mfma_f32_16x16x32_bf16 v[108:111], v[240:243], a[12:15], v[108:111]\n"
"ds_read_b64_tr_b16 v[238:239], v8 offset:41472\n"
"v_mfma_f32_16x16x32_bf16 v[112:115], v[244:247], a[8:11], v[112:115]\n"
"ds_read_b64_tr_b16 v[240:241], v5 offset:45056\n"
"v_cvt_pk_bf16_f32 v172, v172, v173\n"
"v_mfma_f32_16x16x32_bf16 v[116:119], v[244:247], a[12:15], v[116:119]\n"
"ds_read_b64_tr_b16 v[242:243], v5 offset:45568\n"
"s_waitcnt lgkmcnt(6)\n"
"v_mfma_f32_16x16x32_bf16 v[88:91], v[224:227], a[16:19], v[88:91]\n"
"ds_read_b64_tr_b16 v[244:245], v6 offset:45056\n"
"v_cvt_pk_bf16_f32 v173, v174, v175\n"
"v_mfma_f32_16x16x32_bf16 v[92:95], v[224:227], a[20:23], v[92:95]\n"
"ds_read_b64_tr_b16 v[246:247], v6 offset:45568\n"
"v_cvt_pk_bf16_f32 v174, v176, v177\n"
"v_cvt_pk_bf16_f32 v175, v178, v179\n"
"v_mfma_f32_16x16x32_bf16 v[96:99], v[228:231], a[16:19], v[96:99]\n"
"ds_read_b64_tr_b16 v[224:225], v7 offset:45056\n"
"v_mfma_f32_16x16x32_bf16 v[100:103], v[228:231], a[20:23], v[100:103]\n"
"ds_read_b64_tr_b16 v[226:227], v7 offset:45568\n"
"s_waitcnt lgkmcnt(6)\n"
"v_mfma_f32_16x16x32_bf16 v[104:107], v[232:235], a[16:19], v[104:107]\n"
"ds_read_b64_tr_b16 v[228:229], v8 offset:45056\n"
"s_add_u32 s61, s32, 52888576\n"
"v_mfma_f32_16x16x32_bf16 v[108:111], v[232:235], a[20:23], v[108:111]\n"
"ds_read_b64_tr_b16 v[230:231], v8 offset:45568\n"
"v_mfma_f32_16x16x32_bf16 v[112:115], v[236:239], a[16:19], v[112:115]\n"
"ds_read_b64_tr_b16 v[232:233], v5 offset:49152\n"
"buffer_store_dwordx4 v[172:175], v2, s[48:51], s61 offen sc1\n"
"v_mfma_f32_16x16x32_bf16 v[116:119], v[236:239], a[20:23], v[116:119]\n"
"ds_read_b64_tr_b16 v[234:235], v5 offset:49664\n"
"s_waitcnt lgkmcnt(6)\n"
"v_mfma_f32_16x16x32_bf16 v[88:91], v[240:243], a[24:27], v[88:91]\n"
"ds_read_b64_tr_b16 v[236:237], v6 offset:49152\n"
"v_mfma_f32_16x16x32_bf16 v[92:95], v[240:243], a[28:31], v[92:95]\n"
"ds_read_b64_tr_b16 v[238:239], v6 offset:49664\n"
"v_mfma_f32_16x16x32_bf16 v[96:99], v[244:247], a[24:27], v[96:99]\n"
"ds_read_b64_tr_b16 v[240:241], v7 offset:49152\n"
"buffer_load_dwordx4 v9, s[40:43], s64 offen lds\n"
"v_mfma_f32_16x16x32_bf16 v[100:103], v[244:247], a[28:31], v[100:103]\n"
"ds_read_b64_tr_b16 v[242:243], v7 offset:49664\n"
"s_waitcnt lgkmcnt(6)\n"
"s_add_u32 s62, s0, 1622016\n"
"v_mov_b32 v252, 0\n"
"s_mov_b64 exec, 1\n"
"buffer_atomic_or v252, off, s[48:51], s62 sc0 sc1\n"
"s_mov_b64 exec, -1\n"
"v_mfma_f32_16x16x32_bf16 v[104:107], v[224:227], a[24:27], v[104:107]\n"
"ds_read_b64_tr_b16 v[244:245], v8 offset:49152\n"
"s_add_u32 m0, s55, 151552\n"
"s_add_u32 s64, s90, 393216\n"
"v_mfma_f32_16x16x32_bf16 v[108:111], v[224:227], a[28:31], v[108:111]\n"
"ds_read_b64_tr_b16 v[246:247], v8 offset:49664\n"
"v_cvt_pk_bf16_f32 v180, v180, v181\n"
"v_cvt_pk_bf16_f32 v181, v182, v183\n"
"v_cvt_pk_bf16_f32 v182, v184, v185\n"
"v_cvt_pk_bf16_f32 v183, v186, v187\n"
"v_mfma_f32_16x16x32_bf16 v[112:115], v[228:231], a[24:27], v[112:115]\n"
"ds_read_b64_tr_b16 v[224:225], v5 offset:53248\n"
"buffer_load_dwordx4 v9, s[40:43], s64 offen lds\n"
"v_mfma_f32_16x16x32_bf16 v[116:119], v[228:231], a[28:31], v[116:119]\n"
"ds_read_b64_tr_b16 v[226:227], v5 offset:53760\n"
"s_waitcnt lgkmcnt(6)\n"
"v_mfma_f32_16x16x32_bf16 v[88:91], v[232:235], a[32:35], v[88:91]\n"
"ds_read_b64_tr_b16 v[228:229], v6 offset:53248\n"
"s_add_u32 s61, s32, 52889600\n"
"s_add_u32 m0, s55, 155648\n"
"v_mfma_f32_16x16x32_bf16 v[92:95], v[232:235], a[36:39], v[92:95]\n"
"ds_read_b64_tr_b16 v[230:231], v6 offset:53760\n"
"s_add_u32 s64, s90, 589824\n"
"v_mfma_f32_16x16x32_bf16 v[96:99], v[236:239], a[32:35], v[96:99]\n"
"ds_read_b64_tr_b16 v[232:233], v7 offset:53248\n"
"buffer_store_dwordx4 v[180:183], v2, s[48:51], s61 offen sc1\n"
"v_mfma_f32_16x16x32_bf16 v[100:103], v[236:239], a[36:39], v[100:103]\n"
"ds_read_b64_tr_b16 v[234:235], v7 offset:53760\n"
"s_waitcnt lgkmcnt(6)\n"
"v_mfma_f32_16x16x32_bf16 v[104:107], v[240:243], a[32:35], v[104:107]\n"
"ds_read_b64_tr_b16 v[236:237], v8 offset:53248\n"
"v_mfma_f32_16x16x32_bf16 v[108:111], v[240:243], a[36:39], v[108:111]\n"
"ds_read_b64_tr_b16 v[238:239], v8 offset:53760\n"
"v_mfma_f32_16x16x32_bf16 v[112:115], v[244:247], a[32:35], v[112:115]\n"
"ds_read_b64_tr_b16 v[240:241], v5 offset:57344\n"
"buffer_load_dwordx4 v9, s[40:43], s64 offen lds\n"
"v_mfma_f32_16x16x32_bf16 v[116:119], v[244:247], a[36:39], v[116:119]\n"
"ds_read_b64_tr_b16 v[242:243], v5 offset:57856\n"
"s_waitcnt lgkmcnt(6)\n"
"v_mfma_f32_16x16x32_bf16 v[88:91], v[224:227], a[40:43], v[88:91]\n"
"ds_read_b64_tr_b16 v[244:245], v6 offset:57344\n"
"s_add_u32 m0, s55, 159744\n"
"s_add_u32 s64, s90, 851968\n"
"v_mfma_f32_16x16x32_bf16 v[92:95], v[224:227], a[44:47], v[92:95]\n"
"ds_read_b64_tr_b16 v[246:247], v6 offset:57856\n"
"v_cvt_pk_bf16_f32 v188, v188, v189\n"
"v_cvt_pk_bf16_f32 v189, v190, v191\n"
"v_cvt_pk_bf16_f32 v190, v192, v193\n"
"v_cvt_pk_bf16_f32 v191, v194, v195\n"
"v_mfma_f32_16x16x32_bf16 v[96:99], v[228:231], a[40:43], v[96:99]\n"
"ds_read_b64_tr_b16 v[224:225], v7 offset:57344\n"
"buffer_load_dwordx4 v9, s[40:43], s64 offen lds\n"
"v_mfma_f32_16x16x32_bf16 v[100:103], v[228:231], a[44:47], v[100:103]\n"
"ds_read_b64_tr_b16 v[226:227], v7 offset:57856\n"
"s_waitcnt lgkmcnt(6)\n"
"v_mfma_f32_16x16x32_bf16 v[104:107], v[232:235], a[40:43], v[104:107]\n"
"ds_read_b64_tr_b16 v[228:229], v8 offset:57344\n"
"s_add_u32 s61, s32, 52890624\n"
"s_add_u32 m0, s55, 0\n"
"v_mfma_f32_16x16x32_bf16 v[108:111], v[232:235], a[44:47], v[108:111]\n"
"ds_read_b64_tr_b16 v[230:231], v8 offset:57856\n"
"s_add_u32 s64, s90, 1048576\n"
"v_mfma_f32_16x16x32_bf16 v[112:115], v[236:239], a[40:43], v[112:115]\n"
"ds_read_b64_tr_b16 v[232:233], v5 offset:61440\n"
"buffer_store_dwordx4 v[188:191], v2, s[48:51], s61 offen sc1\n"
"v_mfma_f32_16x16x32_bf16 v[116:119], v[236:239], a[44:47], v[116:119]\n"
"ds_read_b64_tr_b16 v[234:235], v5 offset:61952\n"
"s_waitcnt lgkmcnt(6)\n"
"v_mfma_f32_16x16x32_bf16 v[88:91], v[240:243], a[48:51], v[88:91]\n"
"ds_read_b64_tr_b16 v[236:237], v6 offset:61440\n"
"v_mfma_f32_16x16x32_bf16 v[92:95], v[240:243], a[52:55], v[92:95]\n"
"ds_read_b64_tr_b16 v[238:239], v6 offset:61952\n"
"v_mfma_f32_16x16x32_bf16 v[96:99], v[244:247], a[48:51], v[96:99]\n"
"ds_read_b64_tr_b16 v[240:241], v7 offset:61440\n"
"buffer_load_dwordx4 v9, s[40:43], s64 offen lds\n"
"v_mfma_f32_16x16x32_bf16 v[100:103], v[244:247], a[52:55], v[100:103]\n"
"ds_read_b64_tr_b16 v[242:243], v7 offset:61952\n"
"s_waitcnt lgkmcnt(6)\n"
"v_mfma_f32_16x16x32_bf16 v[104:107], v[224:227], a[48:51], v[104:107]\n"
"ds_read_b64_tr_b16 v[244:245], v8 offset:61440\n"
"s_add_u32 m0, s55, 4096\n"
"s_add_u32 s64, s90, 1310720\n"
"v_mfma_f32_16x16x32_bf16 v[108:111], v[224:227], a[52:55], v[108:111]\n"
"ds_read_b64_tr_b16 v[246:247], v8 offset:61952\n"
"v_mfma_f32_16x16x32_bf16 v[112:115], v[228:231], a[48:51], v[112:115]\n"
"s_waitcnt vmcnt(11)\n"
"s_barrier\n"
"v_add_u32 v5, 65536, v5\n"
"v_add_u32 v6, 65536, v6\n"
"v_add_u32 v7, 65536, v7\n"
"v_add_u32 v8, 65536, v8\n"
"ds_read_b64_tr_b16 v[224:225], v5\n"
"v_mfma_f32_16x16x32_bf16 v[116:119], v[228:231], a[52:55], v[116:119]\n"
"ds_read_b64_tr_b16 v[226:227], v5 offset:512\n"
"s_waitcnt lgkmcnt(6)\n"
"v_mfma_f32_16x16x32_bf16 v[88:91], v[232:235], a[56:59], v[88:91]\n"
"ds_read_b64_tr_b16 v[228:229], v6\n"
"buffer_load_dwordx4 v9, s[40:43], s64 offen lds\n"
"v_mfma_f32_16x16x32_bf16 v[92:95], v[232:235], a[60:63], v[92:95]\n"
"ds_read_b64_tr_b16 v[230:231], v6 offset:512\n"
"s_add_u32 m0, s55, 8192\n"
"v_mfma_f32_16x16x32_bf16 v[96:99], v[236:239], a[56:59], v[96:99]\n"
"ds_read_b64_tr_b16 v[232:233], v7\n"
"s_add_u32 s64, s90, 1507328\n"
"v_mfma_f32_16x16x32_bf16 v[100:103], v[236:239], a[60:63], v[100:103]\n"
"ds_read_b64_tr_b16 v[234:235], v7 offset:512\n"
"s_waitcnt lgkmcnt(6)\n"
"v_mfma_f32_16x16x32_bf16 v[104:107], v[240:243], a[56:59], v[104:107]\n"
"ds_read_b64_tr_b16 v[236:237], v8\n"
"buffer_load_dwordx4 v9, s[40:43], s64 offen lds\n"
"v_mfma_f32_16x16x32_bf16 v[108:111], v[240:243], a[60:63], v[108:111]\n"
"ds_read_b64_tr_b16 v[238:239], v8 offset:512\n"
"s_add_u32 m0, s55, 12288\n"
"v_mfma_f32_16x16x32_bf16 v[112:115], v[244:247], a[56:59], v[112:115]\n"
"ds_read_b64_tr_b16 v[240:241], v5 offset:4096\n"
"s_add_u32 s64, s90, 1769472\n"
"v_mfma_f32_16x16x32_bf16 v[116:119], v[244:247], a[60:63], v[116:119]\n"
"ds_read_b64_tr_b16 v[242:243], v5 offset:4608\n"
"s_waitcnt lgkmcnt(6)\n"
"v_mfma_f32_16x16x32_bf16 v[88:91], v[224:227], a[64:67], v[88:91]\n"
"ds_read_b64_tr_b16 v[244:245], v6 offset:4096\n"
"buffer_load_dwordx4 v9, s[40:43], s64 offen lds\n"
"v_mfma_f32_16x16x32_bf16 v[92:95], v[224:227], a[68:71], v[92:95]\n"
"ds_read_b64_tr_b16 v[246:247], v6 offset:4608\n"
"s_add_u32 m0, s55, 114688\n"
"v_mfma_f32_16x16x32_bf16 v[96:99], v[228:231], a[64:67], v[96:99]\n"
"ds_read_b64_tr_b16 v[224:225], v7 offset:4096\n"
"s_add_u32 s64, s56, 196608\n"
"v_mfma_f32_16x16x32_bf16 v[100:103], v[228:231], a[68:71], v[100:103]\n"
"ds_read_b64_tr_b16 v[226:227], v7 offset:4608\n"
"s_waitcnt lgkmcnt(6)\n"
"s_mov_b64 exec, 1\n"
"s_waitcnt vmcnt(9)\n"
"v_cmp_ne_u32 vcc, 16, v252\n"
"s_mov_b64 exec, -1\n"
"s_nop 3\n"
"s_cbranch_vccz Ld30_%=\n"
"s_mov_b32 s65, 0\n"
"Lp29_%=:\n"
"s_add_u32 s62, s0, 1622016\n"
"v_mov_b32 v252, 0\n"
"s_mov_b64 exec, 1\n"
"buffer_atomic_or v252, off, s[48:51], s62 sc0 sc1\n"
"s_mov_b64 exec, -1\n"
"s_add_u32 s65, s65, 1\n"
"s_cmp_gt_u32 s65, 131072\n"
"s_waitcnt vmcnt(0)\n"
"s_cbranch_scc1 Ld30_%=\n"
"s_mov_b64 exec, 1\n"
"v_cmp_ne_u32 vcc, 16, v252\n"
"s_mov_b64 exec, -1\n"
"s_nop 3\n"
"s_cbranch_vccnz Lp29_%=\n"
"Ld30_%=:\n"
"v_mfma_f32_16x16x32_bf16 v[104:107], v[232:235], a[64:67], v[104:107]\n"
"ds_read_b64_tr_b16 v[228:229], v8 offset:4096\n"
"v_mov_b32 v38, s55\n"
"v_mfma_f32_16x16x32_bf16 v[108:111], v[232:235], a[68:71], v[108:111]\n"
"ds_read_b64_tr_b16 v[230:231], v8 offset:4608\n"
"v_lshlrev_b32 v38, 2, v38\n"
"v_mfma_f32_16x16x32_bf16 v[112:115], v[236:239], a[64:67], v[112:115]\n"
"ds_read_b64_tr_b16 v[232:233], v5 offset:8192\n"
"s_add_u32 s63, s52, 15073280\n"
"v_mfma_f32_16x16x32_bf16 v[116:119], v[236:239], a[68:71], v[116:119]\n"
"ds_read_b64_tr_b16 v[234:235], v5 offset:8704\n"
"s_waitcnt lgkmcnt(6)\n"
"v_mfma_f32_16x16x32_bf16 v[88:91], v[240:243], a[72:75], v[88:91]\n"
"ds_read_b64_tr_b16 v[236:237], v6 offset:8192\n"
"v_add_u32 v38, 16384, v38\n"
"v_mfma_f32_16x16x32_bf16 v[92:95], v[240:243], a[76:79], v[92:95]\n"
"ds_read_b64_tr_b16 v[238:239], v6 offset:8704\n"
"v_lshrrev_b32 v36, 1, v4\n"
"v_mfma_f32_16x16x32_bf16 v[96:99], v[244:247], a[72:75], v[96:99]\n"
"ds_read_b64_tr_b16 v[240:241], v7 offset:8192\n"
"v_and_b32 v37, 7, v3\n"
"v_mfma_f32_16x16x32_bf16 v[100:103], v[244:247], a[76:79], v[100:103]\n"
"ds_read_b64_tr_b16 v[242:243], v7 offset:8704\n"
"s_waitcnt lgkmcnt(6)\n"
"v_mfma_f32_16x16x32_bf16 v[104:107], v[224:227], a[72:75], v[104:107]\n"
"ds_read_b64_tr_b16 v[244:245], v8 offset:8192\n"
"v_xor_b32 v36, v36, v37\n"
"v_mfma_f32_16x16x32_bf16 v[108:111], v[224:227], a[76:79], v[108:111]\n"
"ds_read_b64_tr_b16 v[246:247], v8 offset:8704\n"
"v_lshlrev_b32 v36, 4, v36\n"
"v_mfma_f32_16x16x32_bf16 v[112:115], v[228:231], a[72:75], v[112:115]\n"
"ds_read_b64_tr_b16 v[224:225], v5 offset:12288\n"
"v_lshl_add_u32 v36, v3, 7, v36\n"
"v_mfma_f32_16x16x32_bf16 v[116:119], v[228:231], a[76:79], v[116:119]\n"
"ds_read_b64_tr_b16 v[226:227], v5 offset:12800\n"
"s_waitcnt lgkmcnt(6)\n"
"v_mfma_f32_16x16x32_bf16 v[88:91], v[232:235], a[80:83], v[88:91]\n"
"ds_read_b64_tr_b16 v[228:229], v6 offset:12288\n"
"v_and_b32 v37, 1, v4\n"
"v_mfma_f32_16x16x32_bf16 v[92:95], v[232:235], a[84:87], v[92:95]\n"
"ds_read_b64_tr_b16 v[230:231], v6 offset:12800\n"
"v_lshl_add_u32 v36, v37, 3, v36\n"
"v_mfma_f32_16x16x32_bf16 v[96:99], v[236:239], a[80:83], v[96:99]\n"
"ds_read_b64_tr_b16 v[232:233], v7 offset:12288\n"
"v_add_u32 v35, v38, v36\n"
"v_mfma_f32_16x16x32_bf16 v[100:103], v[236:239], a[84:87], v[100:103]\n"
"ds_read_b64_tr_b16 v[234:235], v7 offset:12800\n"
"s_waitcnt lgkmcnt(6)\n"
"v_mfma_f32_16x16x32_bf16 v[104:107], v[240:243], a[80:83], v[104:107]\n"
"ds_read_b64_tr_b16 v[236:237], v8 offset:12288\n"
"v_add_u32 v34, v38, v2\n"
"v_mfma_f32_16x16x32_bf16 v[108:111], v[240:243], a[84:87], v[108:111]\n"
"ds_read_b64_tr_b16 v[238:239], v8 offset:12800\n"
"v_lshrrev_b32 v36, 3, v1\n"
"v_mfma_f32_16x16x32_bf16 v[112:115], v[244:247], a[80:83], v[112:115]\n"
"ds_read_b64_tr_b16 v[240:241], v5 offset:16384\n"
"buffer_load_dwordx4 v9, s[40:43], s64 offen lds\n"
"v_mfma_f32_16x16x32_bf16 v[116:119], v[244:247], a[84:87], v[116:119]\n"
"ds_read_b64_tr_b16 v[242:243], v5 offset:16896\n"
"s_waitcnt lgkmcnt(6)\n"
"v_mfma_f32_16x16x32_bf16 v[88:91], v[224:227], a[88:91], v[88:91]\n"
"ds_read_b64_tr_b16 v[244:245], v6 offset:16384\n"
"s_add_u32 m0, s55, 118784\n"
"s_add_u32 s64, s56, 655360\n"
"v_mfma_f32_16x16x32_bf16 v[92:95], v[224:227], a[92:95], v[92:95]\n"
"ds_read_b64_tr_b16 v[246:247], v6 offset:16896\n"
"v_and_b32 v37, 7, v1\n"
"v_mfma_f32_16x16x32_bf16 v[96:99], v[228:231], a[88:91], v[96:99]\n"
"ds_read_b64_tr_b16 v[224:225], v7 offset:16384\n"
"v_xor_b32 v37, v37, v36\n"
"v_mfma_f32_16x16x32_bf16 v[100:103], v[228:231], a[92:95], v[100:103]\n"
"ds_read_b64_tr_b16 v[226:227], v7 offset:16896\n"
"s_waitcnt lgkmcnt(6)\n"
"v_mfma_f32_16x16x32_bf16 v[104:107], v[232:235], a[88:91], v[104:107]\n"
"ds_read_b64_tr_b16 v[228:229], v8 offset:16384\n"
"buffer_load_dwordx4 v9, s[40:43], s64 offen lds\n"
"v_mfma_f32_16x16x32_bf16 v[108:111], v[232:235], a[92:95], v[108:111]\n"
"ds_read_b64_tr_b16 v[230:231], v8 offset:16896\n"
"s_add_u32 m0, s55, 122880\n"
"v_mfma_f32_16x16x32_bf16 v[112:115], v[236:239], a[88:91], v[112:115]\n"
"ds_read_b64_tr_b16 v[232:233], v5 offset:20480\n"
"s_add_u32 s64, s56, 1114112\n"
"v_mfma_f32_16x16x32_bf16 v[116:119], v[236:239], a[92:95], v[116:119]\n"
"ds_read_b64_tr_b16 v[234:235], v5 offset:20992\n"
"s_waitcnt lgkmcnt(6)\n"
"v_mfma_f32_16x16x32_bf16 v[88:91], v[240:243], a[96:99], v[88:91]\n"
"ds_read_b64_tr_b16 v[236:237], v6 offset:20480\n"
"v_lshlrev_b32 v37, 4, v37\n"
"v_mfma_f32_16x16x32_bf16 v[92:95], v[240:243], a[100:103], v[92:95]\n"
"ds_read_b64_tr_b16 v[238:239], v6 offset:20992\n"
"v_lshl_add_u32 v33, v36, 11, v37\n"
"v_mfma_f32_16x16x32_bf16 v[96:99], v[244:247], a[96:99], v[96:99]\n"
"ds_read_b64_tr_b16 v[240:241], v7 offset:20480\n"
"buffer_load_dwordx4 v9, s[40:43], s64 offen lds\n"
"v_mfma_f32_16x16x32_bf16 v[100:103], v[244:247], a[100:103], v[100:103]\n"
"ds_read_b64_tr_b16 v[242:243], v7 offset:20992\n"
"s_waitcnt lgkmcnt(6)\n"
"v_mfma_f32_16x16x32_bf16 v[104:107], v[224:227], a[96:99], v[104:107]\n"
"ds_read_b64_tr_b16 v[244:245], v8 offset:20480\n"
"s_add_u32 m0, s55, 126976\n"
"s_add_u32 s64, s56, 1572864\n"
"v_mfma_f32_16x16x32_bf16 v[108:111], v[224:227], a[100:103], v[108:111]\n"
"ds_read_b64_tr_b16 v[246:247], v8 offset:20992\n"
"v_xor_b32 v40, 0, v35\n"
"v_mfma_f32_16x16x32_bf16 v[112:115], v[228:231], a[96:99], v[112:115]\n"
"ds_read_b64_tr_b16 v[224:225], v5 offset:24576\n"
"v_xor_b32 v41, 32, v35\n"
"v_mfma_f32_16x16x32_bf16 v[116:119], v[228:231], a[100:103], v[116:119]\n"
"ds_read_b64_tr_b16 v[226:227], v5 offset:25088\n"
"s_waitcnt lgkmcnt(6)\n"
"s_waitcnt vmcnt(7)\n"
"s_add_u32 s62, s18, 1573056\n"
"s_mov_b64 exec, 2\n"
"buffer_atomic_add v1, off, s[48:51], s62 sc1\n"
"s_mov_b64 exec, -1\n"
"v_mfma_f32_16x16x32_bf16 v[88:91], v[232:235], a[104:107], v[88:91]\n"
"ds_read_b64_tr_b16 v[228:229], v6 offset:24576\n"
"buffer_load_dwordx4 v9, s[40:43], s64 offen lds\n"
"v_mfma_f32_16x16x32_bf16 v[92:95], v[232:235], a[108:111], v[92:95]\n"
"ds_read_b64_tr_b16 v[230:231], v6 offset:25088\n"
"s_add_u32 m0, s55, 131072\n"
"v_mfma_f32_16x16x32_bf16 v[96:99], v[236:239], a[104:107], v[96:99]\n"
"ds_read_b64_tr_b16 v[232:233], v7 offset:24576\n"
"s_add_u32 s64, s56, 2031616\n"
"v_mfma_f32_16x16x32_bf16 v[100:103], v[236:239], a[108:111], v[100:103]\n"
"ds_read_b64_tr_b16 v[234:235], v7 offset:25088\n"
"s_waitcnt lgkmcnt(6)\n"
"v_mfma_f32_16x16x32_bf16 v[104:107], v[240:243], a[104:107], v[104:107]\n"
"ds_read_b64_tr_b16 v[236:237], v8 offset:24576\n"
"v_xor_b32 v42, 64, v35\n"
"v_mfma_f32_16x16x32_bf16 v[108:111], v[240:243], a[108:111], v[108:111]\n"
"ds_read_b64_tr_b16 v[238:239], v8 offset:25088\n"
"v_xor_b32 v43, 96, v35\n"
"v_mfma_f32_16x16x32_bf16 v[112:115], v[244:247], a[104:107], v[112:115]\n"
"ds_read_b64_tr_b16 v[240:241], v5 offset:28672\n"
"buffer_load_dwordx4 v9, s[40:43], s64 offen lds\n"
"v_mfma_f32_16x16x32_bf16 v[116:119], v[244:247], a[108:111], v[116:119]\n"
"ds_read_b64_tr_b16 v[242:243], v5 offset:29184\n"
"s_waitcnt lgkmcnt(6)\n"
"v_mfma_f32_16x16x32_bf16 v[88:91], v[224:227], a[112:115], v[88:91]\n"
"ds_read_b64_tr_b16 v[244:245], v6 offset:28672\n"
"s_add_u32 m0, s55, 135168\n"
"s_add_u32 s64, s56, 2490368\n"
"v_mfma_f32_16x16x32_bf16 v[92:95], v[224:227], a[116:119], v[92:95]\n"
"ds_read_b64_tr_b16 v[246:247], v6 offset:29184\n"
"v_mfma_f32_16x16x32_bf16 v[96:99], v[228:231], a[112:115], v[96:99]\n"
"ds_read_b64_tr_b16 v[224:225], v7 offset:28672\n"
"v_mfma_f32_16x16x32_bf16 v[100:103], v[228:231], a[116:119], v[100:103]\n"
"ds_read_b64_tr_b16 v[226:227], v7 offset:29184\n"
"s_waitcnt lgkmcnt(6)\n"
"v_mfma_f32_16x16x32_bf16 v[104:107], v[232:235], a[112:115], v[104:107]\n"
"ds_read_b64_tr_b16 v[228:229], v8 offset:28672\n"
"buffer_load_dwordx4 v9, s[40:43], s64 offen lds\n"
"v_mfma_f32_16x16x32_bf16 v[108:111], v[232:235], a[116:119], v[108:111]\n"
"ds_read_b64_tr_b16 v[230:231], v8 offset:29184\n"
"s_add_u32 m0, s55, 139264\n"
"v_mfma_f32_16x16x32_bf16 v[112:115], v[236:239], a[112:115], v[112:115]\n"
"s_waitcnt vmcnt(7)\n"
"s_barrier\n"
"s_nop 0\n"
"v_add_u32 v5, 65536, v5\n"
"v_add_u32 v6, 65536, v6\n"
"v_add_u32 v7, 65536, v7\n"
"v_add_u32 v8, 65536, v8\n"
"ds_read_b64_tr_b16 v[232:233], v5\n"
"v_mfma_f32_16x16x32_bf16 v[116:119], v[236:239], a[116:119], v[116:119]\n"
"ds_read_b64_tr_b16 v[234:235], v5 offset:512\n"
"s_waitcnt lgkmcnt(6)\n"
"v_mfma_f32_16x16x32_bf16 v[88:91], v[240:243], a[120:123], v[88:91]\n"
"ds_read_b64_tr_b16 v[236:237], v6\n"
"s_add_u32 s64, s56, 2949120\n"
"v_mfma_f32_16x16x32_bf16 v[92:95], v[240:243], a[124:127], v[92:95]\n"
"ds_read_b64_tr_b16 v[238:239], v6 offset:512\n"
"v_mfma_f32_16x16x32_bf16 v[96:99], v[244:247], a[120:123], v[96:99]\n"
"ds_read_b64_tr_b16 v[240:241], v7\n"
"buffer_load_dwordx4 v9, s[40:43], s64 offen lds\n"
"v_mfma_f32_16x16x32_bf16 v[100:103], v[244:247], a[124:127], v[100:103]\n"
"ds_read_b64_tr_b16 v[242:243], v7 offset:512\n"
"s_waitcnt lgkmcnt(6)\n"
"LPG21_%=:\n"
"s_cbranch_execz LWRc_%=\n"
"v_mfma_f32_16x16x32_bf16 v[104:107], v[224:227], a[120:123], v[104:107]\n"
"ds_read_b64_tr_b16 v[244:245], v8\n"
"s_add_u32 m0, s55, 143360\n"
"s_add_u32 s64, s56, 3407872\n"
"v_mfma_f32_16x16x32_bf16 v[108:111], v[224:227], a[124:127], v[108:111]\n"
"ds_read_b64_tr_b16 v[246:247], v8 offset:512\n"
"v_mfma_f32_16x16x32_bf16 v[112:115], v[228:231], a[120:123], v[112:115]\n"
"ds_read_b64_tr_b16 v[224:225], v5 offset:4096\n"
"v_mfma_f32_16x16x32_bf16 v[116:119], v[228:231], a[124:127], v[116:119]\n"
"ds_read_b64_tr_b16 v[226:227], v5 offset:4608\n"
"s_waitcnt lgkmcnt(6)\n"
"v_mfma_f32_16x16x32_bf16 v[88:91], v[232:235], a[128:131], v[88:91]\n"
"ds_read_b64_tr_b16 v[228:229], v6 offset:4096\n"
"buffer_load_dwordx4 v9, s[40:43], s64 offen lds\n"
"v_mfma_f32_16x16x32_bf16 v[92:95], v[232:235], a[132:135], v[92:95]\n"
"ds_read_b64_tr_b16 v[230:231], v6 offset:4608\n"
"v_mfma_f32_16x16x32_bf16 v[96:99], v[236:239], a[128:131], v[96:99]\n"
"ds_read_b64_tr_b16 v[232:233], v7 offset:4096\n"
"v_mfma_f32_16x16x32_bf16 v[100:103], v[236:239], a[132:135], v[100:103]\n"
"ds_read_b64_tr_b16 v[234:235], v7 offset:4608\n"
"s_waitcnt lgkmcnt(6)\n"
"v_mfma_f32_16x16x32_bf16 v[104:107], v[240:243], a[128:131], v[104:107]\n"
"ds_read_b64_tr_b16 v[236:237], v8 offset:4096\n"
"v_mfma_f32_16x16x32_bf16 v[108:111], v[240:243], a[132:135], v[108:111]\n"
"ds_read_b64_tr_b16 v[238:239], v8 offset:4608\n"
"v_mfma_f32_16x16x32_bf16 v[112:115], v[244:247], a[128:131], v[112:115]\n"
"ds_read_b64_tr_b16 v[240:241], v5 offset:8192\n"
"v_mfma_f32_16x16x32_bf16 v[116:119], v[244:247], a[132:135], v[116:119]\n"
"ds_read_b64_tr_b16 v[242:243], v5 offset:8704\n"
"s_waitcnt lgkmcnt(6)\n"
"v_mfma_f32_16x16x32_bf16 v[88:91], v[224:227], a[136:139], v[88:91]\n"
"ds_read_b64_tr_b16 v[244:245], v6 offset:8192\n"
"v_mfma_f32_16x16x32_bf16 v[92:95], v[224:227], a[140:143], v[92:95]\n"
"ds_read_b64_tr_b16 v[246:247], v6 offset:8704\n"
"v_mfma_f32_16x16x32_bf16 v[96:99], v[228:231], a[136:139], v[96:99]\n"
"ds_read_b64_tr_b16 v[224:225], v7 offset:8192\n"
"v_mfma_f32_16x16x32_bf16 v[100:103], v[228:231], a[140:143], v[100:103]\n"
"ds_read_b64_tr_b16 v[226:227], v7 offset:8704\n"
"s_waitcnt lgkmcnt(6)\n"
"v_mfma_f32_16x16x32_bf16 v[104:107], v[232:235], a[136:139], v[104:107]\n"
"ds_read_b64_tr_b16 v[228:229], v8 offset:8192\n"
"v_mfma_f32_16x16x32_bf16 v[108:111], v[232:235], a[140:143], v[108:111]\n"
"ds_read_b64_tr_b16 v[230:231], v8 offset:8704\n"
"v_mfma_f32_16x16x32_bf16 v[112:115], v[236:239], a[136:139], v[112:115]\n"
"ds_read_b64_tr_b16 v[232:233], v5 offset:12288\n"
"v_mfma_f32_16x16x32_bf16 v[116:119], v[236:239], a[140:143], v[116:119]\n"
"ds_read_b64_tr_b16 v[234:235], v5 offset:12800\n"
"s_waitcnt lgkmcnt(6)\n"
"v_mfma_f32_16x16x32_bf16 v[88:91], v[240:243], a[144:147], v[88:91]\n"
"ds_read_b64_tr_b16 v[236:237], v6 offset:12288\n"
"buffer_load_dwordx4 a[192:195], v2, s[48:51], s63 offen sc1\n"
"v_mfma_f32_16x16x32_bf16 v[92:95], v[240:243], a[148:151], v[92:95]\n"
"ds_read_b64_tr_b16 v[238:239], v6 offset:12800\n"
"v_mfma_f32_16x16x32_bf16 v[96:99], v[244:247], a[144:147], v[96:99]\n"
"ds_read_b64_tr_b16 v[240:241], v7 offset:12288\n"
"v_mfma_f32_16x16x32_bf16 v[100:103], v[244:247], a[148:151], v[100:103]\n"
"ds_read_b64_tr_b16 v[242:243], v7 offset:12800\n"
"s_waitcnt lgkmcnt(6)\n"
"v_mfma_f32_16x16x32_bf16 v[104:107], v[224:227], a[144:147], v[104:107]\n"
"ds_read_b64_tr_b16 v[244:245], v8 offset:12288\n"
"buffer_load_dwordx4 a[196:199], v2, s[48:51], s63 offen offset:1024 sc1\n"
"v_mfma_f32_16x16x32_bf16 v[108:111], v[224:227], a[148:151], v[108:111]\n"
"ds_read_b64_tr_b16 v[246:247], v8 offset:12800\n"
"s_add_u32 s63, s52, 15990784\n"
"v_mfma_f32_16x16x32_bf16 v[112:115], v[228:231], a[144:147], v[112:115]\n"
"v_add_u32 v5, 4294819840, v5\n"
"v_add_u32 v6, 4294819840, v6\n"
"v_add_u32 v7, 4294819840, v7\n"
"v_add_u32 v8, 4294819840, v8\n"
"ds_read_b64_tr_b16 v[224:225], v5\n"
"v_mfma_f32_16x16x32_bf16 v[116:119], v[228:231], a[148:151], v[116:119]\n"
"ds_read_b64_tr_b16 v[226:227], v5 offset:512\n"
"s_waitcnt lgkmcnt(6)\n"
"v_mfma_f32_16x16x32_bf16 v[88:91], v[232:235], a[152:155], v[88:91]\n"
"ds_read_b64_tr_b16 v[228:229], v6\n"
"buffer_load_dwordx4 a[200:203], v2, s[48:51], s63 offen sc1\n"
"v_mfma_f32_16x16x32_bf16 v[92:95], v[232:235], a[156:159], v[92:95]\n"
"ds_read_b64_tr_b16 v[230:231], v6 offset:512\n"
"v_mfma_f32_16x16x32_bf16 v[96:99], v[236:239], a[152:155], v[96:99]\n"
"ds_read_b64_tr_b16 v[232:233], v7\n"
"v_mfma_f32_16x16x32_bf16 v[100:103], v[236:239], a[156:159], v[100:103]\n"
"ds_read_b64_tr_b16 v[234:235], v7 offset:512\n"
"s_waitcnt lgkmcnt(6)\n"
"v_mfma_f32_16x16x32_bf16 v[104:107], v[240:243], a[152:155], v[104:107]\n"
"ds_read_b64_tr_b16 v[236:237], v8\n"
"buffer_load_dwordx4 a[204:207], v2, s[48:51], s63 offen offset:1024 sc1\n"
"v_mfma_f32_16x16x32_bf16 v[108:111], v[240:243], a[156:159], v[108:111]\n"
"ds_read_b64_tr_b16 v[238:239], v8 offset:512\n"
"s_add_u32 s63, s52, 16908288\n"
"v_mfma_f32_16x16x32_bf16 v[112:115], v[244:247], a[152:155], v[112:115]\n"
"ds_read_b64_tr_b16 v[240:241], v5 offset:4096\n"
"v_mfma_f32_16x16x32_bf16 v[116:119], v[244:247], a[156:159], v[116:119]\n"
"ds_read_b64_tr_b16 v[242:243], v5 offset:4608\n"
"s_waitcnt lgkmcnt(6)\n"
"v_mfma_f32_16x16x32_bf16 v[88:91], v[224:227], a[160:163], v[88:91]\n"
"ds_read_b64_tr_b16 v[244:245], v6 offset:4096\n"
"buffer_load_dwordx4 a[208:211], v2, s[48:51], s63 offen sc1\n"
"v_mfma_f32_16x16x32_bf16 v[92:95], v[224:227], a[164:167], v[92:95]\n"
"ds_read_b64_tr_b16 v[246:247], v6 offset:4608\n"
"v_mfma_f32_16x16x32_bf16 v[96:99], v[228:231], a[160:163], v[96:99]\n"
"ds_read_b64_tr_b16 v[224:225], v7 offset:4096\n"
"v_mfma_f32_16x16x32_bf16 v[100:103], v[228:231], a[164:167], v[100:103]\n"
"ds_read_b64_tr_b16 v[226:227], v7 offset:4608\n"
"s_waitcnt lgkmcnt(6)\n"
"v_mfma_f32_16x16x32_bf16 v[104:107], v[232:235], a[160:163], v[104:107]\n"
"ds_read_b64_tr_b16 v[228:229], v8 offset:4096\n"
"buffer_load_dwordx4 a[212:215], v2, s[48:51], s63 offen offset:1024 sc1\n"
"v_mfma_f32_16x16x32_bf16 v[108:111], v[232:235], a[164:167], v[108:111]\n"
"ds_read_b64_tr_b16 v[230:231], v8 offset:4608\n"
"s_add_u32 s63, s52, 17825792\n"
"v_mfma_f32_16x16x32_bf16 v[112:115], v[236:239], a[160:163], v[112:115]\n"
"ds_read_b64_tr_b16 v[232:233], v5 offset:8192\n"
"v_mfma_f32_16x16x32_bf16 v[116:119], v[236:239], a[164:167], v[116:119]\n"
"ds_read_b64_tr_b16 v[234:235], v5 offset:8704\n"
"s_waitcnt lgkmcnt(6)\n"
"s_add_u32 s62, s19, 1573056\n"
"v_mov_b32 v252, 0\n"
"s_mov_b64 exec, 1\n"
"buffer_atomic_or v252, off, s[48:51], s62 sc0 sc1\n"
"s_mov_b64 exec, -1\n"
"v_mfma_f32_16x16x32_bf16 v[88:91], v[240:243], a[168:171], v[88:91]\n"
"ds_read_b64_tr_b16 v[236:237], v6 offset:8192\n"
"buffer_load_dwordx4 a[216:219], v2, s[48:51], s63 offen sc1\n"
"v_mfma_f32_16x16x32_bf16 v[92:95], v[240:243], a[172:175], v[92:95]\n"
"ds_read_b64_tr_b16 v[238:239], v6 offset:8704\n"
"v_mfma_f32_16x16x32_bf16 v[96:99], v[244:247], a[168:171], v[96:99]\n"
"ds_read_b64_tr_b16 v[240:241], v7 offset:8192\n"
"v_mfma_f32_16x16x32_bf16 v[100:103], v[244:247], a[172:175], v[100:103]\n"
"ds_read_b64_tr_b16 v[242:243], v7 offset:8704\n"
"s_waitcnt lgkmcnt(6)\n"
"v_mfma_f32_16x16x32_bf16 v[104:107], v[224:227], a[168:171], v[104:107]\n"
"ds_read_b64_tr_b16 v[244:245], v8 offset:8192\n"
"buffer_load_dwordx4 a[220:223], v2, s[48:51], s63 offen offset:1024 sc1\n"
"v_mfma_f32_16x16x32_bf16 v[108:111], v[224:227], a[172:175], v[108:111]\n"
"ds_read_b64_tr_b16 v[246:247], v8 offset:8704\n"
"s_add_u32 s63, s52, 18743296\n"
"v_mfma_f32_16x16x32_bf16 v[112:115], v[228:231], a[168:171], v[112:115]\n"
"ds_read_b64_tr_b16 v[224:225], v5 offset:12288\n"
"v_mfma_f32_16x16x32_bf16 v[116:119], v[228:231], a[172:175], v[116:119]\n"
"ds_read_b64_tr_b16 v[226:227], v5 offset:12800\n"
"s_waitcnt lgkmcnt(6)\n"
"v_mfma_f32_16x16x32_bf16 v[88:91], v[232:235], a[176:179], v[88:91]\n"
"ds_read_b64_tr_b16 v[228:229], v6 offset:12288\n"
"buffer_load_dwordx4 a[224:227], v2, s[48:51], s63 offen sc1\n"
"v_mfma_f32_16x16x32_bf16 v[92:95], v[232:235], a[180:183], v[92:95]\n"
"ds_read_b64_tr_b16 v[230:231], v6 offset:12800\n"
"v_mfma_f32_16x16x32_bf16 v[96:99], v[236:239], a[176:179], v[96:99]\n"
"ds_read_b64_tr_b16 v[232:233], v7 offset:12288\n"
"v_mfma_f32_16x16x32_bf16 v[100:103], v[236:239], a[180:183], v[100:103]\n"
"ds_read_b64_tr_b16 v[234:235], v7 offset:12800\n"
"s_waitcnt lgkmcnt(6)\n"
"v_mfma_f32_16x16x32_bf16 v[104:107], v[240:243], a[176:179], v[104:107]\n"
"ds_read_b64_tr_b16 v[236:237], v8 offset:12288\n"
"buffer_load_dwordx4 a[228:231], v2, s[48:51], s63 offen offset:1024 sc1\n"
"v_mfma_f32_16x16x32_bf16 v[108:111], v[240:243], a[180:183], v[108:111]\n"
"ds_read_b64_tr_b16 v[238:239], v8 offset:12800\n"
"s_add_u32 s63, s52, 19660800\n"
"v_mfma_f32_16x16x32_bf16 v[112:115], v[244:247], a[176:179], v[112:115]\n"
"s_waitcnt vmcnt(11)\n"
"s_barrier\n"
"v_add_u32 v5, 114688, v5\n"
"v_add_u32 v6, 114688, v6\n"
"v_add_u32 v7, 114688, v7\n"
"v_add_u32 v8, 114688, v8\n"
"ds_read_b64_tr_b16 v[240:241], v5\n"
"v_mfma_f32_16x16x32_bf16 v[116:119], v[244:247], a[180:183], v[116:119]\n"
"ds_read_b64_tr_b16 v[242:243], v5 offset:512\n"
"s_waitcnt lgkmcnt(6)\n"
"v_mfma_f32_16x16x32_bf16 v[88:91], v[224:227], a[184:187], v[88:91]\n"
"ds_read_b64_tr_b16 v[244:245], v6\n"
"buffer_load_dwordx4 a[232:235], v2, s[48:51], s63 offen sc1\n"
"v_mfma_f32_16x16x32_bf16 v[92:95], v[224:227], a[188:191], v[92:95]\n"
"ds_read_b64_tr_b16 v[246:247], v6 offset:512\n"
"v_mfma_f32_16x16x32_bf16 v[96:99], v[228:231], a[184:187], v[96:99]\n"
"ds_read_b64_tr_b16 v[224:225], v7\n"
"v_mfma_f32_16x16x32_bf16 v[100:103], v[228:231], a[188:191], v[100:103]\n"
"ds_read_b64_tr_b16 v[226:227], v7 offset:512\n"
"s_waitcnt lgkmcnt(6)\n"
"v_mfma_f32_16x16x32_bf16 v[104:107], v[232:235], a[184:187], v[104:107]\n"
"ds_read_b64_tr_b16 v[228:229], v8\n"
"buffer_load_dwordx4 a[236:239], v2, s[48:51], s63 offen offset:1024 sc1\n"
"v_mfma_f32_16x16x32_bf16 v[108:111], v[232:235], a[188:191], v[108:111]\n"
"ds_read_b64_tr_b16 v[230:231], v8 offset:512\n"
"s_add_u32 s63, s52, 20578304\n"
"v_mfma_f32_16x16x32_bf16 v[112:115], v[236:239], a[184:187], v[112:115]\n"
"ds_read_b64_tr_b16 v[232:233], v5 offset:4096\n"
"v_mfma_f32_16x16x32_bf16 v[116:119], v[236:239], a[188:191], v[116:119]\n"
"ds_read_b64_tr_b16 v[234:235], v5 offset:4608\n"
"s_waitcnt vmcnt(11) lgkmcnt(6)\n"
"v_mfma_f32_16x16x32_bf16 v[88:91], v[240:243], a[192:195], v[88:91]\n"
"ds_read_b64_tr_b16 v[236:237], v6 offset:4096\n"
"buffer_load_dwordx4 a[240:243], v2, s[48:51], s63 offen sc1\n"
"v_mfma_f32_16x16x32_bf16 v[92:95], v[240:243], a[196:199], v[92:95]\n"
"ds_read_b64_tr_b16 v[238:239], v6 offset:4608\n"
"v_mfma_f32_16x16x32_bf16 v[96:99], v[244:247], a[192:195], v[96:99]\n"
"ds_read_b64_tr_b16 v[240:241], v7 offset:4096\n"
"v_mfma_f32_16x16x32_bf16 v[100:103], v[244:247], a[196:199], v[100:103]\n"
"ds_read_b64_tr_b16 v[242:243], v7 offset:4608\n"
"s_waitcnt lgkmcnt(6)\n"
"v_mfma_f32_16x16x32_bf16 v[104:107], v[224:227], a[192:195], v[104:107]\n"
"ds_read_b64_tr_b16 v[244:245], v8 offset:4096\n"
"buffer_load_dwordx4 a[244:247], v2, s[48:51], s63 offen offset:1024 sc1\n"
"v_mfma_f32_16x16x32_bf16 v[108:111], v[224:227], a[196:199], v[108:111]\n"
"ds_read_b64_tr_b16 v[246:247], v8 offset:4608\n"
"s_add_u32 s63, s52, 21495808\n"
"v_mfma_f32_16x16x32_bf16 v[112:115], v[228:231], a[192:195], v[112:115]\n"
"ds_read_b64_tr_b16 v[224:225], v5 offset:8192\n"
"v_mfma_f32_16x16x32_bf16 v[116:119], v[228:231], a[196:199], v[116:119]\n"
"ds_read_b64_tr_b16 v[226:227], v5 offset:8704\n"
"s_waitcnt vmcnt(11) lgkmcnt(6)\n"
"v_mfma_f32_16x16x32_bf16 v[88:91], v[232:235], a[200:203], v[88:91]\n"
"ds_read_b64_tr_b16 v[228:229], v6 offset:8192\n"
"buffer_load_dwordx4 a[248:251], v2, s[48:51], s63 offen sc1\n"
"v_mfma_f32_16x16x32_bf16 v[92:95], v[232:235], a[204:207], v[92:95]\n"
"ds_read_b64_tr_b16 v[230:231], v6 offset:8704\n"
"v_mfma_f32_16x16x32_bf16 v[96:99], v[236:239], a[200:203], v[96:99]\n"
"ds_read_b64_tr_b16 v[232:233], v7 offset:8192\n"
"v_mfma_f32_16x16x32_bf16 v[100:103], v[236:239], a[204:207], v[100:103]\n"
"ds_read_b64_tr_b16 v[234:235], v7 offset:8704\n"
"s_waitcnt lgkmcnt(6)\n"
"v_mfma_f32_16x16x32_bf16 v[104:107], v[240:243], a[200:203], v[104:107]\n"
"ds_read_b64_tr_b16 v[236:237], v8 offset:8192\n"
"buffer_load_dwordx4 a[252:255], v2, s[48:51], s63 offen offset:1024 sc1\n"
"v_mfma_f32_16x16x32_bf16 v[108:111], v[240:243], a[204:207], v[108:111]\n"
"ds_read_b64_tr_b16 v[238:239], v8 offset:8704\n"
"v_mfma_f32_16x16x32_bf16 v[112:115], v[244:247], a[200:203], v[112:115]\n"
"ds_read_b64_tr_b16 v[240:241], v5 offset:12288\n"
"v_mfma_f32_16x16x32_bf16 v[116:119], v[244:247], a[204:207], v[116:119]\n"
"ds_read_b64_tr_b16 v[242:243], v5 offset:12800\n"
"s_waitcnt vmcnt(11) lgkmcnt(6)\n"
"s_mov_b64 exec, 1\n"
"s_waitcnt vmcnt(10)\n"
"v_cmp_ne_u32 vcc, 1, v252\n"
"s_mov_b64 exec, -1\n"
"s_nop 3\n"
"s_cbranch_vccz Ld32_%=\n"
"s_mov_b32 s65, 0\n"
"Lp31_%=:\n"
"s_add_u32 s62, s19, 1573056\n"
"v_mov_b32 v252, 0\n"
"s_mov_b64 exec, 1\n"
"buffer_atomic_or v252, off, s[48:51], s62 sc0 sc1\n"
"s_mov_b64 exec, -1\n"
"s_add_u32 s65, s65, 1\n"
"s_cmp_gt_u32 s65, 131072\n"
"s_waitcnt vmcnt(0)\n"
"s_cbranch_scc1 Ld32_%=\n"
"s_mov_b64 exec, 1\n"
"v_cmp_ne_u32 vcc, 1, v252\n"
"s_mov_b64 exec, -1\n"
"s_nop 3\n"
"s_cbranch_vccnz Lp31_%=\n"
"Ld32_%=:\n"
"v_mfma_f32_16x16x32_bf16 v[88:91], v[224:227], a[208:211], v[88:91]\n"
"ds_read_b64_tr_b16 v[244:245], v6 offset:12288\n"
"v_mfma_f32_16x16x32_bf16 v[92:95], v[224:227], a[212:215], v[92:95]\n"
"ds_read_b64_tr_b16 v[246:247], v6 offset:12800\n"
"v_mfma_f32_16x16x32_bf16 v[96:99], v[228:231], a[208:211], v[96:99]\n"
"ds_read_b64_tr_b16 v[224:225], v7 offset:12288\n"
"s_add_u32 s61, s33, 52887552\n"
"v_mfma_f32_16x16x32_bf16 v[100:103], v[228:231], a[212:215], v[100:103]\n"
"ds_read_b64_tr_b16 v[226:227], v7 offset:12800\n"
"s_waitcnt lgkmcnt(6)\n"
"v_mfma_f32_16x16x32_bf16 v[104:107], v[232:235], a[208:211], v[104:107]\n"
"ds_read_b64_tr_b16 v[228:229], v8 offset:12288\n"
"buffer_load_dwordx4 v[168:171], v2, s[48:51], s61 offen sc1\n"
"v_mfma_f32_16x16x32_bf16 v[108:111], v[232:235], a[212:215], v[108:111]\n"
"ds_read_b64_tr_b16 v[230:231], v8 offset:12800\n"
"v_mfma_f32_16x16x32_bf16 v[112:115], v[236:239], a[208:211], v[112:115]\n"
"ds_read_b64_tr_b16 v[232:233], v5 offset:16384\n"
"v_mfma_f32_16x16x32_bf16 v[116:119], v[236:239], a[212:215], v[116:119]\n"
"ds_read_b64_tr_b16 v[234:235], v5 offset:16896\n"
"s_waitcnt vmcnt(9) lgkmcnt(6)\n"
"v_mfma_f32_16x16x32_bf16 v[88:91], v[240:243], a[216:219], v[88:91]\n"
"ds_read_b64_tr_b16 v[236:237], v6 offset:16384\n"
"buffer_load_dwordx4 v[176:179], v2, s[48:51], s61 offen offset:1024 sc1\n"
"v_mfma_f32_16x16x32_bf16 v[92:95], v[240:243], a[220:223], v[92:95]\n"
"ds_read_b64_tr_b16 v[238:239], v6 offset:16896\n"
"v_mfma_f32_16x16x32_bf16 v[96:99], v[244:247], a[216:219], v[96:99]\n"
"ds_read_b64_tr_b16 v[240:241], v7 offset:16384\n"
"v_mfma_f32_16x16x32_bf16 v[100:103], v[244:247], a[220:223], v[100:103]\n"
"ds_read_b64_tr_b16 v[242:243], v7 offset:16896\n"
"s_waitcnt lgkmcnt(6)\n"
"v_mfma_f32_16x16x32_bf16 v[104:107], v[224:227], a[216:219], v[104:107]\n"
"ds_read_b64_tr_b16 v[244:245], v8 offset:16384\n"
"buffer_load_dwordx4 v[184:187], v2, s[48:51], s61 offen offset:2048 sc1\n"
"v_mfma_f32_16x16x32_bf16 v[108:111], v[224:227], a[220:223], v[108:111]\n"
"ds_read_b64_tr_b16 v[246:247], v8 offset:16896\n"
"v_mfma_f32_16x16x32_bf16 v[112:115], v[228:231], a[216:219], v[112:115]\n"
"ds_read_b64_tr_b16 v[224:225], v5 offset:20480\n"
"buffer_load_dwordx4 v[192:195], v2, s[48:51], s61 offen offset:3072 sc1\n"
"v_mfma_f32_16x16x32_bf16 v[116:119], v[228:231], a[220:223], v[116:119]\n"
"ds_read_b64_tr_b16 v[226:227], v5 offset:20992\n"
"s_waitcnt vmcnt(10) lgkmcnt(6)\n"
"v_mfma_f32_16x16x32_bf16 v[88:91], v[232:235], a[224:227], v[88:91]\n"
"ds_read_b64_tr_b16 v[228:229], v6 offset:20480\n"
"v_mfma_f32_16x16x32_bf16 v[92:95], v[232:235], a[228:231], v[92:95]\n"
"ds_read_b64_tr_b16 v[230:231], v6 offset:20992\n"
"v_mfma_f32_16x16x32_bf16 v[96:99], v[236:239], a[224:227], v[96:99]\n"
"ds_read_b64_tr_b16 v[232:233], v7 offset:20480\n"
"v_mfma_f32_16x16x32_bf16 v[100:103], v[236:239], a[228:231], v[100:103]\n"
"ds_read_b64_tr_b16 v[234:235], v7 offset:20992\n"
"s_waitcnt lgkmcnt(6)\n"
"v_mfma_f32_16x16x32_bf16 v[104:107], v[240:243], a[224:227], v[104:107]\n"
"ds_read_b64_tr_b16 v[236:237], v8 offset:20480\n"
"v_mfma_f32_16x16x32_bf16 v[108:111], v[240:243], a[228:231], v[108:111]\n"
"ds_read_b64_tr_b16 v[238:239], v8 offset:20992\n"
"v_mfma_f32_16x16x32_bf16 v[112:115], v[244:247], a[224:227], v[112:115]\n"
"ds_read_b64_tr_b16 v[240:241], v5 offset:24576\n"
"v_mfma_f32_16x16x32_bf16 v[116:119], v[244:247], a[228:231], v[116:119]\n"
"ds_read_b64_tr_b16 v[242:243], v5 offset:25088\n"
"s_waitcnt vmcnt(8) lgkmcnt(6)\n"
"v_mfma_f32_16x16x32_bf16 v[88:91], v[224:227], a[232:235], v[88:91]\n"
"ds_read_b64_tr_b16 v[244:245], v6 offset:24576\n"
"v_mfma_f32_16x16x32_bf16 v[92:95], v[224:227], a[236:239], v[92:95]\n"
"ds_read_b64_tr_b16 v[246:247], v6 offset:25088\n"
"v_mfma_f32_16x16x32_bf16 v[96:99], v[228:231], a[232:235], v[96:99]\n"
"ds_read_b64_tr_b16 v[224:225], v7 offset:24576\n"
"v_mfma_f32_16x16x32_bf16 v[100:103], v[228:231], a[236:239], v[100:103]\n"
"ds_read_b64_tr_b16 v[226:227], v7 offset:25088\n"
"s_waitcnt lgkmcnt(6)\n"
"v_mfma_f32_16x16x32_bf16 v[104:107], v[232:235], a[232:235], v[104:107]\n"
"ds_read_b64_tr_b16 v[228:229], v8 offset:24576\n"
"v_mfma_f32_16x16x32_bf16 v[108:111], v[232:235], a[236:239], v[108:111]\n"
"ds_read_b64_tr_b16 v[230:231], v8 offset:25088\n"
"v_mfma_f32_16x16x32_bf16 v[112:115], v[236:239], a[232:235], v[112:115]\n"
"ds_read_b64_tr_b16 v[232:233], v5 offset:28672\n"
"v_mfma_f32_16x16x32_bf16 v[116:119], v[236:239], a[236:239], v[116:119]\n"
"ds_read_b64_tr_b16 v[234:235], v5 offset:29184\n"
"s_waitcnt vmcnt(6) lgkmcnt(6)\n"
"v_mfma_f32_16x16x32_bf16 v[88:91], v[240:243], a[240:243], v[88:91]\n"
"ds_read_b64_tr_b16 v[236:237], v6 offset:28672\n"
"v_mfma_f32_16x16x32_bf16 v[92:95], v[240:243], a[244:247], v[92:95]\n"
"ds_read_b64_tr_b16 v[238:239], v6 offset:29184\n"
"v_mfma_f32_16x16x32_bf16 v[96:99], v[244:247], a[240:243], v[96:99]\n"
"ds_read_b64_tr_b16 v[240:241], v7 offset:28672\n"
"v_mfma_f32_16x16x32_bf16 v[100:103], v[244:247], a[244:247], v[100:103]\n"
"ds_read_b64_tr_b16 v[242:243], v7 offset:29184\n"
"s_waitcnt lgkmcnt(6)\n"
"v_mfma_f32_16x16x32_bf16 v[104:107], v[224:227], a[240:243], v[104:107]\n"
"ds_read_b64_tr_b16 v[244:245], v8 offset:28672\n"
"v_mfma_f32_16x16x32_bf16 v[108:111], v[224:227], a[244:247], v[108:111]\n"
"ds_read_b64_tr_b16 v[246:247], v8 offset:29184\n"
"v_mfma_f32_16x16x32_bf16 v[112:115], v[228:231], a[240:243], v[112:115]\n"
"v_mfma_f32_16x16x32_bf16 v[116:119], v[228:231], a[244:247], v[116:119]\n"
"s_waitcnt vmcnt(4) lgkmcnt(6)\n"
"v_mfma_f32_16x16x32_bf16 v[88:91], v[232:235], a[248:251], v[88:91]\n"
"v_mfma_f32_16x16x32_bf16 v[92:95], v[232:235], a[252:255], v[92:95]\n"
"s_waitcnt lgkmcnt(4)\n"
"v_mfma_f32_16x16x32_bf16 v[96:99], v[236:239], a[248:251], v[96:99]\n"
"v_mfma_f32_16x16x32_bf16 v[100:103], v[236:239], a[252:255], v[100:103]\n"
"s_waitcnt lgkmcnt(2)\n"
"v_mfma_f32_16x16x32_bf16 v[104:107], v[240:243], a[248:251], v[104:107]\n"
"v_mfma_f32_16x16x32_bf16 v[108:111], v[240:243], a[252:255], v[108:111]\n"
"s_waitcnt lgkmcnt(0)\n"
"v_mfma_f32_16x16x32_bf16 v[112:115], v[244:247], a[248:251], v[112:115]\n"
"v_mfma_f32_16x16x32_bf16 v[116:119], v[244:247], a[252:255], v[116:119]\n"
"s_waitcnt vmcnt(0)\n"
"v_mfma_f32_16x16x32_bf16 v[88:91], v[152:155], v[168:171], v[88:91]\n"
"v_mfma_f32_16x16x32_bf16 v[92:95], v[156:159], v[168:171], v[92:95]\n"
"v_mfma_f32_16x16x32_bf16 v[96:99], v[152:155], v[176:179], v[96:99]\n"
"v_mfma_f32_16x16x32_bf16 v[100:103], v[156:159], v[176:179], v[100:103]\n"
"v_mfma_f32_16x16x32_bf16 v[104:107], v[152:155], v[184:187], v[104:107]\n"
"v_mfma_f32_16x16x32_bf16 v[108:111], v[156:159], v[184:187], v[108:111]\n"
"v_mfma_f32_16x16x32_bf16 v[112:115], v[152:155], v[192:195], v[112:115]\n"
"v_mfma_f32_16x16x32_bf16 v[116:119], v[156:159], v[192:195], v[116:119]\n"
"s_nop 0\n"
"v_cvt_pk_bf16_f32 v88, v88, v89\n"
"v_cvt_pk_bf16_f32 v89, v90, v91\n"
"ds_write_b64 v40, v[88:89]\n"
"v_cvt_pk_bf16_f32 v96, v96, v97\n"
"v_cvt_pk_bf16_f32 v97, v98, v99\n"
"ds_write_b64 v41, v[96:97]\n"
"v_cvt_pk_bf16_f32 v104, v104, v105\n"
"v_cvt_pk_bf16_f32 v105, v106, v107\n"
"ds_write_b64 v42, v[104:105]\n"
"v_cvt_pk_bf16_f32 v112, v112, v113\n"
"v_cvt_pk_bf16_f32 v113, v114, v115\n"
"ds_write_b64 v43, v[112:113]\n"
"ds_read_b128 v[128:131], v34\n"
"ds_read_b128 v[132:135], v34 offset:1024\n"
"v_cvt_pk_bf16_f32 v92, v92, v93\n"
"v_cvt_pk_bf16_f32 v93, v94, v95\n"
"ds_write_b64 v40, v[92:93] offset:2048\n"
"v_cvt_pk_bf16_f32 v100, v100, v101\n"
"v_cvt_pk_bf16_f32 v101, v102, v103\n"
"ds_write_b64 v41, v[100:101] offset:2048\n"
"v_cvt_pk_bf16_f32 v108, v108, v109\n"
"v_cvt_pk_bf16_f32 v109, v110, v111\n"
"ds_write_b64 v42, v[108:109] offset:2048\n"
"v_cvt_pk_bf16_f32 v116, v116, v117\n"
"v_cvt_pk_bf16_f32 v117, v118, v119\n"
"ds_write_b64 v43, v[116:117] offset:2048\n"
"ds_read_b128 v[136:139], v34 offset:2048\n"
"ds_read_b128 v[140:143], v34 offset:3072\n"
"s_waitcnt lgkmcnt(7)\n"
"buffer_store_dwordx4 v[128:131], v33, s[44:47], s34 offen nt\n"
"s_add_u32 s34, s34, 16384\n"
"s_waitcnt lgkmcnt(6)\n"
"buffer_store_dwordx4 v[132:135], v33, s[44:47], s34 offen nt\n"
"s_add_u32 s34, s34, 16384\n"
"s_waitcnt lgkmcnt(1)\n"
"buffer_store_dwordx4 v[136:139], v33, s[44:47], s34 offen nt\n"
"s_add_u32 s34, s34, 16384\n"
"s_waitcnt lgkmcnt(0)\n"
"buffer_store_dwordx4 v[140:143], v33, s[44:47], s34 offen nt\n"
"s_add_u32 s34, s34, 16384\n"
"s_endpgm\n"
"LEND_%=:\n"
  :: "s"(__builtin_amdgcn_kernarg_segment_ptr()), "s"(blockIdx.x), "v"(threadIdx.x), "s"((unsigned)(size_t)lds)
  : "v1","v2","v3","v4","v5","v6","v7","v8","v9","v10","v11","v12","v13","v14","v15","v16","v17","v18","v19","v20","v21","v22","v23","v24","v25","v26","v27","v28","v29","v30","v31","v32","v33","v34","v35","v36","v37","v38","v39","v40","v41","v42","v43","v44","v45","v46","v47","v48","v49","v50","v51","v52","v53","v54","v55","v56","v57","v58","v59","v60","v61","v62","v63","v64","v65","v66","v67","v68","v69","v70","v71","v72","v73","v74","v75","v76","v77","v78","v79","v80","v81","v82","v83","v84","v85","v86","v87","v88","v89","v90","v91","v92","v93","v94","v95","v96","v97","v98","v99","v100","v101","v102","v103","v104","v105","v106","v107","v108","v109","v110","v111","v112","v113","v114","v115","v116","v117","v118","v119","v120","v121","v122","v123","v124","v125","v126","v127","v128","v129","v130","v131","v132","v133","v134","v135","v136","v137","v138","v139","v140","v141","v142","v143","v144","v145","v146","v147","v148","v149","v150","v151","v152","v153","v154","v155","v156","v157","v158","v159","v160","v161","v162","v163","v164","v165","v166","v167","v168","v169","v170","v171","v172","v173","v174","v175","v176","v177","v178","v179","v180","v181","v182","v183","v184","v185","v186","v187","v188","v189","v190","v191","v192","v193","v194","v195","v196","v197","v198","v199","v200","v201","v202","v203","v204","v205","v206","v207","v208","v209","v210","v211","v212","v213","v214","v215","v216","v217","v218","v219","v220","v221","v222","v223","v224","v225","v226","v227","v228","v229","v230","v231","v232","v233","v234","v235","v236","v237","v238","v239","v240","v241","v242","v243","v244","v245","v246","v247","v248","v249","v250","v251","v252","v253","v254","v255","a0","a1","a2","a3","a4","a5","a6","a7","a8","a9","a10","a11","a12","a13","a14","a15","a16","a17","a18","a19","a20","a21","a22","a23","a24","a25","a26","a27","a28","a29","a30","a31","a32","a33","a34","a35","a36","a37","a38","a39","a40","a41","a42","a43","a44","a45","a46","a47","a48","a49","a50","a51","a52","a53","a54","a55","a56","a57","a58","a59","a60","a61","a62","a63","a64","a65","a66","a67","a68","a69","a70","a71","a72","a73","a74","a75","a76","a77","a78","a79","a80","a81","a82","a83","a84","a85","a86","a87","a88","a89","a90","a91","a92","a93","a94","a95","a96","a97","a98","a99","a100","a101","a102","a103","a104","a105","a106","a107","a108","a109","a110","a111","a112","a113","a114","a115","a116","a117","a118","a119","a120","a121","a122","a123","a124","a125","a126","a127","a128","a129","a130","a131","a132","a133","a134","a135","a136","a137","a138","a139","a140","a141","a142","a143","a144","a145","a146","a147","a148","a149","a150","a151","a152","a153","a154","a155","a156","a157","a158","a159","a160","a161","a162","a163","a164","a165","a166","a167","a168","a169","a170","a171","a172","a173","a174","a175","a176","a177","a178","a179","a180","a181","a182","a183","a184","a185","a186","a187","a188","a189","a190","a191","a192","a193","a194","a195","a196","a197","a198","a199","a200","a201","a202","a203","a204","a205","a206","a207","a208","a209","a210","a211","a212","a213","a214","a215","a216","a217","a218","a219","a220","a221","a222","a223","a224","a225","a226","a227","a228","a229","a230","a231","a232","a233","a234","a235","a236","a237","a238","a239","a240","a241","a242","a243","a244","a245","a246","a247","a248","a249","a250","a251","a252","a253","a254","a255","s4","s5","s6","s7","s8","s9","s10","s11","s12","s13","s14","s15","s16","s17","s18","s19","s20","s21","s22","s23","s24","s25","s26","s27","s28","s29","s30","s31","s33","s34","s35","s36","s37","s38","s39","s40","s41","s42","s43","s44","s45","s46","s47","s48","s49","s50","s51","s52","s53","s54","s55","s56","s57","s58","s59","s60","s61","s62","s63","s64","s65","s66","s67","s68","s69","s70","s71","s72","s73","s74","s75","s76","s77","s78","s79","s80","s81","s82","s83","s84","s85","s86","s87","s88","s89","s90","s91","s92","s93","s94","s95","s96","s97","s98","s99","vcc","scc","memory");
}

extern "C" void kernel_launch(void* const* d_in, const int* in_sizes, int n_in, void* d_out, int out_size, void* d_ws, size_t ws_size,
                              hipStream_t stream) {
  sequential_preLN__grouped_kv_mqa__mixer_gmlp__prompt_kernel<<<256, 256, 0, stream>>>(
      d_in[0], d_in[1], d_in[2], d_in[3], d_in[4], d_in[6], d_in[7], d_in[8], d_in[9], d_out, d_ws, d_ws);
}
```
